```python
import torch

def _softmax(x, axis=-1):
    return torch.softmax(x, dim=axis)
def _gelu(x):
    return torch.nn.functional.gelu(x, approximate="tanh")
def _silu(x):
    return torch.nn.functional.silu(x)
def _sigmoid(x):
    return torch.sigmoid(x)
def _layernorm(x, axis=-1, eps=1e-5):
    m = x.mean(dim=axis, keepdim=True)
    v = x.var(dim=axis, keepdim=True, unbiased=False)
    return (x - m) / torch.sqrt(v + eps)
def _scatter_add(tgt, idx, src, axis=0):
    return tgt.clone().index_add_(axis, idx, src)
def _segment_sum(src, seg, n):
    out = torch.zeros((n,) + src.shape[1:], dtype=src.dtype)
    return out.index_add_(0, seg, src)
def _pad(x, pw):
    flat = tuple(v for pair in reversed(pw) for v in pair)
    return torch.nn.functional.pad(x, flat)

kernel_name = 'interleave_layers__sliding_block__mixer_token_mlp__prompt'

def setup_inputs():

    _w = (lambda a: (lambda t: t.to(torch.bfloat16) if t.is_floating_point() else t)(torch.as_tensor(a)))
    def _A_setup():
        B, S, D = 1, 2048, 448
        return {
            "x": _w((lambda *s: torch.randn(*s, dtype=torch.bfloat16) * 0.05)(B, S, D)),
            "wqkv": _w((lambda *s: torch.randn(*s, dtype=torch.bfloat16) * 0.05)(D, 3 * D)),
            "wo": _w((lambda *s: torch.randn(*s, dtype=torch.bfloat16) * 0.05)(D, D)),
        }
    _A_in = _A_setup()

    def _B_setup():
        B, S, D, F = 1, 2048, 448, 1792
        return {
            "x": _w((lambda *s: torch.randn(*s, dtype=torch.bfloat16) * 0.05)(B, S, D)),
            "w1": _w((lambda *s: torch.randn(*s, dtype=torch.bfloat16) * 1.6)(S, F) * (S ** -0.5)),
            "b1": _w((lambda *s: torch.randn(*s, dtype=torch.bfloat16) * 12.8)(F) * 0.02),
            "w2": _w((lambda *s: torch.randn(*s, dtype=torch.bfloat16) * 0.8)(F, S) * (F ** -0.5)),
            "b2": _w((lambda *s: torch.randn(*s, dtype=torch.bfloat16) * 12.8)(S) * 0.02),
        }
    _B_in = _B_setup()

    return {
        "A_x": _A_in["x"],
        "A_wqkv": _A_in["wqkv"],
        "A_wo": _A_in["wo"],
        "B_x": _B_in["x"],
        "B_w1": _B_in["w1"],
        "B_b1": _B_in["b1"],
        "B_w2": _B_in["w2"],
        "B_b2": _B_in["b2"],
    }

def reference(A_x, A_wqkv, A_wo, B_x, B_w1, B_b1, B_w2, B_b2):
    def _A_ref(x, wqkv, wo):
        B, S, D = x.shape
        H = 16; Dh = D // H; W = 256
        qkv = (lambda x, *d: x.permute(*d))((x @ wqkv).reshape(B, S, 3, H, Dh), 2, 0, 3, 1, 4)
        q, k, v = qkv[0], qkv[1], qkv[2]
        scores = torch.einsum("bhqd,bhkd->bhqk", q, k) * (Dh ** -0.5)
        idx = torch.arange(S)
        mask = (idx[:, None] - idx[None, :] >= 0) & (idx[:, None] - idx[None, :] < W)
        scores = torch.where(mask[None, None], scores, -1e9)
        p = _softmax(scores, axis=-1)
        o = torch.einsum("bhqk,bhkd->bhqd", p, v)
        return (lambda x, *d: x.permute(*d))(o, 0, 2, 1, 3).reshape(B, S, D) @ wo

    def _B_ref(x, w1, b1, w2, b2):
        B, S, D = x.shape
        h = _layernorm(x, axis=-1)
        u = torch.einsum("bsd,sf->bdf", h, w1) + b1
        u = _gelu(u)
        y = torch.einsum("bdf,fs->bds", u, w2) + b2
        return (lambda x, *d: x.permute(*d))(y, 0, 2, 1)

    x = A_x
    x = x + _A_ref(_layernorm(x, axis=-1), A_wqkv, A_wo)
    x = x + _B_ref(_layernorm(x, axis=-1), B_w1, B_b1, B_w2, B_b2)
    x = x + _A_ref(_layernorm(x, axis=-1), A_wqkv, A_wo)
    x = x + _B_ref(_layernorm(x, axis=-1), B_w1, B_b1, B_w2, B_b2)
    return x
```

```cpp
#include <hip/hip_runtime.h>
#include <hip/hip_bf16.h>
typedef unsigned short u16;
constexpr int S=2048, D=448, H=16, DH=28, W=256, F=1792;
__device__ __forceinline__ float bf2f(u16 b){ return __uint_as_float(((unsigned)b)<<16); }
__device__ __forceinline__ u16 f2bf(float f){ unsigned u=__float_as_uint(f); u += 0x7FFFu + ((u>>16)&1u); return (u16)(u>>16); }
__device__ __forceinline__ float rb(float f){ return bf2f(f2bf(f)); }

__global__ void k_ln(const u16* x, u16* y, int reps){
  int row = blockIdx.x; int lane = threadIdx.x;
  float v[7];
  for(int i=0;i<7;i++) v[i]=bf2f(x[row*D+lane+64*i]);
  for(int r=0;r<reps;r++){
    float s=0; for(int i=0;i<7;i++) s+=v[i];
    for(int o=32;o>0;o>>=1) s+=__shfl_xor(s,o);
    float m=s/D; float q=0;
    for(int i=0;i<7;i++){ float d=v[i]-m; q+=d*d; }
    for(int o=32;o>0;o>>=1) q+=__shfl_xor(q,o);
    float rs=rsqrtf(q/D+1e-5f);
    for(int i=0;i<7;i++) v[i]=rb((v[i]-m)*rs);
  }
  for(int i=0;i<7;i++) y[row*D+lane+64*i]=f2bf(v[i]);
}
__global__ void k_gemm_nn(const u16* A, const u16* B, u16* C, const u16* res, int M,int N,int K){
  int n = blockIdx.x*64+threadIdx.x; int m = blockIdx.y;
  if(n>=N) return;
  float acc=0;
  for(int k=0;k<K;k++) acc += bf2f(A[m*K+k])*bf2f(B[k*N+n]);
  acc = rb(acc);
  if(res) acc += bf2f(res[m*N+n]);
  C[m*N+n]=f2bf(acc);
}
__global__ void k_attn(const u16* qkv, u16* o){
  int s = blockIdx.x; int h = blockIdx.y; int t = threadIdx.x;
  __shared__ float p[256]; __shared__ float red[4];
  int key = s - t; float sc=-1e30f;
  if(key>=0){ float a=0; for(int d=0;d<DH;d++) a += bf2f(qkv[s*3*D+h*DH+d])*bf2f(qkv[key*3*D+D+h*DH+d]); sc = a*0.1889822365f; }
  float m=sc; for(int o2=32;o2>0;o2>>=1) m=fmaxf(m,__shfl_xor(m,o2));
  if((t&63)==0) red[t>>6]=m; __syncthreads();
  m=fmaxf(fmaxf(red[0],red[1]),fmaxf(red[2],red[3])); __syncthreads();
  float e = key>=0? __expf(sc-m):0.f; float l=e;
  for(int o2=32;o2>0;o2>>=1) l+=__shfl_xor(l,o2);
  if((t&63)==0) red[t>>6]=l; __syncthreads();
  l=red[0]+red[1]+red[2]+red[3];
  p[t]=rb(e/l); __syncthreads();
  if(t<DH){ float a=0; for(int j=0;j<256;j++){ int k2=s-j; if(k2>=0) a+=p[j]*bf2f(qkv[k2*3*D+2*D+h*DH+t]); } o[s*D+h*DH+t]=f2bf(a); }
}
__global__ void k_g1(const u16* h, const u16* w1, const u16* b1, u16* u){
  int f = blockIdx.x*64+threadIdx.x; int d = blockIdx.y;
  float acc=0; for(int s=0;s<S;s++) acc += bf2f(h[s*D+d])*bf2f(w1[s*F+f]);
  float x = rb(rb(acc)+bf2f(b1[f]));
  float g = 0.5f*x*(1.f+tanhf(0.7978845608f*(x+0.044715f*x*x*x)));
  u[d*F+f]=f2bf(g);
}
__global__ void k_g2(const u16* u, const u16* w2, const u16* b2, const u16* res, u16* out){
  int s = blockIdx.x*64+threadIdx.x; int d = blockIdx.y;
  float acc=0; for(int f=0;f<F;f++) acc += bf2f(u[d*F+f])*bf2f(w2[f*S+s]);
  float y = rb(rb(acc)+bf2f(b2[s]));
  out[s*D+d]=f2bf(y+bf2f(res[s*D+d]));
}
extern "C" void kernel_launch(void* const* d_in, const int* in_sizes, int n_in, void* d_out, int out_size, void* d_ws, size_t ws_size, hipStream_t stream){
  const u16* x=(const u16*)d_in[0]; const u16* wqkv=(const u16*)d_in[1]; const u16* wo=(const u16*)d_in[2];
  const u16* w1=(const u16*)d_in[4]; const u16* b1=(const u16*)d_in[5]; const u16* w2=(const u16*)d_in[6]; const u16* b2=(const u16*)d_in[7];
  u16* ws=(u16*)d_ws; u16* ln=ws; u16* qkv=ln+S*D; u16* o=qkv+S*3*D; u16* x1=o+S*D; u16* u=x1+S*D; u16* x2=u+D*F;
  const u16* cur=x;
  for(int l=0;l<2;l++){
    k_ln<<<S,64,0,stream>>>(cur,ln,1);
    k_gemm_nn<<<dim3(21,S),64,0,stream>>>(ln,wqkv,qkv,nullptr,S,3*D,D);
    k_attn<<<dim3(S,H),256,0,stream>>>(qkv,o);
    k_gemm_nn<<<dim3(7,S),64,0,stream>>>(o,wo,x1,cur,S,D,D);
    k_ln<<<S,64,0,stream>>>(x1,ln,2);
    k_g1<<<dim3(F/64,D),64,0,stream>>>(ln,w1,b1,u);
    u16* dst = l==0? x2 : (u16*)d_out;
    k_g2<<<dim3(S/64,D),64,0,stream>>>(u,w2,b2,x1,dst);
    cur=x2;
  }
}
```

```cpp
#include <hip/hip_runtime.h>

#define GAS __attribute__((address_space(1)))
#define LAS __attribute__((address_space(3)))
#define DI __device__ __forceinline__
#define RLX_AGENT __ATOMIC_RELAXED, __HIP_MEMORY_SCOPE_AGENT
typedef unsigned v4u __attribute__((ext_vector_type(4)));
typedef unsigned v2u __attribute__((ext_vector_type(2)));
typedef float f32x2 __attribute__((ext_vector_type(2)));
typedef float f32x4 __attribute__((ext_vector_type(4)));
typedef float f32x16 __attribute__((ext_vector_type(16)));
typedef short s16x4 __attribute__((ext_vector_type(4)));
typedef __bf16 bf16x2 __attribute__((ext_vector_type(2)));
typedef __bf16 bf16x8 __attribute__((ext_vector_type(8)));
typedef __amdgpu_buffer_rsrc_t rsrc_t;

constexpr int S = 2048, D = 448, F = 1792;
constexpr float EPS = 1e-5f;
constexpr int FLAG_OFF = 4096;
constexpr int KV_OFF = 512 * 1024, KV_SZ = 16 * 64 * 4096;
constexpr int O_OFF = KV_OFF + 2 * KV_SZ, O_SZ = 32 * 16 * 4096;
constexpr int S3_OFF = O_OFF + 2 * O_SZ, S3_SZ = 32 * 8 * 1024;
constexpr int HT_OFF = S3_OFF + 2 * S3_SZ, HT_SZ = 7 * 64 * 4096;
constexpr int U_OFF = HT_OFF + 2 * HT_SZ, U_SZ = 7 * 56 * 4096;
constexpr int X2_OFF = U_OFF + 2 * U_SZ, X2_SZ = 64 * 28 * 1024;
constexpr int S5_OFF = X2_OFF + X2_SZ, S5_SZ = 32 * 8 * 512;
constexpr int DBG_OFF = S5_OFF + S5_SZ;
constexpr int WS_BYTES = DBG_OFF + 256 * 64 * 8;
constexpr int L_WQ = 0;
constexpr int L_KVW = 86016;
constexpr int L_STG = L_KVW + 49152;
constexpr int L_CSP = L_STG + 16384;
constexpr int L_CS = L_CSP + 1536;
constexpr int L_ST = L_CS + 384;
constexpr int LDS_BYTES = L_ST + 1024;

constexpr int SC1 = 16;
#define WIN4 6
#define WIN5 6
#define NPRE4 16
#define NPRE5 4

struct Params {
  const void *x, *wqkv, *wo, *w1, *b1, *w2, *b2;
  void *out, *ws;
};

DI float asf(unsigned u) { return __builtin_bit_cast(float, u); }
DI unsigned asu(float f) { return __builtin_bit_cast(unsigned, f); }
DI bf16x2 asb2(unsigned u) { return __builtin_bit_cast(bf16x2, u); }
DI rsrc_t mk(const void* p, int bytes) { return __builtin_amdgcn_make_buffer_rsrc((void*)p, 0, bytes, 0x00020000); }
template <int AUX> DI v4u ld16(rsrc_t r, int voff, int soff) { return __builtin_amdgcn_raw_buffer_load_b128(r, voff, soff, AUX); }
template <int AUX> DI v2u ld8(rsrc_t r, int voff, int soff) { return __builtin_amdgcn_raw_buffer_load_b64(r, voff, soff, AUX); }
template <int AUX> DI void st16(v4u v, rsrc_t r, int voff, int soff) { __builtin_amdgcn_raw_buffer_store_b128(v, r, voff, soff, AUX); }
template <int AUX> DI void st8(v2u v, rsrc_t r, int voff, int soff) { __builtin_amdgcn_raw_buffer_store_b64(v, r, voff, soff, AUX); }
DI f32x4 mfma16(v4u a, v4u b, f32x4 c) {
  return __builtin_amdgcn_mfma_f32_16x16x32_bf16(__builtin_bit_cast(bf16x8, a), __builtin_bit_cast(bf16x8, b), c, 0, 0, 0);
}
DI f32x16 mfma32(v4u a, v4u b, f32x16 c) {
  return __builtin_amdgcn_mfma_f32_32x32x16_bf16(__builtin_bit_cast(bf16x8, a), __builtin_bit_cast(bf16x8, b), c, 0, 0, 0);
}
DI unsigned pk(float lo, float hi) { return __builtin_bit_cast(unsigned, __builtin_convertvector((f32x2){lo, hi}, bf16x2)); }
DI float bflo(unsigned u) { return asf(u << 16); }
DI float bfhi(unsigned u) { return asf(u & 0xffff0000u); }
DI v2u trrd(LAS char* p) { return __builtin_bit_cast(v2u, __builtin_amdgcn_ds_read_tr16_b64_v4i16((LAS s16x4*)p)); }
DI v4u trfrag(LAS char* p0, LAS char* p1) { v2u a = trrd(p0), b = trrd(p1); return (v4u){a.x, a.y, b.x, b.y}; }
DI float xor32(float v) {
  unsigned u = asu(v);
  auto r = __builtin_amdgcn_permlane32_swap(u, u, false, false);
  return asf((threadIdx.x & 32) ? r[0] : r[1]);
}
DI float sum32(float v) {
  unsigned u = asu(v);
  auto r = __builtin_amdgcn_permlane32_swap(u, u, false, false);
  return asf(r[0]) + asf(r[1]);
}
DI float max32(float v) {
  unsigned u = asu(v);
  auto r = __builtin_amdgcn_permlane32_swap(u, u, false, false);
  return fmaxf(asf(r[0]), asf(r[1]));
}
DI float sum16(float v) {
  unsigned u = asu(v);
  auto r = __builtin_amdgcn_permlane16_swap(u, u, false, false);
  return asf(r[0]) + asf(r[1]);
}
#define FENCE() asm volatile("" ::: "memory")
#define SCHED() __builtin_amdgcn_sched_barrier(0)
#define DRAIN() asm volatile("s_waitcnt vmcnt(0)" ::: "memory")
DI void bar() { asm volatile("s_waitcnt lgkmcnt(0)" ::: "memory"); __builtin_amdgcn_s_barrier(); FENCE(); }

#define STAMP(i) do {} while (0)

DI void publish(GAS unsigned* flag, unsigned epoch) {
  DRAIN();
  bar();
  if (threadIdx.x == 0) __hip_atomic_store(flag, epoch, RLX_AGENT);
  FENCE();
}
DI void wait_flags(GAS unsigned* flag, unsigned epoch, unsigned& budget) {
  FENCE();
  while (true) {
    unsigned f = __hip_atomic_load(flag, RLX_AGENT);
    if (__builtin_amdgcn_ballot_w64(f != epoch) == 0ull) break;
    if (budget == 0u) break;
    --budget;
  }
  __builtin_amdgcn_fence(__ATOMIC_ACQUIRE, "wavefront");
  FENCE();
}

DI void convert(v4u (&r)[4], LAS char* stg, int wa0, int wa1, int ra0, int ra1, int rot) {
  *(LAS v4u*)(stg + wa0) = r[0];
  *(LAS v4u*)(stg + wa1 + 1024) = r[1];
  *(LAS v4u*)(stg + wa0 + 2048) = r[2];
  *(LAS v4u*)(stg + wa1 + 3072) = r[3];
#pragma unroll
  for (int i = 0; i < 4; ++i) {
    const int x = ((i + rot) & 3) << 5;
    r[i] = trfrag(stg + (ra0 ^ x), stg + (ra1 ^ x));
  }
}
DI int swz(int row) { return ((row >> 1) & 1) | (((row >> 3) ^ (row >> 2)) & 1) << 1; }

DI void reduce4(f32x4 (&acc)[4][4], f32x4 (&o)[4], LAS char* L, int w, int lane) {
  LAS char* red = L + L_KVW + lane * 16;
#pragma unroll
  for (int b = 1; b < 4; ++b)
#pragma unroll
    for (int a = 0; a < 4; ++a) *(LAS f32x4*)(red + (((b + w) & 3) * 12 + (b - 1) * 4 + a) * 1024) = acc[a][b];
  bar();
#pragma unroll
  for (int a = 0; a < 4; ++a) {
    f32x4 v = acc[a][0];
#pragma unroll
    for (int s = 0; s < 3; ++s) v += *(LAS f32x4*)(red + (w * 12 + s * 4 + a) * 1024);
    o[a] = v;
  }
}

#define FRESH() \
  int tid = threadIdx.x, bid = blockIdx.x; \
  asm volatile("" : "+v"(tid), "+s"(bid)); \
  const int lane = tid & 63, w = __builtin_amdgcn_readfirstlane(tid >> 6), bx = bid & 7, by = bid >> 3; \
  const int c = lane & 15, g = lane >> 4, q4 = (lane & 15) >> 2, p4 = lane & 3, r32 = lane & 31, hh = lane >> 5; \
  const int t = 2 * bx + (by >> 4), hd = by & 15;                        \
  const int st = 4 * bx + by / 7, dt = by % 7;                           \
  const int dt4 = (bx & 1) * 4 + by / 7, ft = 7 * (bx >> 1) + by % 7;    \
  LAS char* stg = L + L_STG + w * 4096; \
  const int wrow = lane >> 3; \
  const int wa0 = wrow * 128 + (((lane & 7) ^ (swz(wrow) << 1)) << 4); \
  const int wa1 = wrow * 128 + (((lane & 7) ^ (swz(wrow + 8) << 1)) << 4); \
  auto raddr = [&](int row) { return row * 128 + (swz(row) << 5) + 8 * p4; }; \
  (void)c; (void)g; (void)q4; (void)r32; (void)hh; (void)t; (void)hd; (void)st; (void)dt; (void)dt4; (void)ft; (void)stg; (void)wa0; (void)wa1; (void)raddr;

__global__ void __launch_bounds__(256, 1) fused_kernel(Params p) {
  extern __shared__ __attribute__((aligned(16))) char smem[];
  LAS char* L = (LAS char*)smem;
  const bool on35 = (blockIdx.x >> 3) < 28;
  const bool on4 = (blockIdx.x >> 3) < ((blockIdx.x & 1) ? 21 : 28);
  const rsrc_t rws = mk(p.ws, WS_BYTES);
  GAS unsigned* ctrl = (GAS unsigned*)p.ws;
  GAS unsigned* flags = (GAS unsigned*)((GAS char*)p.ws + FLAG_OFF);
  STAMP(0);
  const unsigned epoch = __hip_atomic_load(ctrl, RLX_AGENT) + 1u;
  unsigned budget = 1u << 22;

  v4u xf[28];
  float mean = 0.f, rstd = 0.f;
  {
    FRESH();
    const rsrc_t rwq = mk(p.wqkv, D * 3 * D * 2);
    const rsrc_t rx = mk(p.x, S * D * 2);
    v4u wr[21];
    {
      const int vo = (w * 112 + (lane >> 2)) * 2688 + hd * 56 + (lane & 3) * 16;
#pragma unroll
      for (int i = 0; i < 21; ++i) wr[i] = ld16<0>(rwq, vo, (i % 7) * 16 * 2688 + (i / 7) * 896);
    }
    {
      const int vo = (t * 128 + w * 32 + wrow) * 896 + (lane & 7) * 16;
#pragma unroll
      for (int i = 0; i < 28; ++i) xf[i] = ld16<0>(rx, vo, (i & 3) * 8 * 896 + (i >> 2) * 128);
    }
    SCHED();
#pragma unroll
    for (int i = 0; i < 21; ++i) *(LAS v4u*)(L + L_WQ + (i / 7) * 28672 + (w * 112 + (i % 7) * 16) * 64 + lane * 16) = wr[i];
    {
      const v4u ones = {0x3f803f80u, 0x3f803f80u, 0x3f803f80u, 0x3f803f80u};
      LAS char* wb = L + L_WQ + (w * 112 + 8 * hh + q4) * 64 + (g & 1) * 32 + 8 * p4;
#pragma unroll
      for (int part = 0; part < 3; ++part) {
        f32x16 a = {};
#pragma unroll
        for (int ks = 0; ks < 7; ++ks) a = mfma32(ones, trfrag(wb + part * 28672 + ks * 1024, wb + part * 28672 + ks * 1024 + 256), a);
        if (lane < 32) *(LAS float*)(L + L_CSP + (w * 96 + part * 32 + lane) * 4) = a[0];
      }
    }
    {
      const int xs0 = (wrow >> 1), xs1 = 4 + (wrow >> 1);
      const int xw0 = wrow * 128 + (((lane & 7) ^ xs0) << 4), xw1 = wrow * 128 + (((lane & 7) ^ xs1) << 4);
      const int xr = r32 * 128 + 8 * hh, xsw = (r32 >> 1) & 7;
      float s1 = 0.f, s2 = 0.f;
#pragma unroll
      for (int cc = 0; cc < 7; ++cc) {
        *(LAS v4u*)(stg + xw0) = xf[4 * cc];
        *(LAS v4u*)(stg + xw1 + 1024) = xf[4 * cc + 1];
        *(LAS v4u*)(stg + xw0 + 2048) = xf[4 * cc + 2];
        *(LAS v4u*)(stg + xw1 + 3072) = xf[4 * cc + 3];
#pragma unroll
        for (int e = 0; e < 4; ++e) {
          const int ch = (e >> 1) * 4 + (e & 1);
          v2u a = *(LAS v2u*)(stg + xr + ((ch ^ xsw) << 4)), b = *(LAS v2u*)(stg + xr + (((ch + 2) ^ xsw) << 4));
          v4u f = {a.x, a.y, b.x, b.y};
          xf[4 * cc + e] = f;
#pragma unroll
          for (int j = 0; j < 4; ++j) {
            const bf16x2 v = asb2(f[j]);
            s1 = __builtin_amdgcn_fdot2_f32_bf16(v, asb2(0x3f803f80u), s1, false);
            s2 = __builtin_amdgcn_fdot2_f32_bf16(v, v, s2, false);
          }
        }
      }
      s1 = sum32(s1); s2 = sum32(s2);
      mean = s1 * (1.f / D);
      rstd = __builtin_amdgcn_rsqf(fmaxf(s2 * (1.f / D) - mean * mean, 0.f) + EPS);
    }
    bar();
    if (tid < 96) {
      LAS float* cp = (LAS float*)(L + L_CSP);
      *(LAS float*)(L + L_CS + tid * 4) = (cp[tid] + cp[96 + tid]) + (cp[192 + tid] + cp[288 + tid]);
    }
    bar();
  }
  f32x4 res[4];
#pragma unroll
  for (int a = 0; a < 4; ++a) res[a] = (f32x4){0.f, 0.f, 0.f, 0.f};

#pragma unroll 1
  for (int layer = 0;; ++layer) {
    GAS unsigned* fl = flags + layer * 5 * 256 * 32;
    STAMP(1 + layer * 16);
    v4u qf[2];
    {
      FRESH();
      LAS float* stt = (LAS float*)(L + L_ST + w * 256);
      if (lane < 32) { stt[lane] = mean; stt[32 + lane] = rstd; }
      LAS char* wb = L + L_WQ + (4 * hh + q4) * 64 + (g & 1) * 32 + 8 * p4;
      auto wfrag = [&](int part, int ks) {
        LAS char* a = wb + part * 28672 + (ks >> 1) * 2048 + (ks & 1) * 512;
        return trfrag(a, a + 1024);
      };
      f32x16 ak = {}, av = {};
#pragma unroll
      for (int ks = 0; ks < 28; ++ks) {
        ak = mfma32(wfrag(1, ks), xf[ks], ak);
        av = mfma32(xf[ks], wfrag(2, ks), av);
      }
      v4u kv[4];
      const float nm = -mean * rstd;
#pragma unroll
      for (int a = 0; a < 4; ++a) {
        const f32x4 cs = *(LAS f32x4*)(L + L_CS + (32 + 8 * a + 4 * hh) * 4);
        float k0 = ak[4 * a] * rstd + nm * cs[0], k1 = ak[4 * a + 1] * rstd + nm * cs[1];
        float k2 = ak[4 * a + 2] * rstd + nm * cs[2], k3 = ak[4 * a + 3] * rstd + nm * cs[3];
        kv[a >> 1][(a & 1) * 2] = pk(k0, k1);
        kv[a >> 1][(a & 1) * 2 + 1] = pk(k2, k3);
      }
      const float csv = *(LAS float*)(L + L_CS + (64 + r32) * 4);
#pragma unroll
      for (int a = 0; a < 4; ++a) {
        const f32x4 mm = *(LAS f32x4*)(stt + 8 * a + 4 * hh), rr = *(LAS f32x4*)(stt + 32 + 8 * a + 4 * hh);
        float v[4];
#pragma unroll
        for (int j = 0; j < 4; ++j) v[j] = (av[4 * a + j] - mm[j] * csv) * rr[j];
        unsigned u0 = pk(v[0], v[1]), u1 = pk(v[2], v[3]);
        if (r32 == 28) { u0 = 0x3f803f80u; u1 = 0x3f803f80u; }
        kv[2 + (a >> 1)][(a & 1) * 2] = u0;
        kv[2 + (a >> 1)][(a & 1) * 2 + 1] = u1;
      }
#pragma unroll
      for (int i = 0; i < 4; ++i) {
        if (t < 15) st16<SC1>(kv[i], rws, KV_OFF + layer * KV_SZ + (hd * 64 + 4 * t + w) * 4096 + lane * 16, i * 1024);
        *(LAS v4u*)(L + L_KVW + (8 + w) * 4096 + i * 1024 + lane * 16) = kv[i];
      }
      FENCE();
      f32x16 aq = {};
#pragma unroll
      for (int ks = 0; ks < 28; ++ks) aq = mfma32(wfrag(0, ks), xf[ks], aq);
      const float qs = rstd * (0.18898223650461362f * 1.4426950408889634f), qn = -mean * qs;
#pragma unroll
      for (int a = 0; a < 4; ++a) {
        const f32x4 cs = *(LAS f32x4*)(L + L_CS + (8 * a + 4 * hh) * 4);
        unsigned u0 = pk(aq[4 * a] * qs + qn * cs[0], aq[4 * a + 1] * qs + qn * cs[1]);
        unsigned u1 = pk(aq[4 * a + 2] * qs + qn * cs[2], aq[4 * a + 3] * qs + qn * cs[3]);
        if (a == 3 && hh) { u0 = 0u; u1 = 0u; }
        qf[a >> 1][(a & 1) * 2] = u0;
        qf[a >> 1][(a & 1) * 2 + 1] = u1;
      }
    }
    publish(fl + (0 * 256 + blockIdx.x) * 32, epoch);
    STAMP(3 + layer * 16);
    v4u wo[4][4];
    {
      FRESH();
      const rsrc_t rwo = mk(p.wo, on35 ? D * D * 2 : 0);
      const int vo = (112 * w + wrow) * 896 + dt * 128 + (lane & 7) * 16;
#pragma unroll
      for (int k = 0; k < 4; ++k)
#pragma unroll
        for (int m = 0; m < 4; ++m) wo[k][m] = ld16<0>(rwo, vo, (28 * k + 8 * m) * 896);
    }
    SCHED();
    {
      FRESH();
      f32x16 sc[9];
      LAS char* kvw = L + L_KVW + w * 4096 + lane * 16;
      auto qk = [&](int j) {
        f32x16 a = {};
        a = mfma32(*(LAS v4u*)(kvw + j * 4096), qf[0], a);
        a = mfma32(*(LAS v4u*)(kvw + j * 4096 + 1024), qf[1], a);
        return a;
      };
      f32x16 neg;
#pragma unroll
      for (int i = 0; i < 16; ++i) neg[i] = -1e30f;
#pragma unroll
      for (int j = 0; j < 9; ++j) sc[j] = neg;
#pragma unroll
      for (int j = 5; j < 9; ++j)
        if (w + j >= 8) sc[j] = qk(j);
      if (t > 0) {
        const int tp = (t >= 2 && (lane & 1)) ? t - 2 : t - 1;
        wait_flags(fl + (0 * 256 + (((tp & 1) * 16 + hd) * 8 + (tp >> 1))) * 32, epoch, budget);
        STAMP(4 + layer * 16);
        v4u a[4], b[4];
        const int vo = KV_OFF + layer * KV_SZ + (hd * 64 + 4 * t - 4 + w) * 4096 + lane * 16;
#pragma unroll
        for (int i = 0; i < 4; ++i) a[i] = ld16<SC1>(rws, vo, i * 1024);
        if (t >= 2) {
#pragma unroll
          for (int i = 0; i < 4; ++i) b[i] = ld16<SC1>(rws, vo - 16384, i * 1024);
#pragma unroll
          for (int i = 0; i < 4; ++i) *(LAS v4u*)(kvw + i * 1024) = b[i];
        }
#pragma unroll
        for (int i = 0; i < 4; ++i) *(LAS v4u*)(kvw + 16384 + i * 1024) = a[i];
      }
      bar();
      const int jmin = 8 - 4 * t - w;
#pragma unroll
      for (int j = 0; j < 8; ++j)
        if (w + j < 8 && j >= jmin) sc[j] = qk(j);
#pragma unroll
      for (int i = 0; i < 16; ++i) {
        const int kl = (i & 3) + 8 * (i >> 2) + 4 * hh;
        sc[0][i] = kl > r32 ? sc[0][i] : -1e30f;
        sc[8][i] = kl <= r32 ? sc[8][i] : -1e30f;
      }
      float mx = -1e30f;
#pragma unroll
      for (int j = 0; j < 9; ++j)
#pragma unroll
        for (int i = 0; i < 16; i += 2) mx = __builtin_fmaxf(mx, __builtin_fmaxf(sc[j][i], sc[j][i + 1]));
      mx = max32(mx);
      f32x16 ao = {};
#pragma unroll
      for (int j = 0; j < 9; ++j) {
        if (j >= jmin) {
          v4u pf[2];
#pragma unroll
          for (int i = 0; i < 16; i += 2)
            pf[i >> 3][(i >> 1) & 3] = pk(__builtin_amdgcn_exp2f(sc[j][i] - mx), __builtin_amdgcn_exp2f(sc[j][i + 1] - mx));
          ao = mfma32(*(LAS v4u*)(kvw + j * 4096 + 2048), pf[0], ao);
          ao = mfma32(*(LAS v4u*)(kvw + j * 4096 + 3072), pf[1], ao);
        }
      }
      const float lo = xor32(ao[12]);
      const float inv = __builtin_amdgcn_rcpf(hh ? ao[12] : lo);
      v4u of[2];
#pragma unroll
      for (int i = 0; i < 16; i += 2) of[i >> 3][(i >> 1) & 3] = pk(ao[i] * inv, ao[i + 1] * inv);
      if (hh) { of[1][2] = 0u; of[1][3] = 0u; }
      const int vo = O_OFF + layer * O_SZ + ((2 * t + (w >> 1)) * 16 + hd) * 4096 + (2 * (w & 1) + (r32 >> 4)) * 1024 + hh * 256 + c * 16;
      st16<SC1>(of[0], rws, vo, 0);
      st16<SC1>(of[1], rws, vo, 512);
    }
    publish(fl + (1 * 256 + blockIdx.x) * 32, epoch);
    STAMP(5 + layer * 16);
    v4u wt[16][4];
    const rsrc_t rw1 = mk(p.w1, on4 ? S * F * 2 : 0);
    {
      FRESH();
      const int w1o = (512 * w + wrow) * (F * 2) + ft * 128 + (lane & 7) * 16;
#pragma unroll
      for (int k = 0; k < 8; ++k)
#pragma unroll
        for (int m = 0; m < 4; ++m) wt[k][m] = ld16<0>(rw1, w1o, (32 * k + 8 * m) * (F * 2));
    }
    SCHED();
    if (on35) {
      FRESH();
      {
        const int r0 = 16 * (g >> 1) + 4 * (g & 1) + q4;
        const int ra0 = raddr(r0), ra1 = raddr(r0 + 8);
#pragma unroll
        for (int k = 0; k < 4; ++k) convert(wo[k], stg, wa0, wa1, ra0, ra1, 0);
      }
      if (layer == 0) {
        const rsrc_t rx = mk(p.x, S * D * 2);
#pragma unroll
        for (int a = 0; a < 4; ++a) {
          const v2u v = ld8<0>(rx, (64 * st + 16 * w + c) * 896 + dt * 128 + g * 8, a * 32);
          res[a] = (f32x4){bflo(v.x), bfhi(v.x), bflo(v.y), bfhi(v.y)};
        }
      }
      const int tq = st >> 1;
      wait_flags(fl + (1 * 256 + (((tq & 1) * 16 + 4 * w + (lane & 3)) * 8 + (tq >> 1))) * 32, epoch, budget);
      STAMP(6 + layer * 16);
      v4u of[4][4];
#pragma unroll
      for (int k = 0; k < 4; ++k)
#pragma unroll
        for (int b = 0; b < 4; ++b)
          of[k][b] = ld16<SC1>(rws, O_OFF + layer * O_SZ + (st * 16 + 4 * w) * 4096 + lane * 16, k * 4096 + ((b + w) & 3) * 1024);
      SCHED();
      f32x4 acc[4][4];
#pragma unroll
      for (int a = 0; a < 4; ++a)
#pragma unroll
        for (int b = 0; b < 4; ++b) acc[a][b] = (f32x4){0.f, 0.f, 0.f, 0.f};
#pragma unroll
      for (int k = 0; k < 4; ++k)
#pragma unroll
        for (int b = 0; b < 4; ++b)
#pragma unroll
          for (int a = 0; a < 4; ++a) acc[a][b] = mfma16(wo[k][a], of[k][b], acc[a][b]);
      f32x4 o[4];
      reduce4(acc, o, L, w, lane);
      float s1 = 0.f, s2 = 0.f;
#pragma unroll
      for (int a = 0; a < 4; ++a) {
        res[a] += o[a];
#pragma unroll
        for (int j = 0; j < 4; ++j) { s1 += res[a][j]; s2 += res[a][j] * res[a][j]; }
      }
      s1 = sum32(sum16(s1)); s2 = sum32(sum16(s2));
      const int sb = S3_OFF + layer * S3_SZ + st * 8192 + (16 * w + c) * 16;
      if (g == 0) st16<SC1>((v4u){asu(s1), epoch, asu(s2), epoch}, rws, sb + dt * 1024, 0);
      STAMP(7 + layer * 16);
      FENCE();
      float t1, t2;
      while (true) {
        const v4u a = ld16<SC1>(rws, sb + g * 1024, 0), b = ld16<SC1>(rws, sb + (g < 3 ? g + 4 : g) * 1024, 0);
        t1 = asf(a.x) + (g < 3 ? asf(b.x) : 0.f);
        t2 = asf(a.z) + (g < 3 ? asf(b.z) : 0.f);
        const bool bad = (a.y != epoch) | (a.w != epoch) | (b.y != epoch) | (b.w != epoch);
        if (__builtin_amdgcn_ballot_w64(bad) == 0ull) break;
        if (budget == 0u) break;
        --budget;
        FENCE();
      }
      STAMP(8 + layer * 16);
      t1 = sum32(sum16(t1)); t2 = sum32(sum16(t2));
      const float m = t1 * (1.f / D), var = fmaxf(t2 * (1.f / D) - m * m, 0.f);
      const float r1 = __builtin_amdgcn_rsqf(var + EPS);
      const float r = r1 * __builtin_amdgcn_rsqf(var * r1 * r1 + EPS);
#pragma unroll
      for (int a = 0; a < 4; ++a) {
        const v2u v = {pk((res[a][0] - m) * r, (res[a][1] - m) * r), pk((res[a][2] - m) * r, (res[a][3] - m) * r)};
        *(LAS v2u*)(stg + c * 128 + ((a ^ swz(c)) << 5) + g * 8) = v;
      }
      const int hr = 8 * (g & 1) + q4;
      const int h0 = raddr(hr), h1 = raddr(hr + 4);
#pragma unroll
      for (int e = 0; e < 2; ++e) {
        const int x = (2 * e + (g >> 1)) << 5;
        const v4u f = trfrag(stg + (h0 ^ x), stg + (h1 ^ x));
        st16<SC1>(f, rws, HT_OFF + layer * HT_SZ + (dt * 64 + 2 * st + (w >> 1)) * 4096 + (g >> 1) * 1024 + (2 * (w & 1) + (g & 1)) * 256 + c * 16, e * 2048);
      }
      publish(fl + (2 * 256 + bid) * 32, epoch);
    }
    STAMP(9 + layer * 16);
    if (on4) {
      FRESH();
      const int w1o = (512 * w + wrow) * (F * 2) + ft * 128 + (lane & 7) * 16;
#pragma unroll
      for (int k = 8; k < 16; ++k)
#pragma unroll
        for (int m = 0; m < 4; ++m) wt[k][m] = ld16<0>(rw1, w1o, (32 * k + 8 * m) * (F * 2));
      v2u bb[4];
      {
        const rsrc_t rb1 = mk(p.b1, F * 2);
#pragma unroll
        for (int a = 0; a < 4; ++a) bb[a] = ld8<0>(rb1, ft * 128 + g * 8, a * 32);
      }
      SCHED();
      const int ra0 = raddr(8 * g + q4), ra1 = raddr(8 * g + 4 + q4);
#pragma unroll
      for (int k = 0; k < NPRE4; ++k) convert(wt[k], stg, wa0, wa1, ra0, ra1, 0);
      const int sp = 8 * w + (lane & 7);
      wait_flags(fl + (2 * 256 + ((sp & 3) * 7 + dt4) * 8 + (sp >> 2)) * 32, epoch, budget);
      STAMP(10 + layer * 16);
      v4u af[16][4];
      const int ho = HT_OFF + layer * HT_SZ + (dt4 * 64 + 16 * w) * 4096 + lane * 16;
#pragma unroll
      for (int k = 0; k < WIN4; ++k)
#pragma unroll
        for (int b = 0; b < 4; ++b) af[k][b] = ld16<SC1>(rws, ho, k * 4096 + ((b + w) & 3) * 1024);
      SCHED();
      f32x4 acc[4][4];
#pragma unroll
      for (int a = 0; a < 4; ++a)
#pragma unroll
        for (int b = 0; b < 4; ++b) acc[a][b] = (f32x4){0.f, 0.f, 0.f, 0.f};
#pragma unroll
      for (int k = 0; k < 16; ++k) {
        if (k + NPRE4 < 16) convert(wt[k + NPRE4], stg, wa0, wa1, ra0, ra1, 0);
#pragma unroll
        for (int b = 0; b < 4; ++b)
#pragma unroll
          for (int a = 0; a < 4; ++a) acc[a][b] = mfma16(wt[k][a], af[k][b], acc[a][b]);
        if (k + WIN4 < 16) {
#pragma unroll
          for (int b = 0; b < 4; ++b) af[k + WIN4][b] = ld16<SC1>(rws, ho, (k + WIN4) * 4096 + ((b + w) & 3) * 1024);
        }
        SCHED();
      }
      STAMP(11 + layer * 16);
      f32x4 o[4];
      reduce4(acc, o, L, w, lane);
      v4u uf[2];
#pragma unroll
      for (int a = 0; a < 4; ++a) {
        const f32x4 bv = {bflo(bb[a].x), bfhi(bb[a].x), bflo(bb[a].y), bfhi(bb[a].y)};
        float gl[4];
#pragma unroll
        for (int j = 0; j < 4; ++j) {
          const float u = o[a][j] + bv[j];
          const float e = __builtin_amdgcn_exp2f(u * (u * u * (-0.10294324f) - 2.3022082f));
          gl[j] = u * __builtin_amdgcn_rcpf(1.f + e);
        }
        uf[a >> 1][(a & 1) * 2] = pk(gl[0], gl[1]);
        uf[a >> 1][(a & 1) * 2 + 1] = pk(gl[2], gl[3]);
      }
      const int uo = U_OFF + layer * U_SZ + (dt4 * 56 + 2 * ft) * 4096 + w * 1024 + lane * 16;
      st16<SC1>(uf[0], rws, uo, 0);
      st16<SC1>(uf[1], rws, uo, 4096);
      publish(fl + (3 * 256 + bid) * 32, epoch);
    }
    STAMP(12 + layer * 16);
    if (on35) {
      FRESH();
      v4u wu[14][4];
      const rsrc_t rw2 = mk(p.w2, S * F * 2);
      const int w2o = (448 * w + wrow) * (S * 2) + st * 128 + (lane & 7) * 16;
#pragma unroll
      for (int k = 0; k < 14; ++k)
#pragma unroll
        for (int m = 0; m < 4; ++m) wu[k][m] = ld16<0>(rw2, w2o, (32 * k + 8 * m) * (S * 2));
      const float b2v = bflo((unsigned)__builtin_amdgcn_raw_buffer_load_b16(mk(p.b2, S * 2), (64 * st + 16 * w + c) * 2, 0, 0));
      SCHED();
      const int ra0 = raddr(4 * g + q4), ra1 = raddr(16 + 4 * g + q4);
#pragma unroll
      for (int k = 0; k < NPRE5; ++k) convert(wu[k], stg, wa0, wa1, ra0, ra1, w);
      wait_flags(fl + (3 * 256 + ((dt & 3) * 7 + lane % 7) * 8 + 2 * w + (dt >> 2)) * 32, epoch, budget);
      STAMP(13 + layer * 16);
      v4u af[14][4];
      const int uo = U_OFF + layer * U_SZ + (dt * 56 + 14 * w) * 4096 + lane * 16;
#pragma unroll
      for (int k = 0; k < WIN5; ++k)
#pragma unroll
        for (int a = 0; a < 4; ++a) af[k][a] = ld16<SC1>(rws, uo, k * 4096 + a * 1024);
      SCHED();
      f32x4 acc[4][4];
#pragma unroll
      for (int a = 0; a < 4; ++a)
#pragma unroll
        for (int b = 0; b < 4; ++b) acc[a][b] = (f32x4){0.f, 0.f, 0.f, 0.f};
#pragma unroll
      for (int k = 0; k < 14; ++k) {
        if (k + NPRE5 < 14) convert(wu[k + NPRE5], stg, wa0, wa1, ra0, ra1, w);
#pragma unroll
        for (int a = 0; a < 4; ++a)
#pragma unroll
          for (int b = 0; b < 4; ++b) acc[a][b] = mfma16(af[k][a], wu[k][b], acc[a][b]);
        if (k + WIN5 < 14) {
#pragma unroll
          for (int a = 0; a < 4; ++a) af[k + WIN5][a] = ld16<SC1>(rws, uo, (k + WIN5) * 4096 + a * 1024);
        }
        SCHED();
      }
      STAMP(14 + layer * 16);
      f32x4 o[4];
      reduce4(acc, o, L, w, lane);
      v2u xo[4];
      float s1 = 0.f, s2 = 0.f;
#pragma unroll
      for (int a = 0; a < 4; ++a) {
        res[a] += o[a] + b2v;
        xo[a] = (v2u){pk(res[a][0], res[a][1]), pk(res[a][2], res[a][3])};
#pragma unroll
        for (int j = 0; j < 4; ++j) { s1 += res[a][j]; s2 += res[a][j] * res[a][j]; }
      }
      if (layer == 0) {
        s1 = sum32(sum16(s1)); s2 = sum32(sum16(s2));
        const int xb = X2_OFF + (2 * st + (w >> 1)) * 28672 + (4 * dt + (g >> 1)) * 1024 + ((g & 1) * 32 + (w & 1) * 16 + c) * 16;
        st16<SC1>((v4u){xo[0].x, xo[0].y, xo[1].x, xo[1].y}, rws, xb, 0);
        st16<SC1>((v4u){xo[2].x, xo[2].y, xo[3].x, xo[3].y}, rws, xb, 2048);
        if (g == 0) st8<SC1>((v2u){asu(s1), asu(s2)}, rws, S5_OFF + (st * 8 + dt) * 512 + (16 * w + c) * 8, 0);
        publish(flags + (4 * 256 + bid) * 32, epoch);
      } else {
        const rsrc_t ro = mk(p.out, S * D * 2);
#pragma unroll
        for (int a = 0; a < 4; ++a) st8<0>(xo[a], ro, (64 * st + 16 * w + c) * 896 + dt * 128 + g * 8, a * 32);
      }
    }
    STAMP(15 + layer * 16);
    if (layer) break;
    {
      FRESH();
      const int st1 = 2 * t + (w >> 1);
      wait_flags(flags + (4 * 256 + ((st1 & 3) * 7 + lane % 7) * 8 + (st1 >> 2)) * 32, epoch, budget);
      STAMP(18);
      v2u sp[4];
#pragma unroll
      for (int i = 0; i < 4; ++i) {
        const int dd = hh * 4 + i;
        sp[i] = ld8<SC1>(rws, S5_OFF + (st1 * 8 + (dd < 7 ? dd : 0)) * 512 + ((w & 1) * 32 + r32) * 8, 0);
      }
#pragma unroll
      for (int i = 0; i < 28; ++i) xf[i] = ld16<SC1>(rws, X2_OFF + (4 * t + w) * 28672 + lane * 16, i * 1024);
      SCHED();
      float s1 = 0.f, s2 = 0.f;
#pragma unroll
      for (int i = 0; i < 4; ++i) {
        const bool ok = (hh * 4 + i) < 7;
        s1 += ok ? asf(sp[i].x) : 0.f;
        s2 += ok ? asf(sp[i].y) : 0.f;
      }
      s1 = sum32(s1); s2 = sum32(s2);
      mean = s1 * (1.f / D);
      rstd = __builtin_amdgcn_rsqf(fmaxf(s2 * (1.f / D) - mean * mean, 0.f) + EPS);
    }
  }
  if (blockIdx.x == 0 && threadIdx.x == 0) __hip_atomic_store(ctrl, epoch, RLX_AGENT);
}

extern "C" void kernel_launch(void* const* d_in, const int* in_sizes, int n_in, void* d_out, int out_size, void* d_ws,
                              size_t ws_size, hipStream_t stream) {
  Params p;
  p.x = d_in[0]; p.wqkv = d_in[1]; p.wo = d_in[2];
  p.w1 = d_in[4]; p.b1 = d_in[5]; p.w2 = d_in[6]; p.b2 = d_in[7];
  p.out = d_out; p.ws = d_ws;
  (void)hipFuncSetAttribute((const void*)fused_kernel, hipFuncAttributeMaxDynamicSharedMemorySize, LDS_BYTES);
  fused_kernel<<<256, 256, LDS_BYTES, stream>>>(p);
}
```

```cpp
#include <hip/hip_runtime.h>

#define GAS __attribute__((address_space(1)))
#define LAS __attribute__((address_space(3)))
#define DI __device__ __forceinline__
#define RLX_AGENT __ATOMIC_RELAXED, __HIP_MEMORY_SCOPE_AGENT
typedef unsigned v4u __attribute__((ext_vector_type(4)));
typedef unsigned v2u __attribute__((ext_vector_type(2)));
typedef float f32x2 __attribute__((ext_vector_type(2)));
typedef float f32x4 __attribute__((ext_vector_type(4)));
typedef float f32x16 __attribute__((ext_vector_type(16)));
typedef short s16x4 __attribute__((ext_vector_type(4)));
typedef __bf16 bf16x2 __attribute__((ext_vector_type(2)));
typedef __bf16 bf16x8 __attribute__((ext_vector_type(8)));
typedef __amdgpu_buffer_rsrc_t rsrc_t;

constexpr int S = 2048, D = 448, F = 1792;
constexpr float EPS = 1e-5f;
constexpr int FLAG_OFF = 4096;
constexpr int KV_OFF = 512 * 1024, KV_SZ = 16 * 64 * 4096;
constexpr int O_OFF = KV_OFF + 2 * KV_SZ, O_SZ = 32 * 16 * 4096;
constexpr int S3_OFF = O_OFF + 2 * O_SZ, S3_SZ = 32 * 8 * 1024;
constexpr int HT_OFF = S3_OFF + 2 * S3_SZ, HT_SZ = 7 * 64 * 4096;
constexpr int U_OFF = HT_OFF + 2 * HT_SZ, U_SZ = 7 * 56 * 4096;
constexpr int X2_OFF = U_OFF + 2 * U_SZ, X2_SZ = 64 * 28 * 1024;
constexpr int S5_OFF = X2_OFF + X2_SZ, S5_SZ = 32 * 8 * 512;
constexpr int DBG_OFF = S5_OFF + S5_SZ;
constexpr int WS_BYTES = DBG_OFF + 256 * 64 * 8;
constexpr int L_WQ = 0;
constexpr int L_KVW = 86016;
constexpr int L_STG = L_KVW + 49152;
constexpr int L_CSP = L_STG + 16384;
constexpr int L_CS = L_CSP + 1536;
constexpr int L_ST = L_CS + 384;
constexpr int LDS_BYTES = L_ST + 1024;

constexpr int SC1 = 16;
#define WIN4 12
#define WIN5 12
#define NW1A 7
#define NW1B 13
#define NCV1 6
#define NPRE5 8

#define CODE_BYTES 41000
#define WARM_KEEP(v) warm1 = v
#define WARM_KEEP2(v) warm2 = v
struct Params {
  const void *x, *wqkv, *wo, *w1, *b1, *w2, *b2;
  void *out, *ws;
};

DI float asf(unsigned u) { return __builtin_bit_cast(float, u); }
DI unsigned asu(float f) { return __builtin_bit_cast(unsigned, f); }
DI bf16x2 asb2(unsigned u) { return __builtin_bit_cast(bf16x2, u); }
DI rsrc_t mk(const void* p, int bytes) { return __builtin_amdgcn_make_buffer_rsrc((void*)p, 0, bytes, 0x00020000); }
template <int AUX> DI v4u ld16(rsrc_t r, int voff, int soff) { return __builtin_amdgcn_raw_buffer_load_b128(r, voff, soff, AUX); }
template <int AUX> DI v2u ld8(rsrc_t r, int voff, int soff) { return __builtin_amdgcn_raw_buffer_load_b64(r, voff, soff, AUX); }
template <int AUX> DI void st16(v4u v, rsrc_t r, int voff, int soff) { __builtin_amdgcn_raw_buffer_store_b128(v, r, voff, soff, AUX); }
template <int AUX> DI void st8(v2u v, rsrc_t r, int voff, int soff) { __builtin_amdgcn_raw_buffer_store_b64(v, r, voff, soff, AUX); }
DI f32x4 mfma16(v4u a, v4u b, f32x4 c) {
  return __builtin_amdgcn_mfma_f32_16x16x32_bf16(__builtin_bit_cast(bf16x8, a), __builtin_bit_cast(bf16x8, b), c, 0, 0, 0);
}
DI f32x16 mfma32(v4u a, v4u b, f32x16 c) {
  return __builtin_amdgcn_mfma_f32_32x32x16_bf16(__builtin_bit_cast(bf16x8, a), __builtin_bit_cast(bf16x8, b), c, 0, 0, 0);
}
DI unsigned pk(float lo, float hi) { return __builtin_bit_cast(unsigned, __builtin_convertvector((f32x2){lo, hi}, bf16x2)); }
DI float bflo(unsigned u) { return asf(u << 16); }
DI float bfhi(unsigned u) { return asf(u & 0xffff0000u); }
DI v2u trrd(LAS char* p) { return __builtin_bit_cast(v2u, __builtin_amdgcn_ds_read_tr16_b64_v4i16((LAS s16x4*)p)); }
DI v4u trfrag(LAS char* p0, LAS char* p1) { v2u a = trrd(p0), b = trrd(p1); return (v4u){a.x, a.y, b.x, b.y}; }
DI float xor32(float v) {
  unsigned u = asu(v);
  auto r = __builtin_amdgcn_permlane32_swap(u, u, false, false);
  return asf((threadIdx.x & 32) ? r[0] : r[1]);
}
DI float sum32(float v) {
  unsigned u = asu(v);
  auto r = __builtin_amdgcn_permlane32_swap(u, u, false, false);
  return asf(r[0]) + asf(r[1]);
}
DI float max32(float v) {
  unsigned u = asu(v);
  auto r = __builtin_amdgcn_permlane32_swap(u, u, false, false);
  return fmaxf(asf(r[0]), asf(r[1]));
}
DI float sum16(float v) {
  unsigned u = asu(v);
  auto r = __builtin_amdgcn_permlane16_swap(u, u, false, false);
  return asf(r[0]) + asf(r[1]);
}
#define FENCE() asm volatile("" ::: "memory")
#define SCHED() __builtin_amdgcn_sched_barrier(0)
#define DRAIN() asm volatile("s_waitcnt vmcnt(0)" ::: "memory")
DI void bar() { asm volatile("s_waitcnt lgkmcnt(0)" ::: "memory"); __builtin_amdgcn_s_barrier(); FENCE(); }

#define STAMP(i) do {} while (0)

DI void publish(GAS unsigned* flag, unsigned epoch) {
  DRAIN();
  bar();
  if (threadIdx.x == 0) __hip_atomic_store(flag, epoch, RLX_AGENT);
  FENCE();
}
DI void wait_flags(GAS unsigned* flag, unsigned epoch, unsigned& budget) {
  FENCE();
  while (true) {
    unsigned f = __hip_atomic_load(flag, RLX_AGENT);
    if (__builtin_amdgcn_ballot_w64(f != epoch) == 0ull) break;
    if (budget == 0u) break;
    --budget;
  }
  __builtin_amdgcn_fence(__ATOMIC_ACQUIRE, "wavefront");
  FENCE();
}

DI void convert(v4u (&r)[4], LAS char* stg, int wa0, int wa1, int ra0, int ra1, int rot) {
  *(LAS v4u*)(stg + wa0) = r[0];
  *(LAS v4u*)(stg + wa1 + 1024) = r[1];
  *(LAS v4u*)(stg + wa0 + 2048) = r[2];
  *(LAS v4u*)(stg + wa1 + 3072) = r[3];
#pragma unroll
  for (int i = 0; i < 4; ++i) {
    const int x = ((i + rot) & 3) << 5;
    r[i] = trfrag(stg + (ra0 ^ x), stg + (ra1 ^ x));
  }
}
DI void cv_wr(const v4u& r, int m, LAS char* stg, int wa0, int wa1) { *(LAS v4u*)(stg + ((m & 1) ? wa1 : wa0) + m * 1024) = r; }
DI v4u cv_rd(int i, LAS char* stg, int ra0, int ra1, int rot) {
  const int x = ((i + rot) & 3) << 5;
  return trfrag(stg + (ra0 ^ x), stg + (ra1 ^ x));
}
DI int wrap7(int v) { return v >= 7 ? v - 7 : v; }
DI int wrap14(int v) { return v >= 14 ? v - 14 : v; }
DI int swz(int row) { return ((row >> 1) & 1) | (((row >> 3) ^ (row >> 2)) & 1) << 1; }

DI void reduce4(f32x4 (&acc)[4][4], f32x4 (&o)[4], LAS char* L, int w, int lane) {
  LAS char* red = L + L_KVW + lane * 16;
#pragma unroll
  for (int b = 1; b < 4; ++b)
#pragma unroll
    for (int a = 0; a < 4; ++a) *(LAS f32x4*)(red + (((b + w) & 3) * 12 + (b - 1) * 4 + a) * 1024) = acc[a][b];
  bar();
#pragma unroll
  for (int a = 0; a < 4; ++a) {
    f32x4 v = acc[a][0];
#pragma unroll
    for (int s = 0; s < 3; ++s) v += *(LAS f32x4*)(red + (w * 12 + s * 4 + a) * 1024);
    o[a] = v;
  }
}

#define FRESH() \
  int tid = threadIdx.x, bid = blockIdx.x; \
  asm volatile("" : "+v"(tid), "+s"(bid)); \
  const int lane = tid & 63, w = __builtin_amdgcn_readfirstlane(tid >> 6), bx = bid & 7, by = bid >> 3; \
  const int c = lane & 15, g = lane >> 4, q4 = (lane & 15) >> 2, p4 = lane & 3, r32 = lane & 31, hh = lane >> 5; \
  const int t = 2 * bx + (by >> 4), hd = by & 15;                        \
  const int st = 4 * bx + by / 7, dt = by % 7;                           \
  const int dt4 = (bx & 1) * 4 + by / 7, ft = 7 * (bx >> 1) + by % 7;    \
  LAS char* stg = L + L_STG + w * 4096; \
  const int wrow = lane >> 3; \
  const int wa0 = wrow * 128 + (((lane & 7) ^ (swz(wrow) << 1)) << 4); \
  const int wa1 = wrow * 128 + (((lane & 7) ^ (swz(wrow + 8) << 1)) << 4); \
  auto raddr = [&](int row) { return row * 128 + (swz(row) << 5) + 8 * p4; }; \
  (void)c; (void)g; (void)q4; (void)r32; (void)hh; (void)t; (void)hd; (void)st; (void)dt; (void)dt4; (void)ft; (void)stg; (void)wa0; (void)wa1; (void)raddr;

#define MFMA32V0(d, a, b) asm volatile("s_nop 1\n\tv_mfma_f32_32x32x16_bf16 %0, %1, %2, 0" : "=&v"(d) : "v"(a), "v"(b))
#define MFMA32VC(d, a, b, c) asm volatile("s_nop 1\n\tv_mfma_f32_32x32x16_bf16 %0, %1, %2, %3" : "=&v"(d) : "v"(a), "v"(b), "v"(c))
#define MFMA32VA(d, a, b) asm volatile("s_nop 1\n\tv_mfma_f32_32x32x16_bf16 %0, %1, %2, %0" : "+v"(d) : "v"(a), "v"(b))
#define MFMA_SETTLE(d) asm volatile("s_nop 11" : "+v"(d))
DI float max3(float a, float b, float c) { float r; asm("v_max3_f32 %0, %1, %2, %3" : "=v"(r) : "v"(a), "v"(b), "v"(c)); return r; }
DI float max16(float m, const f32x16& s) {
#pragma unroll
  for (int i = 0; i < 16; i += 2) m = max3(m, s[i], s[i + 1]);
  return m;
}

__global__ void __launch_bounds__(256, 1) fused_kernel(Params p) {
  extern __shared__ __attribute__((aligned(16))) char smem[];
  LAS char* L = (LAS char*)smem;
  const bool on35 = (blockIdx.x >> 3) < 28;
  const bool on4 = (blockIdx.x >> 3) < ((blockIdx.x & 1) ? 21 : 28);
  const rsrc_t rws = mk(p.ws, WS_BYTES);
  GAS unsigned* ctrl = (GAS unsigned*)p.ws;
  GAS unsigned* flags = (GAS unsigned*)((GAS char*)p.ws + FLAG_OFF);
  STAMP(0);
  unsigned warm1 = 0, warm2 = 0;

  {
    unsigned long long pc;
    asm volatile("s_getpc_b64 %0" : "=s"(pc));
    const GAS unsigned* cp = (const GAS unsigned*)(pc & ~127ull) + (threadIdx.x + ((blockIdx.x >> 3) & 1) * 256) * 32;
    unsigned v = 0;
    if ((threadIdx.x + ((blockIdx.x >> 3) & 1) * 256) * 128 < CODE_BYTES) v = *cp;
    WARM_KEEP(v);
  }
  unsigned budget = 1u << 22;

  v4u xf[28];
  f32x16 ak, av;
  float mean = 0.f, rstd = 0.f;
  f32x4 res[4];
  unsigned epoch;
#define WB(cc) (L + L_WQ + (4 * hh + q4) * 64 + (g & 1) * 32 + 8 * p4 + (hd & 1) * 8 + wrap7((cc) + hd % 7) * 4096)
#define WFRAG(wb, part, e) trfrag((wb) + (part) * 28672 + ((e) >> 1) * 2048 + ((e) & 1) * 512, (wb) + (part) * 28672 + ((e) >> 1) * 2048 + ((e) & 1) * 512 + 1024)
  {
    FRESH();
    const rsrc_t rwq = mk(p.wqkv, D * 3 * D * 2);
    const rsrc_t rx = mk(p.x, S * D * 2);
    const int rotw = by % 7, rotx = hd % 7;
    v4u wr[21];
    {
      const int vo = (w * 112 + (lane >> 2)) * 2688 + hd * 56 - (hd & 1) * 8 + (lane & 3) * 16;
#pragma unroll
      for (int i = 0; i < 21; ++i) { wr[i] = ld16<0>(rwq, vo, wrap7(i % 7 + rotw) * 16 * 2688 + (i / 7) * 896); SCHED(); }
    }
    epoch = __hip_atomic_load(ctrl, RLX_AGENT) + 1u;
    f32x16 cs[3] = {};
    {
      const v4u ones = {0x3f803f80u, 0x3f803f80u, 0x3f803f80u, 0x3f803f80u};
      LAS char* cb = L + L_WQ + (w * 112 + 8 * hh + q4) * 64 + (g & 1) * 32 + 8 * p4;
      const int vo = (t * 128 + w * 32 + wrow) * 896 + (lane & 7) * 16;
#pragma unroll
      for (int i = 0; i < 28; ++i) {
        xf[i] = ld16<0>(rx, vo, (i & 3) * 8 * 896 + wrap7((i >> 2) + rotx) * 128);
        SCHED();
        if (i >= 4 && i < 25) {
          const int j = i - 4, o = (j / 7) * 28672 + wrap7(j % 7 + rotw) * 1024;
          *(LAS v4u*)(L + L_WQ + w * 112 * 64 + lane * 16 + o) = wr[j];
          wr[j] = trfrag(cb + o, cb + o + 256);
        }
        if (i >= 6 && i < 27) cs[(i - 6) / 7] = mfma32(ones, wr[i - 6], cs[(i - 6) / 7]);
        SCHED();
      }
    }
    v2u rr[4];
    {
      const rsrc_t rxr = mk(p.x, on35 ? S * D * 2 : 0);
#pragma unroll
      for (int a = 0; a < 4; ++a) rr[a] = ld8<0>(rxr, (64 * st + 16 * w + c) * 896 + dt * 128 + g * 8, a * 32);
    }
    if (lane < 32) {
#pragma unroll
      for (int part = 0; part < 3; ++part) *(LAS float*)(L + L_CSP + (w * 96 + part * 32 + lane) * 4) = cs[part][0];
    }
    STAMP(32);
    asm volatile("" :: "v"(warm1), "v"(warm2));
    const int xs0 = (wrow >> 1), xs1 = 4 + (wrow >> 1);
    const int xw0 = wrow * 128 + (((lane & 7) ^ xs0) << 4), xw1 = wrow * 128 + (((lane & 7) ^ xs1) << 4);
    const int xr = r32 * 128 + 8 * hh, xsw = (r32 >> 1) & 7;
    auto xrd = [&](int e) {
      const int ch = (e >> 1) * 4 + (e & 1);
      const v2u a = *(LAS v2u*)(stg + xr + ((ch ^ xsw) << 4)), b = *(LAS v2u*)(stg + xr + (((ch + 2) ^ xsw) << 4));
      return (v4u){a.x, a.y, b.x, b.y};
    };
#pragma unroll
    for (int m = 0; m < 4; ++m) cv_wr(xf[m], m, stg, xw0, xw1);
#pragma unroll
    for (int e = 0; e < 4; ++e) xf[e] = xrd(e);
    bar();
    if (tid < 96) {
      LAS float* cp = (LAS float*)(L + L_CSP) + ((tid & 31) + 4 * (hd & 1) < 32 ? tid + 4 * (hd & 1) : tid);
      *(LAS float*)(L + L_CS + tid * 4) = (cp[0] + cp[96]) + (cp[192] + cp[288]);
    }
    STAMP(33);
    float s1[2] = {}, s2[2] = {};
    ak = (f32x16){}; av = (f32x16){};
    v4u wk[28], wv[28];
    {
      LAS char* wb = WB(0);
      wk[0] = WFRAG(wb, 1, 0); wv[0] = WFRAG(wb, 2, 0); wk[1] = WFRAG(wb, 1, 1); wv[1] = WFRAG(wb, 2, 1);
    }
    SCHED();
#pragma unroll
    for (int ks = 0; ks < 28; ++ks) {
      const int cc = ks >> 2, e = ks & 3;
      ak = mfma32(wk[ks], xf[ks], ak);
      if (ks + 2 < 28) { LAS char* wb = WB((ks + 2) >> 2); wk[ks + 2] = WFRAG(wb, 1, (ks + 2) & 3); wv[ks + 2] = WFRAG(wb, 2, (ks + 2) & 3); }
#pragma unroll
      for (int j = 0; j < 2; ++j) {
        const bf16x2 v = asb2(xf[ks][j]);
        s1[j] = __builtin_amdgcn_fdot2_f32_bf16(v, asb2(0x3f803f80u), s1[j], false);
        s2[j] = __builtin_amdgcn_fdot2_f32_bf16(v, v, s2[j], false);
      }
      SCHED();
      av = mfma32(xf[ks], wv[ks], av);
      if (cc + 1 < 7) {
        if (e < 2) { cv_wr(xf[4 * cc + 4 + 2 * e], 2 * e, stg, xw0, xw1); cv_wr(xf[4 * cc + 5 + 2 * e], 2 * e + 1, stg, xw0, xw1); }
        else { xf[4 * cc + 2 * e] = xrd(2 * e - 4); xf[4 * cc + 2 * e + 1] = xrd(2 * e - 3); }
      }
#pragma unroll
      for (int j = 0; j < 2; ++j) {
        const bf16x2 v = asb2(xf[ks][2 + j]);
        s1[j] = __builtin_amdgcn_fdot2_f32_bf16(v, asb2(0x3f803f80u), s1[j], false);
        s2[j] = __builtin_amdgcn_fdot2_f32_bf16(v, v, s2[j], false);
      }
      SCHED();
    }
    const float t1 = sum32(s1[0] + s1[1]), t2 = sum32(s2[0] + s2[1]);
    mean = t1 * (1.f / D);
    rstd = __builtin_amdgcn_rsqf(fmaxf(t2 * (1.f / D) - mean * mean, 0.f) + EPS);
#pragma unroll
    for (int a = 0; a < 4; ++a) res[a] = (f32x4){bflo(rr[a].x), bfhi(rr[a].x), bflo(rr[a].y), bfhi(rr[a].y)};
    bar();
  }

#pragma unroll 1
  for (int layer = 0;; ++layer) {
    GAS unsigned* fl = flags + layer * 5 * 256 * 32;
    STAMP(1 + layer * 16);
    v4u qf[2];
    v4u wo[4][4];
    {
      FRESH();
      LAS float* stt = (LAS float*)(L + L_ST + w * 256);
      if (lane < 32) { stt[lane] = mean; stt[32 + lane] = rstd; }
      v4u wq[28];
      {
        LAS char* wb = WB(0);
#pragma unroll
        for (int ks = 0; ks < 4; ++ks) wq[ks] = WFRAG(wb, 0, ks);
      }
      v4u kv[4];
      const float nm = -mean * rstd;
#pragma unroll
      for (int a = 0; a < 4; ++a) {
        const f32x4 cs = *(LAS f32x4*)(L + L_CS + (32 + 8 * a + 4 * hh) * 4);
        unsigned u0 = pk(ak[4 * a] * rstd + nm * cs[0], ak[4 * a + 1] * rstd + nm * cs[1]);
        unsigned u1 = pk(ak[4 * a + 2] * rstd + nm * cs[2], ak[4 * a + 3] * rstd + nm * cs[3]);
        if (a == 3 && hh) { u0 = 0u; u1 = 0u; }
        kv[a >> 1][(a & 1) * 2] = u0;
        kv[a >> 1][(a & 1) * 2 + 1] = u1;
      }
      const float csv = *(LAS float*)(L + L_CS + (64 + r32) * 4);
#pragma unroll
      for (int a = 0; a < 4; ++a) {
        const f32x4 mm = *(LAS f32x4*)(stt + 8 * a + 4 * hh), rr = *(LAS f32x4*)(stt + 32 + 8 * a + 4 * hh);
        float v[4];
#pragma unroll
        for (int j = 0; j < 4; ++j) v[j] = (av[4 * a + j] - mm[j] * csv) * rr[j];
        unsigned u0 = pk(v[0], v[1]), u1 = pk(v[2], v[3]);
        if (r32 == 28) { u0 = 0x3f803f80u; u1 = 0x3f803f80u; }
        kv[2 + (a >> 1)][(a & 1) * 2] = u0;
        kv[2 + (a >> 1)][(a & 1) * 2 + 1] = u1;
      }
      {
        const rsrc_t rkv = mk(p.ws, t < 15 ? WS_BYTES : 0);
#pragma unroll
        for (int i = 0; i < 4; ++i) {
          st16<SC1>(kv[i], rkv, KV_OFF + layer * KV_SZ + (hd * 64 + 4 * t + w) * 4096 + lane * 16, i * 1024);
          *(LAS v4u*)(L + L_KVW + (8 + w) * 4096 + i * 1024 + lane * 16) = kv[i];
        }
      }
      SCHED();
      STAMP(2 + layer * 16 + layer * 27);
      f32x16 aq = {};
#pragma unroll
      for (int ks = 0; ks < 28; ++ks) {
        aq = mfma32(wq[ks], xf[ks], aq);
        if (ks + 4 < 28) { LAS char* wb = WB((ks + 4) >> 2); wq[ks + 4] = WFRAG(wb, 0, ks & 3); }
        SCHED();
      }
      publish(fl + (0 * 256 + bid) * 32, epoch);
      STAMP(3 + layer * 16);
      {
        const rsrc_t rwo = mk(p.wo, on35 ? D * D * 2 : 0);
        const int vo = (112 * w + wrow) * 896 + dt * 128 + (lane & 7) * 16;
#pragma unroll
        for (int k = 0; k < 4; ++k)
#pragma unroll
          for (int m = 0; m < 4; ++m) wo[k][m] = ld16<0>(rwo, vo, (28 * k + 8 * m) * 896);
      }
      const float qs = rstd * (0.18898223650461362f * 1.4426950408889634f), qn = -mean * qs;
#pragma unroll
      for (int a = 0; a < 4; ++a) {
        const f32x4 cs = *(LAS f32x4*)(L + L_CS + (8 * a + 4 * hh) * 4);
        unsigned u0 = pk(aq[4 * a] * qs + qn * cs[0], aq[4 * a + 1] * qs + qn * cs[1]);
        unsigned u1 = pk(aq[4 * a + 2] * qs + qn * cs[2], aq[4 * a + 3] * qs + qn * cs[3]);
        if (a == 3 && hh) { u0 = 0x00003f80u; u1 = 0u; }
        qf[a >> 1][(a & 1) * 2] = u0;
        qf[a >> 1][(a & 1) * 2 + 1] = u1;
      }
    }
    SCHED();
    v4u wt[16][4];
    const rsrc_t rw1 = mk(p.w1, on4 ? S * F * 2 : 0);
#define ROT4 (2 * (by % 7) + 4 * (by / 7))
#define W1O ((512 * w + wrow) * (F * 2) + ft * 128 + (lane & 7) * 16)
#define LOAD_W1(K, M) wt[K][M] = ld16<0>(rw1, w1o, (((K) + rot4) & 15) * (32 * F * 2) + 8 * (M) * (F * 2))
    {
      FRESH();
      const int w1o = W1O, rot4 = ROT4;
      LAS char* kvw = L + L_KVW + w * 4096 + lane * 16;
      f32x16 s8;
      unsigned pm[8];
      {
        const v4u k0 = *(LAS v4u*)(kvw + 8 * 4096), k1 = *(LAS v4u*)(kvw + 8 * 4096 + 1024);
        MFMA32V0(s8, k0, qf[0]);
        MFMA32VA(s8, k1, qf[1]);
#pragma unroll
        for (int i = 0; i < 16; i += 2) {
          const int kl = (i & 3) + 8 * (i >> 2) + 4 * hh;
          pm[i >> 1] = (kl > r32 ? 0xffffu : 0u) | (kl + 1 > r32 ? 0xffff0000u : 0u);
          asm volatile("" : "+v"(pm[i >> 1]));
        }
      }
      if (t > 0) {
        const int tp = (t >= 2 && (lane & 1)) ? t - 2 : t - 1;
        wait_flags(fl + (0 * 256 + (((tp & 1) * 16 + hd) * 8 + (tp >> 1))) * 32, epoch, budget);
      }
      STAMP(4 + layer * 16);
      {
        v4u a[4], b[4];
        const int vo = KV_OFF + layer * KV_SZ + (hd * 64 + 4 * t + w) * 4096 + lane * 16;
        const rsrc_t r1 = mk(p.ws, t >= 1 ? WS_BYTES : 0), r2 = mk(p.ws, t >= 2 ? WS_BYTES : 0);
        b[0] = ld16<SC1>(r2, vo - 32768, 0); b[1] = ld16<SC1>(r2, vo - 32768, 1024);
        a[0] = ld16<SC1>(r1, vo - 16384, 0); a[1] = ld16<SC1>(r1, vo - 16384, 1024);
        b[2] = ld16<SC1>(r2, vo - 32768, 2048); b[3] = ld16<SC1>(r2, vo - 32768, 3072);
        a[2] = ld16<SC1>(r1, vo - 16384, 2048); a[3] = ld16<SC1>(r1, vo - 16384, 3072);
        SCHED();
        if (t < 2 && hh) b[1][2] = 0xf149u;
        if (t < 1 && hh) a[1][2] = 0xf149u;
#pragma unroll
        for (int i = 0; i < 4; ++i) {
          *(LAS v4u*)(kvw + i * 1024) = b[i];
          *(LAS v4u*)(kvw + 16384 + i * 1024) = a[i];
        }
      }
      bar();
      STAMP(37 + layer * 4);
      v4u kf[8][2];
#pragma unroll
      for (int j = 0; j < 8; ++j) { kf[j][0] = *(LAS v4u*)(kvw + j * 4096); kf[j][1] = *(LAS v4u*)(kvw + j * 4096 + 1024); }
      SCHED();
      f32x16 sc[8], se;
      float mx = -1e30f;
#pragma unroll
      for (int j = 0; j < 10; ++j) {
        if (j < 8) MFMA32V0(sc[j], kf[j][0], qf[0]);
        if (j == 9) MFMA_SETTLE(sc[7]);
        if (j == 2) {
#pragma unroll
          for (int i = 0; i < 16; ++i) se[i] = ((i & 3) + 8 * (i >> 2) + 4 * hh) > r32 ? sc[0][i] : s8[i];
        } else if (j > 2) {
#pragma unroll
          for (int i = 0; i < 8; i += 2) mx = max3(mx, sc[j - 2][i], sc[j - 2][i + 1]);
        }
        SCHED();
        if (j < 8) MFMA32VA(sc[j], kf[j][1], qf[1]);
        if (j == 2) mx = max16(mx, se);
        else if (j > 2) {
#pragma unroll
          for (int i = 8; i < 16; i += 2) mx = max3(mx, sc[j - 2][i], sc[j - 2][i + 1]);
        }
        SCHED();
      }
      mx = max32(mx);
      STAMP(38 + layer * 4);
      f32x16 nm;
#pragma unroll
      for (int i = 0; i < 16; ++i) nm[i] = -mx;
      asm volatile("" : "+v"(nm));
      f32x16 ao = {};
      v4u vf[9][2], pf[9][2];
#define VF(j) { vf[j][0] = *(LAS v4u*)(kvw + (j) * 4096 + 2048); vf[j][1] = *(LAS v4u*)(kvw + (j) * 4096 + 3072); }
      VF(0) VF(8)
      SCHED();
#pragma unroll
      for (int j = 0; j < 8; ++j) {
#pragma unroll
        for (int q = 0; q < 4; ++q) {
          if (j == 0) {
            if (q == 0) MFMA32VC(sc[1], kf[1][0], qf[0], nm);
            if (q == 1) MFMA32VA(sc[1], kf[1][1], qf[1]);
            if (q == 2) MFMA32VC(sc[2], kf[2][0], qf[0], nm);
            if (q == 3) MFMA32VA(sc[2], kf[2][1], qf[1]);
          } else {
            if (q == 0) ao = mfma32(vf[j - 1][0], pf[j - 1][0], ao);
            if (q == 1) ao = mfma32(vf[j - 1][1], pf[j - 1][1], ao);
            if (j + 2 < 8) {
              if (q == 2) MFMA32VC(sc[j + 2], kf[j + 2][0], qf[0], nm);
              if (q == 3) MFMA32VA(sc[j + 2], kf[j + 2][1], qf[1]);
            } else if (j == 6) {
              if (q == 2) ao = mfma32(vf[8][0], pf[8][0], ao);
              if (q == 3) ao = mfma32(vf[8][1], pf[8][1], ao);
            }
          }
          SCHED();
#pragma unroll
          for (int i = 4 * q; i < 4 * q + 4; i += 2) {
            if (j == 0) {
              const unsigned u = pk(__builtin_amdgcn_exp2f(se[i] - mx), __builtin_amdgcn_exp2f(se[i + 1] - mx));
              pf[0][i >> 3][(i >> 1) & 3] = u & pm[i >> 1];
              pf[8][i >> 3][(i >> 1) & 3] = u & ~pm[i >> 1];
            } else {
              pf[j][i >> 3][(i >> 1) & 3] = pk(__builtin_amdgcn_exp2f(sc[j][i]), __builtin_amdgcn_exp2f(sc[j][i + 1]));
            }
          }
          if (q == 0 && j > 0) VF(j)
          if (j < NW1A) LOAD_W1(j, q);
          SCHED();
        }
      }
      ao = mfma32(vf[7][0], pf[7][0], ao);
      ao = mfma32(vf[7][1], pf[7][1], ao);
      const float lo = xor32(ao[12]);
      const float inv = __builtin_amdgcn_rcpf(hh ? ao[12] : lo);
      v4u of[2];
#pragma unroll
      for (int i = 0; i < 16; i += 2) of[i >> 3][(i >> 1) & 3] = pk(ao[i] * inv, ao[i + 1] * inv);
      if (hh) { of[1][2] = 0u; of[1][3] = 0u; }
      const int vo = O_OFF + layer * O_SZ + ((2 * t + (w >> 1)) * 16 + hd) * 4096 + (2 * (w & 1) + (r32 >> 4)) * 1024 + hh * 256 + c * 16;
      st16<SC1>(of[0], rws, vo, 0);
      st16<SC1>(of[1], rws, vo, 512);
      STAMP(39 + layer * 4);
      {
        const int r0 = 16 * (g >> 1) + 4 * (g & 1) + q4;
        const int ra0 = raddr(r0), ra1 = raddr(r0 + 8);
#pragma unroll
        for (int k = 0; k < 4; ++k) convert(wo[k], stg, wa0, wa1, ra0, ra1, 0);
      }
      publish(fl + (1 * 256 + bid) * 32, epoch);
      STAMP(5 + layer * 16);
      {
        const int ra0 = raddr(8 * g + q4), ra1 = raddr(8 * g + 4 + q4);
#pragma unroll
        for (int k = 0; k < NW1B - NW1A; ++k) {
#pragma unroll
          for (int m = 0; m < 4; ++m) {
            LOAD_W1(NW1A + k, m);
            if (k < NCV1) cv_wr(wt[k][m], m, stg, wa0, wa1);
            SCHED();
          }
          if (k < NCV1) {
#pragma unroll
            for (int i = 0; i < 4; ++i) wt[k][i] = cv_rd(i, stg, ra0, ra1, 0);
          }
          SCHED();
        }
      }
    }
    if (on35) {
      FRESH();
      const int tq = st >> 1;
      wait_flags(fl + (1 * 256 + (((tq & 1) * 16 + 4 * w + (lane & 3)) * 8 + (tq >> 1))) * 32, epoch, budget);
      STAMP(6 + layer * 16);
      v4u of[16];
      const int oo = O_OFF + layer * O_SZ + (st * 16 + 4 * w) * 4096 + lane * 16;
#define LOAD_O(I) of[I] = ld16<SC1>(rws, oo, ((I) >> 2) * 4096 + (((I) + w) & 3) * 1024)
#pragma unroll
      for (int i = 0; i < 10; ++i) { LOAD_O(i); SCHED(); }
      f32x4 acc[4][4];
#pragma unroll
      for (int a = 0; a < 4; ++a)
#pragma unroll
        for (int b = 0; b < 4; ++b) acc[a][b] = (f32x4){0.f, 0.f, 0.f, 0.f};
#pragma unroll
      for (int i = 0; i < 16; ++i) {
#pragma unroll
        for (int a = 0; a < 4; ++a) acc[a][i & 3] = mfma16(wo[i >> 2][a], of[i], acc[a][i & 3]);
        SCHED();
        if (i + 10 < 16) LOAD_O(i + 10);
        SCHED();
      }
      f32x4 o[4];
      reduce4(acc, o, L, w, lane);
      float s1 = 0.f, s2 = 0.f;
#pragma unroll
      for (int a = 0; a < 4; ++a) {
        res[a] += o[a];
        s1 += (res[a][0] + res[a][1]) + (res[a][2] + res[a][3]);
        s2 += (res[a][0] * res[a][0] + res[a][1] * res[a][1]) + (res[a][2] * res[a][2] + res[a][3] * res[a][3]);
      }
      s1 = sum32(sum16(s1)); s2 = sum32(sum16(s2));
      const int sb = S3_OFF + layer * S3_SZ + st * 8192 + (16 * w + c) * 16;
      if (g == 0) st16<SC1>((v4u){asu(s1), epoch, asu(s2), epoch}, rws, sb + dt * 1024, 0);
      STAMP(7 + layer * 16);
      FENCE();
      float t1, t2;
      while (true) {
        const v4u a = ld16<SC1>(rws, sb + g * 1024, 0), b = ld16<SC1>(rws, sb + (g < 3 ? g + 4 : g) * 1024, 0);
        t1 = asf(a.x) + (g < 3 ? asf(b.x) : 0.f);
        t2 = asf(a.z) + (g < 3 ? asf(b.z) : 0.f);
        const bool bad = (a.y != epoch) | (a.w != epoch) | (b.y != epoch) | (b.w != epoch);
        if (__builtin_amdgcn_ballot_w64(bad) == 0ull) break;
        if (budget == 0u) break;
        --budget;
        FENCE();
      }
      STAMP(8 + layer * 16);
      t1 = sum32(sum16(t1)); t2 = sum32(sum16(t2));
      const float m = t1 * (1.f / D), var = fmaxf(t2 * (1.f / D) - m * m, 0.f);
      const float r1 = __builtin_amdgcn_rsqf(var + EPS);
      const float r = r1 * __builtin_amdgcn_rsqf(var * r1 * r1 + EPS);
#pragma unroll
      for (int a = 0; a < 4; ++a) {
        const v2u v = {pk((res[a][0] - m) * r, (res[a][1] - m) * r), pk((res[a][2] - m) * r, (res[a][3] - m) * r)};
        *(LAS v2u*)(stg + c * 128 + ((a ^ swz(c)) << 5) + g * 8) = v;
      }
      const int hr = 8 * (g & 1) + q4;
      const int h0 = raddr(hr), h1 = raddr(hr + 4);
#pragma unroll
      for (int e = 0; e < 2; ++e) {
        const int x = (2 * e + (g >> 1)) << 5;
        const v4u f = trfrag(stg + (h0 ^ x), stg + (h1 ^ x));
        st16<SC1>(f, rws, HT_OFF + layer * HT_SZ + (dt * 64 + 2 * st + (w >> 1)) * 4096 + (g >> 1) * 1024 + (2 * (w & 1) + (g & 1)) * 256 + c * 16, e * 2048);
      }
      publish(fl + (2 * 256 + bid) * 32, epoch);
    }
    STAMP(9 + layer * 16);
    v4u wu[14][4];
    const rsrc_t rw2 = mk(p.w2, on35 ? S * F * 2 : 0);
#define ROT5 wrap14(3 * (by / 7) + 2 * (by % 7))
#define W2O ((448 * w + wrow) * (S * 2) + st * 128 + (lane & 7) * 16)
#define LOAD_W2(K, M) wu[K][M] = ld16<0>(rw2, w2o, (32 * wrap14((K) + rot5) + 8 * (M)) * (S * 2))
    if (on4) {
      FRESH();
      const int w1o = W1O, rot4 = ROT4, w2o = W2O, rot5 = ROT5;
      v2u bb[4];
      const int ra0 = raddr(8 * g + q4), ra1 = raddr(8 * g + 4 + q4);
#pragma unroll
      for (int k = NCV1; k < 16; ++k) {
#pragma unroll
        for (int m = 0; m < 4; ++m) {
          if (NW1B + k - NCV1 < 16) LOAD_W1(NW1B + k - NCV1, m);
          cv_wr(wt[k][m], m, stg, wa0, wa1);
          SCHED();
        }
#pragma unroll
        for (int i = 0; i < 4; ++i) wt[k][i] = cv_rd(i, stg, ra0, ra1, 0);
        SCHED();
      }
      {
        const rsrc_t rb1 = mk(p.b1, F * 2);
#pragma unroll
        for (int a = 0; a < 4; ++a) bb[a] = ld8<0>(rb1, ft * 128 + g * 8, a * 32);
      }
      const int sp = 8 * w + (lane & 7);
      wait_flags(fl + (2 * 256 + ((sp & 3) * 7 + dt4) * 8 + (sp >> 2)) * 32, epoch, budget);
      STAMP(10 + layer * 16);
      v4u af[64];
      const int ho = HT_OFF + layer * HT_SZ + (dt4 * 64 + 16 * w) * 4096 + lane * 16;
#define LOAD_H(I) af[I] = ld16<SC1>(rws, ho, ((((I) >> 2) + rot4) & 15) * 4096 + (((I) + w) & 3) * 1024)
#pragma unroll
      for (int i = 0; i < WIN4; ++i) { LOAD_H(i); SCHED(); }
      f32x4 acc[4][4];
#pragma unroll
      for (int a = 0; a < 4; ++a)
#pragma unroll
        for (int b = 0; b < 4; ++b) acc[a][b] = (f32x4){0.f, 0.f, 0.f, 0.f};
#pragma unroll
      for (int i = 0; i < 64; ++i) {
#pragma unroll
        for (int a = 0; a < 4; ++a) acc[a][i & 3] = mfma16(wt[i >> 2][a], af[i], acc[a][i & 3]);
        SCHED();
        if (i + WIN4 < 64) LOAD_H(i + WIN4);
        else LOAD_W2((i + WIN4 - 64) >> 2, i & 3);
        SCHED();
      }
      STAMP(11 + layer * 16);
      f32x4 o[4];
      reduce4(acc, o, L, w, lane);
      v4u uf[2];
#pragma unroll
      for (int a = 0; a < 4; ++a) {
        const f32x4 bv = {bflo(bb[a].x), bfhi(bb[a].x), bflo(bb[a].y), bfhi(bb[a].y)};
        float gl[4];
#pragma unroll
        for (int j = 0; j < 4; ++j) {
          const float u = o[a][j] + bv[j];
          const float e = __builtin_amdgcn_exp2f(u * (u * u * (-0.10294324f) - 2.3022082f));
          gl[j] = u * __builtin_amdgcn_rcpf(1.f + e);
        }
        uf[a >> 1][(a & 1) * 2] = pk(gl[0], gl[1]);
        uf[a >> 1][(a & 1) * 2 + 1] = pk(gl[2], gl[3]);
      }
      const int uo = U_OFF + layer * U_SZ + (dt4 * 56 + 2 * ft) * 4096 + w * 1024 + lane * 16;
      st16<SC1>(uf[0], rws, uo, 0);
      st16<SC1>(uf[1], rws, uo, 4096);
      STAMP(40 + layer * 4);
      publish(fl + (3 * 256 + bid) * 32, epoch);
    } else {
      FRESH();
      const int w2o = W2O, rot5 = ROT5;
#pragma unroll
      for (int i = 0; i < WIN4; ++i) { LOAD_W2(i >> 2, i & 3); SCHED(); }
    }
    STAMP(12 + layer * 16);
    if (on35) {
      FRESH();
      const int w2o = W2O, rot5 = ROT5;
      const int ra0 = raddr(4 * g + q4), ra1 = raddr(16 + 4 * g + q4);
#pragma unroll
      for (int k = 0; k < 14 - WIN4 / 4; ++k) {
#pragma unroll
        for (int m = 0; m < 4; ++m) {
          LOAD_W2(WIN4 / 4 + k, m);
          if (k < NPRE5) cv_wr(wu[k][m], m, stg, wa0, wa1);
          SCHED();
        }
        if (k < NPRE5) {
#pragma unroll
          for (int i = 0; i < 4; ++i) wu[k][i] = cv_rd(i, stg, ra0, ra1, w);
        }
        SCHED();
      }
      const float b2v = bflo((unsigned)__builtin_amdgcn_raw_buffer_load_b16(mk(p.b2, S * 2), (64 * st + 16 * w + c) * 2, 0, 0));
      wait_flags(fl + (3 * 256 + ((dt & 3) * 7 + lane % 7) * 8 + 2 * w + (dt >> 2)) * 32, epoch, budget);
      STAMP(13 + layer * 16);
      v4u af[56];
      const int uo = U_OFF + layer * U_SZ + (dt * 56 + 14 * w) * 4096 + lane * 16;
#define LOAD_U(I) af[I] = ld16<SC1>(rws, uo, wrap14(((I) >> 2) + rot5) * 4096 + ((I) & 3) * 1024)
#pragma unroll
      for (int i = 0; i < WIN5; ++i) { LOAD_U(i); SCHED(); }
      f32x4 acc[4][4];
#pragma unroll
      for (int a = 0; a < 4; ++a)
#pragma unroll
        for (int b = 0; b < 4; ++b) acc[a][b] = (f32x4){0.f, 0.f, 0.f, 0.f};
#pragma unroll
      for (int i = 0; i < 56; ++i) {
        const int kc = NPRE5 + (i >> 3);
#pragma unroll
        for (int b = 0; b < 4; ++b) {
          acc[i & 3][b] = mfma16(af[i], wu[i >> 2][b], acc[i & 3][b]);
          if (b == 1 && kc < 14) {
            SCHED();
            if ((i & 7) < 4) cv_wr(wu[kc][i & 3], i & 3, stg, wa0, wa1);
            else wu[kc][i & 3] = cv_rd(i & 3, stg, ra0, ra1, w);
            SCHED();
          }
        }
        SCHED();
        if (i + WIN5 < 56) LOAD_U(i + WIN5);
        SCHED();
      }
      STAMP(14 + layer * 16);
      f32x4 o[4];
      reduce4(acc, o, L, w, lane);
      v2u xo[4];
      float s1 = 0.f, s2 = 0.f;
#pragma unroll
      for (int a = 0; a < 4; ++a) {
        res[a] += o[a] + b2v;
        xo[a] = (v2u){pk(res[a][0], res[a][1]), pk(res[a][2], res[a][3])};
        s1 += (res[a][0] + res[a][1]) + (res[a][2] + res[a][3]);
        s2 += (res[a][0] * res[a][0] + res[a][1] * res[a][1]) + (res[a][2] * res[a][2] + res[a][3] * res[a][3]);
      }
      if (layer == 0) {
        const int xb = X2_OFF + (2 * st + (w >> 1)) * 28672 + (4 * dt + (g >> 1)) * 1024 + ((g & 1) * 32 + (w & 1) * 16 + c) * 16;
        st16<SC1>((v4u){xo[0].x, xo[0].y, xo[1].x, xo[1].y}, rws, xb, 0);
        st16<SC1>((v4u){xo[2].x, xo[2].y, xo[3].x, xo[3].y}, rws, xb, 2048);
        s1 = sum32(sum16(s1)); s2 = sum32(sum16(s2));
        if (g == 0) st8<SC1>((v2u){asu(s1), asu(s2)}, rws, S5_OFF + (st * 8 + dt) * 512 + (16 * w + c) * 8, 0);
        publish(flags + (4 * 256 + bid) * 32, epoch);
      } else {
        const rsrc_t ro = mk(p.out, S * D * 2);
#pragma unroll
        for (int a = 0; a < 4; ++a) st8<0>(xo[a], ro, (64 * st + 16 * w + c) * 896 + dt * 128 + g * 8, a * 32);
      }
    }
    STAMP(15 + layer * 16);
    if (layer) break;
    {
      FRESH();
      const int st1 = 2 * t + (w >> 1), rotx = hd % 7;
      v4u wk[28], wv[28];
      {
        LAS char* wb = WB(0);
        wk[0] = WFRAG(wb, 1, 0); wv[0] = WFRAG(wb, 2, 0); wk[1] = WFRAG(wb, 1, 1); wv[1] = WFRAG(wb, 2, 1);
      }
      wait_flags(flags + (4 * 256 + ((st1 & 3) * 7 + lane % 7) * 8 + (st1 >> 2)) * 32, epoch, budget);
      STAMP(18);
      const int xo = X2_OFF + (4 * t + w) * 28672 + lane * 16;
#define LOAD_X(I) xf[I] = ld16<SC1>(rws, xo, wrap7(((I) >> 2) + rotx) * 4096 + ((I) & 3) * 1024)
#pragma unroll
      for (int i = 0; i < 10; ++i) { LOAD_X(i); SCHED(); }
      v2u sp[4];
      ak = (f32x16){}; av = (f32x16){};
#pragma unroll
      for (int ks = 0; ks < 28; ++ks) {
        ak = mfma32(wk[ks], xf[ks], ak);
        if (ks + 2 < 28) { LAS char* wb = WB((ks + 2) >> 2); wk[ks + 2] = WFRAG(wb, 1, (ks + 2) & 3); wv[ks + 2] = WFRAG(wb, 2, (ks + 2) & 3); }
        SCHED();
        av = mfma32(xf[ks], wv[ks], av);
        SCHED();
        if (ks + 10 < 28) LOAD_X(ks + 10);
        else if (ks + 10 < 32) {
          const int dd = hh * 4 + (ks + 10 - 28);
          sp[ks + 10 - 28] = ld8<SC1>(rws, S5_OFF + (st1 * 8 + (dd < 7 ? dd : 0)) * 512 + ((w & 1) * 32 + r32) * 8, 0);
        }
        SCHED();
      }
      float s1 = 0.f, s2 = 0.f;
#pragma unroll
      for (int i = 0; i < 4; ++i) {
        const bool ok = (hh * 4 + i) < 7;
        s1 += ok ? asf(sp[i].x) : 0.f;
        s2 += ok ? asf(sp[i].y) : 0.f;
      }
      s1 = sum32(s1); s2 = sum32(s2);
      mean = s1 * (1.f / D);
      rstd = __builtin_amdgcn_rsqf(fmaxf(s2 * (1.f / D) - mean * mean, 0.f) + EPS);
    }
  }
  if (blockIdx.x == 0 && threadIdx.x == 0) __hip_atomic_store(ctrl, epoch, RLX_AGENT);
}

extern "C" void kernel_launch(void* const* d_in, const int* in_sizes, int n_in, void* d_out, int out_size, void* d_ws,
                              size_t ws_size, hipStream_t stream) {
  Params p;
  p.x = d_in[0]; p.wqkv = d_in[1]; p.wo = d_in[2];
  p.w1 = d_in[4]; p.b1 = d_in[5]; p.w2 = d_in[6]; p.b2 = d_in[7];
  p.out = d_out; p.ws = d_ws;
  (void)hipFuncSetAttribute((const void*)fused_kernel, hipFuncAttributeMaxDynamicSharedMemorySize, LDS_BYTES);
  fused_kernel<<<256, 256, LDS_BYTES, stream>>>(p);
}
```

```cpp
#include <hip/hip_runtime.h>
#include <utility>

#define GAS __attribute__((address_space(1)))
#define LAS __attribute__((address_space(3)))
#define DI __device__ __forceinline__
#define RLX_AGENT __ATOMIC_RELAXED, __HIP_MEMORY_SCOPE_AGENT
typedef unsigned v4u __attribute__((ext_vector_type(4)));
typedef unsigned v2u __attribute__((ext_vector_type(2)));
typedef float f32x2 __attribute__((ext_vector_type(2)));
typedef float f32x4 __attribute__((ext_vector_type(4)));
typedef float f32x16 __attribute__((ext_vector_type(16)));
typedef short s16x4 __attribute__((ext_vector_type(4)));
typedef __bf16 bf16x2 __attribute__((ext_vector_type(2)));
typedef __bf16 bf16x8 __attribute__((ext_vector_type(8)));
typedef __amdgpu_buffer_rsrc_t rsrc_t;

constexpr int S = 2048, D = 448, F = 1792;
constexpr float EPS = 1e-5f;
constexpr int FLAG_OFF = 4096;
constexpr int KV_OFF = 512 * 1024, KV_SZ = 16 * 64 * 4096;
constexpr int O_OFF = KV_OFF + 2 * KV_SZ, O_SZ = 32 * 16 * 4096;
constexpr int S3_OFF = O_OFF + 2 * O_SZ, S3_SZ = 32 * 8 * 1024;
constexpr int HT_OFF = S3_OFF + 2 * S3_SZ, HT_SZ = 7 * 64 * 4096;
constexpr int U_OFF = HT_OFF + 2 * HT_SZ, U_SZ = 7 * 56 * 4096;
constexpr int X2_OFF = U_OFF + 2 * U_SZ, X2_SZ = 64 * 28 * 1024;
constexpr int S5_OFF = X2_OFF + X2_SZ, S5_SZ = 32 * 8 * 512;
constexpr int DBG_OFF = S5_OFF + S5_SZ;
constexpr int WS_BYTES = DBG_OFF + 256 * 128 * 8;
constexpr int L_WQ = 0;
constexpr int L_KVW = 86016;
constexpr int L_STG = L_KVW + 49152;
constexpr int L_CSP = L_STG + 16384;
constexpr int L_CS = L_CSP + 1536;
constexpr int L_ST = L_CS + 384;
constexpr int LDS_BYTES = L_ST + 1024;

constexpr int SC1 = 16;
#define VT_LAYOUT 1
#define CODE_BYTES 46000
#define WIN4 12
#define WIN5 12
#define LD_C 3
#define LD_E 9
#define LD_G (LD_E + 3)
#define LD_I 19
#define LD_T (LD_I + WIN4 / 4)
#define KM5 0

struct Params {
  const void *x, *wqkv, *wo, *w1, *b1, *w2, *b2;
  void *out, *ws;
};

template <int... I, class Fn> DI void sfor_(std::integer_sequence<int, I...>, Fn&& f) { (f(std::integral_constant<int, I>{}), ...); }
template <int N, class Fn> DI void sfor(Fn&& f) { sfor_(std::make_integer_sequence<int, N>{}, f); }
DI float asf(unsigned u) { return __builtin_bit_cast(float, u); }
DI unsigned asu(float f) { return __builtin_bit_cast(unsigned, f); }
DI bf16x2 asb2(unsigned u) { return __builtin_bit_cast(bf16x2, u); }
DI rsrc_t mk(const void* p, int bytes) { return __builtin_amdgcn_make_buffer_rsrc((void*)p, 0, bytes, 0x00020000); }
template <int AUX> DI v4u ld16(rsrc_t r, int voff, int soff) { return __builtin_amdgcn_raw_buffer_load_b128(r, voff, soff, AUX); }
template <int AUX> DI v2u ld8(rsrc_t r, int voff, int soff) { return __builtin_amdgcn_raw_buffer_load_b64(r, voff, soff, AUX); }
template <int AUX> DI void st16(v4u v, rsrc_t r, int voff, int soff) { __builtin_amdgcn_raw_buffer_store_b128(v, r, voff, soff, AUX); }
template <int AUX> DI void st8(v2u v, rsrc_t r, int voff, int soff) { __builtin_amdgcn_raw_buffer_store_b64(v, r, voff, soff, AUX); }
DI f32x4 mfma16(v4u a, v4u b, f32x4 c) {
  return __builtin_amdgcn_mfma_f32_16x16x32_bf16(__builtin_bit_cast(bf16x8, a), __builtin_bit_cast(bf16x8, b), c, 0, 0, 0);
}
DI f32x16 mfma32(v4u a, v4u b, f32x16 c) {
  return __builtin_amdgcn_mfma_f32_32x32x16_bf16(__builtin_bit_cast(bf16x8, a), __builtin_bit_cast(bf16x8, b), c, 0, 0, 0);
}
DI unsigned pk(float lo, float hi) { return __builtin_bit_cast(unsigned, __builtin_convertvector((f32x2){lo, hi}, bf16x2)); }
DI float bflo(unsigned u) { return asf(u << 16); }
DI float bfhi(unsigned u) { return asf(u & 0xffff0000u); }
DI v2u trrd(LAS char* p) { return __builtin_bit_cast(v2u, __builtin_amdgcn_ds_read_tr16_b64_v4i16((LAS s16x4*)p)); }
DI v4u trfrag(LAS char* p0, LAS char* p1) { v2u a = trrd(p0), b = trrd(p1); return (v4u){a.x, a.y, b.x, b.y}; }
DI float hi32(float v) {
  unsigned u = asu(v);
  auto r = __builtin_amdgcn_permlane32_swap(u, u, false, false);
  return asf(r[1]);
}
DI float sum32(float v) {
  unsigned u = asu(v);
  auto r = __builtin_amdgcn_permlane32_swap(u, u, false, false);
  return asf(r[0]) + asf(r[1]);
}
DI float max32(float v) {
  unsigned u = asu(v);
  auto r = __builtin_amdgcn_permlane32_swap(u, u, false, false);
  return fmaxf(asf(r[0]), asf(r[1]));
}
DI float sum16(float v) {
  unsigned u = asu(v);
  auto r = __builtin_amdgcn_permlane16_swap(u, u, false, false);
  return asf(r[0]) + asf(r[1]);
}
template <int CTRL> DI float dpp_add(float v) { return v + asf((unsigned)__builtin_amdgcn_update_dpp(0, (int)asu(v), CTRL, 0xf, 0xf, true)); }
DI float sum8(float v) { return dpp_add<0x141>(dpp_add<0x4e>(dpp_add<0xb1>(v))); }
#define FENCE() asm volatile("" ::: "memory")
#define SCHED() __builtin_amdgcn_sched_barrier(0)
#define DRAIN() asm volatile("s_waitcnt vmcnt(0)" ::: "memory")
#define PINV(x) asm volatile("" : "+v"(x))
#define PINS(x) asm volatile("" : "+s"(x))
DI void bar() { asm volatile("s_waitcnt lgkmcnt(0)" ::: "memory"); __builtin_amdgcn_s_barrier(); FENCE(); }

template <int N> DI void vmw() { asm volatile("s_waitcnt vmcnt(%0)" :: "n"(N) : "memory"); }
#define LGKM0() asm volatile("s_waitcnt lgkmcnt(0)" ::: "memory")
DI void dma16(rsrc_t r, int voff, int soff, int lds) {
  unsigned keep;
  asm volatile("s_mov_b32 %0, m0\n\ts_mov_b32 m0, %1\n\ts_nop 3\n\tbuffer_load_dwordx4 %2, %3, %4 offen lds\n\ts_mov_b32 m0, %0"
               : "=&s"(keep) : "s"(__builtin_amdgcn_readfirstlane(lds)), "v"(voff), "s"(r), "s"(__builtin_amdgcn_readfirstlane(soff)) : "memory");
}
template <int STEP> DI void dma64(rsrc_t r, int v0, int v1, int soff, int lds) {
  unsigned keep, so;
  asm volatile("s_mov_b32 %0, m0\n\ts_mov_b32 m0, %2\n\ts_mov_b32 %1, %6\n\ts_nop 2\n\tbuffer_load_dwordx4 %3, %5, %1 offen lds\n\t"
               "s_add_u32 m0, m0, 0x400\n\ts_add_u32 %1, %1, %7\n\tbuffer_load_dwordx4 %4, %5, %1 offen lds\n\t"
               "s_add_u32 m0, m0, 0x400\n\ts_add_u32 %1, %1, %7\n\tbuffer_load_dwordx4 %3, %5, %1 offen lds\n\t"
               "s_add_u32 m0, m0, 0x400\n\ts_add_u32 %1, %1, %7\n\tbuffer_load_dwordx4 %4, %5, %1 offen lds\n\ts_mov_b32 m0, %0"
               : "=&s"(keep), "=&s"(so)
               : "s"(__builtin_amdgcn_readfirstlane(lds)), "v"(v0), "v"(v1), "s"(r), "s"(__builtin_amdgcn_readfirstlane(soff)), "n"(STEP)
               : "memory", "scc");
}

#define STAMP(i) do {} while (0)

DI void publish(GAS unsigned* flag, unsigned epoch) {
  DRAIN();
  bar();
  if (threadIdx.x == 0) __hip_atomic_store(flag, epoch, RLX_AGENT);
  FENCE();
}
DI void wait_flags(GAS unsigned* flag, unsigned epoch, unsigned& budget) {
  FENCE();
  while (true) {
    unsigned f = __hip_atomic_load(flag, RLX_AGENT);
    if (__builtin_amdgcn_ballot_w64(f != epoch) == 0ull) break;
    if (budget == 0u) break;
    --budget;
  }
  __builtin_amdgcn_fence(__ATOMIC_ACQUIRE, "wavefront");
  FENCE();
}

DI void cv_wr(const v4u& r, int m, LAS char* stg, int wa0, int wa1) { *(LAS v4u*)(stg + ((m & 1) ? wa1 : wa0) + m * 1024) = r; }
DI v4u cv_rd(int i, LAS char* stg, int ra0, int ra1, int rot) {
  const int x = ((i + rot) & 3) << 5;
  return trfrag(stg + (ra0 ^ x), stg + (ra1 ^ x));
}
DI int wrap7(int v) { return v >= 7 ? v - 7 : v; }
DI int wrap14(int v) { return v >= 14 ? v - 14 : v; }
DI int swz(int row) { return ((row >> 1) & 1) | (((row >> 3) ^ (row >> 2)) & 1) << 1; }

#define FRESH() \
  int tid = threadIdx.x, bid = blockIdx.x; \
  asm volatile("" : "+v"(tid)); \
  asm volatile("" : "+s"(bid)); \
  const int lane = tid & 63, w = __builtin_amdgcn_readfirstlane(tid >> 6), bx = bid & 7, by = bid >> 3; \
  const int c = lane & 15, g = lane >> 4, q4 = (lane & 15) >> 2, p4 = lane & 3, r32 = lane & 31, hh = lane >> 5; \
  const int by7 = __builtin_amdgcn_readfirstlane((by * 37) >> 8), bym = __builtin_amdgcn_readfirstlane(by - 7 * by7); \
  const int t = 2 * bx + (by >> 4), hd = by & 15;                \
  const int st = 4 * bx + by7, dt = bym;                         \
  const int dt4 = (bx & 1) * 4 + by7, ft = 7 * (bx >> 1) + bym;    \
  const int rotx = hd >= 14 ? hd - 14 : wrap7(hd), rot4 = 2 * bym + 4 * by7, rot5 = wrap14(3 * by7 + 2 * bym); \
  LAS char* stga = L + L_STG + w * 4096; \
  LAS char* ring = L + L_KVW + w * 16384; \
  LAS char* stgb = ring + 12288; \
  const int wrow = lane >> 3; \
  const int wa0 = wrow * 128 + (((lane & 7) ^ (swz(wrow) << 1)) << 4); \
  const int wa1 = wrow * 128 + (((lane & 7) ^ (swz(wrow + 8) << 1)) << 4); \
  auto raddr = [&](int row) { return row * 128 + (swz(row) << 5) + 8 * p4; }; \
  const int w1o = (512 * w + wrow) * (F * 2) + ft * 128 + (lane & 7) * 16, w2o = (448 * w + wrow) * (S * 2) + st * 128 + (lane & 7) * 16; \
  const int r1a = raddr(8 * g + q4), r1b = raddr(8 * g + 4 + q4), r2a = raddr(4 * g + q4), r2b = raddr(16 + 4 * g + q4); \
  const int sx0 = swz(wrow) << 5, sx1 = swz(wrow + 8) << 5; \
  LAS char* red = L + L_KVW + lane * 16; \
  (void)c; (void)g; (void)q4; (void)r32; (void)hh; (void)t; (void)hd; (void)st; (void)dt; (void)dt4; (void)ft; (void)stga; (void)stgb; (void)ring; (void)sx0; (void)sx1; (void)wa0; (void)wa1; \
  (void)rotx; (void)rot4; (void)rot5; (void)w1o; (void)w2o; (void)r1a; (void)r1b; (void)r2a; (void)r2b; (void)red;

#define MFMA32V0(d, a, b) asm volatile("s_nop 1\n\tv_mfma_f32_32x32x16_bf16 %0, %1, %2, 0" : "=&v"(d) : "v"(a), "v"(b))
#define MFMA32VC(d, a, b, c) asm volatile("s_nop 1\n\tv_mfma_f32_32x32x16_bf16 %0, %1, %2, %3" : "=&v"(d) : "v"(a), "v"(b), "v"(c))
#define MFMA32VA(d, a, b) asm volatile("s_nop 1\n\tv_mfma_f32_32x32x16_bf16 %0, %1, %2, %0" : "+v"(d) : "v"(a), "v"(b))
#define MFMA_SETTLE(d) asm volatile("s_nop 11" : "+v"(d))
#define MFMA_SETTLE2(d, e) asm volatile("s_nop 11" : "+v"(d), "+v"(e))
DI float max3(float a, float b, float c) { float r; asm("v_max3_f32 %0, %1, %2, %3" : "=v"(r) : "v"(a), "v"(b), "v"(c)); return r; }

#define WB(cc) (L + L_WQ + (4 * hh + q4) * 64 + (g & 1) * 32 + 8 * p4 + (hd & 1) * 8 + wrap7((cc) + rotx) * 4096)
#define WFRAG(wb, part, e) trfrag((wb) + (part) * 28672 + ((e) >> 1) * 2048 + ((e) & 1) * 512, (wb) + (part) * 28672 + ((e) >> 1) * 2048 + ((e) & 1) * 512 + 1024)
#define CSUM(part, a) (*(LAS f32x4*)(L + L_CS + ((part) * 32 + 8 * (a) + 4 * hh) * 4))
#define LOAD_WZ(n, m) wz[n][m] = (n) < 16 ? ld16<0>(rw1, w1o, (((n) + rot4) & 15) * (32 * F * 2) + 8 * (m) * (F * 2)) \
                                          : ld16<0>(rw2, w2o, (32 * wrap14((n) - 16 + rot5) + 8 * (m)) * (S * 2))
#define WZ_RD(n, base) _Pragma("unroll") for (int i_ = 0; i_ < 4; ++i_) wz[n][i_] = (n) < 16 ? cv_rd(i_, base, r1a, r1b, 0) : cv_rd(i_, base, r2a, r2b, w)
#define CONV(C0, C1) \
  _Pragma("unroll") for (int n_ = C0; n_ < (C1); ++n_) { \
    _Pragma("unroll") for (int m_ = 0; m_ < 4; ++m_) cv_wr(wz[n_][m_], m_, stgb, wa0, wa1); \
    WZ_RD(n_, stgb); \
    SCHED(); \
  }
#define DMA_WZ(n, slot) { \
    if ((n) < 16) dma64<8 * F * 2>(rw1, w1o ^ sx0, w1o ^ sx1, (((n) + rot4) & 15) * (32 * F * 2), (int)(size_t)ring + (slot) * 4096); \
    else dma64<8 * S * 2>(rw2, w2o ^ sx0, w2o ^ sx1, wrap14((n) - 16 + rot5) * (32 * S * 2), (int)(size_t)ring + (slot) * 4096); }
#define DPUMP(L0, L1) \
  _Pragma("unroll") for (int n_ = L0; n_ < (L1); ++n_) { \
    if (n_ - (L0) >= 4) { vmw<12>(); WZ_RD(n_ - 4, ring + ((n_ - (L0)) & 3) * 4096); LGKM0(); } \
    DMA_WZ(n_, (n_ - (L0)) & 3) \
    SCHED(); \
  }
#define DTAIL(L0, L1) \
  _Pragma("unroll") for (int n_ = ((L1) - 4 > (L0) ? (L1) - 4 : (L0)); n_ < (L1); ++n_) { WZ_RD(n_, ring + ((n_ - (L0)) & 3) * 4096); } \
  SCHED();
#define REDW(a, b) *(LAS f32x4*)(red + w * 16384 + (((b) - 1) * 4 + (a)) * 1024) = acc[a][b]
#define REDSUM(o) \
  bar(); \
  _Pragma("unroll") for (int a = 0; a < 4; ++a) { \
    const f32x4 r0 = *(LAS f32x4*)(red + ((w + 3) & 3) * 16384 + a * 1024), r1 = *(LAS f32x4*)(red + ((w + 2) & 3) * 16384 + (4 + a) * 1024), \
                r2 = *(LAS f32x4*)(red + ((w + 1) & 3) * 16384 + (8 + a) * 1024); \
    o[a] = (acc[a][0] + r0) + (r1 + r2); \
  }
#define COLSUM(part) { \
    LAS char* cb = L + L_WQ + (part) * 28672 + (w * 112 + 8 * hh + q4) * 64 + (g & 1) * 32 + 8 * p4; \
    v4u wc[7]; \
    _Pragma("unroll") for (int i = 0; i < 7; ++i) wc[i] = trfrag(cb + i * 1024, cb + i * 1024 + 256); \
    f32x16 cs = {}; \
    _Pragma("unroll") for (int i = 0; i < 7; ++i) cs = mfma32((v4u){0x3f803f80u, 0x3f803f80u, 0x3f803f80u, 0x3f803f80u}, wc[i], cs); \
    if (lane < 32) *(LAS float*)(L + L_CSP + (w * 96 + (part) * 32 + lane) * 4) = cs[0]; }
#define COLRED(i) { \
    LAS float* cp = (LAS float*)(L + L_CSP) + (((i) & 31) + 4 * (hd & 1) < 32 ? (i) + 4 * (hd & 1) : (i)); \
    *(LAS float*)(L + L_CS + (i) * 4) = (cp[0] + cp[96]) + (cp[192] + cp[288]); }

__global__ void __launch_bounds__(256, 1) fused_kernel(Params p) {
  extern __shared__ __attribute__((aligned(16))) char smem[];
  LAS char* L = (LAS char*)smem;
  const bool on35 = (blockIdx.x >> 3) < 28;
  const bool on4 = (blockIdx.x >> 3) < ((blockIdx.x & 1) ? 21 : 28);
  const rsrc_t rws = mk(p.ws, WS_BYTES);
  GAS unsigned* ctrl = (GAS unsigned*)p.ws;
  GAS unsigned* flags = (GAS unsigned*)((GAS char*)p.ws + FLAG_OFF);
  STAMP(0);
  unsigned budget = 1u << 22;

  v4u xf[28];
  f32x16 ak, av;
  float mean = 0.f, rstd = 0.f;
  f32x4 res[4];
  unsigned epoch;
  {
    FRESH();
    const rsrc_t rwq = mk(p.wqkv, D * 3 * D * 2);
    const rsrc_t rx = mk(p.x, S * D * 2);
    unsigned warm[2];
    {
      unsigned long long pc;
      asm volatile("s_getpc_b64 %0" : "=s"(pc));
      const rsrc_t rc = w == 0 ? rwq : mk((const void*)(pc & ~127ull), CODE_BYTES);
      const int o1 = w == 0 ? (lane < 38 ? (by * 294 + 256 + lane) * 128 : 0x7ffffff0) : (lane < 5 ? (by * 15 + (w - 1) * 5 + lane) * 128 : 0x7ffffff0);
      asm volatile("s_nop 4\n\tbuffer_load_dword %0, %3, %4, 0 offen\n\tbuffer_load_dword %1, %5, %6, 0 offen\n\tbuffer_load_dword %2, %7, %8, 0 offen sc1"
                   : "=&v"(warm[0]), "=&v"(warm[1]), "=&v"(epoch)
                   : "v"((by * 294 + tid) * 128), "s"(rwq), "v"(o1), "s"(rc), "v"(0), "s"(rws) : "memory");
    }
    SCHED();
    const int wvo = (w * 112 + (lane >> 2)) * 2688 + hd * 56 - (hd & 1) * 8 + (lane & 3) * 16;
    const int wlb = (int)(size_t)(L + L_WQ) + w * 112 * 64;
#define DMA_WQ(part, i) dma16(rwq, wvo, wrap7((i) + bym) * 16 * 2688 + (part) * 896, wlb + (part) * 28672 + wrap7((i) + bym) * 1024)
    const int xvo = (t * 128 + w * 32 + wrow) * 896 + (((lane & 7) ^ (wrow >> 1)) << 4);
#define DMA_X(cc) dma64<8 * 896>(rx, xvo, xvo ^ 64, wrap7((cc) + rotx) * 128, (int)(size_t)ring + ((cc) & 3) * 4096)
    const int xr = r32 * 128 + 8 * hh, xsw = (r32 >> 1) & 7;
#define READ_X(cc) _Pragma("unroll") for (int e = 0; e < 4; ++e) { \
      const int ch = (e >> 1) * 4 + (e & 1); \
      LAS char* xb_ = ring + ((cc) & 3) * 4096 + xr; \
      const v2u a_ = *(LAS v2u*)(xb_ + ((ch ^ xsw) << 4)), b_ = *(LAS v2u*)(xb_ + (((ch + 2) ^ xsw) << 4)); \
      xf[4 * (cc) + e] = (v4u){a_.x, a_.y, b_.x, b_.y}; }
    float s1[2] = {}, s2[2] = {};
#define XSTAT(i) { _Pragma("unroll") for (int j = 0; j < 4; ++j) { \
      const bf16x2 v = asb2(xf[i][j]); \
      s1[j & 1] = __builtin_amdgcn_fdot2_f32_bf16(v, asb2(0x3f803f80u), s1[j & 1], false); \
      s2[j & 1] = __builtin_amdgcn_fdot2_f32_bf16(v, v, s2[j & 1], false); } \
      PINV(s1[0]); PINV(s1[1]); PINV(s2[0]); PINV(s2[1]); }
    DMA_X(0); DMA_X(1); DMA_X(2); DMA_X(3);
#pragma unroll
    for (int i = 0; i < 7; ++i) DMA_WQ(1, i);
    asm volatile("s_waitcnt vmcnt(19)" : "+v"(warm[0]), "+v"(warm[1]), "+v"(epoch) :: "memory");
    epoch += 1u;
    STAMP(32);
#pragma unroll
    for (int cc = 0; cc < 3; ++cc) {
      vmw<19>();
      READ_X(cc)
      LGKM0();
      DMA_X(cc + 4);
#pragma unroll
      for (int e = 0; e < 4; ++e) XSTAT(4 * cc + e)
      SCHED();
    }
    vmw<12>();
    READ_X(3)
#pragma unroll
    for (int i = 0; i < 3; ++i) DMA_WQ(2, i);
    COLSUM(1)
    bar();
    STAMP(33);
    {
      v4u wk[28];
      { LAS char* wb = WB(0); _Pragma("unroll") for (int ks = 0; ks < 4; ++ks) wk[ks] = WFRAG(wb, 1, ks); }
      SCHED();
      sfor<28>([&](auto I) __attribute__((always_inline)) {
        constexpr int ks = I;
        if constexpr (ks == 8) { vmw<8 + 7 + 2>(); READ_X(4) }
        if constexpr (ks == 12) { vmw<4 + 7 + 4>(); READ_X(5) }
        if constexpr (ks == 16) { vmw<7 + 6>(); READ_X(6) }
        if constexpr (ks == 0) MFMA32V0(ak, wk[ks], xf[ks]); else MFMA32VA(ak, wk[ks], xf[ks]);
        if constexpr (ks + 4 < 28) { LAS char* wb = WB((ks + 4) >> 2); wk[ks + 4] = WFRAG(wb, 1, ks & 3); }
        if constexpr (ks < 4) DMA_WQ(2, 3 + ks);
        else if constexpr (ks <= 16 && (ks & 1) == 0) DMA_WQ(0, (ks - 4) / 2);
        if constexpr (ks >= 12) XSTAT(ks)
        SCHED();
      });
    }
    {
      const float t1 = sum32(s1[0] + s1[1]), t2 = sum32(s2[0] + s2[1]);
      mean = t1 * (1.f / D);
      rstd = __builtin_amdgcn_rsqf(fmaxf(t2 * (1.f / D) - mean * mean, 0.f) + EPS);
    }
#pragma unroll
    for (int a = 0; a < 4; ++a) res[a] = (f32x4){0.f, 0.f, 0.f, 0.f};
    STAMP(34);
    vmw<7>();
    COLSUM(2)
    bar();
    STAMP(35);
    if (tid < 64) COLRED(32 + tid)
    {
      v4u wv[28];
      { LAS char* wb = WB(0); _Pragma("unroll") for (int ks = 0; ks < 4; ++ks) wv[ks] = WFRAG(wb, 2, ks); }
      SCHED();
#pragma unroll
      for (int ks = 0; ks < 28; ++ks) {
        if (ks == 0) MFMA32V0(av, wv[ks], xf[ks]); else MFMA32VA(av, wv[ks], xf[ks]);
        if (ks + 4 < 28) { LAS char* wb = WB((ks + 4) >> 2); wv[ks + 4] = WFRAG(wb, 2, ks & 3); }
        SCHED();
      }
    }
    bar();
    MFMA_SETTLE2(ak, av);
#pragma unroll
    for (int a = 0; a < 4; ++a) {
      const f32x4 ck = CSUM(1, a), cv = CSUM(2, a);
#pragma unroll
      for (int j = 0; j < 4; ++j) { ak[4 * a + j] -= mean * ck[j]; av[4 * a + j] -= mean * cv[j]; }
    }
  }

#pragma unroll 1
  for (int layer = 0;; ++layer) {
    GAS unsigned* fl = flags + layer * 5 * 256 * 32;
    STAMP(1 + layer * 16);
    v4u qf[2];
    v4u wo[4][4];
    v2u rr[4];
    {
      FRESH();
      v4u kv[4];
#pragma unroll
      for (int i = 0; i < 16; i += 2) {
        kv[i >> 3][(i >> 1) & 3] = pk(ak[i] * rstd, ak[i + 1] * rstd);
        kv[2 + (i >> 3)][(i >> 1) & 3] = pk(av[i] * rstd, av[i + 1] * rstd);
      }
      if (hh) { kv[1][2] = 0u; kv[1][3] = 0u; kv[3][2] = 0x00003f80u; kv[3][3] = 0u; }
      {
        const rsrc_t rkv = mk(p.ws, t < 15 ? WS_BYTES : 0);
#pragma unroll
        for (int i = 0; i < 4; ++i) st16<SC1>(kv[i], rkv, KV_OFF + layer * KV_SZ + (hd * 64 + 4 * t + w) * 4096 + lane * 16, i * 1024);
      }
      SCHED();
      if (layer == 0) {
        vmw<4>();
        if (t == 15) vmw<0>();
        COLSUM(0)
        bar();
        if (tid < 32) COLRED(tid)
      }
      v4u wq[28];
      {
        LAS char* wb = WB(0);
#pragma unroll
        for (int ks = 0; ks < 4; ++ks) wq[ks] = WFRAG(wb, 0, ks);
      }
      f32x16 aq;
      STAMP(2 + layer * 16 + layer * 27);
      LAS char* kvo = L + L_KVW + (8 + w) * 4096;
#pragma unroll
      for (int ks = 0; ks < 28; ++ks) {
        if (ks == 0) MFMA32V0(aq, wq[ks], xf[ks]); else MFMA32VA(aq, wq[ks], xf[ks]);
        if (ks + 4 < 28) { LAS char* wb = WB((ks + 4) >> 2); wq[ks + 4] = WFRAG(wb, 0, ks & 3); }
        if (ks == 2) *(LAS v4u*)(kvo + lane * 16) = kv[0];
        if (ks == 4) *(LAS v4u*)(kvo + 1024 + lane * 16) = kv[1];
        if (ks == 6) *(LAS v4u*)(kvo + 2048 + ((lane * 16) ^ (hh << 6))) = kv[2];
        if (ks == 8) *(LAS v4u*)(kvo + 3072 + ((lane * 16) ^ (hh << 6) ^ 128)) = kv[3];
        SCHED();
      }
      publish(fl + (0 * 256 + bid) * 32, epoch);
      STAMP(3 + layer * 16);
      {
        const rsrc_t rwo = mk(p.wo, on35 ? D * D * 2 : 0);
        const int vo = (112 * w + wrow) * 896 + dt * 128 + (lane & 7) * 16;
#pragma unroll
        for (int k = 0; k < 4; ++k)
#pragma unroll
          for (int m = 0; m < 4; ++m) wo[k][m] = ld16<0>(rwo, vo, (28 * k + 8 * m) * 896);
      }
      {
        const rsrc_t rxr = mk(p.x, (on35 && layer == 0) ? S * D * 2 : 0);
#pragma unroll
        for (int a = 0; a < 4; ++a) rr[a] = ld8<0>(rxr, (64 * st + 16 * w + c) * 896 + dt * 128 + g * 8, a * 32);
      }
      MFMA_SETTLE(aq);
      const float qs = rstd * (0.18898223650461362f * 1.4426950408889634f), qn = -mean * qs;
#pragma unroll
      for (int a = 0; a < 4; ++a) {
        const f32x4 cs = CSUM(0, a);
        qf[a >> 1][(a & 1) * 2] = pk(aq[4 * a] * qs + qn * cs[0], aq[4 * a + 1] * qs + qn * cs[1]);
        qf[a >> 1][(a & 1) * 2 + 1] = pk(aq[4 * a + 2] * qs + qn * cs[2], aq[4 * a + 3] * qs + qn * cs[3]);
      }
      if (hh) { qf[1][2] = 0x00003f80u; qf[1][3] = 0u; }
    }
    SCHED();
    v4u wz[30][4];
    const rsrc_t rw1 = mk(p.w1, on4 ? S * F * 2 : 0);
    const rsrc_t rw2 = mk(p.w2, S * F * 2);
    {
      FRESH();
      LAS char* kvw = L + L_KVW + w * 4096 + lane * 16;
      LAS char* vw = L + L_KVW + w * 4096 + 2048 + ((lane * 16) ^ (hh << 6));
      const int vx = ((g & 1) * 1024 + (p4 & 1) * 512 + hh * 64 + q4 * 16) ^ ((p4 & 1) << 6) ^ ((g & 1) << 7);
      LAS char* vr0 = L + L_KVW + w * 4096 + 2048 + vx + (p4 >> 1) * 8;
      LAS char* vr1 = L + L_KVW + w * 4096 + 2048 + (vx ^ 128) + (p4 >> 1) * 8;
      f32x16 s8;
      unsigned pm[8];
      {
        const v4u k0 = *(LAS v4u*)(kvw + 8 * 4096), k1 = *(LAS v4u*)(kvw + 8 * 4096 + 1024);
        MFMA32V0(s8, k0, qf[0]);
        MFMA32VA(s8, k1, qf[1]);
#pragma unroll
        for (int i = 0; i < 16; i += 2) {
          const int kl = (i & 3) + 8 * (i >> 2) + 4 * hh;
          pm[i >> 1] = (kl > r32 ? 0xffffu : 0u) | (kl + 1 > r32 ? 0xffff0000u : 0u);
          PINV(pm[i >> 1]);
        }
      }
      int kvo = KV_OFF + layer * KV_SZ + (hd * 64 + 4 * t + w) * 4096 + lane * 16 - 32768;
      int oo = O_OFF + layer * O_SZ + ((2 * t + (w >> 1)) * 16 + hd) * 4096 + (2 * (w & 1) + (r32 >> 4)) * 1024 + hh * 256 + c * 16;
      unsigned nk2 = (t < 2 && hh) ? 0xf149u : 0u, nk1 = (t < 1 && hh) ? 0xf149u : 0u;
      PINV(kvo); PINV(oo); PINV(nk1); PINV(nk2); PINV(kvw); PINV(vw); PINV(vr0); PINV(vr1);
      if (t > 0) {
        const int tp = (t >= 2 && (lane & 1)) ? t - 2 : t - 1;
        wait_flags(fl + (0 * 256 + (((tp & 1) * 16 + hd) * 8 + (tp >> 1))) * 32, epoch, budget);
      }
      STAMP(4 + layer * 16);
      {
        v4u a[4], b[4];
        const rsrc_t r1 = mk(p.ws, t >= 1 ? WS_BYTES : 0), r2 = mk(p.ws, t >= 2 ? WS_BYTES : 0);
        b[0] = ld16<SC1>(r2, kvo, 0); b[1] = ld16<SC1>(r2, kvo, 1024);
        a[0] = ld16<SC1>(r1, kvo, 16384); a[1] = ld16<SC1>(r1, kvo, 16384 + 1024);
        SCHED();
        b[2] = ld16<SC1>(r2, kvo, 2048); b[3] = ld16<SC1>(r2, kvo, 3072);
        a[2] = ld16<SC1>(r1, kvo, 16384 + 2048); a[3] = ld16<SC1>(r1, kvo, 16384 + 3072);
        SCHED();
        b[1][2] |= nk2; a[1][2] |= nk1;
        *(LAS v4u*)(kvw) = b[0]; *(LAS v4u*)(kvw + 1024) = b[1];
        *(LAS v4u*)(kvw + 16384) = a[0]; *(LAS v4u*)(kvw + 16384 + 1024) = a[1];
        LAS char* vw1 = (LAS char*)(size_t)((unsigned)(size_t)vw ^ 128u) + 1024;
        *(LAS v4u*)(vw) = b[2]; *(LAS v4u*)(vw1) = b[3];
        *(LAS v4u*)(vw + 16384) = a[2]; *(LAS v4u*)(vw1 + 16384) = a[3];
      }
      bar();
      STAMP(37 + layer * 4);
      v4u kf[8][2];
#pragma unroll
      for (int j = 0; j < 8; ++j) { kf[j][0] = *(LAS v4u*)(kvw + j * 4096); kf[j][1] = *(LAS v4u*)(kvw + j * 4096 + 1024); }
      SCHED();
      f32x16 sc[8], se;
      float mx = -1e30f, mx2 = -1e30f;
#pragma unroll
      for (int j = 0; j < 10; ++j) {
        if (j < 8) MFMA32V0(sc[j], kf[j][0], qf[0]);
        if (j == 9) MFMA_SETTLE(sc[7]);
        if (j == 2) {
          asm volatile("s_nop 3" : "+v"(sc[0]));
#pragma unroll
          for (int i = 0; i < 16; ++i) se[i] = ((i & 3) + 8 * (i >> 2) + 4 * hh) > r32 ? sc[0][i] : s8[i];
        } else if (j > 2) {
#pragma unroll
          for (int i = 0; i < 8; i += 4) { mx = max3(mx, sc[j - 2][i], sc[j - 2][i + 1]); mx2 = max3(mx2, sc[j - 2][i + 2], sc[j - 2][i + 3]); }
        }
        SCHED();
        if (j < 8) MFMA32VA(sc[j], kf[j][1], qf[1]);
        if (j == 2) {
#pragma unroll
          for (int i = 0; i < 16; i += 4) { mx = max3(mx, se[i], se[i + 1]); mx2 = max3(mx2, se[i + 2], se[i + 3]); }
        } else if (j > 2) {
#pragma unroll
          for (int i = 8; i < 16; i += 4) { mx = max3(mx, sc[j - 2][i], sc[j - 2][i + 1]); mx2 = max3(mx2, sc[j - 2][i + 2], sc[j - 2][i + 3]); }
        }
        SCHED();
      }
      mx = max32(max3(mx, mx2, mx2));
      STAMP(38 + layer * 4);
      f32x16 nm;
#pragma unroll
      for (int i = 0; i < 16; ++i) nm[i] = -mx;
      PINV(nm);
      f32x16 ao;
      v4u vf[9][2], pf[9][2];
      float ex[33][4];
#define VF(j, s) vf[j][s] = trfrag(vr0 + (j) * 4096 + (s) * 256, vr1 + (j) * 4096 + (s) * 256)
      VF(0, 0); VF(0, 1); VF(8, 0); VF(8, 1);
      SCHED();
#pragma unroll
      for (int n = 0; n < 33; ++n) {
        const int j = n >> 2, q = n & 3;
        if (j == 0) {
          if (q == 0) MFMA32VC(sc[1], kf[1][0], qf[0], nm);
          if (q == 1) MFMA32VA(sc[1], kf[1][1], qf[1]);
          if (q == 2) MFMA32VC(sc[2], kf[2][0], qf[0], nm);
          if (q == 3) MFMA32VA(sc[2], kf[2][1], qf[1]);
        } else if (j < 8) {
          if (q == 1) { if (j == 1) MFMA32V0(ao, vf[0][0], pf[0][0]); else MFMA32VA(ao, vf[j - 1][0], pf[j - 1][0]); }
          if (q == 2) MFMA32VA(ao, vf[j - 1][1], pf[j - 1][1]);
          if (j + 2 < 8) {
            if (q == 0) MFMA32VC(sc[j + 2], kf[j + 2][0], qf[0], nm);
            if (q == 3) MFMA32VA(sc[j + 2], kf[j + 2][1], qf[1]);
          } else if (j == 6) {
            if (q == 0) MFMA32VA(ao, vf[8][0], pf[8][0]);
            if (q == 3) MFMA32VA(ao, vf[8][1], pf[8][1]);
          } else if (q == 3) MFMA32VA(ao, vf[7][0], pf[7][0]);
        }
        SCHED();
        if (n < 32) {
#pragma unroll
          for (int i = 0; i < 4; ++i) ex[n][i] = __builtin_amdgcn_exp2f(j == 0 ? se[4 * q + i] - mx : sc[j][4 * q + i]);
        }
        SCHED();
        if (n > 0) {
          const int jp = (n - 1) >> 2, qp = (n - 1) & 3;
#pragma unroll
          for (int i = 0; i < 2; ++i) {
            const unsigned u = pk(ex[n - 1][2 * i], ex[n - 1][2 * i + 1]);
            if (jp == 0) { pf[0][qp >> 1][(2 * qp + i) & 3] = u & pm[2 * qp + i]; pf[8][qp >> 1][(2 * qp + i) & 3] = u & ~pm[2 * qp + i]; }
            else pf[jp][qp >> 1][(2 * qp + i) & 3] = u;
          }
        }
        if (n == 22) asm volatile("" :: "v"(nm));
        if (j > 0 && j < 8 && q < 2) VF(j, q);
        if ((n & 1) == 0 && n / 8 < LD_C && LD_C <= 4) LOAD_WZ(n / 8, (n >> 1) & 3);
        if (LD_C > 4 && j < LD_C) LOAD_WZ(j, q);
        SCHED();
      }
      MFMA32VA(ao, vf[7][1], pf[7][1]);
      MFMA_SETTLE(ao);
      const float inv = __builtin_amdgcn_rcpf(hi32(ao[12]));
      v4u of[2];
#pragma unroll
      for (int i = 0; i < 16; i += 2) of[i >> 3][(i >> 1) & 3] = pk(ao[i] * inv, ao[i + 1] * inv);
      if (hh) { of[1][2] = 0u; of[1][3] = 0u; }
      st16<SC1>(of[0], rws, oo, 0);
      st16<SC1>(of[1], rws, oo, 512);
      SCHED();
      STAMP(39 + layer * 4);
      {
        const int r0 = 16 * (g >> 1) + 4 * (g & 1) + q4;
        const int ra0 = raddr(r0), ra1 = raddr(r0 + 8);
#pragma unroll
        for (int k = 0; k < 4; ++k) {
#pragma unroll
          for (int m = 0; m < 4; ++m) cv_wr(wo[k][m], m, stga, wa0, wa1);
#pragma unroll
          for (int i = 0; i < 4; ++i) wo[k][i] = cv_rd(i, stga, ra0, ra1, 0);
        }
      }
#pragma unroll
      for (int a = 0; a < 4; ++a) { res[a] += (f32x4){bflo(rr[a].x), bfhi(rr[a].x), bflo(rr[a].y), bfhi(rr[a].y)}; PINV(res[a]); }
      publish(fl + (1 * 256 + bid) * 32, epoch);
      STAMP(5 + layer * 16);
    }
    if (on35) {
      {
        FRESH();
        DPUMP(LD_C, LD_E)
        const int tq = st >> 1;
        int oo = O_OFF + layer * O_SZ + (st * 16 + 4 * w) * 4096 + lane * 16;
        int sb = S3_OFF + layer * S3_SZ + st * 8192 + (16 * w + c) * 16;
        int ob[4];
#pragma unroll
        for (int b = 0; b < 4; ++b) { ob[b] = ((b + w) & 3) * 1024; PINS(ob[b]); }
        PINV(oo); PINV(sb); PINV(red);
        wait_flags(fl + (1 * 256 + (((tq & 1) * 16 + 4 * w + (lane & 3)) * 8 + (tq >> 1))) * 32, epoch, budget);
        STAMP(6 + layer * 16);
        v4u of[16];
#define LOAD_O(I) of[I] = ld16<SC1>(rws, oo + ((I) & 3) * 4096, ob[(((I) >> 2) + 1) & 3])
#pragma unroll
        for (int i = 0; i < 10; ++i) { LOAD_O(i); SCHED(); }
        DTAIL(LD_C, LD_E)
        f32x4 acc[4][4];
#pragma unroll
        for (int i = 0; i < 16; ++i) {
          const int b = ((i >> 2) + 1) & 3, k = i & 3;
#pragma unroll
          for (int a = 0; a < 4; ++a) {
            acc[a][b] = mfma16(wo[k][a], of[i], k == 0 ? (f32x4){0.f, 0.f, 0.f, 0.f} : acc[a][b]);
            SCHED();
            if (a == 0 && i + 10 < 16) LOAD_O(i + 10);
            if (a == 2 && i >= 4) REDW(k, i >> 2);
            SCHED();
          }
        }
        f32x4 o[4];
        REDSUM(o)
        float s1 = 0.f, s2 = 0.f;
#pragma unroll
        for (int a = 0; a < 4; ++a) {
          res[a] += o[a];
          s1 += (res[a][0] + res[a][1]) + (res[a][2] + res[a][3]);
          s2 += (res[a][0] * res[a][0] + res[a][1] * res[a][1]) + (res[a][2] * res[a][2] + res[a][3] * res[a][3]);
        }
        s1 = sum32(sum16(s1)); s2 = sum32(sum16(s2));
        if (g == 0) st16<SC1>((v4u){asu(s1), epoch, asu(s2), epoch}, rws, sb + dt * 1024, 0);
        STAMP(7 + layer * 16);
        FENCE();
        SCHED();
        bar();
        DPUMP(LD_E, LD_G)
        const int hr = 8 * (g & 1) + q4;
        int h0 = raddr(hr) ^ ((g >> 1) << 5), h1 = raddr(hr + 4) ^ ((g >> 1) << 5), hw = c * 128 + (swz(c) << 5) + g * 8;
        int ho = HT_OFF + layer * HT_SZ + (dt * 64 + 2 * st + (w >> 1)) * 4096 + (g >> 1) * 1024 + (2 * (w & 1) + (g & 1)) * 256 + c * 16;
        PINV(h0); PINV(h1); PINV(hw); PINV(ho);
        float t1, t2;
        while (true) {
          const v4u a = ld16<SC1>(rws, sb + g * 1024, 0), b = ld16<SC1>(rws, sb + (g < 3 ? g + 4 : g) * 1024, 0);
          t1 = asf(a.x) + (g < 3 ? asf(b.x) : 0.f);
          t2 = asf(a.z) + (g < 3 ? asf(b.z) : 0.f);
          const bool bad = (a.y != epoch) | (a.w != epoch) | (b.y != epoch) | (b.w != epoch);
          if (__builtin_amdgcn_ballot_w64(bad) == 0ull) break;
          if (budget == 0u) break;
          --budget;
          FENCE();
        }
        STAMP(8 + layer * 16);
        t1 = sum32(sum16(t1)); t2 = sum32(sum16(t2));
        const float m = t1 * (1.f / D), var = fmaxf(t2 * (1.f / D) - m * m, 0.f);
        const float r1 = __builtin_amdgcn_rsqf(var + EPS);
        const float r = r1 * __builtin_amdgcn_rsqf(var * r1 * r1 + EPS), nr = -m * r;
#pragma unroll
        for (int a = 0; a < 4; ++a) {
          const v2u v = {pk(res[a][0] * r + nr, res[a][1] * r + nr), pk(res[a][2] * r + nr, res[a][3] * r + nr)};
          *(LAS v2u*)(stgb + (hw ^ (a << 5))) = v;
        }
#pragma unroll
        for (int e = 0; e < 2; ++e) st16<SC1>(trfrag(stgb + (h0 ^ (e << 6)), stgb + (h1 ^ (e << 6))), rws, ho, e * 2048);
        SCHED();
        DTAIL(LD_E, LD_G)
        CONV(0, LD_C)
        publish(fl + (2 * 256 + bid) * 32, epoch);
        STAMP(9 + layer * 16);
        DPUMP(LD_G, LD_I)
      }
      if (on4) {
        FRESH();
        v2u bb[4];
        {
          const rsrc_t rb1 = mk(p.b1, F * 2);
#pragma unroll
          for (int a = 0; a < 4; ++a) bb[a] = ld8<0>(rb1, ft * 128 + g * 8, a * 32);
        }
        int hv[4], hk[16];
#pragma unroll
        for (int b = 0; b < 4; ++b) { hv[b] = HT_OFF + layer * HT_SZ + (dt4 * 64 + 16 * w) * 4096 + ((b + w) & 3) * 1024 + lane * 16; PINV(hv[b]); }
#pragma unroll
        for (int k = 0; k < 16; ++k) { hk[k] = ((k + rot4) & 15) * 4096; PINS(hk[k]); }
        int uo = U_OFF + layer * U_SZ + (dt4 * 56 + 2 * ft) * 4096 + w * 1024 + lane * 16;
        PINV(uo); PINV(red);
        const int sp = 8 * w + (lane & 7);
        wait_flags(fl + (2 * 256 + ((sp & 3) * 7 + dt4) * 8 + (sp >> 2)) * 32, epoch, budget);
        STAMP(10 + layer * 16);
        v4u af[64];
#define LOAD_H(I) af[I] = ld16<SC1>(rws, hv[(((I) >> 4) + 1) & 3], hk[(I) & 15])
#pragma unroll
        for (int i = 0; i < WIN4; ++i) { LOAD_H(i); SCHED(); }
        DTAIL(LD_G, LD_I)
        f32x4 acc[4][4];
#pragma unroll
        for (int i = 0; i < 64; ++i) {
          const int b = ((i >> 4) + 1) & 3, k = i & 15;
#pragma unroll
          for (int a = 0; a < 4; ++a) {
            acc[a][b] = mfma16(wz[k][a], af[i], k == 0 ? (f32x4){0.f, 0.f, 0.f, 0.f} : acc[a][b]);
            SCHED();
            if (a == 0) {
              if (i + WIN4 < 64) LOAD_H(i + WIN4);
              else LOAD_WZ(LD_I + ((i + WIN4 - 64) >> 2), i & 3);
            }
            if (a == 2 && i >= 16 && (k & 3) == 1) REDW(k >> 2, i >> 4);
            SCHED();
          }
        }
        STAMP(11 + layer * 16);
        f32x4 o[4];
        REDSUM(o)
        v4u uf[2];
#pragma unroll
        for (int a = 0; a < 4; ++a) {
          const f32x4 bv = {bflo(bb[a].x), bfhi(bb[a].x), bflo(bb[a].y), bfhi(bb[a].y)};
          float gl[4];
#pragma unroll
          for (int j = 0; j < 4; ++j) {
            const float u = o[a][j] + bv[j];
            const float e = __builtin_amdgcn_exp2f(u * (u * u * (-0.10294324f) - 2.3022082f));
            gl[j] = u * __builtin_amdgcn_rcpf(1.f + e);
          }
          uf[a >> 1][(a & 1) * 2] = pk(gl[0], gl[1]);
          uf[a >> 1][(a & 1) * 2 + 1] = pk(gl[2], gl[3]);
        }
        st16<SC1>(uf[0], rws, uo, 0);
        st16<SC1>(uf[1], rws, uo, 4096);
        SCHED();
        STAMP(40 + layer * 4);
        CONV(LD_I, LD_T)
        publish(fl + (3 * 256 + bid) * 32, epoch);
      } else {
        FRESH();
        DRAIN();
        DTAIL(LD_G, LD_I)
        LGKM0();
        DPUMP(LD_I, LD_T)
        DRAIN();
        DTAIL(LD_I, LD_T)
        LGKM0();
      }
      STAMP(12 + layer * 16);
      {
        FRESH();
        DPUMP(LD_T, 30)
        const float b2v = bflo((unsigned)__builtin_amdgcn_raw_buffer_load_b16(mk(p.b2, S * 2), (64 * st + 16 * w + c) * 2, 0, 0));
        int uk[14];
#pragma unroll
        for (int k = 0; k < 14; ++k) { uk[k] = wrap14(k + rot5) * 4096; PINS(uk[k]); }
        int uo = U_OFF + layer * U_SZ + (dt * 56 + 14 * w) * 4096 + lane * 16;
        int xb = layer ? (64 * st + 16 * w + c) * 896 + dt * 128 + g * 8
                       : X2_OFF + (2 * st + (w >> 1)) * 28672 + (4 * dt + (g >> 1)) * 1024 + ((g & 1) * 32 + (w & 1) * 16 + c) * 16;
        PINV(uo); PINV(xb); PINV(red);
        wait_flags(fl + (3 * 256 + ((dt & 3) * 7 + wrap7(lane & 7)) * 8 + 2 * w + (dt >> 2)) * 32, epoch, budget);
        STAMP(13 + layer * 16);
        v4u af[56];
#define SLOT_A(I) ((I) < 4 * KM5 ? (I) & 3 : ((I) - 4 * KM5) / (14 - KM5))
#define SLOT_K(I) ((I) < 4 * KM5 ? (I) >> 2 : KM5 + ((I) - 4 * KM5) % (14 - KM5))
#define LOAD_U(I) af[I] = ld16<SC1>(rws, uo + SLOT_A(I) * 1024, uk[SLOT_K(I)])
#pragma unroll
        for (int i = 0; i < WIN5; ++i) { LOAD_U(i); SCHED(); }
        DTAIL(LD_T, 30)
        f32x4 acc[4][4];
        sfor<56>([&](auto I) __attribute__((always_inline)) {
          constexpr int i = I, a = SLOT_A(i), k = SLOT_K(i);
#pragma unroll
          for (int b = 0; b < 4; ++b) {
            acc[a][b] = mfma16(af[i], wz[16 + k][b], k == 0 ? (f32x4){0.f, 0.f, 0.f, 0.f} : acc[a][b]);
            SCHED();
            if constexpr (i + WIN5 < 56) { if (b == 0) LOAD_U(i + WIN5); }
            if constexpr (i >= 4 * KM5 && a > 0 && k > KM5 && k <= KM5 + 3) { if (b == 2) REDW(a - 1, k - KM5); }
            SCHED();
          }
        });
        STAMP(14 + layer * 16);
        REDW(3, 1); REDW(3, 2); REDW(3, 3);
        f32x4 o[4];
        REDSUM(o)
        v2u xo[4];
        float s1 = 0.f, s2 = 0.f;
#pragma unroll
        for (int a = 0; a < 4; ++a) {
          res[a] += o[a] + b2v;
          xo[a] = (v2u){pk(res[a][0], res[a][1]), pk(res[a][2], res[a][3])};
        }
        if (layer == 0) {
          st16<SC1>((v4u){xo[0].x, xo[0].y, xo[1].x, xo[1].y}, rws, xb, 0);
          st16<SC1>((v4u){xo[2].x, xo[2].y, xo[3].x, xo[3].y}, rws, xb, 2048);
          SCHED();
#pragma unroll
          for (int a = 0; a < 4; ++a) {
            s1 += (res[a][0] + res[a][1]) + (res[a][2] + res[a][3]);
            s2 += (res[a][0] * res[a][0] + res[a][1] * res[a][1]) + (res[a][2] * res[a][2] + res[a][3] * res[a][3]);
          }
          s1 = sum32(sum16(s1)); s2 = sum32(sum16(s2));
          if (g == 0) st8<SC1>((v2u){asu(s1), asu(s2)}, rws, S5_OFF + (st * 8 + dt) * 512 + (16 * w + c) * 8, 0);
          publish(flags + (4 * 256 + bid) * 32, epoch);
        } else {
          const rsrc_t ro = mk(p.out, S * D * 2);
#pragma unroll
          for (int a = 0; a < 4; ++a) st8<0>(xo[a], ro, xb, a * 32);
        }
      }
    }
    STAMP(15 + layer * 16);
    if (layer) break;
    {
      FRESH();
      const int st1 = 2 * t + (w >> 1);
      v4u wk[28], wv[28];
      {
        LAS char* wb = WB(0);
        wk[0] = WFRAG(wb, 1, 0); wv[0] = WFRAG(wb, 2, 0); wk[1] = WFRAG(wb, 1, 1); wv[1] = WFRAG(wb, 2, 1);
      }
#pragma unroll
      for (int a = 0; a < 4; ++a) {
        const f32x4 ck = CSUM(1, a), cv = CSUM(2, a);
#pragma unroll
        for (int j = 0; j < 4; ++j) { ak[4 * a + j] = ck[j]; av[4 * a + j] = cv[j]; }
      }
      int xo = X2_OFF + (4 * t + w) * 28672 + lane * 16, so = S5_OFF + (st1 * 8 + hh * 4) * 512 + ((w & 1) * 32 + r32) * 8, xk[7];
#pragma unroll
      for (int k = 0; k < 7; ++k) { xk[k] = wrap7(k + rotx) * 4096; PINS(xk[k]); }
      PINV(xo); PINV(so); PINV(ak); PINV(av);
      wait_flags(flags + (4 * 256 + ((st1 & 3) * 7 + wrap7(lane & 7)) * 8 + (st1 >> 2)) * 32, epoch, budget);
      STAMP(18);
      v2u sp[4];
#pragma unroll
      for (int i = 0; i < 4; ++i) sp[i] = ld8<SC1>(rws, (i == 3 && hh) ? so : so + i * 512, 0);
      SCHED();
#define LOAD_X(I) xf[I] = ld16<SC1>(rws, xo + ((I) & 3) * 1024, xk[(I) >> 2])
#pragma unroll
      for (int i = 0; i < 10; ++i) { LOAD_X(i); SCHED(); }
      {
        float s1 = (asf(sp[0].x) + asf(sp[1].x)) + (asf(sp[2].x) + (hh ? 0.f : asf(sp[3].x)));
        float s2 = (asf(sp[0].y) + asf(sp[1].y)) + (asf(sp[2].y) + (hh ? 0.f : asf(sp[3].y)));
        s1 = sum32(s1); s2 = sum32(s2);
        mean = s1 * (1.f / D);
        rstd = __builtin_amdgcn_rsqf(fmaxf(s2 * (1.f / D) - mean * mean, 0.f) + EPS);
        ak *= -mean; av *= -mean;
      }
      SCHED();
#pragma unroll
      for (int ks = 0; ks < 28; ++ks) {
        MFMA32VA(ak, wk[ks], xf[ks]);
        if (ks + 2 < 28) { LAS char* wb = WB((ks + 2) >> 2); wk[ks + 2] = WFRAG(wb, 1, (ks + 2) & 3); wv[ks + 2] = WFRAG(wb, 2, (ks + 2) & 3); }
        SCHED();
        MFMA32VA(av, wv[ks], xf[ks]);
        SCHED();
        if (ks + 10 < 28) LOAD_X(ks + 10);
        SCHED();
      }
      MFMA_SETTLE2(ak, av);
    }
  }
  if (blockIdx.x == 0 && threadIdx.x == 0) __hip_atomic_store(ctrl, epoch, RLX_AGENT);
}

extern "C" void kernel_launch(void* const* d_in, const int* in_sizes, int n_in, void* d_out, int out_size, void* d_ws,
                              size_t ws_size, hipStream_t stream) {
  Params p;
  p.x = d_in[0]; p.wqkv = d_in[1]; p.wo = d_in[2];
  p.w1 = d_in[4]; p.b1 = d_in[5]; p.w2 = d_in[6]; p.b2 = d_in[7];
  p.out = d_out; p.ws = d_ws;
  (void)hipFuncSetAttribute((const void*)fused_kernel, hipFuncAttributeMaxDynamicSharedMemorySize, LDS_BYTES);
  fused_kernel<<<256, 256, LDS_BYTES, stream>>>(p);
}
```

```cpp
#include <hip/hip_runtime.h>
#include <utility>

#define GAS __attribute__((address_space(1)))
#define LAS __attribute__((address_space(3)))
#define DI __device__ __forceinline__
#define INL __attribute__((always_inline))
#define RLX_AGENT __ATOMIC_RELAXED, __HIP_MEMORY_SCOPE_AGENT
typedef unsigned v4u __attribute__((ext_vector_type(4)));
typedef unsigned v2u __attribute__((ext_vector_type(2)));
typedef float f32x2 __attribute__((ext_vector_type(2)));
typedef float f32x4 __attribute__((ext_vector_type(4)));
typedef float f32x16 __attribute__((ext_vector_type(16)));
typedef short s16x4 __attribute__((ext_vector_type(4)));
typedef __bf16 bf16x2 __attribute__((ext_vector_type(2)));
typedef __bf16 bf16x8 __attribute__((ext_vector_type(8)));
typedef __amdgpu_buffer_rsrc_t rsrc_t;

constexpr int S = 2048, D = 448, F = 1792;
constexpr float EPS = 1e-5f;
constexpr int FLAG_OFF = 4096;
constexpr int KV_OFF = 512 * 1024, KV_SZ = 16 * 64 * 4096;
constexpr int O_OFF = KV_OFF + 2 * KV_SZ, O_SZ = 32 * 16 * 4096;
constexpr int S3_OFF = O_OFF + 2 * O_SZ, S3_SZ = 32 * 8 * 1024;
constexpr int HT_OFF = S3_OFF + 2 * S3_SZ, HT_SZ = 7 * 64 * 4096;
constexpr int U_OFF = HT_OFF + 2 * HT_SZ, U_SZ = 7 * 56 * 4096;
constexpr int X2_OFF = U_OFF + 2 * U_SZ, X2_SZ = 64 * 28 * 1024;
constexpr int S5_OFF = X2_OFF + X2_SZ, S5_SZ = 32 * 8 * 512;
constexpr int DBG_OFF = S5_OFF + S5_SZ;
constexpr int WS_BYTES = DBG_OFF + 256 * 128 * 8;
constexpr int L_WQ = 0;
constexpr int L_KVW = 86016;
constexpr int L_STG = L_KVW + 49152;
constexpr int L_CSP = L_STG + 16384;
constexpr int L_CS = L_CSP + 1536 + 64;
constexpr int L_HST = L_CS + 384;
constexpr int L_DUMP = L_HST + 8192;
constexpr int LDS_BYTES = L_DUMP + 256;

constexpr int SC1 = 16, OOB = 0x7ffff000;
#define VT_LAYOUT 1
#define WIN4 12
#define WIN5 12
#define WINX 10
#define QPUB 20
#define NO1 3
#define NO2 3
#define NO3 2
#define NG2 3
#define NG3 2
#define NH1 3
#define NH2 2
#define NH3 2
#define NU1 3
#define NU3 3
#define TCH0 1
#define TCH1 2
#define TCH2 1
constexpr int Z_G = NO1 + NO2 + NO3, Z_H = Z_G + NG2 + NG3, Z_U = Z_H + NH1 + NH2 + NH3;
static_assert(Z_U >= 16 && Z_U + NU1 + NU3 <= 30 && NO1 <= 4 && NU1 <= 4, "schedule");

struct Params {
  const void *x, *wqkv, *wo, *w1, *b1, *w2, *b2;
  void *out, *ws;
};

template <int... I, class Fn> DI void sfor_(std::integer_sequence<int, I...>, Fn&& f) { (f(std::integral_constant<int, I>{}), ...); }
template <int N, class Fn> DI void sfor(Fn&& f) { sfor_(std::make_integer_sequence<int, N>{}, f); }
DI float asf(unsigned u) { return __builtin_bit_cast(float, u); }
DI unsigned asu(float f) { return __builtin_bit_cast(unsigned, f); }
DI bf16x2 asb2(unsigned u) { return __builtin_bit_cast(bf16x2, u); }
DI rsrc_t mk(const void* p, int bytes) { return __builtin_amdgcn_make_buffer_rsrc((void*)p, 0, bytes, 0x00020000); }
template <int AUX> DI v4u ld16(rsrc_t r, int voff, int soff) { return __builtin_amdgcn_raw_buffer_load_b128(r, voff, soff, AUX); }
template <int AUX> DI v2u ld8(rsrc_t r, int voff, int soff) { return __builtin_amdgcn_raw_buffer_load_b64(r, voff, soff, AUX); }
template <int AUX> DI void st16(v4u v, rsrc_t r, int voff, int soff) { __builtin_amdgcn_raw_buffer_store_b128(v, r, voff, soff, AUX); }
template <int AUX> DI void st8(v2u v, rsrc_t r, int voff, int soff) { __builtin_amdgcn_raw_buffer_store_b64(v, r, voff, soff, AUX); }
DI f32x4 mfma16(v4u a, v4u b, f32x4 c) {
  return __builtin_amdgcn_mfma_f32_16x16x32_bf16(__builtin_bit_cast(bf16x8, a), __builtin_bit_cast(bf16x8, b), c, 0, 0, 0);
}
DI f32x16 mfma32(v4u a, v4u b, f32x16 c) {
  return __builtin_amdgcn_mfma_f32_32x32x16_bf16(__builtin_bit_cast(bf16x8, a), __builtin_bit_cast(bf16x8, b), c, 0, 0, 0);
}
DI unsigned pk(float lo, float hi) { return __builtin_bit_cast(unsigned, __builtin_convertvector((f32x2){lo, hi}, bf16x2)); }
DI float bflo(unsigned u) { return asf(u << 16); }
DI float bfhi(unsigned u) { return asf(u & 0xffff0000u); }
DI f32x4 unpack4(v2u v) { return (f32x4){bflo(v.x), bfhi(v.x), bflo(v.y), bfhi(v.y)}; }
DI v2u trrd(LAS char* p) { return __builtin_bit_cast(v2u, __builtin_amdgcn_ds_read_tr16_b64_v4i16((LAS s16x4*)p)); }
DI v4u trfrag(LAS char* p0, LAS char* p1) { v2u a = trrd(p0), b = trrd(p1); return (v4u){a.x, a.y, b.x, b.y}; }
DI float hi32(float v) {
  unsigned u = asu(v);
  auto r = __builtin_amdgcn_permlane32_swap(u, u, false, false);
  return asf(r[1]);
}
DI float sum32(float v) {
  unsigned u = asu(v);
  auto r = __builtin_amdgcn_permlane32_swap(u, u, false, false);
  return asf(r[0]) + asf(r[1]);
}
DI float max32(float v) {
  unsigned u = asu(v);
  auto r = __builtin_amdgcn_permlane32_swap(u, u, false, false);
  return fmaxf(asf(r[0]), asf(r[1]));
}
DI float sum16(float v) {
  unsigned u = asu(v);
  auto r = __builtin_amdgcn_permlane16_swap(u, u, false, false);
  return asf(r[0]) + asf(r[1]);
}
#define FENCE() asm volatile("" ::: "memory")
#define SCHED() __builtin_amdgcn_sched_barrier(0)
#define DRAIN() asm volatile("s_waitcnt vmcnt(0)" ::: "memory")
#define PINV(x) asm volatile("" : "+v"(x))
#define PINS(x) asm volatile("" : "+s"(x))
#define KEEP(x) asm volatile("" :: "v"(x))
#define LGKM0() asm volatile("s_waitcnt lgkmcnt(0)" ::: "memory")
DI void bar() { LGKM0(); __builtin_amdgcn_s_barrier(); FENCE(); }
template <int N> DI void vmw() { asm volatile("s_waitcnt vmcnt(%0)" :: "n"(N) : "memory"); }
template <int STEP> DI void dma1(rsrc_t r, int voff, int& so, int& lds) {
  asm volatile("s_mov_b32 m0, %1\n\ts_add_u32 %1, %1, 0x400\n\tbuffer_load_dwordx4 %2, %3, %0 offen lds\n\ts_add_u32 %0, %0, %4"
               : "+s"(so), "+s"(lds) : "v"(voff), "s"(r), "n"(STEP) : "memory", "scc");
}
DI void touch(rsrc_t r, int voff, int soff, LAS char* L) {
  asm volatile("s_mov_b32 m0, %0\n\ts_nop 0\n\tbuffer_load_dword %1, %2, %3 offen lds"
               :: "s"((int)(size_t)(L + L_DUMP)), "v"(voff), "s"(r), "s"(soff) : "memory");
}
template <int STEP> DI void dma64(rsrc_t r, int v0, int v1, int soff, int lds) {
  unsigned so;
  asm volatile("s_mov_b32 m0, %1\n\ts_mov_b32 %0, %5\n\tbuffer_load_dwordx4 %2, %4, %0 offen lds\n\t"
               "s_add_u32 m0, m0, 0x400\n\ts_add_u32 %0, %0, %6\n\tbuffer_load_dwordx4 %3, %4, %0 offen lds\n\t"
               "s_add_u32 m0, m0, 0x400\n\ts_add_u32 %0, %0, %6\n\tbuffer_load_dwordx4 %2, %4, %0 offen lds\n\t"
               "s_add_u32 m0, m0, 0x400\n\ts_add_u32 %0, %0, %6\n\tbuffer_load_dwordx4 %3, %4, %0 offen lds"
               : "=&s"(so) : "s"(lds), "v"(v0), "v"(v1), "s"(r), "s"(soff), "n"(STEP) : "memory", "scc");
}

#define STAMP(i) do {} while (0)

DI void publish(GAS unsigned* flag, unsigned epoch) {
  DRAIN();
  __builtin_amdgcn_s_waitcnt(0x0F70);
  bar();
  if (threadIdx.x == 0) __hip_atomic_store(flag, epoch, RLX_AGENT);
  FENCE();
}
template <int N> DI void publish_n(GAS unsigned* flag, unsigned epoch) {
  vmw<N>();
  __builtin_amdgcn_s_barrier();
  FENCE();
  if (threadIdx.x == 0) __hip_atomic_store(flag, epoch, RLX_AGENT);
  FENCE();
}
#define POLL(f, ptr) asm volatile("global_load_dword %0, %1, off sc1" : "=&v"(f) : "v"(ptr) : "memory")
#define POLLED(f, N, ptr) { \
    asm volatile("s_waitcnt vmcnt(%1)" : "+v"(f) : "n"(N) : "memory"); \
    if (__builtin_amdgcn_ballot_w64(f != epoch) != 0ull) wait_flags(ptr, epoch, budget); \
    FENCE(); }
DI void wait_flags(GAS unsigned* flag, unsigned epoch, unsigned& budget) {
  FENCE();
  while (true) {
    unsigned f = __hip_atomic_load(flag, RLX_AGENT);
    if (__builtin_amdgcn_ballot_w64(f != epoch) == 0ull) break;
    if (budget == 0u) break;
    --budget;
  }
  __builtin_amdgcn_fence(__ATOMIC_ACQUIRE, "wavefront");
  FENCE();
}

DI void cv_wr(const v4u& r, int m, LAS char* stg, int wa0, int wa1) { *(LAS v4u*)(stg + ((m & 1) ? wa1 : wa0) + m * 1024) = r; }
DI v4u cv_rd(int x, LAS char* stg, int ra0, int ra1) { return trfrag(stg + (ra0 ^ (x << 5)), stg + (ra1 ^ (x << 5))); }
DI int wrap7(int v) { return v >= 7 ? v - 7 : v; }
DI int wrap14(int v) { return v >= 14 ? v - 14 : v; }
DI int swz(int row) { return ((row >> 1) & 1) | (((row >> 3) ^ (row >> 2)) & 1) << 1; }

#define FRESH() \
  int tid = threadIdx.x, bid = blockIdx.x; \
  asm volatile("" : "+v"(tid)); \
  asm volatile("" : "+s"(bid)); \
  const int lane = tid & 63, w = __builtin_amdgcn_readfirstlane(tid >> 6), bx = bid & 7, by = bid >> 3; \
  const int c = lane & 15, g = lane >> 4, q4 = (lane & 15) >> 2, p4 = lane & 3, r32 = lane & 31, hh = lane >> 5; \
  const int by7 = __builtin_amdgcn_readfirstlane((by * 37) >> 8), bym = __builtin_amdgcn_readfirstlane(by - 7 * by7); \
  const int t = 2 * bx + (by >> 4), hd = by & 15;                \
  const int st = 4 * bx + by7, dt = bym;                         \
  const int dt4 = by < 21 ? bym : bym + 4 * (bx & 1), ft = 7 * (bx >> 1) + (by < 21 ? by7 + 4 * (bx & 1) : 3);    \
  const int rotx = hd >= 14 ? hd - 14 : wrap7(hd), rot4 = ROT4, rot5 = ROT5; \
  const int w4 = (w + G4 * 2 * (by7 & 1)) & 3, pa = G5 * 2 * (by7 & 1);    \
  LAS char* stga = L + L_STG + w * 4096; \
  LAS char* ring = L + L_KVW + w * 16384; \
  LAS char* stgb = ring + 12288; \
  const int wrow = lane >> 3; \
  const int sx0 = swz(wrow) << 5, sx1 = swz(wrow + 8) << 5; \
  const int wa0 = (wrow * 128 + (lane & 7) * 16) ^ sx0, wa1 = (wrow * 128 + (lane & 7) * 16) ^ sx1; \
  auto raddr = [&](int row) { return row * 128 + (swz(row) << 5) + 8 * p4; }; \
  const int w1o = (512 * w + wrow) * (F * 2) + ft * 128 + (lane & 7) * 16, w2o = (448 * w + wrow) * (S * 2) + st * 128 + (lane & 7) * 16; \
  const int r1a = raddr(8 * g + q4), r1b = raddr(8 * g + 4 + q4), r2a = raddr(4 * g + q4), r2b = raddr(16 + 4 * g + q4); \
  LAS char* red = L + L_KVW + lane * 16; \
  (void)c; (void)g; (void)q4; (void)r32; (void)hh; (void)t; (void)hd; (void)st; (void)dt; (void)dt4; (void)ft; (void)stga; (void)stgb; (void)ring; \
  (void)sx0; (void)sx1; (void)wa0; (void)wa1; (void)rotx; (void)rot4; (void)rot5; (void)w1o; (void)w2o; (void)r1a; (void)r1b; (void)r2a; (void)r2b; \
  (void)red; (void)w4; (void)pa;
#define G4 0
#define G5 0
#define ROT4 0
#define ROT5 0

#define MNOP ""
#define MFMA32V0(d, a, b) asm volatile(MNOP "v_mfma_f32_32x32x16_bf16 %0, %1, %2, 0" : "=&v"(d) : "v"(a), "v"(b))
#define MFMA32VC(d, a, b, c) asm volatile(MNOP "v_mfma_f32_32x32x16_bf16 %0, %1, %2, %3" : "=&v"(d) : "v"(a), "v"(b), "v"(c))
#define MFMA32VA(d, a, b) asm volatile(MNOP "v_mfma_f32_32x32x16_bf16 %0, %1, %2, %0" : "+v"(d) : "v"(a), "v"(b))
#define MFMA_SETTLE(d) asm volatile("s_nop 11" : "+v"(d))
#define MFMA_SETTLE2(d, e) asm volatile("s_nop 11" : "+v"(d), "+v"(e))
DI float max3(float a, float b, float c) { float r; asm("v_max3_f32 %0, %1, %2, %3" : "=v"(r) : "v"(a), "v"(b), "v"(c)); return r; }

#define WB(cc) (L + L_WQ + (4 * hh + q4) * 64 + (g & 1) * 32 + 8 * p4 + (hd & 1) * 8 + wrap7((cc) + rotx) * 4096)
#define WFRAG(wb, part, e) trfrag((wb) + (part) * 28672 + ((e) >> 1) * 2048 + ((e) & 1) * 512, (wb) + (part) * 28672 + ((e) >> 1) * 2048 + ((e) & 1) * 512 + 1024)
#define T1O ((16 * (by * 4 + w) + (lane >> 2)) * (F * 2) + (7 * (bx >> 1) + 3 * (bx & 1) + (lane & 3)) * 128)
#define T1H ((512 * (by >> 3) + 8 * ((by & 7) * 4 + w) + (lane >> 2)) * (F * 2) + (7 * (bx >> 1) + 3 * (bx & 1) + (lane & 3)) * 128)
#define CSUM(part, a) (*(LAS f32x4*)(L + L_CS + ((part) * 32 + 8 * (a) + 4 * hh) * 4))
#define WZ_RD1(n, i, base) wz[n][i] = (n) < 16 ? cv_rd(i, base, r1a, r1b) : cv_rd(((i) + w) & 3, base, r2a, r2b)
#define WZ_RD(n, base) _Pragma("unroll") for (int i_ = 0; i_ < 4; ++i_) WZ_RD1(n, i_, base)
#define DMA_WZ(n, slot) { \
    if ((n) < 16) dma64<8 * F * 2>(rw1, w1o ^ sx0, w1o ^ sx1, (((n) + rot4) & 15) * (32 * F * 2), (int)(size_t)ring + (slot) * 4096); \
    else dma64<8 * S * 2>(rw2, w2o ^ sx0, w2o ^ sx1, wrap14((n) - 16 + rot5) * (32 * S * 2), (int)(size_t)ring + (slot) * 4096); }
#define DPUMP(L0, A, B) \
  _Pragma("unroll") for (int n_ = A; n_ < (B); ++n_) { \
    if (n_ - (L0) >= 4) { vmw<12>(); WZ_RD(n_ - 4, ring + ((n_ - (L0)) & 3) * 4096); LGKM0(); } \
    DMA_WZ(n_, (n_ - (L0)) & 3) \
    SCHED(); \
  }
#define DTSTEP(L0, L1, sl) ((L1) - 1 - (((L1) - 1 - (L0) - (sl)) & 3))
#define DTAIL1(L0, L1, j) { if (DTSTEP(L0, L1, (j) >> 2) >= (L0)) WZ_RD1(DTSTEP(L0, L1, (j) >> 2), (j) & 3, ring + ((j) >> 2) * 4096); }
#define DTAIL(L0, L1) _Pragma("unroll") for (int j_ = 0; j_ < 16; ++j_) DTAIL1(L0, L1, j_) SCHED();
#define DMA_WO(k) { const int wvo_ = (112 * w + wrow) * 896 + dt * 128 + (lane & 7) * 16; \
    dma64<8 * 896>(mk(p.wo, on35 ? D * D * 2 : 0), wvo_ ^ sx0, wvo_ ^ sx1, 28 * (k) * 896, (int)(size_t)ring + (k) * 4096); }
#define WO_RD1(j) wo[(j) >> 2][(j) & 3] = cv_rd(((j) & 3) ^ pa, ring + ((j) >> 2) * 4096, raddr(16 * (g >> 1) + 4 * (g & 1) + q4), raddr(16 * (g >> 1) + 4 * (g & 1) + q4 + 8))
#define REDW(a, b) *(LAS f32x4*)(red + w * 16384 + (((b) - 1) * 4 + (a)) * 1024) = acc[a][b]
#define REDR(a, b) (*(LAS f32x4*)(red + ((w + 4 - (b)) & 3) * 16384 + (((b) - 1) * 4 + (a)) * 1024))
#define COLSUM(part) { \
    LAS char* cb = L + L_WQ + (part) * 28672 + (w * 112 + 8 * hh + q4) * 64 + (g & 1) * 32 + 8 * p4; \
    v4u wc[7]; \
    _Pragma("unroll") for (int i = 0; i < 7; ++i) wc[i] = trfrag(cb + i * 1024, cb + i * 1024 + 256); \
    f32x16 cs = {}; \
    _Pragma("unroll") for (int i = 0; i < 7; ++i) cs = mfma32((v4u){0x3f803f80u, 0x3f803f80u, 0x3f803f80u, 0x3f803f80u}, wc[i], cs); \
    if (lane < 32) *(LAS float*)(L + L_CSP + (w * 96 + (part) * 32 + lane) * 4) = cs[0]; }
#define COLRED(i) { \
    LAS float* cp = (LAS float*)(L + L_CSP) + (((i) & 31) + 4 * (hd & 1) < 32 ? (i) + 4 * (hd & 1) : (i)); \
    *(LAS float*)(L + L_CS + (i) * 4) = (cp[0] + cp[96]) + (cp[192] + cp[288]); }

__global__ void __launch_bounds__(256, 1) fused_kernel(Params p) {
  extern __shared__ __attribute__((aligned(16))) char smem[];
  LAS char* L = (LAS char*)smem;
  const bool on35 = (blockIdx.x >> 3) < 28;
  const bool on4 = (blockIdx.x >> 3) < ((blockIdx.x & 1) ? 24 : 25);
  const rsrc_t rws = mk(p.ws, WS_BYTES);
  GAS unsigned* ctrl = (GAS unsigned*)p.ws;
  GAS unsigned* flags = (GAS unsigned*)((GAS char*)p.ws + FLAG_OFF);
  STAMP(0);
  unsigned budget = 1u << 22;

  v4u xf[28];
  f32x16 ak, av;
  float mean = 0.f, rstd = 0.f;
  f32x4 res[4];
  unsigned epoch;
  {
    FRESH();
    const rsrc_t rwq = mk(p.wqkv, D * 3 * D * 2);
    const rsrc_t rx = mk(p.x, S * D * 2);
    const int xvo = (t * 128 + w * 32 + wrow) * 896 + (((lane & 7) ^ (wrow >> 1)) << 4);
#define DMA_X(cc) dma64<8 * 896>(rx, xvo, xvo ^ 64, wrap7((cc) + rotx) * 128, (int)(size_t)ring + ((cc) & 3) * 4096)
    DMA_X(0);
    const int tl = by * 98 + w * 25 + lane, trow = (tl * 9363) >> 16;
    const int tvo = (lane < 25 && w * 25 + lane < 98) ? trow * 2688 + (tl - 7 * trow) * 128 : OOB;
    touch(rwq, tvo, 896, L);
    DMA_X(1);
    touch(rwq, tvo, 2 * 896, L);
    DMA_X(2);
    {
      unsigned long long pc;
      int len;
      asm volatile("s_getpc_b64 %0\n.Lfk_code_from:\n\ts_mov_b32 %1, .Lfk_code_to-.Lfk_code_from" : "=s"(pc), "=s"(len));
      touch(mk((const void*)pc, len > 0 ? len : 0), lane < 4 ? (by * 16 + w * 4 + lane) * 128 : OOB, 0, L);
    }
    DMA_X(3);
    asm volatile("buffer_load_dword %0, %1, %2, 0 offen sc1" : "=&v"(epoch) : "v"(0), "s"(rws) : "memory");
    SCHED();
    const int wvo = (w * 112 + (lane >> 2)) * 2688 + hd * 56 - (hd & 1) * 8 + (lane & 3) * 16;
    int wso = 896, wlb = (int)(size_t)(L + L_WQ) + w * 112 * 64 + 28672;
#define DMA_WQ() dma1<16 * 2688>(rwq, wvo, wso, wlb)
#define WQ_PART(from, to) { wso = (to) * 896; wlb += ((to) - (from)) * 28672 - 7168; }
    const int xr = r32 * 128 + 8 * hh, xsw = (r32 >> 1) & 7;
#define READ_X(cc) _Pragma("unroll") for (int e = 0; e < 4; ++e) { \
      const int ch = (e >> 1) * 4 + (e & 1); \
      LAS char* xb_ = ring + ((cc) & 3) * 4096 + xr; \
      const v2u a_ = *(LAS v2u*)(xb_ + ((ch ^ xsw) << 4)), b_ = *(LAS v2u*)(xb_ + (((ch + 2) ^ xsw) << 4)); \
      xf[4 * (cc) + e] = (v4u){a_.x, a_.y, b_.x, b_.y}; }
    const v4u ones = {0x3f803f80u, 0x3f803f80u, 0x3f803f80u, 0x3f803f80u};
    f32x16 gs = {}, ss = {};
#define XSTAT(i) { gs = mfma32(xf[i], xf[i], gs); ss = mfma32(ones, xf[i], ss); }
    STAMP(32);
    vmw<16>(); READ_X(0) LGKM0(); DMA_X(4); XSTAT(0) XSTAT(1) XSTAT(2) XSTAT(3) SCHED();
    vmw<15>(); READ_X(1) LGKM0(); DMA_X(5); XSTAT(4) XSTAT(5) XSTAT(6) XSTAT(7) SCHED();
    vmw<14>(); READ_X(2) LGKM0(); DMA_X(6); touch(rwq, tvo, 0, L); SCHED();
    asm volatile("s_waitcnt vmcnt(13)" : "+v"(epoch) :: "memory");
    epoch += 1u;
    READ_X(3)
    SCHED();
    sfor<20>([&](auto I) INL {
      constexpr int i = I;
      if constexpr (i == 6) { vmw<8 + 1 + 3>(); READ_X(4) }
      if constexpr (i == 10) { vmw<4 + 1 + 5>(); READ_X(5) }
      if constexpr (i == 14) { vmw<1 + 7>(); READ_X(6) WQ_PART(1, 2) }
      if constexpr ((i & 1) == 0) DMA_WQ();
      XSTAT(8 + i)
      SCHED();
    });
    vmw<3>();
    bar();
    STAMP(33);
    LAS char* cb = L + L_WQ + (w * 112 + 8 * hh + q4) * 64 + (g & 1) * 32 + 8 * p4;
    {
      v4u wk[28], wc[7];
      f32x16 cs = {};
      float dg = 0.f;
      { LAS char* wb = WB(0); _Pragma("unroll") for (int ks = 0; ks < 4; ++ks) wk[ks] = WFRAG(wb, 1, ks); }
      SCHED();
      sfor<28>([&](auto I) INL {
        constexpr int ks = I;
        if constexpr (ks == 0) MFMA32V0(ak, wk[ks], xf[ks]); else MFMA32VA(ak, wk[ks], xf[ks]);
        if constexpr (ks + 4 < 28) { LAS char* wb = WB((ks + 4) >> 2); wk[ks + 4] = WFRAG(wb, 1, ks & 3); }
        if constexpr (ks == 12) WQ_PART(2, 0)
        if constexpr ((ks <= 9 && ks % 3 == 0) || (ks >= 14 && ks % 5 == 4)) DMA_WQ();
        if constexpr (ks >= 1 && ks < 17) { dg = ((ks - 1) & 3) + 8 * ((ks - 1) >> 2) + 4 * hh == r32 ? gs[ks - 1] : dg; PINV(dg); }
        if constexpr (ks == 17) { mean = ss[0] * (1.f / D); rstd = sum32(dg) * (1.f / D) - mean * mean; PINV(mean); PINV(rstd); }
        if constexpr (ks == 18) { rstd = __builtin_amdgcn_rsqf(fmaxf(rstd, 0.f) + EPS); PINV(rstd); }
        if constexpr (ks >= 17 && ks < 24) wc[ks - 17] = trfrag(cb + 28672 + (ks - 17) * 1024, cb + 28672 + (ks - 17) * 1024 + 256);
        if constexpr (ks >= 19 && ks < 26) cs = mfma32(ones, wc[ks - 19], cs);
        SCHED();
      });
      *(LAS float*)(L + (hh ? L_DUMP : L_CSP + (w * 96 + 32) * 4) + r32 * 4) = cs[0];
    }
    STAMP(34);
    vmw<3>();
    bar();
    STAMP(35);
    f32x4 cc[8];
    {
      v4u wv[28], wc[7];
      f32x16 cs = {};
      float pp[4];
      LAS float* pb = (LAS float*)(L + L_CSP) + 32 + 16 * w + c + 4 * (hd & 1);
      int hto = lane < 32 ? T1H : OOB;
      PINV(pb); PINV(hto);
      { LAS char* wb = WB(0); _Pragma("unroll") for (int ks = 0; ks < 4; ++ks) wv[ks] = WFRAG(wb, 2, ks); }
      SCHED();
      sfor<28>([&](auto I) INL {
        constexpr int ks = I;
        if constexpr (ks == 0) MFMA32V0(av, wv[ks], xf[ks]); else MFMA32VA(av, wv[ks], xf[ks]);
        if constexpr (ks + 4 < 28) { LAS char* wb = WB((ks + 4) >> 2); wv[ks + 4] = WFRAG(wb, 2, ks & 3); }
        if constexpr (ks < 7) wc[ks] = trfrag(cb + 2 * 28672 + ks * 1024, cb + 2 * 28672 + ks * 1024 + 256);
        if constexpr (ks >= 2 && ks < 9) cs = mfma32(ones, wc[ks - 2], cs);
        if constexpr (ks % 3 == 1 && ks / 3 < 4) DMA_WQ();
        if constexpr (TCH0 && ks == 13) touch(mk(p.w1, S * F * 2), hto, 0, L);
        if constexpr (ks == 11) *(LAS float*)(L + (hh ? L_DUMP : L_CSP + (w * 96 + 64) * 4) + r32 * 4) = cs[0];
        if constexpr (ks == 12) bar();
        if constexpr (ks == 13) { pp[0] = pb[0]; pp[1] = pb[96]; pp[2] = pb[192]; pp[3] = pb[288]; }
        if constexpr (ks == 17) *(LAS float*)(L + L_CS + (32 + 16 * w + c) * 4) = (pp[0] + pp[1]) + (pp[2] + pp[3]);
        if constexpr (ks == 19) bar();
        if constexpr (ks >= 20) cc[ks - 20] = CSUM(1, ks - 20);
        SCHED();
      });
    }
#pragma unroll
    for (int a = 0; a < 4; ++a) res[a] = (f32x4){0.f, 0.f, 0.f, 0.f};
#pragma unroll
    for (int i = 0; i < 16; ++i) ak[i] = (ak[i] - mean * cc[i >> 2][i & 3]) * rstd;
    PINV(ak);
    asm volatile("s_nop 3" : "+v"(av));
#pragma unroll
    for (int i = 0; i < 16; ++i) av[i] = (av[i] - mean * cc[4 + (i >> 2)][i & 3]) * rstd;
  }

#pragma unroll 1
  for (int layer = 0;; ++layer) {
    GAS unsigned* fl = flags + layer * 5 * 256 * 32;
    STAMP(1 + layer * 16);
    v4u qf[2], kv[4];
    v4u wo[4][4];
    v2u rr[4];
    unsigned b2r;
    v4u wz[30][4];
    const rsrc_t rw1 = mk(p.w1, on4 ? S * F * 2 : 0);
    const rsrc_t rw2 = mk(p.w2, on35 ? S * F * 2 : 0);
    {
      FRESH();
#pragma unroll
      for (int i = 0; i < 16; i += 2) {
        kv[i >> 3][(i >> 1) & 3] = pk(ak[i], ak[i + 1]);
        kv[2 + (i >> 3)][(i >> 1) & 3] = pk(av[i], av[i + 1]);
      }
      if (hh) { kv[1][2] = 0u; kv[1][3] = 0u; kv[3][2] = 0x00003f80u; kv[3][3] = 0u; }
      {
        const rsrc_t rkv = mk(p.ws, t < 15 ? WS_BYTES : 0);
#pragma unroll
        for (int i = 0; i < 4; ++i) st16<SC1>(kv[i], rkv, KV_OFF + layer * KV_SZ + (hd * 64 + 4 * t + w) * 4096 + lane * 16, i * 1024);
      }
      SCHED();
      if (layer == 0) {
        DMA_WO(0) DMA_WO(1)
        vmw<8 + 4>();
        if (t == 15) vmw<8>();
        COLSUM(0)
        LGKM0();
        publish_n<8>(fl + (0 * 256 + bid) * 32, epoch);
        DMA_WO(2) DMA_WO(3)
        if (tid < 32) COLRED(tid)
      }
      v4u wq[28];
      {
        LAS char* wb = WB(0);
#pragma unroll
        for (int ks = 0; ks < 4; ++ks) wq[ks] = WFRAG(wb, 0, ks);
      }
      f32x16 aq;
      STAMP(2 + layer * 16 + layer * 27);
      LAS char* kvo = L + L_KVW + (8 + w) * 4096;
      sfor<28>([&](auto I) INL {
        constexpr int ks = I;
        if constexpr (ks == 0) MFMA32V0(aq, wq[ks], xf[ks]); else MFMA32VA(aq, wq[ks], xf[ks]);
        if constexpr (ks + 4 < 28) { LAS char* wb = WB((ks + 4) >> 2); wq[ks + 4] = WFRAG(wb, 0, ks & 3); }
        if constexpr (ks == QPUB) {
          if (layer != 0) publish(fl + (0 * 256 + bid) * 32, epoch);
          STAMP(3 + layer * 16);
        }
        SCHED();
      });
      if (layer == 0) vmw<0>();
#pragma unroll
      for (int j = 0; j < 16; ++j) WO_RD1(j);
      bar();
      *(LAS v4u*)(kvo + lane * 16) = kv[0];
      *(LAS v4u*)(kvo + 1024 + lane * 16) = kv[1];
      *(LAS v4u*)(kvo + 2048 + ((lane * 16) ^ (hh << 6))) = kv[2];
      *(LAS v4u*)(kvo + 3072 + ((lane * 16) ^ (hh << 6) ^ 128)) = kv[3];
      const float qs = rstd * (0.18898223650461362f * 1.4426950408889634f), qn = -mean * qs;
      f32x4 cq[4];
#pragma unroll
      for (int a = 0; a < 4; ++a) cq[a] = CSUM(0, a) * qn;
      {
        const rsrc_t rxr = mk(p.x, (on35 && layer == 0) ? S * D * 2 : 0);
#pragma unroll
        for (int a = 0; a < 4; ++a) rr[a] = ld8<0>(rxr, (64 * st + 16 * w + c) * 896 + dt * 128 + g * 8, (a ^ pa) * 32);
      }
      b2r = (unsigned)__builtin_amdgcn_raw_buffer_load_b16(mk(p.b2, S * 2), (64 * st + 16 * w + c) * 2, 0, 0);
      MFMA_SETTLE(aq);
#pragma unroll
      for (int a = 0; a < 4; ++a) {
        qf[a >> 1][(a & 1) * 2] = pk(aq[4 * a] * qs + cq[a][0], aq[4 * a + 1] * qs + cq[a][1]);
        qf[a >> 1][(a & 1) * 2 + 1] = pk(aq[4 * a + 2] * qs + cq[a][2], aq[4 * a + 3] * qs + cq[a][3]);
      }
      if (hh) { qf[1][2] = 0x00003f80u; qf[1][3] = 0u; }
    }
    SCHED();
    {
      FRESH();
      LAS char* kvw = L + L_KVW + w * 4096 + lane * 16;
      LAS char* vw = L + L_KVW + w * 4096 + 2048 + ((lane * 16) ^ (hh << 6));
      LAS char* vw1 = L + L_KVW + w * 4096 + 3072 + ((lane * 16) ^ (hh << 6) ^ 128);
      const int vx = ((g & 1) * 1024 + (p4 & 1) * 512 + hh * 64 + q4 * 16) ^ ((p4 & 1) << 6) ^ ((g & 1) << 7);
      LAS char* vr0 = L + L_KVW + w * 4096 + 2048 + vx + (p4 >> 1) * 8;
      LAS char* vr1 = L + L_KVW + w * 4096 + 2048 + (vx ^ 128) + (p4 >> 1) * 8;
      f32x16 s8;
      unsigned long long em[16];
      unsigned pm[8];
      {
        asm volatile("s_nop 1" : "+v"(qf[0]), "+v"(qf[1]));
        MFMA32V0(s8, kv[0], qf[0]);
        MFMA32VA(s8, kv[1], qf[1]);
#pragma unroll
        for (int i = 0; i < 16; i += 2) {
          const int kl = (i & 3) + 8 * (i >> 2) + 4 * hh;
          pm[i >> 1] = (kl > r32 ? 0xffffu : 0u) | (kl + 1 > r32 ? 0xffff0000u : 0u);
          PINV(pm[i >> 1]);
        }
#pragma unroll
        for (int i = 0; i < 16; ++i) { em[i] = __builtin_amdgcn_ballot_w64(((i & 3) + 8 * (i >> 2) + 4 * hh) > r32); PINS(em[i]); }
      }
      int kvo = KV_OFF + layer * KV_SZ + (hd * 64 + 4 * t + w) * 4096 + lane * 16 - 32768;
      int oo = O_OFF + layer * O_SZ + ((2 * t + (w >> 1)) * 16 + hd) * 4096 + (2 * (w & 1) + (r32 >> 4)) * 1024 + hh * 256 + c * 16;
      unsigned nk2 = (t < 2 && hh) ? 0xf149u : 0u, nk1 = (t < 1 && hh) ? 0xf149u : 0u;
      int t1o = T1O;
      PINV(kvo); PINV(oo); PINV(nk1); PINV(nk2); PINV(kvw); PINV(vw); PINV(vw1); PINV(vr0); PINV(vr1); PINV(t1o);
      if (t > 0) {
        const int tp = (t >= 2 && (lane & 1)) ? t - 2 : t - 1;
        wait_flags(fl + (0 * 256 + (((tp & 1) * 16 + hd) * 8 + (tp >> 1))) * 32, epoch, budget);
      }
      STAMP(4 + layer * 16);
      v4u ra[4], rb[4];
      unsigned tw = 0u;
      {
        const rsrc_t r1 = mk(p.ws, t >= 1 ? WS_BYTES : 0), r2 = mk(p.ws, t >= 2 ? WS_BYTES : 0);
        rb[0] = ld16<SC1>(r2, kvo, 0); rb[1] = ld16<SC1>(r2, kvo, 1024);
        ra[0] = ld16<SC1>(r1, kvo, 16384); ra[1] = ld16<SC1>(r1, kvo, 16384 + 1024);
        SCHED();
        rb[2] = ld16<SC1>(r2, kvo, 2048); rb[3] = ld16<SC1>(r2, kvo, 3072);
        ra[2] = ld16<SC1>(r1, kvo, 16384 + 2048); ra[3] = ld16<SC1>(r1, kvo, 16384 + 3072);
        SCHED();
        if (TCH1 == 1) tw = __builtin_amdgcn_raw_buffer_load_b32(mk(p.w1, S * F * 2), t1o, 0, 0);
        SCHED();
        rb[1][2] |= nk2; ra[1][2] |= nk1;
        *(LAS v4u*)(kvw) = rb[0]; *(LAS v4u*)(kvw + 1024) = rb[1];
        *(LAS v4u*)(kvw + 16384) = ra[0]; *(LAS v4u*)(kvw + 16384 + 1024) = ra[1];
      }
      bar();
      STAMP(37 + layer * 4);
      v4u kf[8][2];
#pragma unroll
      for (int jj = 1; jj <= 8; ++jj) { const int j = jj & 7; kf[j][0] = *(LAS v4u*)(kvw + j * 4096); kf[j][1] = *(LAS v4u*)(kvw + j * 4096 + 1024); }
      SCHED();
      f32x16 sc[8], se;
      float mx = -1e30f, mx2 = -1e30f;
#define SEL4(i) asm("v_cndmask_b32 %0, %4, %8, %12\n\tv_cndmask_b32 %1, %5, %9, %13\n\tv_cndmask_b32 %2, %6, %10, %14\n\tv_cndmask_b32 %3, %7, %11, %15" \
        : "=&v"(se[i]), "=&v"(se[(i) + 1]), "=&v"(se[(i) + 2]), "=&v"(se[(i) + 3]) \
        : "v"(s8[i]), "v"(s8[(i) + 1]), "v"(s8[(i) + 2]), "v"(s8[(i) + 3]), "v"(sc[0][i]), "v"(sc[0][(i) + 1]), "v"(sc[0][(i) + 2]), "v"(sc[0][(i) + 3]), \
          "s"(em[i]), "s"(em[(i) + 1]), "s"(em[(i) + 2]), "s"(em[(i) + 3]))
#define MAX8(v, i) asm volatile("v_max3_f32 %0, %0, %2, %3\n\tv_max3_f32 %1, %1, %4, %5\n\tv_max3_f32 %0, %0, %6, %7\n\tv_max3_f32 %1, %1, %8, %9" \
        : "+v"(mx), "+v"(mx2) : "v"(v[i]), "v"(v[(i) + 1]), "v"(v[(i) + 2]), "v"(v[(i) + 3]), "v"(v[(i) + 4]), "v"(v[(i) + 5]), "v"(v[(i) + 6]), "v"(v[(i) + 7]))
      sfor<16>([&](auto N) INL {
        constexpr int n = N, j = ((n >> 1) + 1) & 7, hf = n & 1;
        if constexpr (hf == 0) MFMA32V0(sc[j], kf[j][0], qf[0]); else MFMA32VA(sc[j], kf[j][1], qf[1]);
        if constexpr (n == 0) *(LAS v4u*)(vw) = rb[2];
        if constexpr (n == 1) *(LAS v4u*)(vw1) = rb[3];
        if constexpr (n == 2) *(LAS v4u*)(vw + 16384) = ra[2];
        if constexpr (n == 3) *(LAS v4u*)(vw1 + 16384) = ra[3];
        if constexpr (TCH1 == 2 && n == 4) touch(mk(p.w1, S * F * 2), t1o, 0, L);
        if constexpr (n == 5) asm volatile("s_nop 3");
        if constexpr (n >= 6) MAX8(sc[(n >> 1) - 2], 8 * hf);
        SCHED();
      });
      MAX8(sc[6], 0); MAX8(sc[6], 8); MAX8(sc[7], 0); MAX8(sc[7], 8);
      SCHED();
      SEL4(0); SEL4(4); SEL4(8); SEL4(12);
      MAX8(se, 0); MAX8(se, 8);
      mx = max32(max3(mx, mx2, mx2));
      f32x16 nm;
#pragma unroll
      for (int i = 0; i < 16; ++i) nm[i] = -mx;
      asm volatile("s_nop 1" : "+v"(nm));
      bar();
      STAMP(38 + layer * 4);
      f32x16 ao;
      v4u vf[9][2], pf[9][2];
      float ex[32][4];
#define VF(j, s) vf[j][s] = trfrag(vr0 + (j) * 4096 + (s) * 256, vr1 + (j) * 4096 + (s) * 256)
      sfor<33>([&](auto N) INL {
        constexpr int n = N, j = n >> 2, q = n & 3;
        if constexpr (j == 0) {
          if constexpr (q == 0) MFMA32VC(sc[1], kf[1][0], qf[0], nm);
          if constexpr (q == 1) MFMA32VA(sc[1], kf[1][1], qf[1]);
          if constexpr (q == 2) MFMA32VC(sc[2], kf[2][0], qf[0], nm);
          if constexpr (q == 3) MFMA32VA(sc[2], kf[2][1], qf[1]);
        } else if constexpr (j < 8) {
          if constexpr (q == 1) { if constexpr (j == 1) MFMA32V0(ao, vf[0][0], pf[0][0]); else MFMA32VA(ao, vf[j - 1][0], pf[j - 1][0]); }
          if constexpr (q == 2) MFMA32VA(ao, vf[j - 1][1], pf[j - 1][1]);
          if constexpr (j + 2 < 8) {
            if constexpr (q == 0) MFMA32VC(sc[j + 2], kf[j + 2][0], qf[0], nm);
            if constexpr (q == 3) MFMA32VA(sc[j + 2], kf[j + 2][1], qf[1]);
          } else if constexpr (j == 6) {
            if constexpr (q == 0) MFMA32VA(ao, vf[8][0], pf[8][0]);
            if constexpr (q == 3) MFMA32VA(ao, vf[8][1], pf[8][1]);
          }
        } else MFMA32VA(ao, vf[7][0], pf[7][0]);
        SCHED();
        if constexpr (n < 32) {
#pragma unroll
          for (int i = 0; i < 4; ++i) { if constexpr (j == 0) ex[n][i] = __builtin_amdgcn_exp2f(se[4 * q + i] - mx); else ex[n][i] = __builtin_amdgcn_exp2f(sc[j][4 * q + i]); }
        }
        SCHED();
        if constexpr (n > 0) {
          constexpr int jp = (n - 1) >> 2, qp = (n - 1) & 3;
#pragma unroll
          for (int i = 0; i < 2; ++i) {
            const unsigned u = pk(ex[n - 1][2 * i], ex[n - 1][2 * i + 1]);
            if constexpr (jp == 0) { pf[0][qp >> 1][(2 * qp + i) & 3] = u & pm[2 * qp + i]; pf[8][qp >> 1][(2 * qp + i) & 3] = u & ~pm[2 * qp + i]; }
            else pf[jp][qp >> 1][(2 * qp + i) & 3] = u;
          }
        }
        if constexpr (n == 22) KEEP(nm);
        if constexpr (j < 8 && q < 2) VF(j, q);
        if constexpr (j == 5 && q >= 2) VF(8, q - 2);
        SCHED();
      });
      asm volatile("s_nop 1" : "+v"(pf[7][1]));
      MFMA32VA(ao, vf[7][1], pf[7][1]);
      MFMA_SETTLE(ao);
      const float inv = __builtin_amdgcn_rcpf(hi32(ao[12]));
      v4u of[2];
#pragma unroll
      for (int i = 0; i < 16; i += 2) of[i >> 3][(i >> 1) & 3] = pk(ao[i] * inv, ao[i + 1] * inv);
      if (hh) { of[1][2] = 0u; of[1][3] = 0u; }
      st16<SC1>(of[0], rws, oo, 0);
      st16<SC1>(of[1], rws, oo, 512);
      SCHED();
      STAMP(39 + layer * 4);
#pragma unroll
      for (int a = 0; a < 4; ++a) { res[a] += unpack4(rr[a]); PINV(res[a]); }
      PINV(b2r); KEEP(tw);
      bar();
      DPUMP(0, 0, NO1)
      publish_n<4 * NO1>(fl + (1 * 256 + bid) * 32, epoch);
      STAMP(5 + layer * 16);
    }
    if (on35) {
      {
        FRESH();
        const int tq = st >> 1;
        int oo = O_OFF + layer * O_SZ + (st * 16 + 4 * w) * 4096 + lane * 16;
        int sb = S3_OFF + layer * S3_SZ + st * 8192 + (16 * w + c) * 16;
        int sw = g == 0 ? sb + dt * 1024 : OOB;
        int ob[4];
#pragma unroll
        for (int b = 0; b < 4; ++b) { ob[b] = ((b + w) & 3) * 1024; PINS(ob[b]); }
        PINV(oo); PINV(sb); PINV(sw); PINV(red);
        GAS unsigned* fp = fl + (1 * 256 + (((tq & 1) * 16 + 4 * w + (lane & 3)) * 8 + (tq >> 1))) * 32;
        unsigned pf;
        DPUMP(0, NO1, NO1 + NO2)
        POLL(pf, fp);
        DPUMP(0, NO1 + NO2, Z_G)
        POLLED(pf, 4 * NO3, fp)
        STAMP(6 + layer * 16);
        v4u of[16];
#define LOAD_O(I) of[I] = ld16<SC1>(rws, oo + ((I) & 3) * 4096, ob[(((I) >> 2) + 1) & 3])
#pragma unroll
        for (int i = 0; i < 12; ++i) { LOAD_O(i); SCHED(); }
        vmw<12>();
        f32x4 acc[4][4];
        sfor<16>([&](auto I) INL {
          constexpr int i = I, b = ((i >> 2) + 1) & 3, k = i & 3;
#pragma unroll
          for (int a = 0; a < 4; ++a) {
            acc[a][b] = mfma16(wo[k][a], of[i], k > 0 ? acc[a][b] : b ? (f32x4){0.f, 0.f, 0.f, 0.f} : res[a]);
            SCHED();
            if constexpr (i < 4) { if (a == 0) LOAD_O(i + 12); }
            if (a == 1) DTAIL1(0, Z_G, i)
            if constexpr (i >= 4) { if (a == 2) REDW(k, i >> 2); }
            SCHED();
          }
        });
        bar();
        float s1 = 0.f, s2 = 0.f;
#pragma unroll
        for (int a = 0; a < 4; ++a) {
          res[a] = (acc[a][0] + REDR(a, 1)) + (REDR(a, 2) + REDR(a, 3));
          s1 += (res[a][0] + res[a][1]) + (res[a][2] + res[a][3]);
          s2 += (res[a][0] * res[a][0] + res[a][1] * res[a][1]) + (res[a][2] * res[a][2] + res[a][3] * res[a][3]);
        }
        s1 = sum32(sum16(s1)); s2 = sum32(sum16(s2));
        st16<SC1>((v4u){asu(s1), epoch, asu(s2), epoch}, rws, sw, 0);
        STAMP(7 + layer * 16);
        bar();
        LAS char* hst = L + L_HST + w * 2048;
        const int hr = 8 * (g & 1) + q4;
        int h0 = raddr(hr) ^ ((g >> 1) << 5), h1 = raddr(hr + 4) ^ ((g >> 1) << 5), hw = (c * 128 + (swz(c) << 5) + g * 8) ^ (pa << 5);
        int ho = HT_OFF + layer * HT_SZ + (dt * 64 + 2 * st + (w >> 1)) * 4096 + (g >> 1) * 1024 + (2 * (w & 1) + (g & 1)) * 256 + c * 16;
        PINV(h0); PINV(h1); PINV(hw); PINV(ho);
        int sa = sb + g * 1024, sc = sb + (g < 3 ? g + 4 : g) * 1024;
        PINV(sa); PINV(sc);
        DPUMP(Z_G, Z_G, Z_G + NG2)
        v4u ga, gb;
        asm volatile("buffer_load_dwordx4 %0, %2, %4, 0 offen sc1\n\tbuffer_load_dwordx4 %1, %3, %4, 0 offen sc1" : "=&v"(ga), "=&v"(gb) : "v"(sa), "v"(sc), "s"(rws) : "memory");
        DPUMP(Z_G, Z_G + NG2, Z_H)
        asm volatile("s_waitcnt vmcnt(%2)" : "+v"(ga), "+v"(gb) : "n"(4 * NG3) : "memory");
        while (__builtin_amdgcn_ballot_w64((ga.y != epoch) | (ga.w != epoch) | (gb.y != epoch) | (gb.w != epoch)) != 0ull) {
          if (budget == 0u) break;
          --budget;
          asm volatile("buffer_load_dwordx4 %0, %2, %4, 0 offen sc1\n\tbuffer_load_dwordx4 %1, %3, %4, 0 offen sc1\n\ts_waitcnt vmcnt(0)"
                       : "=&v"(ga), "=&v"(gb) : "v"(sa), "v"(sc), "s"(rws) : "memory");
        }
        float t1 = asf(ga.x) + (g < 3 ? asf(gb.x) : 0.f), t2 = asf(ga.z) + (g < 3 ? asf(gb.z) : 0.f);
        STAMP(8 + layer * 16);
        t1 = sum32(sum16(t1)); t2 = sum32(sum16(t2));
        const float m = t1 * (1.f / D), var = fmaxf(t2 * (1.f / D) - m * m, 0.f);
        const float r1 = __builtin_amdgcn_rsqf(var + EPS);
        const float r = r1 * __builtin_amdgcn_rsqf(var * r1 * r1 + EPS), nr = -m * r;
#pragma unroll
        for (int a = 0; a < 4; ++a) {
          const v2u v = {pk(res[a][0] * r + nr, res[a][1] * r + nr), pk(res[a][2] * r + nr, res[a][3] * r + nr)};
          *(LAS v2u*)(hst + (hw ^ (a << 5))) = v;
        }
#pragma unroll
        for (int e = 0; e < 2; ++e) st16<SC1>(trfrag(hst + (h0 ^ (e << 6)), hst + (h1 ^ (e << 6))), rws, ho, e * 2048);
        SCHED();
        DPUMP(Z_G, Z_H, Z_H + NH1)
        publish_n<4 * NH1>(fl + (2 * 256 + bid) * 32, epoch);
        STAMP(9 + layer * 16);
      }
      if (on4) {
        FRESH();
        v2u bb[4];
        {
          const rsrc_t rb1 = mk(p.b1, F * 2);
#pragma unroll
          for (int a = 0; a < 4; ++a) bb[a] = ld8<0>(rb1, ft * 128 + g * 8, a * 32);
        }
        int hv[4], hk[16];
#pragma unroll
        for (int b = 0; b < 4; ++b) { hv[b] = HT_OFF + layer * HT_SZ + (dt4 * 64 + 16 * w) * 4096 + ((b + w4) & 3) * 1024 + lane * 16; PINV(hv[b]); }
#pragma unroll
        for (int k = 0; k < 16; ++k) { hk[k] = ((k + rot4) & 15) * 4096; PINS(hk[k]); }
        int uo = U_OFF + layer * U_SZ + (dt4 * 56 + 2 * ft) * 4096 + w4 * 1024 + lane * 16;
        PINV(uo); PINV(red);
        const int sp = 8 * w + (lane & 7);
        GAS unsigned* fp = fl + (2 * 256 + ((sp & 3) * 7 + dt4) * 8 + (sp >> 2)) * 32;
        unsigned pf;
        DPUMP(Z_G, Z_H + NH1, Z_H + NH1 + NH2)
        POLL(pf, fp);
        DPUMP(Z_G, Z_H + NH1 + NH2, Z_U)
        POLLED(pf, 4 * NH3, fp)
        STAMP(10 + layer * 16);
        v4u af[64];
#define LOAD_H(I) af[I] = ld16<SC1>(rws, hv[(((I) >> 4) + 1) & 3], hk[(I) & 15])
#pragma unroll
        for (int i = 0; i < WIN4; ++i) { LOAD_H(i); SCHED(); }
        vmw<WIN4>();
        f32x4 acc[4][4], part[4][3];
        sfor<64>([&](auto I) INL {
          constexpr int i = I, b = ((i >> 4) + 1) & 3, k = i & 15;
#pragma unroll
          for (int a = 0; a < 4; ++a) {
            acc[a][b] = mfma16(wz[k][a], af[i], k > 0 ? acc[a][b] : b ? (f32x4){0.f, 0.f, 0.f, 0.f} : acc[a][0]);
            SCHED();
            if constexpr (i + WIN4 < 64) { if (a == 0) LOAD_H(i + WIN4); }
            if constexpr (i < 8) { if (a == 1) DTAIL1(Z_G, Z_U, 2 * i) if (a == 2) DTAIL1(Z_G, Z_U, 2 * i + 1) }
            if constexpr (i >= 16 && k < 4) { if (a == 2) REDW(k, i >> 4); }
            if constexpr (i >= 43 && i < 47) { if (a == 3) { PINV(bb[i - 43]); acc[i - 43][0] = unpack4(bb[i - 43]); } }
            if constexpr (i == 54) { if (a == 3) bar(); }
            if constexpr (i >= 55 && i < 59) { if (a > 0) part[i - 55][a - 1] = REDR(i - 55, a); }
            if constexpr (i >= 59 && i < 63) { if (a == 2) { part[i - 59][0] = (part[i - 59][0] + part[i - 59][1]) + part[i - 59][2]; PINV(part[i - 59][0]); } }
            SCHED();
          }
        });
        STAMP(11 + layer * 16);
        v4u uf[2];
#pragma unroll
        for (int a = 0; a < 4; ++a) {
          const f32x4 u4 = acc[a][0] + part[a][0];
          float gl[4];
#pragma unroll
          for (int j = 0; j < 4; ++j) {
            const float u = u4[j];
            const float e = __builtin_amdgcn_exp2f(u * (u * u * (-0.10294324f) - 2.3022082f));
            gl[j] = u * __builtin_amdgcn_rcpf(1.f + e);
          }
          uf[a >> 1][(a & 1) * 2] = pk(gl[0], gl[1]);
          uf[a >> 1][(a & 1) * 2 + 1] = pk(gl[2], gl[3]);
          if (a == 1) { st16<SC1>(uf[0], rws, uo, 0); SCHED(); }
        }
        st16<SC1>(uf[1], rws, uo, 4096);
        SCHED();
        STAMP(40 + layer * 4);
        bar();
        DPUMP(Z_U, Z_U, Z_U + NU1)
        publish_n<4 * NU1>(fl + (3 * 256 + bid) * 32, epoch);
      } else {
        FRESH();
        DPUMP(Z_G, Z_H + NH1, Z_U)
        DRAIN();
        DTAIL(Z_G, Z_U)
        LGKM0();
        DPUMP(Z_U, Z_U, Z_U + NU1)
      }
      STAMP(12 + layer * 16);
      {
        FRESH();
        int uk[14], uv[4];
#pragma unroll
        for (int k = 0; k < 14; ++k) { uk[k] = wrap14(k + rot5) * 4096; PINS(uk[k]); }
#pragma unroll
        for (int a = 0; a < 4; a += 2) { uv[a] = U_OFF + layer * U_SZ + (dt * 56 + 14 * w) * 4096 + (a ^ pa) * 1024 + lane * 16; PINV(uv[a]); uv[a + 1] = uv[a] + 1024; }
        int xb = layer ? (64 * st + 16 * w + c) * 896 + dt * 128 + g * 8
                       : X2_OFF + (2 * st + (w >> 1)) * 28672 + (4 * dt + (g >> 1)) * 1024 + ((g & 1) * 32 + (w & 1) * 16 + c) * 16;
        int so = g == 0 ? S5_OFF + (st * 8 + dt) * 512 + (16 * w + c) * 8 : OOB;
#pragma unroll
        for (int a = 0; a < 4; ++a) { res[a] += bflo(b2r); PINV(res[a]); }
        PINV(xb); PINV(so); PINV(red);
        const int pi = wrap7(lane & 7);
        GAS unsigned* fp = fl + (3 * 256 + (pi == 3 ? (21 + (dt & 3)) * 8 + (dt >> 2) : (7 * (pi & 3) + dt) * 8 + (pi >> 2)) + 2 * w) * 32;
        unsigned pf;
        DPUMP(Z_U, Z_U + NU1, 30 - NU3)
        POLL(pf, fp);
        DPUMP(Z_U, 30 - NU3, 30)
        POLLED(pf, 4 * NU3, fp)
        STAMP(13 + layer * 16);
        v4u af[56];
#define LOAD_U(I) af[I] = ld16<SC1>(rws, uv[(I) / 14], uk[(I) % 14])
#pragma unroll
        for (int i = 0; i < WIN5; ++i) { LOAD_U(i); SCHED(); }
        vmw<WIN5>();
        f32x4 acc[4][4], part[3][3];
        sfor<56>([&](auto I) INL {
          constexpr int i = I, a = i / 14, k = i % 14;
#pragma unroll
          for (int bq = 0; bq < 4; ++bq) {
            const int b = (bq + 1) & 3;
            acc[a][b] = mfma16(af[i], wz[16 + k][b], k > 0 ? acc[a][b] : b ? (f32x4){0.f, 0.f, 0.f, 0.f} : res[a]);
            SCHED();
            if constexpr (i + WIN5 < 56) { if (bq == 0) LOAD_U(i + WIN5); }
            if constexpr (i < 8) { if (bq == 1) DTAIL1(Z_U, 30, 2 * i) if (bq == 2) DTAIL1(Z_U, 30, 2 * i + 1) }
            if constexpr (a > 0 && k < 3) { if (bq == 2) REDW(a - 1, k + 1); }
            if constexpr (i == 47) { if (bq == 3) bar(); }
            if constexpr (i >= 48 && i < 51) { if (bq > 0) part[i - 48][bq - 1] = REDR(i - 48, bq); }
            if constexpr (i >= 51 && i < 54) { if (bq == 2) { res[i - 51] = (acc[i - 51][0] + part[i - 51][0]) + (part[i - 51][1] + part[i - 51][2]); PINV(res[i - 51]); } }
            SCHED();
          }
        });
        STAMP(14 + layer * 16);
        REDW(3, 1); REDW(3, 2); REDW(3, 3);
        v2u xo[4];
        float s1 = 0.f, s2 = 0.f;
#pragma unroll
        for (int a = 0; a < 3; ++a) {
          xo[a] = (v2u){pk(res[a][0], res[a][1]), pk(res[a][2], res[a][3])};
          s1 += (res[a][0] + res[a][1]) + (res[a][2] + res[a][3]);
          s2 += (res[a][0] * res[a][0] + res[a][1] * res[a][1]) + (res[a][2] * res[a][2] + res[a][3] * res[a][3]);
        }
        PINV(s1); PINV(s2);
        const rsrc_t ro = mk(p.out, S * D * 2);
        if (layer == 0) st16<SC1>((v4u){xo[0].x, xo[0].y, xo[1].x, xo[1].y}, rws, xb, pa * 1024);
        else { st8<0>(xo[0], ro, xb, pa * 32); st8<0>(xo[1], ro, xb, pa * 32 + 32); st8<0>(xo[2], ro, xb, (2 ^ pa) * 32); }
        bar();
        res[3] = (acc[3][0] + REDR(3, 1)) + (REDR(3, 2) + REDR(3, 3));
        xo[3] = (v2u){pk(res[3][0], res[3][1]), pk(res[3][2], res[3][3])};
        if (layer == 0) {
          st16<SC1>((v4u){xo[2].x, xo[2].y, xo[3].x, xo[3].y}, rws, xb, (2 ^ pa) * 1024);
          SCHED();
          s1 += (res[3][0] + res[3][1]) + (res[3][2] + res[3][3]);
          s2 += (res[3][0] * res[3][0] + res[3][1] * res[3][1]) + (res[3][2] * res[3][2] + res[3][3] * res[3][3]);
          s1 = sum32(sum16(s1)); s2 = sum32(sum16(s2));
          st8<SC1>((v2u){asu(s1), asu(s2)}, rws, so, 0);
          publish(flags + (4 * 256 + bid) * 32, epoch);
        } else {
          st8<0>(xo[3], ro, xb, (2 ^ pa) * 32 + 32);
        }
      }
    }
    else if (TCH2) {
      FRESH();
      const int vo = (448 * w + 4 * (by - 28) + (lane >> 4) + 16 * ((lane >> 2) & 3)) * (S * 2) + bx * 512 + (lane & 3) * 128;
#pragma unroll
      for (int i = 0; i < 7; ++i) touch(mk(p.w2, S * F * 2), vo, i * 64 * S * 2, L);
    }
    STAMP(15 + layer * 16);
    if (layer) break;
    {
      FRESH();
      DMA_WO(0) DMA_WO(1) DMA_WO(2)
      const int st1 = 2 * t + (w >> 1);
      v4u wk[28], wv[28];
      {
        LAS char* wb = WB(0);
        wk[0] = WFRAG(wb, 1, 0); wv[0] = WFRAG(wb, 2, 0); wk[1] = WFRAG(wb, 1, 1); wv[1] = WFRAG(wb, 2, 1);
      }
      f32x4 ck[4], cv[4];
#pragma unroll
      for (int a = 0; a < 4; ++a) { ck[a] = CSUM(1, a); cv[a] = CSUM(2, a); PINV(ck[a]); PINV(cv[a]); }
      int xo = X2_OFF + (4 * t + w) * 28672 + lane * 16, so = S5_OFF + (st1 * 8 + hh * 4) * 512 + ((w & 1) * 32 + r32) * 8, xk[7];
      int so3 = hh ? OOB : so + 3 * 512;
#pragma unroll
      for (int k = 0; k < 7; ++k) { xk[k] = wrap7(k + rotx) * 4096; PINS(xk[k]); }
      PINV(xo); PINV(so); PINV(so3);
      GAS unsigned* fp = flags + (4 * 256 + ((st1 & 3) * 7 + wrap7(lane & 7)) * 8 + (st1 >> 2)) * 32;
      unsigned pf;
      POLL(pf, fp);
      DMA_WO(3)
      POLLED(pf, 4, fp)
      STAMP(18);
      v2u sp[4];
#define LOAD_X(I) xf[I] = ld16<SC1>(rws, xo + ((I) & 3) * 1024, xk[(I) >> 2])
#pragma unroll
      for (int i = 0; i < WINX; ++i) { LOAD_X(i); SCHED(); }
      sfor<28>([&](auto I) INL {
        constexpr int ks = I;
        if constexpr (ks == 0) MFMA32V0(ak, wk[ks], xf[ks]); else MFMA32VA(ak, wk[ks], xf[ks]);
        if constexpr (ks + 2 < 28) { LAS char* wb = WB((ks + 2) >> 2); wk[ks + 2] = WFRAG(wb, 1, (ks + 2) & 3); wv[ks + 2] = WFRAG(wb, 2, (ks + 2) & 3); }
        if constexpr (ks < 4) sp[ks] = ld8<SC1>(rws, ks == 3 ? so3 : so + ks * 512, 0);
        SCHED();
        if constexpr (ks == 0) MFMA32V0(av, wv[ks], xf[ks]); else MFMA32VA(av, wv[ks], xf[ks]);
        SCHED();
        if constexpr (ks + WINX < 28) LOAD_X(ks + WINX);
        if constexpr (ks == 18) {
          float s1 = (asf(sp[0].x) + asf(sp[1].x)) + (asf(sp[2].x) + asf(sp[3].x));
          float s2 = (asf(sp[0].y) + asf(sp[1].y)) + (asf(sp[2].y) + asf(sp[3].y));
          s1 = sum32(s1); s2 = sum32(s2);
          mean = s1 * (1.f / D);
          rstd = s2 * (1.f / D) - mean * mean;
          PINV(mean); PINV(rstd);
        }
        if constexpr (ks == 19) { rstd = __builtin_amdgcn_rsqf(fmaxf(rstd, 0.f) + EPS); PINV(rstd); }
        if constexpr (ks >= 20 && ks < 24) { ck[ks - 20] *= -mean * rstd; cv[ks - 20] *= -mean * rstd; PINV(ck[ks - 20]); PINV(cv[ks - 20]); }
        SCHED();
      });
      MFMA_SETTLE2(ak, av);
#pragma unroll
      for (int i = 0; i < 16; ++i) { ak[i] = ak[i] * rstd + ck[i >> 2][i & 3]; av[i] = av[i] * rstd + cv[i >> 2][i & 3]; }
    }
  }
  asm volatile(".Lfk_code_to:");
  if (blockIdx.x == 0 && threadIdx.x == 0) __hip_atomic_store(ctrl, epoch, RLX_AGENT);
}

extern "C" void kernel_launch(void* const* d_in, const int* in_sizes, int n_in, void* d_out, int out_size, void* d_ws,
                              size_t ws_size, hipStream_t stream) {
  Params p;
  p.x = d_in[0]; p.wqkv = d_in[1]; p.wo = d_in[2];
  p.w1 = d_in[4]; p.b1 = d_in[5]; p.w2 = d_in[6]; p.b2 = d_in[7];
  p.out = d_out; p.ws = d_ws;
  (void)hipFuncSetAttribute((const void*)fused_kernel, hipFuncAttributeMaxDynamicSharedMemorySize, LDS_BYTES);
  fused_kernel<<<256, 256, LDS_BYTES, stream>>>(p);
}
```

```cpp
#include <hip/hip_runtime.h>
#include <utility>

#define GAS __attribute__((address_space(1)))
#define LAS __attribute__((address_space(3)))
#define DI __device__ __forceinline__
#define INL __attribute__((always_inline))
#define RLX_AGENT __ATOMIC_RELAXED, __HIP_MEMORY_SCOPE_AGENT
typedef unsigned v4u __attribute__((ext_vector_type(4)));
typedef unsigned v2u __attribute__((ext_vector_type(2)));
typedef float f32x2 __attribute__((ext_vector_type(2)));
typedef float f32x4 __attribute__((ext_vector_type(4)));
typedef float f32x16 __attribute__((ext_vector_type(16)));
typedef short s16x4 __attribute__((ext_vector_type(4)));
typedef __bf16 bf16x2 __attribute__((ext_vector_type(2)));
typedef __bf16 bf16x8 __attribute__((ext_vector_type(8)));
typedef __amdgpu_buffer_rsrc_t rsrc_t;

constexpr int S = 2048, D = 448, F = 1792;
constexpr float EPS = 1e-5f;
constexpr int FLAG_OFF = 4096;
constexpr int KV_OFF = 512 * 1024, KV_SZ = 16 * 64 * 4096;
constexpr int O_OFF = KV_OFF + 2 * KV_SZ, O_SZ = 32 * 16 * 4096;
constexpr int S3_OFF = O_OFF + 2 * O_SZ, S3_SZ = 32 * 8 * 1024;
constexpr int HT_OFF = S3_OFF + 2 * S3_SZ, HT_SZ = 7 * 64 * 4096;
constexpr int U_OFF = HT_OFF + 2 * HT_SZ, U_SZ = 7 * 56 * 4096;
constexpr int X2_OFF = U_OFF + 2 * U_SZ, X2_SZ = 64 * 28 * 1024;
constexpr int S5_OFF = X2_OFF + X2_SZ, S5_SZ = 32 * 8 * 512;
constexpr int DBG_OFF = S5_OFF + S5_SZ;
constexpr int WS_BYTES = DBG_OFF + 256 * 128 * 8;
constexpr int L_WQ = 0;
constexpr int L_KVW = 86016;
constexpr int L_STG = L_KVW + 49152;
constexpr int L_CSP = L_STG + 16384;
constexpr int L_CS = L_CSP + 1536 + 64;
constexpr int L_HST = L_CS + 384;
constexpr int L_DUMP = L_HST + 8192;
constexpr int LDS_BYTES = L_DUMP + 256;

constexpr int SC1 = 16, OOB = 0x7ffff000;
#define VT_LAYOUT 1
#define WIN4 12
#define WIN5 12
#define WINX 12
#define QPUB 20
#define NO1 2
#define NO2 4
#define NO3 2
#define NG2 3
#define NG3 2
#define NH1 2
#define NH2 3
#define NH3 2
#define NU1 3
#define NU3 2
#define PACE 1
#define FTCH 1
#define XTCH 0
#define SUMDOT 0
#define TCH0 1
#define TCH1 1
#define TCH2 1
constexpr int Z_G = NO1 + NO2 + NO3, Z_H = Z_G + NG2 + NG3, Z_U = Z_H + NH1 + NH2 + NH3;
static_assert(QPUB / 2 + 27 - QPUB >= 16 && NO1 <= 3 && NU1 <= 3 && NO2 >= 2 && NG2 >= 2 && NH2 >= 2 && Z_U >= 16 && Z_U + NU1 + 2 + NU3 <= 30 && NO1 <= 4 && NU1 <= 4, "schedule");

struct Params {
  const void *x, *wqkv, *wo, *w1, *b1, *w2, *b2;
  void *out, *ws;
};

template <int... I, class Fn> DI void sfor_(std::integer_sequence<int, I...>, Fn&& f) { (f(std::integral_constant<int, I>{}), ...); }
template <int N, class Fn> DI void sfor(Fn&& f) { sfor_(std::make_integer_sequence<int, N>{}, f); }
DI float asf(unsigned u) { return __builtin_bit_cast(float, u); }
DI unsigned asu(float f) { return __builtin_bit_cast(unsigned, f); }
DI bf16x2 asb2(unsigned u) { return __builtin_bit_cast(bf16x2, u); }
DI rsrc_t mk(const void* p, int bytes) { return __builtin_amdgcn_make_buffer_rsrc((void*)p, 0, bytes, 0x00020000); }
template <int AUX> DI v4u ld16(rsrc_t r, int voff, int soff) { return __builtin_amdgcn_raw_buffer_load_b128(r, voff, soff, AUX); }
template <int AUX> DI v2u ld8(rsrc_t r, int voff, int soff) { return __builtin_amdgcn_raw_buffer_load_b64(r, voff, soff, AUX); }
template <int AUX> DI void st16(v4u v, rsrc_t r, int voff, int soff) { __builtin_amdgcn_raw_buffer_store_b128(v, r, voff, soff, AUX); }
template <int AUX> DI void st8(v2u v, rsrc_t r, int voff, int soff) { __builtin_amdgcn_raw_buffer_store_b64(v, r, voff, soff, AUX); }
DI f32x4 mfma16(v4u a, v4u b, f32x4 c) {
  return __builtin_amdgcn_mfma_f32_16x16x32_bf16(__builtin_bit_cast(bf16x8, a), __builtin_bit_cast(bf16x8, b), c, 0, 0, 0);
}
DI f32x16 mfma32(v4u a, v4u b, f32x16 c) {
  return __builtin_amdgcn_mfma_f32_32x32x16_bf16(__builtin_bit_cast(bf16x8, a), __builtin_bit_cast(bf16x8, b), c, 0, 0, 0);
}
DI unsigned pk(float lo, float hi) { return __builtin_bit_cast(unsigned, __builtin_convertvector((f32x2){lo, hi}, bf16x2)); }
DI float bflo(unsigned u) { return asf(u << 16); }
DI float bfhi(unsigned u) { return asf(u & 0xffff0000u); }
DI f32x4 unpack4(v2u v) { return (f32x4){bflo(v.x), bfhi(v.x), bflo(v.y), bfhi(v.y)}; }
DI v2u trrd(LAS char* p) { return __builtin_bit_cast(v2u, __builtin_amdgcn_ds_read_tr16_b64_v4i16((LAS s16x4*)p)); }
DI v4u trfrag(LAS char* p0, LAS char* p1) { v2u a = trrd(p0), b = trrd(p1); return (v4u){a.x, a.y, b.x, b.y}; }
DI float hi32(float v) {
  unsigned u = asu(v);
  auto r = __builtin_amdgcn_permlane32_swap(u, u, false, false);
  return asf(r[1]);
}
DI float sum32(float v) {
  unsigned u = asu(v);
  auto r = __builtin_amdgcn_permlane32_swap(u, u, false, false);
  return asf(r[0]) + asf(r[1]);
}
DI float max32(float v) {
  unsigned u = asu(v);
  auto r = __builtin_amdgcn_permlane32_swap(u, u, false, false);
  return fmaxf(asf(r[0]), asf(r[1]));
}
DI float sum16(float v) {
  unsigned u = asu(v);
  auto r = __builtin_amdgcn_permlane16_swap(u, u, false, false);
  return asf(r[0]) + asf(r[1]);
}
#define FENCE() asm volatile("" ::: "memory")
#define SCHED() __builtin_amdgcn_sched_barrier(0)
#define DRAIN() asm volatile("s_waitcnt vmcnt(0)" ::: "memory")
#define PINV(x) asm volatile("" : "+v"(x))
#define PINS(x) asm volatile("" : "+s"(x))
#define KEEP(x) asm volatile("" :: "v"(x))
#define LGKM0() asm volatile("s_waitcnt lgkmcnt(0)" ::: "memory")
DI void bar() { LGKM0(); __builtin_amdgcn_s_barrier(); FENCE(); }
template <int N> DI void vmw() { asm volatile("s_waitcnt vmcnt(%0)" :: "n"(N) : "memory"); }
template <int STEP> DI void dma1(rsrc_t r, int voff, int& so, int& lds) {
  asm volatile("s_mov_b32 m0, %1\n\ts_add_u32 %1, %1, 0x400\n\tbuffer_load_dwordx4 %2, %3, %0 offen lds\n\ts_add_u32 %0, %0, %4"
               : "+s"(so), "+s"(lds) : "v"(voff), "s"(r), "n"(STEP) : "memory", "scc");
}
template <int LO, int SO> DI void dmai(rsrc_t r, int voff, int lds) {
  int so;
  asm volatile("s_add_u32 m0, %1, %4\n\ts_mov_b32 %0, %5\n\tbuffer_load_dwordx4 %2, %3, %0 offen lds"
               : "=&s"(so) : "s"(lds), "v"(voff), "s"(r), "n"(LO), "n"(SO) : "memory", "scc");
}
DI void touch(rsrc_t r, int voff, int soff, LAS char* L) {
  asm volatile("s_mov_b32 m0, %0\n\ts_nop 3\n\tbuffer_load_dword %1, %2, %3 offen lds"
               :: "s"((int)(size_t)(L + L_DUMP)), "v"(voff), "s"(r), "s"(soff) : "memory");
}
template <int STEP> DI void dma64(rsrc_t r, int v0, int v1, int soff, int lds) {
  unsigned so;
  asm volatile("s_mov_b32 m0, %1\n\ts_mov_b32 %0, %5\n\tbuffer_load_dwordx4 %2, %4, %0 offen lds\n\t"
               "s_add_u32 m0, m0, 0x400\n\ts_add_u32 %0, %0, %6\n\tbuffer_load_dwordx4 %3, %4, %0 offen lds\n\t"
               "s_add_u32 m0, m0, 0x400\n\ts_add_u32 %0, %0, %6\n\tbuffer_load_dwordx4 %2, %4, %0 offen lds\n\t"
               "s_add_u32 m0, m0, 0x400\n\ts_add_u32 %0, %0, %6\n\tbuffer_load_dwordx4 %3, %4, %0 offen lds"
               : "=&s"(so) : "s"(lds), "v"(v0), "v"(v1), "s"(r), "s"(soff), "n"(STEP) : "memory", "scc");
}

#define STAMP(i) do {} while (0)

DI void publish(GAS unsigned* flag, unsigned epoch) {
  DRAIN();
  __builtin_amdgcn_s_waitcnt(0x0F70);
  bar();
  if (threadIdx.x == 0) __hip_atomic_store(flag, epoch, RLX_AGENT);
  FENCE();
}
template <int N> DI void publish_n(GAS unsigned* flag, unsigned epoch) {
  vmw<N>();
  __builtin_amdgcn_s_barrier();
  FENCE();
  if (threadIdx.x == 0) __hip_atomic_store(flag, epoch, RLX_AGENT);
  FENCE();
}
#define POLL(f, ptr) asm volatile("global_load_dword %0, %1, off sc1" : "=&v"(f) : "v"(ptr) : "memory")
#define POLLED(f, N, ptr) { \
    unsigned f2_; \
    POLL(f2_, ptr); \
    asm volatile("s_waitcnt vmcnt(%1)" : "+v"(f) : "n"((N) + 1) : "memory"); \
    if (__builtin_amdgcn_ballot_w64(f != epoch) != 0ull) { \
      asm volatile("s_waitcnt vmcnt(0)" : "+v"(f2_) :: "memory"); \
      if (__builtin_amdgcn_ballot_w64(f2_ != epoch) != 0ull) wait_flags(ptr, epoch, budget); \
    } \
    FENCE(); \
    late = f2_; }
DI void wait_flags(GAS unsigned* flag, unsigned epoch, unsigned& budget) {
  FENCE();
  while (true) {
    unsigned f = __hip_atomic_load(flag, RLX_AGENT);
    if (__builtin_amdgcn_ballot_w64(f != epoch) == 0ull) break;
    if (budget == 0u) break;
    --budget;
  }
  __builtin_amdgcn_fence(__ATOMIC_ACQUIRE, "wavefront");
  FENCE();
}

DI void cv_wr(const v4u& r, int m, LAS char* stg, int wa0, int wa1) { *(LAS v4u*)(stg + ((m & 1) ? wa1 : wa0) + m * 1024) = r; }
DI v4u cv_rd(int x, LAS char* stg, int ra0, int ra1) { return trfrag(stg + (ra0 ^ (x << 5)), stg + (ra1 ^ (x << 5))); }
DI int wrap7(int v) { return v >= 7 ? v - 7 : v; }
DI int wrap14(int v) { return v >= 14 ? v - 14 : v; }
DI int swz(int row) { return ((row >> 1) & 1) | (((row >> 3) ^ (row >> 2)) & 1) << 1; }

#define FRESH() \
  int tid = threadIdx.x, bid = blockIdx.x; \
  asm volatile("" : "+v"(tid)); \
  asm volatile("" : "+s"(bid)); \
  const int lane = tid & 63, w = __builtin_amdgcn_readfirstlane(tid >> 6), bx = bid & 7, by = bid >> 3; \
  const int c = lane & 15, g = lane >> 4, q4 = (lane & 15) >> 2, p4 = lane & 3, r32 = lane & 31, hh = lane >> 5; \
  const int by7 = __builtin_amdgcn_readfirstlane((by * 37) >> 8), bym = __builtin_amdgcn_readfirstlane(by - 7 * by7); \
  const int t = 2 * bx + (by >> 4), hd = by & 15;                \
  const int st = 4 * bx + by7, dt = bym;                         \
  const int dt4 = by < 21 ? bym : bym + 4 * (bx & 1), ft = 7 * (bx >> 1) + (by < 21 ? by7 + 4 * (bx & 1) : 3);    \
  const int rotx = hd >= 14 ? hd - 14 : wrap7(hd), rot4 = ROT4, rot5 = ROT5; \
  const int w4 = (w + G4 * 2 * (by7 & 1)) & 3, pa = G5 * 2 * (by7 & 1);    \
  LAS char* stga = L + L_STG + w * 4096; \
  LAS char* ring = L + L_KVW + w * 16384; \
  LAS char* stgb = ring + 12288; \
  const int wrow = lane >> 3; \
  const int sx0 = swz(wrow) << 5, sx1 = swz(wrow + 8) << 5; \
  const int wa0 = (wrow * 128 + (lane & 7) * 16) ^ sx0, wa1 = (wrow * 128 + (lane & 7) * 16) ^ sx1; \
  auto raddr = [&](int row) { return row * 128 + (swz(row) << 5) + 8 * p4; }; \
  const int w1o = (512 * w + wrow) * (F * 2) + ft * 128 + (lane & 7) * 16, w2o = (448 * w + wrow) * (S * 2) + st * 128 + (lane & 7) * 16; \
  const int r1a = raddr(8 * g + q4), r1b = raddr(8 * g + 4 + q4), r2a = raddr(4 * g + q4), r2b = raddr(16 + 4 * g + q4); \
  LAS char* red = L + L_KVW + lane * 16; \
  (void)c; (void)g; (void)q4; (void)r32; (void)hh; (void)t; (void)hd; (void)st; (void)dt; (void)dt4; (void)ft; (void)stga; (void)stgb; (void)ring; \
  (void)sx0; (void)sx1; (void)wa0; (void)wa1; (void)rotx; (void)rot4; (void)rot5; (void)w1o; (void)w2o; (void)r1a; (void)r1b; (void)r2a; (void)r2b; \
  (void)red; (void)w4; (void)pa;
#define G4 0
#define G5 0
#define ROT4 0
#define ROT5 0

#define MNOP ""
#define MFMA32V0(d, a, b) asm volatile(MNOP "v_mfma_f32_32x32x16_bf16 %0, %1, %2, 0" : "=&v"(d) : "v"(a), "v"(b))
#define MFMA32VC(d, a, b, c) asm volatile(MNOP "v_mfma_f32_32x32x16_bf16 %0, %1, %2, %3" : "=&v"(d) : "v"(a), "v"(b), "v"(c))
#define MFMA32VA(d, a, b) asm volatile(MNOP "v_mfma_f32_32x32x16_bf16 %0, %1, %2, %0" : "+v"(d) : "v"(a), "v"(b))
#define MFMA_SETTLE(d) asm volatile("s_nop 11" : "+v"(d))
#define MFMA_SETTLE2(d, e) asm volatile("s_nop 11" : "+v"(d), "+v"(e))
DI float max3(float a, float b, float c) { float r; asm("v_max3_f32 %0, %1, %2, %3" : "=v"(r) : "v"(a), "v"(b), "v"(c)); return r; }

#define WB(cc) (L + L_WQ + (4 * hh + q4) * 64 + (g & 1) * 32 + 8 * p4 + (hd & 1) * 8 + (cc) * 4096)
#define WFRAG(wb, part, e) trfrag((wb) + (part) * 28672 + ((e) >> 1) * 2048 + ((e) & 1) * 512, (wb) + (part) * 28672 + ((e) >> 1) * 2048 + ((e) & 1) * 512 + 1024)
#define T1O ((16 * (by * 4 + w) + (lane >> 2)) * (F * 2) + (7 * (bx >> 1) + 3 * (bx & 1) + (lane & 3)) * 128)
#define T1H ((512 * (by >> 3) + 8 * ((by & 7) * 4 + w) + (lane >> 2)) * (F * 2) + (7 * (bx >> 1) + 3 * (bx & 1) + (lane & 3)) * 128)
#define CSUM(part, a) (*(LAS f32x4*)(L + L_CS + ((part) * 32 + 8 * (a) + 4 * hh) * 4))
#define WZ_RD1(n, i, sl) wz[n][i] = (n) < 16 ? trfrag(pr1[i] + (sl) * 4096, pr1[(i) ^ 2] + (sl) * 4096 + 512) : trfrag(pr2[i] + (sl) * 4096, pr2[i] + (sl) * 4096 + 2048)
#define DMA_WZ1(n, m, sl) { \
    if ((n) < 16) dmai<(sl) * 4096 + (m) * 1024, (32 * ((n) & 15) + 8 * (m)) * F * 2>(rw1, ((m) & 1) ? pw1 ^ 64 : pw1, pring); \
    else dmai<(sl) * 4096 + (m) * 1024, (32 * ((n) & 15) + 8 * (m)) * S * 2>(rw2, ((m) & 1) ? pw2 ^ 64 : pw2, pring); }
#define DPUMP(L0, A, B) \
  sfor<(B) - (A)>([&](auto I_) INL { \
    constexpr int n_ = (A) + I_, r_ = n_ - (L0); \
    if constexpr (r_ >= 4) asm volatile("s_waitcnt vmcnt(8) lgkmcnt(0)" ::: "memory"); \
    else if constexpr (r_ == 3) vmw<8>(); \
    sfor<4>([&](auto M_) INL { \
      constexpr int m_ = M_; \
      DMA_WZ1(n_, m_, r_ & 3) \
      if constexpr (r_ >= 3) WZ_RD1(n_ - 3, m_, (r_ - 3) & 3); \
      SCHED(); \
    }); \
  });
#define DTSTEP(L0, L1, sl) ((L1) - 1 - (((L1) - 1 - (L0) - (sl)) & 3))
#define DTAIL1(L0, L1, j) { if constexpr (DTSTEP(L0, L1, (j) >> 2) >= (L0) && DTSTEP(L0, L1, (j) >> 2) >= (L1) - 3) WZ_RD1(DTSTEP(L0, L1, (j) >> 2), (j) & 3, (j) >> 2); }
#define DTAIL(L0, L1) sfor<16>([&](auto J_) INL { constexpr int j_ = J_; DTAIL1(L0, L1, j_) }); SCHED();
#define REDW(a, b) *(LAS f32x4*)(red + w * 16384 + (((b) - 1) * 4 + (a)) * 1024) = acc[a][b]
#define REDR(a, b) (*(LAS f32x4*)(red + ((w + 4 - (b)) & 3) * 16384 + (((b) - 1) * 4 + (a)) * 1024))
#define COLSUM(part) { \
    LAS char* cb = L + L_WQ + (part) * 28672 + (w * 16 + 8 * hh + q4) * 64 + (g & 1) * 32 + 8 * p4; \
    v4u wc[7]; \
    _Pragma("unroll") for (int i = 0; i < 7; ++i) wc[i] = trfrag(cb + i * 4096, cb + i * 4096 + 256); \
    f32x16 cs = {}; \
    _Pragma("unroll") for (int i = 0; i < 7; ++i) cs = mfma32((v4u){0x3f803f80u, 0x3f803f80u, 0x3f803f80u, 0x3f803f80u}, wc[i], cs); \
    if (lane < 32) *(LAS float*)(L + L_CSP + (w * 96 + (part) * 32 + lane) * 4) = cs[0]; }
#define COLRED(i) { \
    LAS float* cp = (LAS float*)(L + L_CSP) + (((i) & 31) + 4 * (hd & 1) < 32 ? (i) + 4 * (hd & 1) : (i)); \
    *(LAS float*)(L + L_CS + (i) * 4) = (cp[0] + cp[96]) + (cp[192] + cp[288]); }

__global__ void __launch_bounds__(256, 1) fused_kernel(Params p) {
  extern __shared__ __attribute__((aligned(16))) char smem[];
  LAS char* L = (LAS char*)smem;
  const bool on35 = (blockIdx.x >> 3) < 28;
  const bool on4 = (blockIdx.x >> 3) < ((blockIdx.x & 1) ? 24 : 25);
  const rsrc_t rws = mk(p.ws, WS_BYTES);
  GAS unsigned* ctrl = (GAS unsigned*)p.ws;
  GAS unsigned* flags = (GAS unsigned*)((GAS char*)p.ws + FLAG_OFF);
  STAMP(0);
  unsigned budget = 1u << 22;

  v4u xf[28];
  f32x16 ak, av;
  float mean = 0.f, rstd = 0.f;
  f32x4 res[4];
  unsigned epoch;
  {
    FRESH();
    const rsrc_t rwq = mk(p.wqkv, D * 3 * D * 2);
    const rsrc_t rx = mk(p.x, S * D * 2);
    const int gid = by * 4 + w, rg = (int)(size_t)ring;
    touch(rx, lane < 14 ? (t * 896 + hd * 56 + w * 14 + lane) * 128 : OOB, 0, L);
    const int xvo = (t * 128 + w * 32 + wrow) * 896 + (((lane & 7) ^ (wrow >> 1)) << 4);
#define DMA_X(cc) sfor<4>([&](auto M) INL { constexpr int m = M; dmai<((cc) & 3) * 4096 + m * 1024, m * 8 * 896 + (cc) * 128>(rx, (m & 1) ? xvo ^ 64 : xvo, rg); })
    DMA_X(0); DMA_X(1);
    touch(rwq, lane < 28 ? (2 * gid) * 2688 + 896 + lane * 128 + (lane >= 14 ? 896 : 0) : OOB, 0, L);
    DMA_X(2); DMA_X(3);
    {
      unsigned long long pc;
      int len;
      asm volatile("s_getpc_b64 %0\n.Lfk_code_from:\n\ts_mov_b32 %1, .Lfk_code_to-.Lfk_code_from" : "=s"(pc), "=s"(len));
      touch(mk((const void*)pc, len > 0 ? len : 0), lane < 4 ? (gid * 4 + lane) * 128 : OOB, 0, L);
    }
    asm volatile("buffer_load_dword %0, %1, %2, 0 offen sc1" : "=&v"(epoch) : "v"(tid >= 1 && tid <= 10 ? FLAG_OFF + ((tid - 1) * 256 + bid) * 128 : on35 && (tid >> 4) == 1 ? S3_OFF + ((tid >> 3) & 1) * S3_SZ + (st * 8 + dt) * 1024 + (tid & 7) * 128 : 0), "s"(rws) : "memory");
    SCHED();
    const int tb = gid * 21 + lane, tbr = (tb * 4682) >> 16;
    int tvb = lane < 21 ? (256 + tbr) * 2688 + 896 + (tb - 14 * tbr) * 128 : OOB;
    const int tl = by * 98 + w * 25 + lane, trow = (tl * 9363) >> 16;
    int tvq = (lane < 25 && w * 25 + lane < 98) ? trow * 2688 + (tl - 7 * trow) * 128 : OOB, tvw = (lane < 25 && w * 25 + lane < 98) ? tl * 128 : OOB;
    int wvo = (w * 16 + (lane >> 2)) * 2688 + hd * 56 - (hd & 1) * 8 + (lane & 3) * 16;
    const int wlb = (int)(size_t)(L + L_WQ) + w * 1024;
#define DMA_W(part, cc) dmai<(part) * 28672 + (cc) * 4096, (cc) * 64 * 2688 + (part) * 896>(rwq, wvo, wlb)
    LAS char* xa[8];
#pragma unroll
    for (int e = 0; e < 8; ++e) { xa[e] = ring + r32 * 128 + 8 * hh + ((e ^ ((r32 >> 1) & 7)) << 4); PINV(xa[e]); }
    LAS char* cb = L + L_WQ + (w * 16 + 8 * hh + q4) * 64 + (g & 1) * 32 + 8 * p4;
    cb += 28672;
    LAS char* wb = WB(0) + 28672;
    PINV(tvb); PINV(tvq); PINV(tvw); PINV(wvo); PINV(cb); PINV(wb);
#define READ_X(cc) _Pragma("unroll") for (int e = 0; e < 4; ++e) { \
      const int ch = (e >> 1) * 4 + (e & 1); \
      const v2u a_ = *(LAS v2u*)(xa[ch] + ((cc) & 3) * 4096), b_ = *(LAS v2u*)(xa[ch + 2] + ((cc) & 3) * 4096); \
      xf[4 * (cc) + e] = (v4u){a_.x, a_.y, b_.x, b_.y}; }
    const v4u ones = {0x3f803f80u, 0x3f803f80u, 0x3f803f80u, 0x3f803f80u};
    f32x16 gs, ss = {};
    float s1[2] = {};
#define XSTAT(i) { if constexpr ((i) == 0) MFMA32V0(gs, xf[i], xf[i]); else MFMA32VA(gs, xf[i], xf[i]); ss = mfma32(ones, xf[i], ss); }
#define XSUM ss[0]
    STAMP(32);
    vmw<15>(); READ_X(0) LGKM0(); DMA_X(4); XSTAT(0) XSTAT(1) XSTAT(2) XSTAT(3) SCHED();
    vmw<15>(); READ_X(1) LGKM0(); DMA_X(5); touch(rwq, tvb, 0, L); XSTAT(4) XSTAT(5) XSTAT(6) XSTAT(7) SCHED();
    vmw<15>(); READ_X(2) LGKM0(); DMA_X(6); SCHED();
    asm volatile("s_waitcnt vmcnt(13)" : "+v"(epoch) :: "memory");
    epoch = __builtin_amdgcn_readfirstlane(epoch) + 1u;
    READ_X(3)
    SCHED();
    sfor<20>([&](auto I) INL {
      constexpr int i = I;
      if constexpr (i == 6) { vmw<4 + 1 + 4 + 3>(); READ_X(4) }
      if constexpr (i == 10) { vmw<1 + 4 + 5>(); READ_X(5) }
      if constexpr (i == 14) { vmw<7>(); READ_X(6) }
      if constexpr ((i & 1) == 0 && i < 16) DMA_W(1 + ((i >> 1) & 1), i >> 2);
      XSTAT(8 + i)
      SCHED();
    });
    STAMP(33);
    v4u wk[28], wv[28], wc[14];
    f32x16 csk = {}, csv = {};
    float dg = 0.f, d8[8], d4[4], pp[4];
    f32x4 cc[8];
    LAS float* pb = (LAS float*)(L + L_CSP) + 32 + 16 * w + c + 4 * (hd & 1);
    LAS float* pw = (LAS float*)(L + (hh ? L_DUMP : L_CSP + (w * 96 + 32) * 4)) + r32;
#define PICK4(o, at, a0, a1, a2, a3, b0, b1, b2, b3, m) \
    asm volatile("v_cndmask_b32 %0, %4, %8, %12\n\tv_cndmask_b32 %1, %5, %9, %12\n\tv_cndmask_b32 %2, %6, %10, %12\n\tv_cndmask_b32 %3, %7, %11, %12" \
                 : "=&v"(o[at]), "=&v"(o[(at) + 1]), "=&v"(o[(at) + 2]), "=&v"(o[(at) + 3]) \
                 : "v"(a0), "v"(a1), "v"(a2), "v"(a3), "v"(b0), "v"(b1), "v"(b2), "v"(b3), "s"(m))
    sfor<8>([&](auto R) INL {
      constexpr int r = R;
      if constexpr (r < 7) {
        vmw<(r <= 3 ? 6 : 6)>();
        if constexpr (r == 6) {
          wc[12] = trfrag(cb + r * 4096, cb + r * 4096 + 256);
          wc[13] = trfrag(cb + 28672 + r * 4096, cb + 28672 + r * 4096 + 256);
          csk = mfma32(ones, wc[12], csk); csv = mfma32(ones, wc[13], csv);
          pw[0] = csk[0]; pw[32] = csv[0];
          LGKM0();
        }
        __builtin_amdgcn_s_barrier();
        FENCE();
        SCHED();
      }
      sfor<4>([&](auto E) INL {
        constexpr int e = E, kr = 4 * r + e, km = kr - 4;
        if constexpr (r > 0) { if constexpr (km == 0) MFMA32V0(ak, wk[km], xf[km]); else MFMA32VA(ak, wk[km], xf[km]); }
        if constexpr (r < 7) wk[kr] = WFRAG(wb + r * 4096, 0, e);
        if constexpr (r < 6 && (e & 1) == 0) wc[2 * r + e / 2] = trfrag(cb + (e / 2) * 28672 + r * 4096, cb + (e / 2) * 28672 + r * 4096 + 256);
        SCHED();
        if constexpr (r > 0) { if constexpr (km == 0) MFMA32V0(av, wv[km], xf[km]); else MFMA32VA(av, wv[km], xf[km]); }
        if constexpr (r < 7) wv[kr] = WFRAG(wb + r * 4096, 1, e);
        SCHED();
        if constexpr (r < 6 && e == 1) csk = mfma32(ones, wc[2 * r], csk);
        if constexpr (r < 6 && e == 3) csv = mfma32(ones, wc[2 * r + 1], csv);
        SCHED();
        if constexpr (e == 1 || e == 3) {
          if constexpr (r < 3) DMA_W(1 + e / 2, r + 4);
          else if constexpr (r == 3) { if constexpr (e == 1) touch(rwq, tvq, 0, L); else touch(mk(p.wo, D * D * 2), tvw, 0, L); }
          else if constexpr (2 * (r - 4) + e / 2 < 7) DMA_W(0, 2 * (r - 4) + e / 2);
        }
        if constexpr (r == 1 && (e & 1) == 0) { constexpr int j = 2 * e; PICK4(d8, j, gs[j], gs[j + 1], gs[j + 2], gs[j + 3], gs[j + 8], gs[j + 9], gs[j + 10], gs[j + 11], 0xffff0000ffff0000ull); }
        if constexpr (r == 2 && e == 0) PICK4(d4, 0, d8[0], d8[1], d8[2], d8[3], d8[4], d8[5], d8[6], d8[7], 0xff00ff00ff00ff00ull);
        if constexpr (r == 2 && e == 2)
          asm volatile("v_cndmask_b32 %1, %1, %3, %5\n\tv_cndmask_b32 %2, %2, %4, %5\n\tv_cndmask_b32 %0, %1, %2, %6\n\tv_cndmask_b32 %0, 0, %0, %7"
                       : "=&v"(dg), "+v"(d4[0]), "+v"(d4[1]) : "v"(d4[2]), "v"(d4[3]), "s"(0xccccccccccccccccull), "s"(0xaaaaaaaaaaaaaaaaull), "s"(0xf0f0f0f00f0f0f0full));
        if constexpr (r == 3 && e == 0) { mean = XSUM * (1.f / D); rstd = sum32(dg) * (1.f / D) - mean * mean; PINV(mean); PINV(rstd); }
        if constexpr (r == 3 && e == 2) { rstd = __builtin_amdgcn_rsqf(fmaxf(rstd, 0.f) + EPS); PINV(rstd); }
        if constexpr (r == 4 && e == 0) { PINV(pb); PINV(pw); }
        if constexpr (r == 6 && e == 0) { pp[0] = pb[0]; pp[1] = pb[96]; pp[2] = pb[192]; pp[3] = pb[288]; }
        if constexpr (r == 6 && e == 2) *(LAS float*)(L + L_CS + (32 + 16 * w + c) * 4) = (pp[0] + pp[1]) + (pp[2] + pp[3]);
        if constexpr (r == 6 && e == 3) bar();
        if constexpr (r == 7 && e < 2) { _Pragma("unroll") for (int j = 0; j < 4; ++j) cc[4 * e + j] = CSUM(1, 4 * e + j); }
        SCHED();
      });
    });
    STAMP(34);
#pragma unroll
    for (int a = 0; a < 4; ++a) res[a] = (f32x4){0.f, 0.f, 0.f, 0.f};
    MFMA_SETTLE2(ak, av);
#pragma unroll
    for (int i = 0; i < 16; ++i) { ak[i] = (ak[i] - mean * cc[i >> 2][i & 3]) * rstd; av[i] = (av[i] - mean * cc[4 + (i >> 2)][i & 3]) * rstd; }
  }

#pragma unroll 1
  for (int layer = 0;; ++layer) {
    GAS unsigned* fl = flags + layer * 5 * 256 * 32;
    STAMP(1 + layer * 16);
    v4u qf[2], kv[4];
    v4u wo[4][4];
    v2u rr[4];
    unsigned b2r;
    v4u wz[30][4];
    int pw1, pw2, pring;
    LAS char *pr1[4], *pr2[4];
    const rsrc_t rw1 = mk(p.w1, on4 ? S * F * 2 : 0);
    const rsrc_t rw2 = mk(p.w2, on35 ? S * F * 2 : 0);
    {
      FRESH();
#pragma unroll
      for (int i = 0; i < 16; i += 2) {
        kv[i >> 3][(i >> 1) & 3] = pk(ak[i], ak[i + 1]);
        kv[2 + (i >> 3)][(i >> 1) & 3] = pk(av[i], av[i + 1]);
      }
      const int kvs = KV_OFF + layer * KV_SZ + (hd * 64 + 4 * t + w) * 4096 + lane * 16;
      if (hh) { kv[1][2] = 0u; kv[1][3] = 0u; kv[3][2] = 0x00003f80u; kv[3][3] = 0u; }
      {
        const rsrc_t rkv = mk(p.ws, t < 15 ? WS_BYTES : 0);
#pragma unroll
        for (int i = 0; i < 4; ++i) st16<SC1>(kv[i], rkv, kvs, i * 1024);
      }
      SCHED();
      if (layer == 0) {
        vmw<4>();
        if (t == 15) vmw<0>();
        COLSUM(0)
        publish(fl + (0 * 256 + bid) * 32, epoch);
        if (tid < 32) COLRED(tid)
      }
      v4u wq[28];
      {
        LAS char* wb = WB(0);
#pragma unroll
        for (int ks = 0; ks < 4; ++ks) wq[ks] = WFRAG(wb, 0, ks);
      }
      f32x16 aq;
      STAMP(2 + layer * 16 + layer * 27);
      LAS char* kvw = L + L_KVW + w * 4096 + lane * 16;
      LAS char* vw = L + L_KVW + w * 4096 + 2048 + ((lane * 16) ^ (hh << 6));
      LAS char* vw1 = L + L_KVW + w * 4096 + 3072 + ((lane * 16) ^ (hh << 6) ^ 128);
      const int tp = (t >= 2 && (lane & 1)) ? t - 2 : t > 0 ? t - 1 : 0;
      GAS unsigned* fp = fl + (0 * 256 + (((tp & 1) * 16 + hd) * 8 + (tp >> 1))) * 32;
      const rsrc_t rwo = mk(p.wo, on35 ? D * D * 2 : 0);
      const int wvo = ((112 * w + wrow) * 896 + dt * 128 + (lane & 7) * 16) ^ sx0;
      sfor<28>([&](auto I) INL {
        constexpr int ks = I;
        if constexpr (ks == 0) MFMA32V0(aq, wq[ks], xf[ks]); else MFMA32VA(aq, wq[ks], xf[ks]);
        if constexpr (ks + 4 < 28) { LAS char* wb = WB((ks + 4) >> 2); wq[ks + 4] = WFRAG(wb, 0, ks & 3); }
        constexpr int wi = ks < QPUB ? ((ks & 1) ? ks / 2 : -1) : ks == QPUB ? -1 : QPUB / 2 + ks - QPUB - 1;
        if constexpr (wi >= 0 && wi < 16) dmai<wi * 1024, (28 * (wi >> 2) + 8 * (wi & 3)) * 896>(rwo, (wi & 1) ? wvo ^ 64 : wvo, (int)(size_t)ring);
        if constexpr (ks == 18) PINV(fp);
        if constexpr (ks == QPUB) {
          if (layer != 0) publish_n<QPUB / 2>(fl + (0 * 256 + bid) * 32, epoch);
          else bar();
          STAMP(3 + layer * 16);
        }
        SCHED();
      });
      unsigned pf;
      POLL(pf, fp);
      f32x4 cq[4];
#pragma unroll
      for (int a = 0; a < 4; ++a) cq[a] = CSUM(0, a);
      int woa = raddr(16 * (g >> 1) + 4 * (g & 1) + q4), wob = raddr(16 * (g >> 1) + 4 * (g & 1) + q4 + 8);
      PINV(woa); PINV(wob); PINV(kvw); PINV(vw); PINV(vw1);
      MFMA_SETTLE(aq);
      const float qs = rstd * (0.18898223650461362f * 1.4426950408889634f);
#pragma unroll
      for (int a = 0; a < 4; ++a) {
        qf[a >> 1][(a & 1) * 2] = pk((aq[4 * a] - mean * cq[a][0]) * qs, (aq[4 * a + 1] - mean * cq[a][1]) * qs);
        qf[a >> 1][(a & 1) * 2 + 1] = pk((aq[4 * a + 2] - mean * cq[a][2]) * qs, (aq[4 * a + 3] - mean * cq[a][3]) * qs);
      }
      if (hh) { qf[1][2] = 0x00003f80u; qf[1][3] = 0u; }
      PINV(qf[0]); PINV(qf[1]);
      f32x16 s8;
      unsigned long long em[16];
      unsigned pm[8];
#pragma unroll
      for (int i = 0; i < 16; ++i) { em[i] = __builtin_amdgcn_ballot_w64(((i & 3) + 8 * (i >> 2) + 4 * hh) > r32); PINS(em[i]); }
      MFMA32V0(s8, kv[0], qf[0]);
      MFMA32VA(s8, kv[1], qf[1]);
#pragma unroll
      for (int i = 0; i < 16; i += 2) {
        const int kl = (i & 3) + 8 * (i >> 2) + 4 * hh;
        pm[i >> 1] = (kl > r32 ? 0xffffu : 0u) | (kl + 1 > r32 ? 0xffff0000u : 0u);
        PINV(pm[i >> 1]);
      }
      SCHED();
      unsigned late;
      POLLED(pf, 0, fp)
      STAMP(4 + layer * 16);
      v4u ra[4], rb[4];
      {
        const rsrc_t r1 = mk(p.ws, t >= 1 ? WS_BYTES : 0), r2 = mk(p.ws, t >= 2 ? WS_BYTES : 0);
        const int kvo = kvs - 32768;
        rb[0] = ld16<SC1>(r2, kvo, 0); rb[1] = ld16<SC1>(r2, kvo, 1024);
        ra[0] = ld16<SC1>(r1, kvo, 16384); ra[1] = ld16<SC1>(r1, kvo, 16384 + 1024);
        SCHED();
        rb[2] = ld16<SC1>(r2, kvo, 2048); rb[3] = ld16<SC1>(r2, kvo, 3072);
        ra[2] = ld16<SC1>(r1, kvo, 16384 + 2048); ra[3] = ld16<SC1>(r1, kvo, 16384 + 3072);
        SCHED();
      }
#pragma unroll
      for (int j = 0; j < 16; ++j) wo[j >> 2][j & 3] = cv_rd((j & 3) ^ pa, ring + (j >> 2) * 4096, woa, wob);
      bar();
      *(LAS v4u*)(kvw + 32768) = kv[0];
      *(LAS v4u*)(kvw + 32768 + 1024) = kv[1];
      *(LAS v4u*)(vw + 32768) = kv[2];
      *(LAS v4u*)(vw1 + 32768) = kv[3];
      const int vx = ((g & 1) * 1024 + (p4 & 1) * 512 + hh * 64 + q4 * 16) ^ ((p4 & 1) << 6) ^ ((g & 1) << 7);
      LAS char* vr0 = L + L_KVW + w * 4096 + 2048 + vx + (p4 >> 1) * 8;
      LAS char* vr1 = L + L_KVW + w * 4096 + 2048 + (vx ^ 128) + (p4 >> 1) * 8;
      int oo = O_OFF + layer * O_SZ + ((2 * t + (w >> 1)) * 16 + hd) * 4096 + (2 * (w & 1) + (r32 >> 4)) * 1024 + hh * 256 + c * 16;
      unsigned nk2 = (t < 2 && hh) ? 0xf149u : 0u, nk1 = (t < 1 && hh) ? 0xf149u : 0u;
      int t1o = TCH1 == 2 ? T1O : lane < 32 ? T1H : OOB, rro = (64 * st + 16 * w + c) * 896 + dt * 128 + g * 8;
      pw1 = w1o ^ sx0; pw2 = w2o ^ sx0; pring = (int)(size_t)ring;
#pragma unroll
      for (int i = 0; i < 4; ++i) { pr1[i] = ring + (r1a ^ (i << 5)); pr2[i] = ring + (r2a ^ (((i + w) & 3) << 5)); PINV(pr1[i]); PINV(pr2[i]); }
      PINV(oo); PINV(nk1); PINV(nk2); PINV(vr0); PINV(vr1); PINV(t1o); PINV(rro); PINV(pw1); PINV(pw2); PINS(pring);
      SCHED();
      rb[1][2] |= nk2; ra[1][2] |= nk1;
      *(LAS v4u*)(kvw) = rb[0]; *(LAS v4u*)(kvw + 1024) = rb[1];
      *(LAS v4u*)(kvw + 16384) = ra[0]; *(LAS v4u*)(kvw + 16384 + 1024) = ra[1];
      bar();
      KEEP(late);
      STAMP(37 + layer * 4);
      v4u kf[8][2];
#pragma unroll
      for (int jj = 1; jj <= 8; ++jj) { const int j = jj & 7; kf[j][0] = *(LAS v4u*)(kvw + j * 4096); kf[j][1] = *(LAS v4u*)(kvw + j * 4096 + 1024); }
      SCHED();
      f32x16 sc[8], se;
      float mx = -1e30f, mx2 = -1e30f;
#define SEL4(i) asm("v_cndmask_b32 %0, %4, %8, %12\n\tv_cndmask_b32 %1, %5, %9, %13\n\tv_cndmask_b32 %2, %6, %10, %14\n\tv_cndmask_b32 %3, %7, %11, %15" \
        : "=&v"(se[i]), "=&v"(se[(i) + 1]), "=&v"(se[(i) + 2]), "=&v"(se[(i) + 3]) \
        : "v"(s8[i]), "v"(s8[(i) + 1]), "v"(s8[(i) + 2]), "v"(s8[(i) + 3]), "v"(sc[0][i]), "v"(sc[0][(i) + 1]), "v"(sc[0][(i) + 2]), "v"(sc[0][(i) + 3]), \
          "s"(em[i]), "s"(em[(i) + 1]), "s"(em[(i) + 2]), "s"(em[(i) + 3]))
#define MAX8(v, i) asm volatile("v_max3_f32 %0, %0, %2, %3\n\tv_max3_f32 %1, %1, %4, %5\n\tv_max3_f32 %0, %0, %6, %7\n\tv_max3_f32 %1, %1, %8, %9" \
        : "+v"(mx), "+v"(mx2) : "v"(v[i]), "v"(v[(i) + 1]), "v"(v[(i) + 2]), "v"(v[(i) + 3]), "v"(v[(i) + 4]), "v"(v[(i) + 5]), "v"(v[(i) + 6]), "v"(v[(i) + 7]))
      sfor<16>([&](auto N) INL {
        constexpr int n = N, j = ((n >> 1) + 1) & 7, hf = n & 1;
        if constexpr (hf == 0) MFMA32V0(sc[j], kf[j][0], qf[0]); else MFMA32VA(sc[j], kf[j][1], qf[1]);
        if constexpr (n == 0) *(LAS v4u*)(vw) = rb[2];
        if constexpr (n == 1) *(LAS v4u*)(vw1) = rb[3];
        if constexpr (n == 2) *(LAS v4u*)(vw + 16384) = ra[2];
        if constexpr (n == 3) *(LAS v4u*)(vw1 + 16384) = ra[3];
        if constexpr (n == 4) { const rsrc_t rxr = mk(p.x, (on35 && layer == 0) ? S * D * 2 : 0); _Pragma("unroll") for (int a = 0; a < 4; ++a) rr[a] = ld8<0>(rxr, rro, (a ^ pa) * 32); }
        if constexpr (n == 5) b2r = (unsigned)__builtin_amdgcn_raw_buffer_load_b16(mk(p.b2, S * 2), (64 * st + 16 * w + c) * 2, 0, 0);
        if constexpr (TCH1 && n == 5) touch(mk(p.w1, S * F * 2), t1o, 0, L);
        if constexpr (n >= 6) MAX8(sc[(n >> 1) - 2], 8 * hf);
        SCHED();
      });
      MAX8(sc[6], 0); MAX8(sc[6], 8); MAX8(sc[7], 0); MAX8(sc[7], 8);
      SCHED();
      SEL4(0); SEL4(4); SEL4(8); SEL4(12);
      MAX8(se, 0); MAX8(se, 8);
      mx = max32(max3(mx, mx2, mx2));
      f32x16 nm;
#pragma unroll
      for (int i = 0; i < 16; ++i) nm[i] = -mx;
      asm volatile("s_nop 1" : "+v"(nm));
      bar();
      STAMP(38 + layer * 4);
      f32x16 ao;
      v4u vf[9][2], pp[9][2];
      float ex[32][4];
#define VF(j, s) vf[j][s] = trfrag(vr0 + (j) * 4096 + (s) * 256, vr1 + (j) * 4096 + (s) * 256)
      sfor<33>([&](auto N) INL {
        constexpr int n = N, j = n >> 2, q = n & 3;
        if constexpr (j == 0) {
          if constexpr (q == 0) MFMA32VC(sc[1], kf[1][0], qf[0], nm);
          if constexpr (q == 1) MFMA32VA(sc[1], kf[1][1], qf[1]);
          if constexpr (q == 2) MFMA32VC(sc[2], kf[2][0], qf[0], nm);
          if constexpr (q == 3) MFMA32VA(sc[2], kf[2][1], qf[1]);
        } else if constexpr (j < 8) {
          if constexpr (q == 1) { if constexpr (j == 1) MFMA32V0(ao, vf[0][0], pp[0][0]); else MFMA32VA(ao, vf[j - 1][0], pp[j - 1][0]); }
          if constexpr (q == 2) MFMA32VA(ao, vf[j - 1][1], pp[j - 1][1]);
          if constexpr (j + 2 < 8) {
            if constexpr (q == 0) MFMA32VC(sc[j + 2], kf[j + 2][0], qf[0], nm);
            if constexpr (q == 3) MFMA32VA(sc[j + 2], kf[j + 2][1], qf[1]);
          } else if constexpr (j == 6) {
            if constexpr (q == 0) MFMA32VA(ao, vf[8][0], pp[8][0]);
            if constexpr (q == 3) MFMA32VA(ao, vf[8][1], pp[8][1]);
          }
        } else MFMA32VA(ao, vf[7][0], pp[7][0]);
        SCHED();
        if constexpr (n < 32) {
#pragma unroll
          for (int i = 0; i < 4; ++i) { if constexpr (j == 0) ex[n][i] = __builtin_amdgcn_exp2f(se[4 * q + i] - mx); else ex[n][i] = __builtin_amdgcn_exp2f(sc[j][4 * q + i]); }
        }
        SCHED();
        if constexpr (n > 0) {
          constexpr int jp = (n - 1) >> 2, qp = (n - 1) & 3;
#pragma unroll
          for (int i = 0; i < 2; ++i) {
            const unsigned u = pk(ex[n - 1][2 * i], ex[n - 1][2 * i + 1]);
            if constexpr (jp == 0) { pp[0][qp >> 1][(2 * qp + i) & 3] = u & pm[2 * qp + i]; pp[8][qp >> 1][(2 * qp + i) & 3] = u & ~pm[2 * qp + i]; }
            else pp[jp][qp >> 1][(2 * qp + i) & 3] = u;
          }
        }
        if constexpr (n == 24) __builtin_amdgcn_s_waitcnt(0x0F70);
        if constexpr (n == 22) KEEP(nm);
        if constexpr (j < 8 && q < 2) VF(j, q);
        if constexpr (j == 5 && q >= 2) VF(8, q - 2);
        SCHED();
      });
      asm volatile("s_nop 1" : "+v"(pp[7][1]));
      MFMA32VA(ao, vf[7][1], pp[7][1]);
      MFMA_SETTLE(ao);
      const float inv = __builtin_amdgcn_rcpf(hi32(ao[12]));
      v4u of[2];
#pragma unroll
      for (int i = 0; i < 16; i += 2) of[i >> 3][(i >> 1) & 3] = pk(ao[i] * inv, ao[i + 1] * inv);
      if (hh) { of[1][2] = 0u; of[1][3] = 0u; }
      st16<SC1>(of[0], rws, oo, 0);
      st16<SC1>(of[1], rws, oo, 512);
      SCHED();
      STAMP(39 + layer * 4);
      bar();
      DPUMP(0, 0, NO1)
      publish_n<4 * NO1>(fl + (1 * 256 + bid) * 32, epoch);
      STAMP(5 + layer * 16);
    }
    if (on35) {
      {
        DPUMP(0, NO1, NO1 + 1)
        FRESH();
        const int tq = st >> 1;
        GAS unsigned* fp = fl + (1 * 256 + (((tq & 1) * 16 + 4 * w + (lane & 3)) * 8 + (tq >> 1))) * 32;
        int oo = O_OFF + layer * O_SZ + (st * 16 + 4 * w) * 4096 + lane * 16;
        PINV(fp); PINV(oo);
        DPUMP(0, NO1 + 1, NO1 + 2)
        int sb = S3_OFF + layer * S3_SZ + st * 8192 + (16 * w + c) * 16;
        int sw = g == 0 ? sb + dt * 1024 : OOB;
        int ob[4];
#pragma unroll
        for (int b = 0; b < 4; ++b) { ob[b] = ((b + w) & 3) * 1024; PINS(ob[b]); }
        PINV(sb); PINV(sw); PINV(red);
#pragma unroll
        for (int a = 0; a < 4; ++a) { res[a] += unpack4(rr[a]); PINV(res[a]); }
        unsigned pf;
        DPUMP(0, NO1 + 2, NO1 + NO2)
        POLL(pf, fp);
        DPUMP(0, NO1 + NO2, Z_G)
        unsigned late;
        POLLED(pf, 4 * NO3, fp)
        STAMP(6 + layer * 16);
        v4u of[16];
#define LOAD_O(I) of[I] = ld16<SC1>(rws, oo + ((I) & 3) * 4096, ob[(((I) >> 2) + 1) & 3])
#pragma unroll
        for (int i = 0; i < 12; ++i) { LOAD_O(i); SCHED(); }
        vmw<12>();
        KEEP(late);
        STAMP(50 + layer * 4);
        f32x4 acc[4][4];
        sfor<16>([&](auto I) INL {
          constexpr int i = I, b = ((i >> 2) + 1) & 3, k = i & 3;
#pragma unroll
          for (int a = 0; a < 4; ++a) {
            acc[a][b] = mfma16(wo[k][a], of[i], k > 0 ? acc[a][b] : b ? (f32x4){0.f, 0.f, 0.f, 0.f} : res[a]);
            SCHED();
            if constexpr (i < 4) { if (a == 0) LOAD_O(i + 12); }
            if (a == 1) DTAIL1(0, Z_G, i)
            if constexpr (i >= 4) { if (a == 2) REDW(k, i >> 2); }
            SCHED();
          }
        });
        STAMP(51 + layer * 4);
        bar();
        STAMP(52 + layer * 4);
        float s1 = 0.f, s2 = 0.f;
#pragma unroll
        for (int a = 0; a < 4; ++a) {
          res[a] = (acc[a][0] + REDR(a, 1)) + (REDR(a, 2) + REDR(a, 3));
          s1 += (res[a][0] + res[a][1]) + (res[a][2] + res[a][3]);
          s2 += (res[a][0] * res[a][0] + res[a][1] * res[a][1]) + (res[a][2] * res[a][2] + res[a][3] * res[a][3]);
        }
        s1 = sum32(sum16(s1)); s2 = sum32(sum16(s2));
        st16<SC1>((v4u){asu(s1), epoch, asu(s2), epoch}, rws, sw, 0);
        STAMP(7 + layer * 16);
        bar();
        DPUMP(Z_G, Z_G, Z_G + 1)
        int sa = sb + g * 1024, sc = sb + (g < 3 ? g + 4 : g) * 1024;
        int ho = HT_OFF + layer * HT_SZ + (dt * 64 + 2 * st + (w >> 1)) * 4096 + (g >> 1) * 1024 + (2 * (w & 1) + (g & 1)) * 256 + c * 16;
        PINV(sa); PINV(sc); PINV(ho);
        DPUMP(Z_G, Z_G + 1, Z_G + 2)
        LAS char* hst = L + L_HST + w * 2048;
        const int hr = 8 * (g & 1) + q4;
        int h0 = raddr(hr) ^ ((g >> 1) << 5), h1 = raddr(hr + 4) ^ ((g >> 1) << 5), hw = (c * 128 + (swz(c) << 5) + g * 8) ^ (pa << 5);
        PINV(h0); PINV(h1); PINV(hw);
        DPUMP(Z_G, Z_G + 2, Z_G + NG2)
        v4u ga, gb;
        asm volatile("buffer_load_dwordx4 %0, %2, %4, 0 offen sc1\n\tbuffer_load_dwordx4 %1, %3, %4, 0 offen sc1" : "=&v"(ga), "=&v"(gb) : "v"(sa), "v"(sc), "s"(rws) : "memory");
        DPUMP(Z_G, Z_G + NG2, Z_H)
        asm volatile("s_waitcnt vmcnt(%2)" : "+v"(ga), "+v"(gb) : "n"(4 * NG3) : "memory");
        while (__builtin_amdgcn_ballot_w64((ga.y != epoch) | (ga.w != epoch) | (gb.y != epoch) | (gb.w != epoch)) != 0ull) {
          if (budget == 0u) break;
          --budget;
          asm volatile("buffer_load_dwordx4 %0, %2, %4, 0 offen sc1\n\tbuffer_load_dwordx4 %1, %3, %4, 0 offen sc1\n\ts_waitcnt vmcnt(0)"
                       : "=&v"(ga), "=&v"(gb) : "v"(sa), "v"(sc), "s"(rws) : "memory");
        }
        float t1 = asf(ga.x) + (g < 3 ? asf(gb.x) : 0.f), t2 = asf(ga.z) + (g < 3 ? asf(gb.z) : 0.f);
        STAMP(8 + layer * 16);
        t1 = sum32(sum16(t1)); t2 = sum32(sum16(t2));
        const float m = t1 * (1.f / D), var = fmaxf(t2 * (1.f / D) - m * m, 0.f);
        const float r1 = __builtin_amdgcn_rsqf(var + EPS);
        const float r = r1 * __builtin_amdgcn_rsqf(var * r1 * r1 + EPS), nr = -m * r;
#pragma unroll
        for (int a = 0; a < 4; ++a) {
          const v2u v = {pk(res[a][0] * r + nr, res[a][1] * r + nr), pk(res[a][2] * r + nr, res[a][3] * r + nr)};
          *(LAS v2u*)(hst + (hw ^ (a << 5))) = v;
        }
#pragma unroll
        for (int e = 0; e < 2; ++e) st16<SC1>(trfrag(hst + (h0 ^ (e << 6)), hst + (h1 ^ (e << 6))), rws, ho, e * 2048);
        SCHED();
        DPUMP(Z_G, Z_H, Z_H + NH1)
        publish_n<4 * NH1>(fl + (2 * 256 + bid) * 32, epoch);
        STAMP(9 + layer * 16);
      }
      DPUMP(Z_G, Z_H + NH1, Z_H + NH1 + 1)
      if (on4) {
        FRESH();
        const int sp = 8 * w + (lane & 7);
        GAS unsigned* fp = fl + (2 * 256 + ((sp & 3) * 7 + dt4) * 8 + (sp >> 2)) * 32;
        int hv[4];
#pragma unroll
        for (int b = 0; b < 4; ++b) { hv[b] = HT_OFF + layer * HT_SZ + (dt4 * 64 + 16 * w) * 4096 + ((b + w4) & 3) * 1024 + lane * 16; PINV(hv[b]); }
        PINV(fp);
        DPUMP(Z_G, Z_H + NH1 + 1, Z_H + NH1 + 2)
        int uo = U_OFF + layer * U_SZ + (dt4 * 56 + 2 * ft) * 4096 + w4 * 1024 + lane * 16, bo = ft * 128 + g * 8;
        PINV(uo); PINV(red); PINV(bo);
        unsigned pf;
        DPUMP(Z_G, Z_H + NH1 + 2, Z_H + NH1 + NH2)
        POLL(pf, fp);
        DPUMP(Z_G, Z_H + NH1 + NH2, Z_U)
        unsigned late;
        POLLED(pf, 4 * NH3, fp)
        STAMP(10 + layer * 16);
        v4u af[64];
#define LOAD_H(I) af[I] = ld16<SC1>(rws, hv[(((I) >> 4) + 1) & 3], ((I) & 15) * 4096)
        v2u bb[4];
#pragma unroll
        for (int i = 0; i < WIN4; ++i) { LOAD_H(i); SCHED(); }
        vmw<WIN4>();
        KEEP(late);
        f32x4 acc[4][4], part[4][3];
        sfor<64>([&](auto I) INL {
          constexpr int i = I, b = ((i >> 4) + 1) & 3, k = i & 15;
#pragma unroll
          for (int a = 0; a < 4; ++a) {
            acc[a][b] = mfma16(wz[k][a], af[i], k > 0 ? acc[a][b] : b ? (f32x4){0.f, 0.f, 0.f, 0.f} : acc[a][0]);
            SCHED();
            if constexpr (i + WIN4 < 64) { if (a == 0) LOAD_H(i + WIN4); }
            if constexpr (i < 8) { if (a == 1) DTAIL1(Z_G, Z_U, 2 * i) if (a == 2) DTAIL1(Z_G, Z_U, 2 * i + 1) }
            if constexpr (i >= 16 && k < 4) { if (a == 2) REDW(k, i >> 4); }
            if constexpr (i >= 24 && i < 28) { if (a == 2) bb[i - 24] = ld8<0>(mk(p.b1, F * 2), bo, (i - 24) * 32); }
            if constexpr (i >= 43 && i < 47) { if (a == 3) { PINV(bb[i - 43]); acc[i - 43][0] = unpack4(bb[i - 43]); } }
            if constexpr (i == 54) { if (a == 3) bar(); }
            if constexpr (i >= 55 && i < 59) { if (a > 0) part[i - 55][a - 1] = REDR(i - 55, a); }
            if constexpr (i >= 59 && i < 63) { if (a == 2) { part[i - 59][0] = (part[i - 59][0] + part[i - 59][1]) + part[i - 59][2]; PINV(part[i - 59][0]); } }
            SCHED();
          }
        });
        STAMP(11 + layer * 16);
        v4u uf[2];
#pragma unroll
        for (int a = 0; a < 4; ++a) {
          const f32x4 u4 = acc[a][0] + part[a][0];
          float gl[4];
#pragma unroll
          for (int j = 0; j < 4; ++j) {
            const float u = u4[j];
            const float e = __builtin_amdgcn_exp2f(u * (u * u * (-0.10294324f) - 2.3022082f));
            gl[j] = u * __builtin_amdgcn_rcpf(1.f + e);
          }
          uf[a >> 1][(a & 1) * 2] = pk(gl[0], gl[1]);
          uf[a >> 1][(a & 1) * 2 + 1] = pk(gl[2], gl[3]);
          if (a == 1) { st16<SC1>(uf[0], rws, uo, 0); SCHED(); }
        }
        st16<SC1>(uf[1], rws, uo, 4096);
        SCHED();
        STAMP(40 + layer * 4);
        bar();
        DPUMP(Z_U, Z_U, Z_U + NU1)
        publish_n<4 * NU1>(fl + (3 * 256 + bid) * 32, epoch);
      } else {
        DPUMP(Z_G, Z_H + NH1 + 1, Z_U)
        DRAIN();
        DTAIL(Z_G, Z_U)
        LGKM0();
        DPUMP(Z_U, Z_U, Z_U + NU1)
      }
      STAMP(12 + layer * 16);
      {
        DPUMP(Z_U, Z_U + NU1, Z_U + NU1 + 1)
        FRESH();
        const int pi = wrap7(lane & 7);
        GAS unsigned* fp = fl + (3 * 256 + (pi == 3 ? (21 + (dt & 3)) * 8 + (dt >> 2) : (7 * (pi & 3) + dt) * 8 + (pi >> 2)) + 2 * w) * 32;
        int uv[4];
#pragma unroll
        for (int a = 0; a < 4; a += 2) { uv[a] = U_OFF + layer * U_SZ + (dt * 56 + 14 * w) * 4096 + (a ^ pa) * 1024 + lane * 16; PINV(uv[a]); uv[a + 1] = uv[a] + 1024; }
        PINV(fp);
        DPUMP(Z_U, Z_U + NU1 + 1, Z_U + NU1 + 2)
        int xb = layer ? (64 * st + 16 * w + c) * 896 + dt * 128 + g * 8
                       : X2_OFF + (2 * st + (w >> 1)) * 28672 + (4 * dt + (g >> 1)) * 1024 + ((g & 1) * 32 + (w & 1) * 16 + c) * 16;
        int so = g == 0 ? S5_OFF + (st * 8 + dt) * 512 + (16 * w + c) * 8 : OOB;
#pragma unroll
        for (int a = 0; a < 4; ++a) { res[a] += bflo(b2r); PINV(res[a]); }
        PINV(xb); PINV(so); PINV(red);
        unsigned pf;
        DPUMP(Z_U, Z_U + NU1 + 2, 30 - NU3)
        POLL(pf, fp);
        DPUMP(Z_U, 30 - NU3, 30)
        unsigned late;
        POLLED(pf, 4 * NU3, fp)
        STAMP(13 + layer * 16);
        v4u af[56];
#define LOAD_U(I) af[I] = ld16<SC1>(rws, uv[(I) / 14], ((I) % 14) * 4096)
#pragma unroll
        for (int i = 0; i < WIN5; ++i) { LOAD_U(i); SCHED(); }
        vmw<WIN5>();
        KEEP(late);
        f32x4 acc[4][4], part[3][3];
        sfor<56>([&](auto I) INL {
          constexpr int i = I, a = i / 14, k = i % 14;
#pragma unroll
          for (int bq = 0; bq < 4; ++bq) {
            const int b = (bq + 1) & 3;
            acc[a][b] = mfma16(af[i], wz[16 + k][b], k > 0 ? acc[a][b] : b ? (f32x4){0.f, 0.f, 0.f, 0.f} : res[a]);
            SCHED();
            if constexpr (i + WIN5 < 56) { if (bq == 0) LOAD_U(i + WIN5); }
            if constexpr (i < 8) { if (bq == 1) DTAIL1(Z_U, 30, 2 * i) if (bq == 2) DTAIL1(Z_U, 30, 2 * i + 1) }
            if constexpr (a > 0 && k < 3) { if (bq == 2) REDW(a - 1, k + 1); }
            if constexpr (i == 47) { if (bq == 3) bar(); }
            if constexpr (i >= 48 && i < 51) { if (bq > 0) part[i - 48][bq - 1] = REDR(i - 48, bq); }
            if constexpr (i >= 51 && i < 54) { if (bq == 2) { res[i - 51] = (acc[i - 51][0] + part[i - 51][0]) + (part[i - 51][1] + part[i - 51][2]); PINV(res[i - 51]); } }
            SCHED();
          }
        });
        STAMP(14 + layer * 16);
        REDW(3, 1); REDW(3, 2); REDW(3, 3);
        v2u xo[4];
        float s1 = 0.f, s2 = 0.f;
#pragma unroll
        for (int a = 0; a < 3; ++a) {
          xo[a] = (v2u){pk(res[a][0], res[a][1]), pk(res[a][2], res[a][3])};
          s1 += (res[a][0] + res[a][1]) + (res[a][2] + res[a][3]);
          s2 += (res[a][0] * res[a][0] + res[a][1] * res[a][1]) + (res[a][2] * res[a][2] + res[a][3] * res[a][3]);
        }
        PINV(s1); PINV(s2);
        const rsrc_t ro = mk(p.out, S * D * 2);
        if (layer == 0) st16<SC1>((v4u){xo[0].x, xo[0].y, xo[1].x, xo[1].y}, rws, xb, pa * 1024);
        else { st8<0>(xo[0], ro, xb, pa * 32); st8<0>(xo[1], ro, xb, pa * 32 + 32); st8<0>(xo[2], ro, xb, (2 ^ pa) * 32); }
        bar();
        res[3] = (acc[3][0] + REDR(3, 1)) + (REDR(3, 2) + REDR(3, 3));
        xo[3] = (v2u){pk(res[3][0], res[3][1]), pk(res[3][2], res[3][3])};
        if (layer == 0) {
          st16<SC1>((v4u){xo[2].x, xo[2].y, xo[3].x, xo[3].y}, rws, xb, (2 ^ pa) * 1024);
          SCHED();
          s1 += (res[3][0] + res[3][1]) + (res[3][2] + res[3][3]);
          s2 += (res[3][0] * res[3][0] + res[3][1] * res[3][1]) + (res[3][2] * res[3][2] + res[3][3] * res[3][3]);
          s1 = sum32(sum16(s1)); s2 = sum32(sum16(s2));
          st8<SC1>((v2u){asu(s1), asu(s2)}, rws, so, 0);
          publish(flags + (4 * 256 + bid) * 32, epoch);
        } else {
          st8<0>(xo[3], ro, xb, (2 ^ pa) * 32 + 32);
        }
      }
    }
    else {
      FRESH();
      const int rw = 4 * (by - 28) + (lane >> 4) + 16 * ((lane >> 2) & 3);
      if (TCH1 == 1) {
        const int vo = (512 * w + 256 + rw) * (F * 2) + (7 * (bx >> 1) + 3 * (bx & 1) + (lane & 3)) * 128;
#pragma unroll 1
        for (int i = 0; i < 4; ++i) { touch(mk(p.w1, S * F * 2), vo, i * 64 * F * 2, L); vmw<PACE>(); }
      }
      const int vo = (448 * w + rw) * (S * 2) + bx * 512 + (lane & 3) * 128;
#pragma unroll 1
      for (int i = 0; i < 7; ++i) { touch(mk(p.w2, S * F * 2), vo, i * 64 * S * 2, L); vmw<PACE>(); }
    }
    STAMP(15 + layer * 16);
    if (layer) break;
    {
      FRESH();
      const int st1 = 2 * t + (w >> 1);
      v4u wk[28], wv[28];
      LAS char* wb = WB(0) + 28672;
      PINV(wb);
      wk[0] = WFRAG(wb, 0, 0); wv[0] = WFRAG(wb, 1, 0); wk[1] = WFRAG(wb, 0, 1); wv[1] = WFRAG(wb, 1, 1);
      f32x4 ck[4], cv[4];
#pragma unroll
      for (int a = 0; a < 4; ++a) { ck[a] = CSUM(1, a); cv[a] = CSUM(2, a); PINV(ck[a]); PINV(cv[a]); }
      int xo = X2_OFF + (4 * t + w) * 28672 + lane * 16, so = S5_OFF + (st1 * 8 + hh * 4) * 512 + ((w & 1) * 32 + r32) * 8;
      int xt = lane < 14 ? X2_OFF + (4 * t + w) * 28672 + (hd * 14 + lane) * 128 : OOB;
      int so3 = hh ? OOB : so + 3 * 512;
      PINV(xo); PINV(so); PINV(so3); PINV(xt);
      GAS unsigned* fp = flags + (4 * 256 + ((st1 & 3) * 7 + wrap7(lane & 7)) * 8 + (st1 >> 2)) * 32;
      wait_flags(fp, epoch, budget);
      STAMP(18);
      v2u sp[4];
      const unsigned xtv = XTCH ? __builtin_amdgcn_raw_buffer_load_b32(rws, xt, 0, SC1) : 0u;
      SCHED();
#define LOAD_X(I) xf[I] = ld16<SC1>(rws, xo + ((I) & 3) * 1024, ((I) >> 2) * 4096)
#pragma unroll
      for (int i = 0; i < WINX; ++i) { LOAD_X(i); SCHED(); }
      sfor<28>([&](auto I) INL {
        constexpr int ks = I;
        if constexpr (ks == 0) MFMA32V0(ak, wk[ks], xf[ks]); else MFMA32VA(ak, wk[ks], xf[ks]);
        if constexpr (ks + 2 < 28) wk[ks + 2] = WFRAG(wb + ((ks + 2) >> 2) * 4096, 0, (ks + 2) & 3);
        if constexpr (ks + WINX < 28) LOAD_X(ks + WINX);
        SCHED();
        if constexpr (ks == 0) MFMA32V0(av, wv[ks], xf[ks]); else MFMA32VA(av, wv[ks], xf[ks]);
        if constexpr (ks + 2 < 28) wv[ks + 2] = WFRAG(wb + ((ks + 2) >> 2) * 4096, 1, (ks + 2) & 3);
        if constexpr (ks < 4) sp[ks] = ld8<SC1>(rws, ks == 3 ? so3 : so + ks * 512, 0);
        if constexpr (ks == 2) KEEP(xtv);
        if constexpr (ks == 18) {
          float s1 = (asf(sp[0].x) + asf(sp[1].x)) + (asf(sp[2].x) + asf(sp[3].x));
          float s2 = (asf(sp[0].y) + asf(sp[1].y)) + (asf(sp[2].y) + asf(sp[3].y));
          s1 = sum32(s1); s2 = sum32(s2);
          mean = s1 * (1.f / D);
          rstd = s2 * (1.f / D) - mean * mean;
          PINV(mean); PINV(rstd);
        }
        if constexpr (ks == 19) { rstd = __builtin_amdgcn_rsqf(fmaxf(rstd, 0.f) + EPS); PINV(rstd); }
        if constexpr (ks >= 20 && ks < 24) { ck[ks - 20] *= -mean * rstd; cv[ks - 20] *= -mean * rstd; PINV(ck[ks - 20]); PINV(cv[ks - 20]); }
        SCHED();
      });
      MFMA_SETTLE2(ak, av);
#pragma unroll
      for (int i = 0; i < 16; ++i) { ak[i] = ak[i] * rstd + ck[i >> 2][i & 3]; av[i] = av[i] * rstd + cv[i >> 2][i & 3]; }
    }
  }
  asm volatile(".Lfk_code_to:");
  if (blockIdx.x == 0 && threadIdx.x == 0) __hip_atomic_store(ctrl, epoch, RLX_AGENT);
}

extern "C" void kernel_launch(void* const* d_in, const int* in_sizes, int n_in, void* d_out, int out_size, void* d_ws,
                              size_t ws_size, hipStream_t stream) {
  Params p;
  p.x = d_in[0]; p.wqkv = d_in[1]; p.wo = d_in[2];
  p.w1 = d_in[4]; p.b1 = d_in[5]; p.w2 = d_in[6]; p.b2 = d_in[7];
  p.out = d_out; p.ws = d_ws;
  (void)hipFuncSetAttribute((const void*)fused_kernel, hipFuncAttributeMaxDynamicSharedMemorySize, LDS_BYTES);
  fused_kernel<<<256, 256, LDS_BYTES, stream>>>(p);
}
```

```cpp
#include <hip/hip_runtime.h>
#include <utility>

#define GAS __attribute__((address_space(1)))
#define LAS __attribute__((address_space(3)))
#define DI __device__ __forceinline__
#define INL __attribute__((always_inline))
#define RLX_AGENT __ATOMIC_RELAXED, __HIP_MEMORY_SCOPE_AGENT
typedef unsigned v4u __attribute__((ext_vector_type(4)));
typedef unsigned v2u __attribute__((ext_vector_type(2)));
typedef float f32x2 __attribute__((ext_vector_type(2)));
typedef float f32x4 __attribute__((ext_vector_type(4)));
typedef float f32x16 __attribute__((ext_vector_type(16)));
typedef short s16x4 __attribute__((ext_vector_type(4)));
typedef __bf16 bf16x2 __attribute__((ext_vector_type(2)));
typedef __bf16 bf16x8 __attribute__((ext_vector_type(8)));
typedef __amdgpu_buffer_rsrc_t rsrc_t;

constexpr int S = 2048, D = 448, F = 1792;
constexpr float EPS = 1e-5f;
constexpr int FLAG_OFF = 4096;
constexpr int KV_OFF = 512 * 1024, KV_SZ = 16 * 64 * 4096;
constexpr int O_OFF = KV_OFF + 2 * KV_SZ, O_SZ = 32 * 16 * 4096;
constexpr int S3_OFF = O_OFF + 2 * O_SZ, S3_SZ = 32 * 8 * 1024;
constexpr int HT_OFF = S3_OFF + 2 * S3_SZ, HT_SZ = 7 * 64 * 4096;
constexpr int U_OFF = HT_OFF + 2 * HT_SZ, U_SZ = 7 * 56 * 4096;
constexpr int X2_OFF = U_OFF + 2 * U_SZ, X2_SZ = 64 * 28 * 1024;
constexpr int S5_OFF = X2_OFF + X2_SZ, S5_SZ = 32 * 8 * 512;
constexpr int DBG_OFF = S5_OFF + S5_SZ;
constexpr int WS_BYTES = DBG_OFF + 256 * 128 * 8;
constexpr int L_WQ = 0;
constexpr int L_KVW = 86016;
constexpr int L_STG = L_KVW + 49152;
constexpr int L_CSP = L_STG + 16384;
constexpr int L_CS = L_CSP + 1536 + 128;
constexpr int L_HST = L_CS + 384;
constexpr int L_DUMP = L_HST + 8192;
constexpr int LDS_BYTES = L_DUMP + 256;

static_assert(L_HST % 128 == 0 && LDS_BYTES <= 163840, "LDS map");
constexpr int SC1 = 16, OOB = 0x7ffff000;
#define VT_LAYOUT 1
#define WIN4 12
#define WIN5 12
#define WINX 12
#define QPUB 14
#define NO1 2
#define NO2 4
#define NO3 2
#define NG2 3
#define NG3 2
#define NH1 2
#define NH2 3
#define NH3 2
#define NU0 3
#define NU1 2
#define NU3 1
#define PACE 1
#define FTCH 1
#define XTCH 0
#define SUMDOT 0
#define OAUX 16
#define OFULL 1
#define MAP2D 0
#define TCH0 1
#define TCH1 1
#define TCH2 1
constexpr int Z_G = NO1 + NO2 + NO3, Z_H = Z_G + NG2 + NG3, Z_U = Z_H + NH1 + NH2 + NH3;
static_assert(QPUB / 2 + 27 - QPUB >= 16 && NO1 <= 3 && NU1 <= 3 && NO2 >= 2 && NG2 >= 2 && NH2 >= 2 && Z_U >= 16 && Z_U + NU0 + NU1 + 2 + NU3 <= 30 && NU0 >= 3 && NU0 <= 6 && NO1 <= 4 && NU1 <= 4, "schedule");

struct Params {
  const void *x, *wqkv, *wo, *w1, *b1, *w2, *b2;
  void *out, *ws;
};

template <int... I, class Fn> DI void sfor_(std::integer_sequence<int, I...>, Fn&& f) { (f(std::integral_constant<int, I>{}), ...); }
template <int N, class Fn> DI void sfor(Fn&& f) { sfor_(std::make_integer_sequence<int, N>{}, f); }
DI float asf(unsigned u) { return __builtin_bit_cast(float, u); }
DI unsigned asu(float f) { return __builtin_bit_cast(unsigned, f); }
DI bf16x2 asb2(unsigned u) { return __builtin_bit_cast(bf16x2, u); }
DI rsrc_t mk(const void* p, int bytes) { return __builtin_amdgcn_make_buffer_rsrc((void*)p, 0, bytes, 0x00020000); }
template <int AUX> DI v4u ld16(rsrc_t r, int voff, int soff) { return __builtin_amdgcn_raw_buffer_load_b128(r, voff, soff, AUX); }
template <int AUX> DI v2u ld8(rsrc_t r, int voff, int soff) { return __builtin_amdgcn_raw_buffer_load_b64(r, voff, soff, AUX); }
template <int AUX> DI void st16(v4u v, rsrc_t r, int voff, int soff) { __builtin_amdgcn_raw_buffer_store_b128(v, r, voff, soff, AUX); }
template <int AUX> DI void st8(v2u v, rsrc_t r, int voff, int soff) { __builtin_amdgcn_raw_buffer_store_b64(v, r, voff, soff, AUX); }
DI f32x4 mfma16(v4u a, v4u b, f32x4 c) {
  return __builtin_amdgcn_mfma_f32_16x16x32_bf16(__builtin_bit_cast(bf16x8, a), __builtin_bit_cast(bf16x8, b), c, 0, 0, 0);
}
DI f32x16 mfma32(v4u a, v4u b, f32x16 c) {
  return __builtin_amdgcn_mfma_f32_32x32x16_bf16(__builtin_bit_cast(bf16x8, a), __builtin_bit_cast(bf16x8, b), c, 0, 0, 0);
}
DI unsigned pk(float lo, float hi) { return __builtin_bit_cast(unsigned, __builtin_convertvector((f32x2){lo, hi}, bf16x2)); }
DI float bflo(unsigned u) { return asf(u << 16); }
DI float bfhi(unsigned u) { return asf(u & 0xffff0000u); }
DI f32x4 unpack4(v2u v) { return (f32x4){bflo(v.x), bfhi(v.x), bflo(v.y), bfhi(v.y)}; }
DI v2u trrd(LAS char* p) { return __builtin_bit_cast(v2u, __builtin_amdgcn_ds_read_tr16_b64_v4i16((LAS s16x4*)p)); }
DI v4u trfrag(LAS char* p0, LAS char* p1) { v2u a = trrd(p0), b = trrd(p1); return (v4u){a.x, a.y, b.x, b.y}; }
DI float hi32(float v) {
  unsigned u = asu(v);
  auto r = __builtin_amdgcn_permlane32_swap(u, u, false, false);
  return asf(r[1]);
}
DI float sum32(float v) {
  unsigned u = asu(v);
  auto r = __builtin_amdgcn_permlane32_swap(u, u, false, false);
  return asf(r[0]) + asf(r[1]);
}
DI float max32(float v) {
  unsigned u = asu(v);
  auto r = __builtin_amdgcn_permlane32_swap(u, u, false, false);
  return fmaxf(asf(r[0]), asf(r[1]));
}
DI float sum16(float v) {
  unsigned u = asu(v);
  auto r = __builtin_amdgcn_permlane16_swap(u, u, false, false);
  return asf(r[0]) + asf(r[1]);
}
DI float sum_ab(float a, float b) {
  auto r = __builtin_amdgcn_permlane16_swap(asu(a), asu(b), false, false);
  return sum32(asf(r[0]) + asf(r[1]));
}
DI void stats16(const f32x4 (&v)[4], int n, f32x2& s1, f32x2& s2) {
#pragma unroll
  for (int a = 0; a < 4; ++a)
    if (n >> a & 1) {
      const f32x2 lo = {v[a][0], v[a][1]}, hi = {v[a][2], v[a][3]};
      s1 += lo; s2 += lo * lo; s1 += hi; s2 += hi * hi;
    }
}
#define FENCE() asm volatile("" ::: "memory")
#define SCHED() __builtin_amdgcn_sched_barrier(0)
#define DRAIN() asm volatile("s_waitcnt vmcnt(0)" ::: "memory")
#define PINV(x) asm volatile("" : "+v"(x))
#define PINA(x) asm volatile("" : "+a"(x))
#define PINS(x) asm volatile("" : "+s"(x))
#define KEEP(x) asm volatile("" :: "v"(x))
#define LGKM0() asm volatile("s_waitcnt lgkmcnt(0)" ::: "memory")
DI void bar() { LGKM0(); __builtin_amdgcn_s_barrier(); FENCE(); }
template <int N> DI void vmw() { asm volatile("s_waitcnt vmcnt(%0)" :: "n"(N) : "memory"); }
template <int STEP> DI void dma1(rsrc_t r, int voff, int& so, int& lds) {
  asm volatile("s_mov_b32 m0, %1\n\ts_add_u32 %1, %1, 0x400\n\tbuffer_load_dwordx4 %2, %3, %0 offen lds\n\ts_add_u32 %0, %0, %4"
               : "+s"(so), "+s"(lds) : "v"(voff), "s"(r), "n"(STEP) : "memory", "scc");
}
template <int LO, int SO> DI void dmai(rsrc_t r, int voff, int lds) {
  int so;
  asm volatile("s_add_u32 m0, %1, %4\n\ts_mov_b32 %0, %5\n\tbuffer_load_dwordx4 %2, %3, %0 offen lds"
               : "=&s"(so) : "s"(lds), "v"(voff), "s"(r), "n"(LO), "n"(SO) : "memory", "scc");
}
DI void touch(rsrc_t r, int voff, int soff, LAS char* L) {
  asm volatile("s_mov_b32 m0, %0\n\ts_nop 3\n\tbuffer_load_dword %1, %2, %3 offen lds"
               :: "s"((int)(size_t)(L + L_DUMP)), "v"(voff), "s"(r), "s"(soff) : "memory");
}
template <int STEP> DI void dma64(rsrc_t r, int v0, int v1, int soff, int lds) {
  unsigned so;
  asm volatile("s_mov_b32 m0, %1\n\ts_mov_b32 %0, %5\n\tbuffer_load_dwordx4 %2, %4, %0 offen lds\n\t"
               "s_add_u32 m0, m0, 0x400\n\ts_add_u32 %0, %0, %6\n\tbuffer_load_dwordx4 %3, %4, %0 offen lds\n\t"
               "s_add_u32 m0, m0, 0x400\n\ts_add_u32 %0, %0, %6\n\tbuffer_load_dwordx4 %2, %4, %0 offen lds\n\t"
               "s_add_u32 m0, m0, 0x400\n\ts_add_u32 %0, %0, %6\n\tbuffer_load_dwordx4 %3, %4, %0 offen lds"
               : "=&s"(so) : "s"(lds), "v"(v0), "v"(v1), "s"(r), "s"(soff), "n"(STEP) : "memory", "scc");
}

#define STAMP(i) do {} while (0)

DI void publish(GAS unsigned* flag, unsigned epoch) {
  DRAIN();
  __builtin_amdgcn_s_waitcnt(0x0F70);
  bar();
  if (threadIdx.x == 0) __hip_atomic_store(flag, epoch, RLX_AGENT);
  FENCE();
}
template <int N> DI void publish_n(GAS unsigned* flag, unsigned epoch) {
  vmw<N>();
  __builtin_amdgcn_s_barrier();
  FENCE();
  if (threadIdx.x == 0) __hip_atomic_store(flag, epoch, RLX_AGENT);
  FENCE();
}
#define POLL(f, ptr) asm volatile("global_load_dword %0, %1, off sc1" : "=&v"(f) : "v"(ptr) : "memory")
#define POLLED(f, N, ptr) { \
    unsigned f2_; \
    POLL(f2_, ptr); \
    asm volatile("s_waitcnt vmcnt(%1)" : "+v"(f) : "n"((N) + 1) : "memory"); \
    if (__builtin_amdgcn_ballot_w64(f != epoch) != 0ull) { \
      asm volatile("s_waitcnt vmcnt(0)" : "+v"(f2_) :: "memory"); \
      if (__builtin_amdgcn_ballot_w64(f2_ != epoch) != 0ull) wait_flags(ptr, epoch, budget); \
    } \
    FENCE(); \
    late = f2_; }
DI void wait_flags(GAS unsigned* flag, unsigned epoch, unsigned& budget) {
  FENCE();
  while (true) {
    unsigned f = __hip_atomic_load(flag, RLX_AGENT);
    if (__builtin_amdgcn_ballot_w64(f != epoch) == 0ull) break;
    if (budget == 0u) break;
    --budget;
  }
  __builtin_amdgcn_fence(__ATOMIC_ACQUIRE, "wavefront");
  FENCE();
}

DI void cv_wr(const v4u& r, int m, LAS char* stg, int wa0, int wa1) { *(LAS v4u*)(stg + ((m & 1) ? wa1 : wa0) + m * 1024) = r; }
DI v4u cv_rd(int x, LAS char* stg, int ra0, int ra1) { return trfrag(stg + (ra0 ^ (x << 5)), stg + (ra1 ^ (x << 5))); }
DI int wrap7(int v) { return v >= 7 ? v - 7 : v; }
DI int wrap14(int v) { return v >= 14 ? v - 14 : v; }
DI int swz(int row) { return ((row >> 1) & 1) | (((row >> 3) ^ (row >> 2)) & 1) << 1; }

#define FRESH() \
  int tid = threadIdx.x, bid = blockIdx.x; \
  asm volatile("" : "+v"(tid)); \
  asm volatile("" : "+s"(bid)); \
  const int lane = tid & 63, w = __builtin_amdgcn_readfirstlane(tid >> 6), bx = bid & 7, by = bid >> 3; \
  const int c = lane & 15, g = lane >> 4, q4 = (lane & 15) >> 2, p4 = lane & 3, r32 = lane & 31, hh = lane >> 5; \
  const int by7 = __builtin_amdgcn_readfirstlane((by * 37) >> 8), bym = __builtin_amdgcn_readfirstlane(by - 7 * by7); \
  const int t = 2 * bx + (by >> 4), hd = by & 15;                \
  const int st = 4 * bx + by7, dt = bym;                         \
  const int dt4 = MAP2D ? (bx & 1) * 4 + by7 : by < 21 ? bym : bym + 4 * (bx & 1), ft = 7 * (bx >> 1) + (MAP2D ? bym : by < 21 ? by7 + 4 * (bx & 1) : 3);    \
  const int rotx = hd >= 14 ? hd - 14 : wrap7(hd), rot4 = ROT4, rot5 = ROT5; \
  const int w4 = (w + G4 * 2 * ((MAP2D ? bym : by7) & 1)) & 3, pa = G5 * 2 * (by7 & 1);    \
  LAS char* stga = L + L_STG + w * 4096; \
  LAS char* ring = L + L_KVW + w * 16384; \
  LAS char* stgb = ring + 12288; \
  const int wrow = lane >> 3; \
  const int sx0 = swz(wrow) << 5, sx1 = swz(wrow + 8) << 5; \
  const int wa0 = (wrow * 128 + (lane & 7) * 16) ^ sx0, wa1 = (wrow * 128 + (lane & 7) * 16) ^ sx1; \
  auto raddr = [&](int row) { return row * 128 + (swz(row) << 5) + 8 * p4; }; \
  const int w1o = (512 * w + wrow) * (F * 2) + ft * 128 + (lane & 7) * 16, w2o = (448 * w + wrow) * (S * 2) + st * 128 + (lane & 7) * 16; \
  const int r1a = raddr(8 * g + q4), r1b = raddr(8 * g + 4 + q4), r2a = raddr(4 * g + q4), r2b = raddr(16 + 4 * g + q4); \
  LAS char* red = L + L_KVW + lane * 16; \
  (void)c; (void)g; (void)q4; (void)r32; (void)hh; (void)t; (void)hd; (void)st; (void)dt; (void)dt4; (void)ft; (void)stga; (void)stgb; (void)ring; \
  (void)sx0; (void)sx1; (void)wa0; (void)wa1; (void)rotx; (void)rot4; (void)rot5; (void)w1o; (void)w2o; (void)r1a; (void)r1b; (void)r2a; (void)r2b; \
  (void)red; (void)w4; (void)pa;
#define G4 0
#define G5 0
#define ROT4 0
#define ROT5 0

#define MNOP ""
#define MFMA32V0(d, a, b) asm volatile(MNOP "v_mfma_f32_32x32x16_bf16 %0, %1, %2, 0" : "=&v"(d) : "v"(a), "v"(b))
#define MFMA32VC(d, a, b, c) asm volatile(MNOP "v_mfma_f32_32x32x16_bf16 %0, %1, %2, %3" : "=&v"(d) : "v"(a), "v"(b), "v"(c))
#define MFMA32VA(d, a, b) asm volatile(MNOP "v_mfma_f32_32x32x16_bf16 %0, %1, %2, %0" : "+v"(d) : "v"(a), "v"(b))
#define MFMA_SETTLE(d) asm volatile("s_nop 11" : "+v"(d))
#define MFMA_SETTLE2(d, e) asm volatile("s_nop 11" : "+v"(d), "+v"(e))
DI float max3(float a, float b, float c) { float r; asm("v_max3_f32 %0, %1, %2, %3" : "=v"(r) : "v"(a), "v"(b), "v"(c)); return r; }

#define WB(cc) (L + L_WQ + (4 * hh + q4) * 64 + (g & 1) * 32 + 8 * p4 + (hd & 1) * 8 + (cc) * 4096)
#define WFRAG(wb, part, e) trfrag((wb) + (part) * 28672 + ((e) >> 1) * 2048 + ((e) & 1) * 512, (wb) + (part) * 28672 + ((e) >> 1) * 2048 + ((e) & 1) * 512 + 1024)
#define T1O ((16 * (by * 4 + w) + (lane >> 2)) * (F * 2) + (7 * (bx >> 1) + 3 * (bx & 1) + (lane & 3)) * 128)
#define T1H ((512 * (by >> 3) + 8 * ((by & 7) * 4 + w) + (lane >> 2)) * (F * 2) + (7 * (bx >> 1) + 3 * (bx & 1) + (lane & 3)) * 128)
#define CSUM(part, a) (*(LAS f32x4*)(L + L_CS + ((part) * 32 + 8 * (a) + 4 * hh) * 4))
#define WZ_RD1(n, i, sl) wz[n][i] = (n) < 16 ? trfrag(pr1[i] + (sl) * 4096, pr1[(i) ^ 2] + (sl) * 4096 + 512) : trfrag(pr2[i] + (sl) * 4096, pr2[i] + (sl) * 4096 + 2048)
#define DMA_WZ1(n, m, sl) { \
    if ((n) < 16) dmai<(sl) * 4096 + (m) * 1024, (32 * ((n) & 15) + 8 * (m)) * F * 2>(rw1, ((m) & 1) ? pw1 ^ 64 : pw1, pring); \
    else dmai<(sl) * 4096 + (m) * 1024, (32 * ((n) & 15) + 8 * (m)) * S * 2>(rw2, ((m) & 1) ? pw2 ^ 64 : pw2, pring); }
#define PIECE(L0, q) { \
    constexpr int r_ = (q) / 4, m_ = (q) % 4, n_ = (L0) + r_; \
    if constexpr (m_ == 0 && r_ >= 4) asm volatile("s_waitcnt vmcnt(8) lgkmcnt(0)" ::: "memory"); \
    else if constexpr (m_ == 0 && r_ == 3) vmw<8>(); \
    DMA_WZ1(n_, m_, r_ & 3) \
    if constexpr (r_ >= 3) WZ_RD1(n_ - 3, m_, (r_ - 3) & 3); \
    SCHED(); }
#define DPUMP(L0, A, B) sfor<4 * ((B) - (A))>([&](auto I_) INL { PIECE(L0, 4 * ((A) - (L0)) + I_) });
#define DTSTEP(L0, L1, sl) ((L1) - 1 - (((L1) - 1 - (L0) - (sl)) & 3))
#define DTAIL1(L0, L1, j) { if constexpr (DTSTEP(L0, L1, (j) >> 2) >= (L0) && DTSTEP(L0, L1, (j) >> 2) >= (L1) - 3) WZ_RD1(DTSTEP(L0, L1, (j) >> 2), (j) & 3, (j) >> 2); }
#define DTAIL(L0, L1) sfor<16>([&](auto J_) INL { constexpr int j_ = J_; DTAIL1(L0, L1, j_) }); SCHED();
#define REDW(a, b) *(LAS f32x4*)(red + w * 16384 + (((b) - 1) * 4 + (a)) * 1024) = acc[a][b]
#define REDR(a, b) (*(LAS f32x4*)(red + ((w + 4 - (b)) & 3) * 16384 + (((b) - 1) * 4 + (a)) * 1024))
#define COLSUM(part) { \
    LAS char* cb = L + L_WQ + (part) * 28672 + (w * 16 + 8 * hh + q4) * 64 + (g & 1) * 32 + 8 * p4; \
    v4u wc[7]; \
    _Pragma("unroll") for (int i = 0; i < 7; ++i) wc[i] = trfrag(cb + i * 4096, cb + i * 4096 + 256); \
    f32x16 cs = {}; \
    _Pragma("unroll") for (int i = 0; i < 7; ++i) cs = mfma32((v4u){0x3f803f80u, 0x3f803f80u, 0x3f803f80u, 0x3f803f80u}, wc[i], cs); \
    if (lane < 32) *(LAS float*)(L + L_CSP + (w * 96 + (part) * 32 + lane) * 4) = cs[0]; }
#define COLRED(i) { \
    LAS float* cp = (LAS float*)(L + L_CSP) + (((i) & 31) + 4 * (hd & 1) < 32 ? (i) + 4 * (hd & 1) : (i)); \
    *(LAS float*)(L + L_CS + (i) * 4) = (cp[0] + cp[96]) + (cp[192] + cp[288]); }

__global__ void __launch_bounds__(256, 1) fused_kernel(Params p) {
  extern __shared__ __attribute__((aligned(16))) char smem[];
  LAS char* L = (LAS char*)smem;
  {
    unsigned long long pc;
    int len;
    asm volatile("s_getpc_b64 %0\n.Lfk_code_from:\n\ts_mov_b32 %1, .Lfk_code_to-.Lfk_code_from" : "=s"(pc), "=s"(len));
    touch(mk((const void*)pc, len > 0 ? len : 0), (threadIdx.x & 63) < 4 ? ((blockIdx.x >> 3) * 16 + (threadIdx.x >> 4) + (threadIdx.x & 3)) * 128 : OOB, 0, L);
  }
  SCHED();
  const bool on35 = (blockIdx.x >> 3) < 28;
  const bool on4 = (blockIdx.x >> 3) < (MAP2D ? ((blockIdx.x & 1) ? 21 : 28) : (blockIdx.x & 1) ? 24 : 25);
  const rsrc_t rws = mk(p.ws, WS_BYTES);
  GAS unsigned* ctrl = (GAS unsigned*)p.ws;
  GAS unsigned* flags = (GAS unsigned*)((GAS char*)p.ws + FLAG_OFF);
  STAMP(0);
  unsigned budget = 1u << 22;

  v4u xf[28];
  v4u kv[4];
  float mean = 0.f, rstd = 0.f;
  f32x4 res[4];
  unsigned epoch;
  int kvs, oo, t1o, wvo2, pw1, pw2, fo;
  unsigned nk1, nk2, pm[8];
  LAS char *kvw, *vw, *vr0, *wor[4], *pr1[4], *pr2[4], *wbq;
#define SETUP(LAYER) { \
    kvs = KV_OFF + (LAYER) * KV_SZ + (hd * 64 + 4 * t + w) * 4096 + lane * 16; \
    oo = O_OFF + (LAYER) * O_SZ + ((2 * t + (w >> 1)) * 16 + hd) * 4096 + (2 * (w & 1) + (r32 >> 4)) * 1024 + hh * 256 + c * 16; \
    t1o = MAP2D ? (lane < 56 ? (512 * (by >> 3) + 8 * ((by & 7) * 4 + w) + ((lane * 37) >> 8)) * (F * 2) + (7 * (bx >> 1) + lane - 7 * ((lane * 37) >> 8)) * 128 : OOB) : lane < 32 ? T1H : OOB;    \
    wvo2 = ((112 * w + wrow) * 896 + dt * 128 + (lane & 7) * 16) ^ sx0; \
    pw1 = w1o ^ sx0; pw2 = w2o ^ sx0; \
    const int tp = (t >= 2 && (lane & 1)) ? t - 2 : t > 0 ? t - 1 : 0; \
    fo = FLAG_OFF + ((LAYER) * 5 * 256 + ((tp & 1) * 16 + hd) * 8 + (tp >> 1)) * 128; \
    nk2 = (t < 2 && hh) ? 0xf149u : 0u; nk1 = (t < 1 && hh) ? 0xf149u : 0u;    \
    _Pragma("unroll") for (int i = 0; i < 8; ++i) { \
      const int d_ = r32 - ((2 * i & 3) + 8 * (i >> 1)) - 4 * hh; \
      pm[i] = ((unsigned)(d_ >> 31) & 0xffffu) | ((unsigned)((d_ - 1) >> 31) & 0xffff0000u); PINA(pm[i]); } \
    kvw = L + L_KVW + w * 4096 + lane * 16; \
    vw = L + L_KVW + w * 4096 + 2048 + ((lane * 16) ^ (hh << 6)); \
    vr0 = L + L_KVW + w * 4096 + 2048 + ((((g & 1) * 1024 + (p4 & 1) * 512 + hh * 64 + q4 * 16) ^ ((p4 & 1) << 6) ^ ((g & 1) << 7)) + (p4 >> 1) * 8); \
    wbq = WB(0); \
    _Pragma("unroll") for (int i = 0; i < 4; ++i) { \
      wor[i] = ring + (raddr(16 * (g >> 1) + 4 * (g & 1) + q4) ^ ((i ^ pa) << 5)); \
      pr1[i] = ring + (r1a ^ (i << 5)); pr2[i] = ring + (r2a ^ (((i + w) & 3) << 5)); \
      PINA(wor[i]); PINA(pr1[i]); PINA(pr2[i]); } \
    PINV(kvs); PINA(oo); PINA(t1o); PINA(wvo2); PINA(pw1); PINA(pw2); PINA(fo); PINA(nk1); PINA(nk2); PINA(kvw); PINA(vw); PINA(vr0); PINV(wbq); }
#define KVOUT(acc, h) { \
    _Pragma("unroll") for (int i = 0; i < 16; i += 2) kv[2 * (h) + (i >> 3)][(i >> 1) & 3] = pk(acc[i], acc[i + 1]); \
    if (hh) { kv[2 * (h) + 1][2] = (h) ? 0x00003f80u : 0u; kv[2 * (h) + 1][3] = 0u; } \
    const rsrc_t rkv = mk(p.ws, t < 15 ? WS_BYTES : 0);    \
    st16<SC1>(kv[2 * (h)], rkv, kvs + (h) * 2048, 0); \
    st16<SC1>(kv[2 * (h) + 1], rkv, kvs + (h) * 2048 + 1024, 0); \
    SCHED(); }
#define X128(p) ((LAS char*)(size_t)((unsigned)(size_t)(p) ^ 128u))
  {
    FRESH();
    const rsrc_t rwq = mk(p.wqkv, D * 3 * D * 2);
    const rsrc_t rx = mk(p.x, S * D * 2);
    const int gid = by * 4 + w, rg = (int)(size_t)ring;
    touch(rx, lane < 14 ? (t * 896 + hd * 56 + w * 14 + lane) * 128 : OOB, 0, L);
    const int xvo = (t * 128 + w * 32 + wrow) * 896 + (((lane & 7) ^ (wrow >> 1)) << 4);
#define DMA_X(cc) sfor<4>([&](auto M) INL { constexpr int m = M; dmai<((cc) & 3) * 4096 + m * 1024, m * 8 * 896 + (cc) * 128>(rx, (m & 1) ? xvo ^ 64 : xvo, rg); })
    DMA_X(0); DMA_X(1);
    touch(rwq, lane < 28 ? (2 * gid) * 2688 + 896 + lane * 128 + (lane >= 14 ? 896 : 0) : OOB, 0, L);
    DMA_X(2);
    SCHED();
    SETUP(0)
    const int tb = gid * 21 + lane, tbr = (tb * 4682) >> 16;
    int tvb = lane < 21 ? (256 + tbr) * 2688 + 896 + (tb - 14 * tbr) * 128 : OOB;
    const int tl = by * 98 + w * 25 + lane, trow = (tl * 9363) >> 16;
    int tvq = (lane < 25 && w * 25 + lane < 98) ? trow * 2688 + (tl - 7 * trow) * 128 : OOB, tvw = (lane < 25 && w * 25 + lane < 98) ? tl * 128 : OOB;
    int wvo = (w * 16 + (lane >> 2)) * 2688 + hd * 56 - (hd & 1) * 8 + (lane & 3) * 16;
    const int wlb = (int)(size_t)(L + L_WQ) + w * 1024;
#define DMA_W(part, cc) dmai<(part) * 28672 + (cc) * 4096, (cc) * 64 * 2688 + (part) * 896>(rwq, wvo, wlb)
    LAS char* xa[8];
#pragma unroll
    for (int e = 0; e < 8; ++e) { xa[e] = ring + r32 * 128 + 8 * hh + ((e ^ ((r32 >> 1) & 7)) << 4); PINV(xa[e]); }
    LAS char* cb = L + L_WQ + (w * 16 + 8 * hh + q4) * 64 + (g & 1) * 32 + 8 * p4;
    cb += 28672;
    LAS char* wb = WB(0) + 28672;
    PINV(tvb); PINV(tvq); PINV(tvw); PINV(wvo); PINV(cb); PINV(wb);
    SCHED();
    DMA_X(3);
    asm volatile("buffer_load_dword %0, %1, %2, 0 offen sc1" : "=&v"(epoch) : "v"(tid >= 1 && tid <= 10 ? FLAG_OFF + ((tid - 1) * 256 + bid) * 128 : on35 && (tid >> 4) == 1 ? S3_OFF + ((tid >> 3) & 1) * S3_SZ + (st * 8 + dt) * 1024 + (tid & 7) * 128 : 0), "s"(rws) : "memory");
    SCHED();
#define READ_X(cc) _Pragma("unroll") for (int e = 0; e < 4; ++e) { \
      const int ch = (e >> 1) * 4 + (e & 1); \
      const v2u a_ = *(LAS v2u*)(xa[ch] + ((cc) & 3) * 4096), b_ = *(LAS v2u*)(xa[ch + 2] + ((cc) & 3) * 4096); \
      xf[4 * (cc) + e] = (v4u){a_.x, a_.y, b_.x, b_.y}; }
    const v4u ones = {0x3f803f80u, 0x3f803f80u, 0x3f803f80u, 0x3f803f80u};
    f32x16 gs, ss = {};
    float s1[2] = {};
#define XSTAT(i) { if constexpr ((i) == 0) MFMA32V0(gs, xf[i], xf[i]); else MFMA32VA(gs, xf[i], xf[i]); ss = mfma32(ones, xf[i], ss); }
#define XSUM ss[0]
    STAMP(32);
    vmw<14>(); READ_X(0) LGKM0(); DMA_X(4); XSTAT(0) XSTAT(1) XSTAT(2) XSTAT(3) SCHED();
    vmw<14>(); READ_X(1) LGKM0(); DMA_X(5); touch(rwq, tvb, 0, L); XSTAT(4) XSTAT(5) XSTAT(6) XSTAT(7) SCHED();
    vmw<14>(); READ_X(2) LGKM0(); DMA_X(6); SCHED();
    asm volatile("s_waitcnt vmcnt(13)" : "+v"(epoch) :: "memory");
    epoch = __builtin_amdgcn_readfirstlane(epoch) + 1u;
    READ_X(3)
    SCHED();
    sfor<20>([&](auto I) INL {
      constexpr int i = I;
      if constexpr (i == 6) { vmw<4 + 1 + 4 + 3>(); READ_X(4) }
      if constexpr (i == 10) { vmw<1 + 4 + 5>(); READ_X(5) }
      if constexpr (i == 14) { vmw<7>(); READ_X(6) }
      if constexpr ((i & 1) == 0 && i < 16) DMA_W(1 + ((i >> 1) & 1), i >> 2);
      XSTAT(8 + i)
      SCHED();
    });
    STAMP(33);
    f32x16 ak, av;
    v4u wk[28], wv[28], wc[14];
    f32x16 csk = {}, csv = {};
    float dg = 0.f, d8[8], d4[4], pp[4];
    f32x4 cc[8];
    LAS float* pb = (LAS float*)(L + L_CSP) + 32 + 16 * w + c + 4 * (hd & 1);
    LAS float* pw = (LAS float*)(L + (hh ? L_DUMP : L_CSP + (w * 96 + 32) * 4)) + r32;
#define PICK4(o, at, a0, a1, a2, a3, b0, b1, b2, b3, m) \
    asm volatile("v_cndmask_b32 %0, %4, %8, %12\n\tv_cndmask_b32 %1, %5, %9, %12\n\tv_cndmask_b32 %2, %6, %10, %12\n\tv_cndmask_b32 %3, %7, %11, %12" \
                 : "=&v"(o[at]), "=&v"(o[(at) + 1]), "=&v"(o[(at) + 2]), "=&v"(o[(at) + 3]) \
                 : "v"(a0), "v"(a1), "v"(a2), "v"(a3), "v"(b0), "v"(b1), "v"(b2), "v"(b3), "s"(m))
    sfor<8>([&](auto R) INL {
      constexpr int r = R;
      if constexpr (r < 7) {
        vmw<(r <= 3 ? 6 : 6)>();
        if constexpr (r == 6) {
          wc[12] = trfrag(cb + r * 4096, cb + r * 4096 + 256);
          wc[13] = trfrag(cb + 28672 + r * 4096, cb + 28672 + r * 4096 + 256);
          csk = mfma32(ones, wc[12], csk); csv = mfma32(ones, wc[13], csv);
          pw[0] = csk[0]; pw[32] = csv[0];
          LGKM0();
        }
        __builtin_amdgcn_s_barrier();
        FENCE();
        SCHED();
      }
      sfor<4>([&](auto E) INL {
        constexpr int e = E, kr = 4 * r + e, km = kr - 4;
        if constexpr (r > 0) { if constexpr (km == 0) MFMA32V0(ak, wk[km], xf[km]); else MFMA32VA(ak, wk[km], xf[km]); }
        if constexpr (r < 7) wk[kr] = WFRAG(wb + r * 4096, 0, e);
        if constexpr (r < 6 && (e & 1) == 0) wc[2 * r + e / 2] = trfrag(cb + (e / 2) * 28672 + r * 4096, cb + (e / 2) * 28672 + r * 4096 + 256);
        SCHED();
        if constexpr (r > 0) { if constexpr (km == 0) MFMA32V0(av, wv[km], xf[km]); else MFMA32VA(av, wv[km], xf[km]); }
        if constexpr (r < 7) wv[kr] = WFRAG(wb + r * 4096, 1, e);
        SCHED();
        if constexpr (r < 6 && e == 1) csk = mfma32(ones, wc[2 * r], csk);
        if constexpr (r < 6 && e == 3) csv = mfma32(ones, wc[2 * r + 1], csv);
        SCHED();
        if constexpr (e == 1 || e == 3) {
          if constexpr (r < 3) DMA_W(1 + e / 2, r + 4);
          else if constexpr (r == 3) { if constexpr (e == 1) touch(rwq, tvq, 0, L); else touch(mk(p.wo, D * D * 2), tvw, 0, L); }
          else if constexpr (2 * (r - 4) + e / 2 < 7) DMA_W(0, 2 * (r - 4) + e / 2);
        }
        if constexpr (r == 1 && (e & 1) == 0) { constexpr int j = 2 * e; PICK4(d8, j, gs[j], gs[j + 1], gs[j + 2], gs[j + 3], gs[j + 8], gs[j + 9], gs[j + 10], gs[j + 11], 0xffff0000ffff0000ull); }
        if constexpr (r == 2 && e == 0) PICK4(d4, 0, d8[0], d8[1], d8[2], d8[3], d8[4], d8[5], d8[6], d8[7], 0xff00ff00ff00ff00ull);
        if constexpr (r == 2 && e == 2)
          asm volatile("v_cndmask_b32 %1, %1, %3, %5\n\tv_cndmask_b32 %2, %2, %4, %5\n\tv_cndmask_b32 %0, %1, %2, %6\n\tv_cndmask_b32 %0, 0, %0, %7"
                       : "=&v"(dg), "+v"(d4[0]), "+v"(d4[1]) : "v"(d4[2]), "v"(d4[3]), "s"(0xccccccccccccccccull), "s"(0xaaaaaaaaaaaaaaaaull), "s"(0xf0f0f0f00f0f0f0full));
        if constexpr (r == 3 && e == 0) { mean = XSUM * (1.f / D); rstd = sum32(dg) * (1.f / D) - mean * mean; PINV(mean); PINV(rstd); }
        if constexpr (r == 3 && e == 2) { rstd = __builtin_amdgcn_rsqf(fmaxf(rstd, 0.f) + EPS); PINV(rstd); }
        if constexpr (r == 4 && e == 0) { PINV(pb); PINV(pw); }
        if constexpr (r == 6 && e == 0) { pp[0] = pb[0]; pp[1] = pb[96]; pp[2] = pb[192]; pp[3] = pb[288]; }
        if constexpr (r == 6 && e == 2) *(LAS float*)(L + L_CS + (32 + 16 * w + c) * 4) = (pp[0] + pp[1]) + (pp[2] + pp[3]);
        if constexpr (r == 6 && e == 3) bar();
        if constexpr (r == 7 && e < 2) { _Pragma("unroll") for (int j = 0; j < 4; ++j) cc[4 * e + j] = CSUM(1, 4 * e + j); }
        SCHED();
      });
    });
    STAMP(34);
    MFMA_SETTLE2(ak, av);
#pragma unroll
    for (int i = 0; i < 16; ++i) ak[i] = (ak[i] - mean * cc[i >> 2][i & 3]) * rstd;
    KVOUT(ak, 0)
#pragma unroll
    for (int i = 0; i < 16; ++i) av[i] = (av[i] - mean * cc[4 + (i >> 2)][i & 3]) * rstd;
    KVOUT(av, 1)
#pragma unroll
    for (int a = 0; a < 4; ++a) res[a] = (f32x4){0.f, 0.f, 0.f, 0.f};
  }

#pragma unroll 1
  for (int layer = 0;; ++layer) {
    GAS unsigned* fl = flags + layer * 5 * 256 * 32;
    STAMP(1 + layer * 16);
    v4u qf[2];
    v4u wo[4][4];
    v2u rr[4];
    unsigned b2r;
    v2u bb[4];
    v4u wz[30][4];
    int pring;
    const rsrc_t rw1 = mk(p.w1, on4 ? S * F * 2 : 0);
    const rsrc_t rw2 = mk(p.w2, on35 ? S * F * 2 : 0);
    {
      const bool hi = threadIdx.x & 32;
      FRESH();
      if (layer == 0) {
        vmw<4>();
        if (t == 15) vmw<0>();
        COLSUM(0)
        publish(fl + (0 * 256 + bid) * 32, epoch);
        if (tid < 32) COLRED(tid)
      }
      v4u wq[28];
#pragma unroll
      for (int ks = 0; ks < 4; ++ks) wq[ks] = WFRAG(wbq, 0, ks);
      f32x16 aq;
      STAMP(2 + layer * 16 + layer * 27);
      const rsrc_t rwo = mk(p.wo, on35 ? D * D * 2 : 0);
      pring = (int)(size_t)ring;
      PINS(pring);
      sfor<28>([&](auto I) INL {
        constexpr int ks = I;
        if constexpr (ks == 0) MFMA32V0(aq, wq[ks], xf[ks]); else MFMA32VA(aq, wq[ks], xf[ks]);
        if constexpr (ks + 4 < 28) wq[ks + 4] = WFRAG(wbq + ((ks + 4) >> 2) * 4096, 0, ks & 3);
        constexpr int wi = ks < QPUB ? ((ks & 1) ? ks / 2 : -1) : ks == QPUB ? -1 : QPUB / 2 + ks - QPUB - 1;
        if constexpr (wi >= 0 && wi < 16) dmai<wi * 1024, (28 * (wi >> 2) + 8 * (wi & 3)) * 896>(rwo, (wi & 1) ? wvo2 ^ 64 : wvo2, pring);
        if constexpr (ks == QPUB) {
          if (layer != 0) publish_n<QPUB / 2>(fl + (0 * 256 + bid) * 32, epoch);
          else bar();
          STAMP(3 + layer * 16);
        }
        SCHED();
      });
      f32x4 cq[4];
#pragma unroll
      for (int a = 0; a < 4; ++a) cq[a] = CSUM(0, a);
      GAS unsigned* fp = (GAS unsigned*)((GAS char*)p.ws + fo);
      PINV(fp);
      MFMA_SETTLE(aq);
      const float qs = rstd * (0.18898223650461362f * 1.4426950408889634f);
#pragma unroll
      for (int a = 0; a < 4; ++a) {
        qf[a >> 1][(a & 1) * 2] = pk((aq[4 * a] - mean * cq[a][0]) * qs, (aq[4 * a + 1] - mean * cq[a][1]) * qs);
        qf[a >> 1][(a & 1) * 2 + 1] = pk((aq[4 * a + 2] - mean * cq[a][2]) * qs, (aq[4 * a + 3] - mean * cq[a][3]) * qs);
      }
      if (hi) { qf[1][2] = 0x00003f80u; qf[1][3] = 0u; }
      PINV(qf[0]); PINV(qf[1]);
      unsigned pf;
      POLL(pf, fp);
      f32x16 s8;
      asm volatile("s_waitcnt vmcnt(1)" ::: "memory");
      MFMA32V0(s8, kv[0], qf[0]);
      MFMA32VA(s8, kv[1], qf[1]);
#pragma unroll
      for (int j = 0; j < 16; ++j) wo[j >> 2][j & 3] = trfrag(wor[j & 3] + (j >> 2) * 4096, wor[(j & 3) ^ 2] + (j >> 2) * 4096 + 1024);
      SCHED();
      unsigned late;
      POLLED(pf, 0, fp)
      STAMP(4 + layer * 16);
      v4u ra[4], rb[4];
      {
        const rsrc_t r1 = mk(p.ws, t >= 1 ? WS_BYTES : 0), r2 = mk(p.ws, t >= 2 ? WS_BYTES : 0);
        const int kvo = kvs - 32768;
        rb[0] = ld16<SC1>(r2, kvo, 0); rb[1] = ld16<SC1>(r2, kvo, 1024);
        ra[0] = ld16<SC1>(r1, kvo, 16384); ra[1] = ld16<SC1>(r1, kvo, 16384 + 1024);
        SCHED();
        rb[2] = ld16<SC1>(r2, kvo, 2048); rb[3] = ld16<SC1>(r2, kvo, 3072);
        ra[2] = ld16<SC1>(r1, kvo, 16384 + 2048); ra[3] = ld16<SC1>(r1, kvo, 16384 + 3072);
        SCHED();
      }
      bar();
      LAS char* vw1 = X128(vw) + 1024;
      LAS char* vr1 = X128(vr0);
      *(LAS v4u*)(kvw + 32768) = kv[0];
      *(LAS v4u*)(kvw + 32768 + 1024) = kv[1];
      *(LAS v4u*)(vw + 32768) = kv[2];
      *(LAS v4u*)(vw1 + 32768) = kv[3];
      int rro = (64 * st + 16 * w + c) * 896 + dt * 128 + g * 8, b2o = (64 * st + 16 * w + c) * 2, b1o = ft * 128 + g * 8;
      unsigned pml[8], mk32[16], nk1l = nk1, nk2l = nk2;
      int ool = oo, t1l = t1o;
      LAS char* vr0l = vr0;
      PINV(rro); PINV(b2o); PINV(b1o); PINV(vw1); PINV(vr1); PINV(vr0l); PINV(ool); PINV(t1l); PINV(nk1l); PINV(nk2l);
#pragma unroll
      for (int i = 0; i < 8; ++i) { pml[i] = pm[i]; PINV(pml[i]); mk32[2 * i] = (unsigned)((int)(pml[i] << 16) >> 16); mk32[2 * i + 1] = (unsigned)((int)pml[i] >> 16); PINV(mk32[2 * i]); PINV(mk32[2 * i + 1]); }
      SCHED();
      f32x16 sc[8], se;
      v4u kf[8][2];
      float mx = -1e30f, mx2 = -1e30f;
#define MAX8(v, i) asm volatile("v_max3_f32 %0, %0, %2, %3\n\tv_max3_f32 %1, %1, %4, %5\n\tv_max3_f32 %0, %0, %6, %7\n\tv_max3_f32 %1, %1, %8, %9" \
        : "+v"(mx), "+v"(mx2) : "v"(v[i]), "v"(v[(i) + 1]), "v"(v[(i) + 2]), "v"(v[(i) + 3]), "v"(v[(i) + 4]), "v"(v[(i) + 5]), "v"(v[(i) + 6]), "v"(v[(i) + 7]))
      *(LAS v4u*)(kvw) = rb[0];
      MFMA32V0(sc[0], rb[0], qf[0]);
      rb[1][2] |= nk2l;
      *(LAS v4u*)(kvw + 1024) = rb[1];
      *(LAS v4u*)(kvw + 16384) = ra[0];
      SCHED();
      MFMA32VA(sc[0], rb[1], qf[1]);
      ra[1][2] |= nk1l;
      *(LAS v4u*)(kvw + 16384 + 1024) = ra[1];
      kf[4][0] = ra[0]; kf[4][1] = ra[1];
      MFMA32V0(sc[4], kf[4][0], qf[0]);
      bar();
      KEEP(late);
      STAMP(37 + layer * 4);
      MFMA32VA(sc[4], kf[4][1], qf[1]);
#pragma unroll
      for (int j = 1; j < 8; ++j) if (j != 4) { kf[j][0] = *(LAS v4u*)(kvw + j * 4096); kf[j][1] = *(LAS v4u*)(kvw + j * 4096 + 1024); }
      PINV(sc[0]);
#pragma unroll
      for (int i = 0; i < 16; ++i) se[i] = asf((asu(sc[0][i]) & mk32[i]) | (asu(s8[i]) & ~mk32[i]));
      PINV(se);
      SCHED();
      sfor<12>([&](auto N) INL {
        constexpr int n = N, jj = n >> 1, j = jj < 3 ? jj + 1 : jj + 2, hf = n & 1;
        if constexpr (hf == 0) MFMA32V0(sc[j], kf[j][0], qf[0]); else MFMA32VA(sc[j], kf[j][1], qf[1]);
        if constexpr (n < 2) MAX8(se, 8 * hf);
        else if constexpr (n < 4) MAX8(sc[4], 8 * hf);
        else if constexpr (n >= 6) MAX8(sc[jj - 2], 8 * hf);
        if constexpr (n == 4) { *(LAS v4u*)(vw) = rb[2]; *(LAS v4u*)(vw1) = rb[3]; }
        if constexpr (n == 5) { *(LAS v4u*)(vw + 16384) = ra[2]; *(LAS v4u*)(vw1 + 16384) = ra[3]; SCHED(); touch(mk(p.w1, S * F * 2), t1l, 0, L); }
        SCHED();
      });
      MAX8(sc[5], 0); MAX8(sc[5], 8); MAX8(sc[6], 0); MAX8(sc[6], 8); MAX8(sc[7], 0); MAX8(sc[7], 8);
      mx = max32(max3(mx, mx2, mx2));
      f32x16 nm;
#pragma unroll
      for (int i = 0; i < 16; ++i) nm[i] = -mx;
      PINV(nm);
      bar();
      STAMP(38 + layer * 4);
      f32x16 ao;
      v4u vf[9][2], pp[9][2];
      float ex[32][4];
#define VF(j, s) vf[j][s] = trfrag(vr0l + (j) * 4096 + (s) * 256, vr1 + (j) * 4096 + (s) * 256)
      sfor<33>([&](auto N) INL {
        constexpr int n = N, j = n >> 2, q = n & 3;
        if constexpr (j == 0) {
          if constexpr (q == 0) MFMA32VC(sc[1], kf[1][0], qf[0], nm);
          if constexpr (q == 1) MFMA32VA(sc[1], kf[1][1], qf[1]);
          if constexpr (q == 2) MFMA32VC(sc[2], kf[2][0], qf[0], nm);
          if constexpr (q == 3) MFMA32VA(sc[2], kf[2][1], qf[1]);
        } else if constexpr (j < 8) {
          if constexpr (q == 1) { if constexpr (j == 1) MFMA32V0(ao, vf[0][0], pp[0][0]); else MFMA32VA(ao, vf[j - 1][0], pp[j - 1][0]); }
          if constexpr (q == 2) MFMA32VA(ao, vf[j - 1][1], pp[j - 1][1]);
          if constexpr (j + 2 < 8) {
            if constexpr (q == 0) MFMA32VC(sc[j + 2], kf[j + 2][0], qf[0], nm);
            if constexpr (q == 3) MFMA32VA(sc[j + 2], kf[j + 2][1], qf[1]);
          } else if constexpr (j == 6) {
            if constexpr (q == 0) MFMA32VA(ao, vf[8][0], pp[8][0]);
            if constexpr (q == 3) MFMA32VA(ao, vf[8][1], pp[8][1]);
          }
        } else MFMA32VA(ao, vf[7][0], pp[7][0]);
        SCHED();
        if constexpr (n < 32) {
#pragma unroll
          for (int i = 0; i < 4; ++i) { if constexpr (j == 0) ex[n][i] = __builtin_amdgcn_exp2f(se[4 * q + i] - mx); else ex[n][i] = __builtin_amdgcn_exp2f(sc[j][4 * q + i]); }
        }
        SCHED();
        if constexpr (n > 0) {
          constexpr int jp = (n - 1) >> 2, qp = (n - 1) & 3;
#pragma unroll
          for (int i = 0; i < 2; ++i) {
            const unsigned u = pk(ex[n - 1][2 * i], ex[n - 1][2 * i + 1]);
            if constexpr (jp == 0) { pp[0][qp >> 1][(2 * qp + i) & 3] = u & pml[2 * qp + i]; pp[8][qp >> 1][(2 * qp + i) & 3] = u & ~pml[2 * qp + i]; }
            else pp[jp][qp >> 1][(2 * qp + i) & 3] = u;
          }
        }
        if constexpr (n >= 2 && n < 10 && q >= 2) rr[(n >> 2) * 2 + q - 2] = ld8<0>(mk(p.x, (on35 && layer == 0) ? S * D * 2 : 0), rro, (((n >> 2) * 2 + q - 2) ^ pa) * 32);
        if constexpr (n == 10) b2r = (unsigned)__builtin_amdgcn_raw_buffer_load_b16(mk(p.b2, S * 2), b2o, 0, 0);
        if constexpr (n >= 14 && n < 22 && q >= 2) bb[((n >> 2) - 3) * 2 + q - 2] = ld8<0>(mk(p.b1, F * 2), b1o, (((n >> 2) - 3) * 2 + q - 2) * 32);
        if constexpr (n == 31) __builtin_amdgcn_s_waitcnt(0x0F70);
        if constexpr (n == 22) KEEP(nm);
        if constexpr (j < 8 && q < 2) VF(j, q);
        if constexpr (j == 5 && q >= 2) VF(8, q - 2);
        SCHED();
      });
      asm volatile("s_nop 1" : "+v"(pp[7][1]));
      MFMA32VA(ao, vf[7][1], pp[7][1]);
      MFMA_SETTLE(ao);
      const float inv = __builtin_amdgcn_rcpf(hi32(ao[12]));
      v4u of[2];
#pragma unroll
      for (int i = 0; i < 16; i += 2) of[i >> 3][(i >> 1) & 3] = pk(ao[i] * inv, ao[i + 1] * inv);
      if (hi) { of[1][2] = 0u; of[1][3] = 0u; }
      st16<SC1>(of[0], rws, ool, 0);
      st16<SC1>(of[1], rws, ool, 512);
      SCHED();
      STAMP(39 + layer * 4);
      bar();
      DPUMP(0, 0, NO1)
      publish_n<4 * NO1>(fl + (1 * 256 + bid) * 32, epoch);
      STAMP(5 + layer * 16);
    }
    if (on35) {
      {
        DPUMP(0, NO1, NO1 + 1)
        FRESH();
        const int tq = st >> 1;
        GAS unsigned* fp = fl + (1 * 256 + (((tq & 1) * 16 + 4 * w + (lane & 3)) * 8 + (tq >> 1))) * 32;
        int oo = O_OFF + layer * O_SZ + (st * 16 + 4 * w) * 4096 + lane * 16;
        PINV(fp); PINV(oo);
        DPUMP(0, NO1 + 1, NO1 + 2)
        int sb = S3_OFF + layer * S3_SZ + st * 8192 + (16 * w + c) * 16;
        int sw = g < 2 ? sb + dt * 1024 + g * 8 : OOB;
        int ob[4];
#pragma unroll
        for (int b = 0; b < 4; ++b) { ob[b] = ((b + w) & 3) * 1024; PINS(ob[b]); }
        PINV(sb); PINV(sw); PINV(red);
#pragma unroll
        for (int a = 0; a < 4; ++a) { res[a] += unpack4(rr[a]); PINV(res[a]); }
        unsigned pf;
        DPUMP(0, NO1 + 2, NO1 + NO2)
        POLL(pf, fp);
        DPUMP(0, NO1 + NO2, Z_G)
        unsigned late;
        POLLED(pf, 4 * NO3, fp)
        STAMP(6 + layer * 16);
        v4u of[16];
#define LOAD_O(I) of[I] = ld16<SC1>(rws, oo + ((I) & 3) * 4096, ob[(((I) >> 2) + 1) & 3])
#pragma unroll
        for (int i = 0; i < 12; ++i) { LOAD_O(i); SCHED(); }
        vmw<12>();
        KEEP(late);
        STAMP(50 + layer * 4);
        f32x4 acc[4][4], part[12];
        sfor<16>([&](auto I) INL {
          constexpr int i = I, b = ((i >> 2) + 1) & 3, k = i & 3;
#pragma unroll
          for (int a = 0; a < 4; ++a) {
            acc[a][b] = mfma16(wo[k][a], of[i], k > 0 ? acc[a][b] : (f32x4){0.f, 0.f, 0.f, 0.f});
            SCHED();
            if constexpr (i < 4) { if (a == 0) LOAD_O(i + 12); }
            if constexpr (i < 12) { if (a == 0) DTAIL1(0, Z_G, i) }
            if constexpr (i >= 4 && k < 2) { if (a == 1 || a == 3) REDW(2 * k + (a >> 1), i >> 2); }
            if constexpr (i == 14) { if (a == 0) bar(); }
            if constexpr (i >= 14) { if (a < 3) { const int j = 6 * (i - 14) + 2 * a; part[j] = REDR(j / 3, j % 3 + 1); part[j + 1] = REDR((j + 1) / 3, (j + 1) % 3 + 1); } }
            SCHED();
          }
        });
        STAMP(51 + layer * 4);
#pragma unroll
        for (int a = 0; a < 4; ++a) res[a] = ((res[a] + part[3 * a]) + (part[3 * a + 1] + part[3 * a + 2])) + acc[a][0];
        f32x2 s1 = {0.f, 0.f}, s2 = {0.f, 0.f};
        stats16(res, 15, s1, s2);
        st8<SC1>((v2u){asu(sum_ab(s1[0] + s1[1], s2[0] + s2[1])), epoch}, rws, sw, 0);
        SCHED();
        DTAIL1(0, Z_G, 12) DTAIL1(0, Z_G, 13) DTAIL1(0, Z_G, 14) DTAIL1(0, Z_G, 15)
        STAMP(7 + layer * 16);
        bar();
        DPUMP(Z_G, Z_G, Z_G + 1)
        int sa = sb + g * 1024, sc = g < 3 ? sb + (g + 4) * 1024 : OOB;
        int ho = HT_OFF + layer * HT_SZ + (dt * 64 + 2 * st + (w >> 1)) * 4096 + (g >> 1) * 1024 + (2 * (w & 1) + (g & 1)) * 256 + c * 16;
        PINV(sa); PINV(sc); PINV(ho);
        DPUMP(Z_G, Z_G + 1, Z_G + 2)
        LAS char* hst = L + L_HST + w * 2048;
        const int hr = 8 * (g & 1) + q4;
        int h0 = raddr(hr) ^ ((g >> 1) << 5), h1 = raddr(hr + 4) ^ ((g >> 1) << 5), hw = (c * 128 + (swz(c) << 5) + g * 8) ^ (pa << 5);
        PINV(h0); PINV(h1); PINV(hw);
        DPUMP(Z_G, Z_G + 2, Z_G + NG2)
        v4u ga, gb;
        asm volatile("buffer_load_dwordx4 %0, %2, %4, 0 offen sc1\n\tbuffer_load_dwordx4 %1, %3, %4, 0 offen sc1" : "=&v"(ga), "=&v"(gb) : "v"(sa), "v"(sc), "s"(rws) : "memory");
        DPUMP(Z_G, Z_G + NG2, Z_H)
        asm volatile("s_waitcnt vmcnt(%2)" : "+v"(ga), "+v"(gb) : "n"(4 * NG3) : "memory");
        while ((__builtin_amdgcn_ballot_w64((ga.y != epoch) | (ga.w != epoch)) | (__builtin_amdgcn_ballot_w64((gb.y != epoch) | (gb.w != epoch)) << 16)) != 0ull) {
          if (budget == 0u) break;
          --budget;
          asm volatile("buffer_load_dwordx4 %0, %2, %4, 0 offen sc1\n\tbuffer_load_dwordx4 %1, %3, %4, 0 offen sc1\n\ts_waitcnt vmcnt(0)"
                       : "=&v"(ga), "=&v"(gb) : "v"(sa), "v"(sc), "s"(rws) : "memory");
        }
        STAMP(8 + layer * 16);
        const unsigned t12 = asu(sum_ab(asf(ga.x) + asf(gb.x), asf(ga.z) + asf(gb.z)));
        const auto tt = __builtin_amdgcn_permlane16_swap(t12, t12, false, false);
        const float t1 = asf(tt[0]), t2 = asf(tt[1]);
        const float m = t1 * (1.f / D), var = fmaxf(t2 * (1.f / D) - m * m, 0.f);
        const float r1 = __builtin_amdgcn_rsqf(var + EPS);
        const float r = r1 * __builtin_amdgcn_rsqf(var * r1 * r1 + EPS), nr = -m * r;
#pragma unroll
        for (int a = 0; a < 4; ++a) {
          const v2u v = {pk(res[a][0] * r + nr, res[a][1] * r + nr), pk(res[a][2] * r + nr, res[a][3] * r + nr)};
          *(LAS v2u*)(hst + (hw ^ (a << 5))) = v;
        }
#pragma unroll
        for (int e = 0; e < 2; ++e) st16<SC1>(trfrag(hst + (h0 ^ (e << 6)), hst + (h1 ^ (e << 6))), rws, ho, e * 2048);
        SCHED();
        DPUMP(Z_G, Z_H, Z_H + NH1)
        publish_n<4 * NH1>(fl + (2 * 256 + bid) * 32, epoch);
        STAMP(9 + layer * 16);
      }
      DPUMP(Z_G, Z_H + NH1, Z_H + NH1 + 1)
      if (on4) {
        FRESH();
        const int sp = 8 * w + (lane & 7);
        GAS unsigned* fp = fl + (2 * 256 + ((sp & 3) * 7 + dt4) * 8 + (sp >> 2)) * 32;
        int hv[4];
#pragma unroll
        for (int b = 0; b < 4; ++b) { hv[b] = HT_OFF + layer * HT_SZ + (dt4 * 64 + 16 * w) * 4096 + ((b + w4) & 3) * 1024 + lane * 16; PINV(hv[b]); }
        PINV(fp);
        DPUMP(Z_G, Z_H + NH1 + 1, Z_H + NH1 + 2)
        int uo = U_OFF + layer * U_SZ + (dt4 * 56 + 2 * ft) * 4096 + w4 * 1024 + lane * 16;
        PINV(uo); PINV(red);
        unsigned pf;
        DPUMP(Z_G, Z_H + NH1 + 2, Z_H + NH1 + NH2)
        POLL(pf, fp);
        DPUMP(Z_G, Z_H + NH1 + NH2, Z_U)
        unsigned late;
        POLLED(pf, 4 * NH3, fp)
        STAMP(10 + layer * 16);
        v4u af[64];
#define LOAD_H(I) { int so_; asm volatile("s_mov_b32 %1, %4\n\tbuffer_load_dwordx4 %0, %2, %3, %1 offen sc1" \
          : "=&v"(af[I]), "=&s"(so_) : "v"(hv[(((I) >> 4) + 1) & 3]), "s"(rws), "n"(((I) & 15) * 4096) : "memory"); }
        sfor<WIN4>([&](auto I) INL { LOAD_H(I) SCHED(); });
        f32x4 acc[4][4], part[12];
        sfor<64>([&](auto I) INL {
          constexpr int i = I, b = ((i >> 4) + 1) & 3, k = i & 15;
          constexpr int np = i <= 54 ? 0 : i <= 62 ? i - 54 : 10;
          asm volatile("s_waitcnt vmcnt(%1)" : "+v"(af[i]) : "n"((i + WIN4 <= 64 ? WIN4 - 1 : 63 - i) + np) : "memory");
          if constexpr (i == 0) KEEP(late);
          SCHED();
          sfor<4>([&](auto A) INL {
            constexpr int a = A;
            acc[a][b] = mfma16(wz[k][a], af[i], k > 0 ? acc[a][b] : (f32x4){0.f, 0.f, 0.f, 0.f});
            SCHED();
            if constexpr (i + WIN4 < 64 && a == 0) LOAD_H(i + WIN4)
            if constexpr (i < 8) { if constexpr (a == 1) DTAIL1(Z_G, Z_U, 2 * i) if constexpr (a == 2) DTAIL1(Z_G, Z_U, 2 * i + 1) }
            if constexpr (i >= 16 && k < 2 && (a & 1)) REDW(2 * k + (a >> 1), i >> 4);
            if constexpr (i == 50 && a == 3) bar();
            if constexpr (i >= 51 && i < 54) part[4 * (i - 51) + a] = REDR((4 * (i - 51) + a) / 3, (4 * (i - 51) + a) % 3 + 1);
            if constexpr (i == 54 && a == 1) bar();
            if constexpr (i >= 54 && a == 2) PIECE(Z_U, np)
            if constexpr (i >= 62 && a == 3) PIECE(Z_U, np + 1)
            if constexpr (i >= 55 && i < 59 && a == 0) {
              constexpr int q = i - 55;
              part[3 * q] = (part[3 * q] + unpack4(bb[q])) + (part[3 * q + 1] + part[3 * q + 2]);
              PINV(part[3 * q]);
            }
            SCHED();
          });
        });
        STAMP(11 + layer * 16);
        v4u uf[2];
        sfor<8>([&](auto H) INL {
          constexpr int h = H, a = h >> 1, j0 = 2 * (h & 1);
          float gl[2];
#pragma unroll
          for (int j = 0; j < 2; ++j) {
            const float u = acc[a][0][j0 + j] + part[3 * a][j0 + j];
            const float e = __builtin_amdgcn_exp2f(u * (u * u * (-0.10294324f) - 2.3022082f));
            gl[j] = u * __builtin_amdgcn_rcpf(1.f + e);
          }
          uf[h >> 2][h & 3] = pk(gl[0], gl[1]);
          if constexpr (h & 1) {
            PINV(uf[h >> 2]);
            if constexpr (h == 3) st16<SC1>(uf[0], rws, uo, 0);
            if constexpr (h == 7) st16<SC1>(uf[1], rws, uo, 4096);
            SCHED();
            if constexpr (h < 7 && 3 + h / 2 < NU0) DPUMP(Z_U, Z_U + 3 + h / 2, Z_U + 4 + h / 2)
          }
        });
        STAMP(40 + layer * 4);
        DPUMP(Z_U, Z_U + NU0, Z_U + NU0 + NU1)
        publish_n<4 * NU1>(fl + (3 * 256 + bid) * 32, epoch);
      } else {
        DPUMP(Z_G, Z_H + NH1 + 1, Z_U)
        DRAIN();
        DTAIL(Z_G, Z_U)
        LGKM0();
        DPUMP(Z_U, Z_U, Z_U + NU0 + NU1)
      }
      STAMP(12 + layer * 16);
      {
        DPUMP(Z_U, Z_U + NU0 + NU1, Z_U + NU0 + NU1 + 1)
        FRESH();
        const int pi = wrap7(lane & 7);
        GAS unsigned* fp = fl + (3 * 256 + (MAP2D ? ((dt & 3) * 7 + pi) * 8 + (dt >> 2) : pi == 3 ? (21 + (dt & 3)) * 8 + (dt >> 2) : (7 * (pi & 3) + dt) * 8 + (pi >> 2)) + 2 * w) * 32;
        int uv[4];
#pragma unroll
        for (int a = 0; a < 4; a += 2) { uv[a] = U_OFF + layer * U_SZ + (dt * 56 + 14 * w) * 4096 + (a ^ pa) * 1024 + lane * 16; PINV(uv[a]); uv[a + 1] = uv[a] + 1024; }
        PINV(fp);
        DPUMP(Z_U, Z_U + NU0 + NU1 + 1, Z_U + NU0 + NU1 + 2)
        int ow = L_HST + w * 2048 + ((c * 128 + (swz(c) << 5) + g * 8) ^ (pa << 5)), orr = L_HST + w * 2048 + wa0;
        PINV(ow); PINV(orr);
        int xb = layer ? (OFULL ? (64 * st + 16 * w + wrow) * 896 + dt * 128 + (lane & 7) * 16 : (64 * st + 16 * w + c) * 896 + dt * 128 + g * 8)
                       : X2_OFF + (2 * st + (w >> 1)) * 28672 + (4 * dt + (g >> 1)) * 1024 + ((g & 1) * 32 + (w & 1) * 16 + c) * 16;
        int so = g < 2 ? S5_OFF + (st * 8 + dt) * 512 + (16 * w + c) * 8 + g * 4 : OOB;
#pragma unroll
        for (int a = 0; a < 4; ++a) { res[a] += bflo(b2r); PINV(res[a]); }
        PINV(xb); PINV(so); PINV(red);
        unsigned pf;
        DPUMP(Z_U, Z_U + NU0 + NU1 + 2, 30 - NU3)
        POLL(pf, fp);
        DPUMP(Z_U, 30 - NU3, 30)
        unsigned late;
        POLLED(pf, 4 * NU3, fp)
        STAMP(13 + layer * 16);
        v4u af[56];
#define LOAD_U(I) af[I] = ld16<SC1>(rws, uv[(I) / 14], ((I) % 14) * 4096)
#pragma unroll
        for (int i = 0; i < WIN5; ++i) { LOAD_U(i); SCHED(); }
        vmw<WIN5>();
        KEEP(late);
        f32x4 acc[4][4], part[4][3];
        v2u xo[4];
        f32x2 s1 = {0.f, 0.f}, s2 = {0.f, 0.f};
        const rsrc_t ro = mk(p.out, S * D * 2);
        sfor<56>([&](auto I) INL {
          constexpr int i = I, a = i / 14, k = i % 14;
#pragma unroll
          for (int bq = 0; bq < 4; ++bq) {
            const int b = (bq + 1) & 3;
            if (a < 3 || b) acc[a][b] = mfma16(af[i], wz[16 + k][b], k > 0 ? acc[a][b] : (f32x4){0.f, 0.f, 0.f, 0.f});
            SCHED();
            if constexpr (i + WIN5 < 56) { if (bq == 0) LOAD_U(i + WIN5); }
            if constexpr (i < 8) { if (bq == 1) DTAIL1(Z_U, 30, 2 * i) if (bq == 2) DTAIL1(Z_U, 30, 2 * i + 1) }
            if constexpr (a > 0 && k < 3) { if (bq == 2) REDW(a - 1, k + 1); }
            if constexpr (i == 46) { if (bq == 2) bar(); }
            if constexpr (i >= 47 && i < 50) { if (bq < 3) part[i - 47][bq] = REDR(i - 47, bq + 1); }
            if constexpr (i >= 50 && i < 53) { if (bq == 2) { res[i - 50] = ((res[i - 50] + part[i - 50][0]) + (part[i - 50][1] + part[i - 50][2])) + acc[i - 50][0]; PINV(res[i - 50]); } }
            if constexpr (i >= 53) { if (bq == 2) { xo[i - 53] = (v2u){pk(res[i - 53][0], res[i - 53][1]), pk(res[i - 53][2], res[i - 53][3])}; stats16(res, 1 << (i - 53), s1, s2); PINV(xo[i - 53]); PINV(s1); PINV(s2); } }
            SCHED();
          }
        });
        STAMP(14 + layer * 16);
        sfor<14>([&](auto K) INL {
          constexpr int k = K;
          acc[3][0] = mfma16(af[42 + k], wz[16 + k][0], k > 0 ? acc[3][0] : (f32x4){0.f, 0.f, 0.f, 0.f});
          SCHED();
          if constexpr (k >= 1 && k < 4) REDW(3, k);
          if constexpr (k == 5) {
            if (layer == 0) st16<SC1>((v4u){xo[0].x, xo[0].y, xo[1].x, xo[1].y}, rws, xb, pa * 1024);
            else if (!OFULL) { st8<OAUX>(xo[0], ro, xb, pa * 32); st8<OAUX>(xo[1], ro, xb, pa * 32 + 32); st8<OAUX>(xo[2], ro, xb, (2 ^ pa) * 32); }
            if (OFULL) { _Pragma("unroll") for (int a = 0; a < 3; ++a) *(LAS v2u*)(L + (ow ^ (a << 5))) = xo[a]; }
          }
          if constexpr (k == 7) { bar(); part[3][0] = REDR(3, 1); part[3][1] = REDR(3, 2); part[3][2] = REDR(3, 3); }
          SCHED();
        });
        res[3] = ((res[3] + part[3][0]) + (part[3][1] + part[3][2])) + acc[3][0];
        xo[3] = (v2u){pk(res[3][0], res[3][1]), pk(res[3][2], res[3][3])};
        if (layer == 0) {
          st16<SC1>((v4u){xo[2].x, xo[2].y, xo[3].x, xo[3].y}, rws, xb, (2 ^ pa) * 1024);
          SCHED();
          stats16(res, 8, s1, s2);
          __builtin_amdgcn_raw_buffer_store_b32(asu(sum_ab(s1[0] + s1[1], s2[0] + s2[1])), rws, so, 0, SC1);
          publish(flags + (4 * 256 + bid) * 32, epoch);
        } else {
          if (!OFULL) st8<OAUX>(xo[3], ro, xb, (2 ^ pa) * 32 + 32);
          else {
            *(LAS v2u*)(L + (ow ^ (3 << 5))) = xo[3];
            const v4u r0 = *(LAS v4u*)(L + orr), r1 = *(LAS v4u*)(L + (orr ^ 64) + 1024);
            st16<OAUX>(r0, ro, xb, 0);
            st16<OAUX>(r1, ro, xb, 8 * 896);
          }
        }
      }
    }
    else {
      FRESH();
      const int rw = 4 * (by - 28) + (lane >> 4) + 16 * ((lane >> 2) & 3);
      if (MAP2D) {
        const int l7 = (lane * 37) >> 8, vo = lane < 56 ? (512 * w + 256 + 8 * (by - 28) + l7) * (F * 2) + (7 * (bx >> 1) + lane - 7 * l7) * 128 : OOB;
#pragma unroll 1
        for (int i = 0; i < 8; ++i) { touch(mk(p.w1, S * F * 2), vo, i * 32 * F * 2, L); vmw<PACE>(); }
      } else {
        const int vo = (512 * w + 256 + rw) * (F * 2) + (7 * (bx >> 1) + 3 * (bx & 1) + (lane & 3)) * 128;
#pragma unroll 1
        for (int i = 0; i < 4; ++i) { touch(mk(p.w1, S * F * 2), vo, i * 64 * F * 2, L); vmw<PACE>(); }
      }
      const int vo = (448 * w + rw) * (S * 2) + bx * 512 + (lane & 3) * 128;
#pragma unroll 1
      for (int i = 0; i < 7; ++i) { touch(mk(p.w2, S * F * 2), vo, i * 64 * S * 2, L); vmw<PACE>(); }
    }
    STAMP(15 + layer * 16);
    if (layer) break;
    {
      FRESH();
      const int st1 = 2 * t + (w >> 1);
      f32x16 ak, av;
      v4u wk[28], wv[28];
      LAS char* wb = WB(0) + 28672;
      PINV(wb);
      wk[0] = WFRAG(wb, 0, 0); wv[0] = WFRAG(wb, 1, 0); wk[1] = WFRAG(wb, 0, 1); wv[1] = WFRAG(wb, 1, 1);
      f32x4 ck[4], cv[4];
#pragma unroll
      for (int a = 0; a < 4; ++a) { ck[a] = CSUM(1, a); cv[a] = CSUM(2, a); PINV(ck[a]); PINV(cv[a]); }
      int xo = X2_OFF + (4 * t + w) * 28672 + lane * 16, so = S5_OFF + (st1 * 8 + hh * 4) * 512 + ((w & 1) * 32 + r32) * 8;
      int xt = lane < 14 ? X2_OFF + (4 * t + w) * 28672 + (hd * 14 + lane) * 128 : OOB;
      int so3 = hh ? OOB : so + 3 * 512;
      PINV(xo); PINV(so); PINV(so3); PINV(xt);
      kvs += KV_SZ; oo += O_SZ; fo += 5 * 256 * 128;
      PINV(kvs); PINA(oo); PINA(fo);
      GAS unsigned* fp = flags + (4 * 256 + ((st1 & 3) * 7 + wrap7(lane & 7)) * 8 + (st1 >> 2)) * 32;
      wait_flags(fp, epoch, budget);
      STAMP(18);
      v2u sp[4];
      const unsigned xtv = XTCH ? __builtin_amdgcn_raw_buffer_load_b32(rws, xt, 0, SC1) : 0u;
      SCHED();
#define LOAD_X(I) xf[I] = ld16<SC1>(rws, xo + ((I) & 3) * 1024, ((I) >> 2) * 4096)
#pragma unroll
      for (int i = 0; i < WINX; ++i) { LOAD_X(i); SCHED(); }
      sfor<28>([&](auto I) INL {
        constexpr int ks = I;
        if constexpr (ks == 0) MFMA32V0(ak, wk[ks], xf[ks]); else MFMA32VA(ak, wk[ks], xf[ks]);
        if constexpr (ks + 2 < 28) wk[ks + 2] = WFRAG(wb + ((ks + 2) >> 2) * 4096, 0, (ks + 2) & 3);
        if constexpr (ks + WINX < 28) LOAD_X(ks + WINX);
        SCHED();
        if constexpr (ks == 0) MFMA32V0(av, wv[ks], xf[ks]); else MFMA32VA(av, wv[ks], xf[ks]);
        if constexpr (ks + 2 < 28) wv[ks + 2] = WFRAG(wb + ((ks + 2) >> 2) * 4096, 1, (ks + 2) & 3);
        if constexpr (ks < 4) sp[ks] = ld8<SC1>(rws, ks == 3 ? so3 : so + ks * 512, 0);
        if constexpr (ks == 2) KEEP(xtv);
        if constexpr (ks == 18) {
          float s1 = (asf(sp[0].x) + asf(sp[1].x)) + (asf(sp[2].x) + asf(sp[3].x));
          float s2 = (asf(sp[0].y) + asf(sp[1].y)) + (asf(sp[2].y) + asf(sp[3].y));
          s1 = sum32(s1); s2 = sum32(s2);
          mean = s1 * (1.f / D);
          rstd = s2 * (1.f / D) - mean * mean;
          PINV(mean); PINV(rstd);
        }
        if constexpr (ks == 19) { rstd = __builtin_amdgcn_rsqf(fmaxf(rstd, 0.f) + EPS); PINV(rstd); }
        if constexpr (ks >= 20 && ks < 24) { ck[ks - 20] *= -mean * rstd; cv[ks - 20] *= -mean * rstd; PINV(ck[ks - 20]); PINV(cv[ks - 20]); }
        SCHED();
      });
      MFMA_SETTLE2(ak, av);
#pragma unroll
      for (int i = 0; i < 16; ++i) ak[i] = ak[i] * rstd + ck[i >> 2][i & 3];
      KVOUT(ak, 0)
#pragma unroll
      for (int i = 0; i < 16; ++i) av[i] = av[i] * rstd + cv[i >> 2][i & 3];
      KVOUT(av, 1)
    }
  }
  asm volatile(".Lfk_code_to:");
  if (blockIdx.x == 0 && threadIdx.x == 0) __hip_atomic_store(ctrl, epoch, RLX_AGENT);
}

extern "C" void kernel_launch(void* const* d_in, const int* in_sizes, int n_in, void* d_out, int out_size, void* d_ws,
                              size_t ws_size, hipStream_t stream) {
  Params p;
  p.x = d_in[0]; p.wqkv = d_in[1]; p.wo = d_in[2];
  p.w1 = d_in[4]; p.b1 = d_in[5]; p.w2 = d_in[6]; p.b2 = d_in[7];
  p.out = d_out; p.ws = d_ws;
  (void)hipFuncSetAttribute((const void*)fused_kernel, hipFuncAttributeMaxDynamicSharedMemorySize, LDS_BYTES);
  fused_kernel<<<256, 256, LDS_BYTES, stream>>>(p);
}
```

```cpp
#include <hip/hip_runtime.h>
#include <utility>

#define GAS __attribute__((address_space(1)))
#define LAS __attribute__((address_space(3)))
#define DI __device__ __forceinline__
#define INL __attribute__((always_inline))
#define RLX_AGENT __ATOMIC_RELAXED, __HIP_MEMORY_SCOPE_AGENT
typedef unsigned v4u __attribute__((ext_vector_type(4)));
typedef unsigned v2u __attribute__((ext_vector_type(2)));
typedef float f32x2 __attribute__((ext_vector_type(2)));
typedef float f32x4 __attribute__((ext_vector_type(4)));
typedef float f32x16 __attribute__((ext_vector_type(16)));
typedef short s16x4 __attribute__((ext_vector_type(4)));
typedef __bf16 bf16x2 __attribute__((ext_vector_type(2)));
typedef __bf16 bf16x8 __attribute__((ext_vector_type(8)));
typedef __amdgpu_buffer_rsrc_t rsrc_t;

constexpr int S = 2048, D = 448, F = 1792;
constexpr float EPS = 1e-5f;
constexpr int FLAG_OFF = 4096;
constexpr int KV_OFF = 512 * 1024, KV_SZ = 16 * 64 * 4096;
constexpr int O_OFF = KV_OFF + 2 * KV_SZ, O_SZ = 32 * 16 * 4096;
constexpr int S3_OFF = O_OFF + 2 * O_SZ, S3_SZ = 32 * 8 * 1024;
constexpr int HT_OFF = S3_OFF + 2 * S3_SZ, HT_SZ = 7 * 64 * 4096;
constexpr int U_OFF = HT_OFF + 2 * HT_SZ, U_SZ = 7 * 56 * 4096;
constexpr int X2_OFF = U_OFF + 2 * U_SZ, X2_SZ = 64 * 28 * 1024;
constexpr int S5_OFF = X2_OFF + X2_SZ, S5_SZ = 32 * 8 * 512;
constexpr int DBG_OFF = S5_OFF + S5_SZ;
constexpr int WS_BYTES = DBG_OFF + 256 * 128 * 8;
constexpr int L_WQ = 0;
constexpr int L_KVW = 86016;
constexpr int L_STG = L_KVW + 49152;
constexpr int L_CSP = L_STG + 16384;
constexpr int L_CS = L_CSP + 1536 + 128;
constexpr int L_HST = L_CS + 384;
constexpr int L_DUMP = L_HST + 8192;
constexpr int LDS_BYTES = L_DUMP + 256;

static_assert(L_HST % 128 == 0 && LDS_BYTES <= 163840, "LDS map");
constexpr int SC1 = 16, OOB = 0x7ffff000;
#define VT_LAYOUT 1
#define WIN4 12
#define WIN5 12
#define WINX 12
#define QPUB 10
#define NO1 2
#define NO2 3
#define NO3 2
#define NG2 3
#define NG3 1
#define NH1 2
#define NH2 3
#define NH3 2
#define NU0 3
#define NU1 2
#define NU3 2
#define PACE 1
#define FTCH 1
#define XTCH 0
#define SUMDOT 0
#define OAUX 16
#define OFULL 1
#define MAP2D 0
#define WSLEEP 0
#define ISLEEP 80
#define QPOLL 24
#define QEARLY 1
#define KP 5
#define MO1 2
#define MO2 3
#define MO3 0
#define MG2 2
#define MG3 0
#define MH1 2
#define NS 5
#define SO 0
#define SG 1
#define SH 2
#define TCH0 1
#define TCH1 1
#define TCH2 1
constexpr int Z_G = NO1 + NO2 + NO3, Z_H = Z_G + NG2 + NG3, Z_U = Z_H + NH1 + NH2 + NH3;
static_assert(QPUB / 2 + QPOLL - QPUB >= 16 && NO1 <= 3 && NU1 <= 3 && NO2 >= 2 && NG2 >= 2 && NH2 >= 2 && Z_U >= 16 && Z_U + NU0 + NU1 + 2 + NU3 <= 30 && NU0 >= 3 && NU0 <= 6 && NO1 <= 4 && NU1 <= 4, "schedule");

struct Params {
  const void *x, *wqkv, *wo, *w1, *b1, *w2, *b2;
  void *out, *ws;
};

template <int... I, class Fn> DI void sfor_(std::integer_sequence<int, I...>, Fn&& f) { (f(std::integral_constant<int, I>{}), ...); }
template <int N, class Fn> DI void sfor(Fn&& f) { sfor_(std::make_integer_sequence<int, N>{}, f); }
DI float asf(unsigned u) { return __builtin_bit_cast(float, u); }
DI unsigned asu(float f) { return __builtin_bit_cast(unsigned, f); }
DI bf16x2 asb2(unsigned u) { return __builtin_bit_cast(bf16x2, u); }
DI rsrc_t mk(const void* p, int bytes) { return __builtin_amdgcn_make_buffer_rsrc((void*)p, 0, bytes, 0x00020000); }
template <int AUX> DI v4u ld16(rsrc_t r, int voff, int soff) { return __builtin_amdgcn_raw_buffer_load_b128(r, voff, soff, AUX); }
template <int AUX> DI v2u ld8(rsrc_t r, int voff, int soff) { return __builtin_amdgcn_raw_buffer_load_b64(r, voff, soff, AUX); }
template <int AUX> DI void st16(v4u v, rsrc_t r, int voff, int soff) { __builtin_amdgcn_raw_buffer_store_b128(v, r, voff, soff, AUX); }
template <int AUX> DI void st8(v2u v, rsrc_t r, int voff, int soff) { __builtin_amdgcn_raw_buffer_store_b64(v, r, voff, soff, AUX); }
DI f32x4 mfma16(v4u a, v4u b, f32x4 c) {
  return __builtin_amdgcn_mfma_f32_16x16x32_bf16(__builtin_bit_cast(bf16x8, a), __builtin_bit_cast(bf16x8, b), c, 0, 0, 0);
}
DI f32x16 mfma32(v4u a, v4u b, f32x16 c) {
  return __builtin_amdgcn_mfma_f32_32x32x16_bf16(__builtin_bit_cast(bf16x8, a), __builtin_bit_cast(bf16x8, b), c, 0, 0, 0);
}
DI unsigned pk(float lo, float hi) { return __builtin_bit_cast(unsigned, __builtin_convertvector((f32x2){lo, hi}, bf16x2)); }
DI float bflo(unsigned u) { return asf(u << 16); }
DI float bfhi(unsigned u) { return asf(u & 0xffff0000u); }
DI f32x4 unpack4(v2u v) { return (f32x4){bflo(v.x), bfhi(v.x), bflo(v.y), bfhi(v.y)}; }
DI v2u trrd(LAS char* p) { return __builtin_bit_cast(v2u, __builtin_amdgcn_ds_read_tr16_b64_v4i16((LAS s16x4*)p)); }
DI v4u trfrag(LAS char* p0, LAS char* p1) { v2u a = trrd(p0), b = trrd(p1); return (v4u){a.x, a.y, b.x, b.y}; }
DI float hi32(float v) {
  unsigned u = asu(v);
  auto r = __builtin_amdgcn_permlane32_swap(u, u, false, false);
  return asf(r[1]);
}
DI float sum32(float v) {
  unsigned u = asu(v);
  auto r = __builtin_amdgcn_permlane32_swap(u, u, false, false);
  return asf(r[0]) + asf(r[1]);
}
DI float max32(float v) {
  unsigned u = asu(v);
  auto r = __builtin_amdgcn_permlane32_swap(u, u, false, false);
  return fmaxf(asf(r[0]), asf(r[1]));
}
DI float sum16(float v) {
  unsigned u = asu(v);
  auto r = __builtin_amdgcn_permlane16_swap(u, u, false, false);
  return asf(r[0]) + asf(r[1]);
}
DI float sum_ab(float a, float b) {
  auto r = __builtin_amdgcn_permlane16_swap(asu(a), asu(b), false, false);
  return sum32(asf(r[0]) + asf(r[1]));
}
DI void stats16(const f32x4 (&v)[4], int n, f32x2& s1, f32x2& s2) {
#pragma unroll
  for (int a = 0; a < 4; ++a)
    if (n >> a & 1) {
      const f32x2 lo = {v[a][0], v[a][1]}, hi = {v[a][2], v[a][3]};
      s1 += lo; s2 += lo * lo; s1 += hi; s2 += hi * hi;
    }
}
#define FENCE() asm volatile("" ::: "memory")
#define SCHED() __builtin_amdgcn_sched_barrier(0)
#define DRAIN() asm volatile("s_waitcnt vmcnt(0)" ::: "memory")
#define PINV(x) asm volatile("" : "+v"(x))
#define PINA(x) asm volatile("" : "+a"(x))
#define PINS(x) asm volatile("" : "+s"(x))
#define KEEP(x) asm volatile("" :: "v"(x))
#define LGKM0() asm volatile("s_waitcnt lgkmcnt(0)" ::: "memory")
DI void bar() { LGKM0(); __builtin_amdgcn_s_barrier(); FENCE(); }
template <int N> DI void vmw() { asm volatile("s_waitcnt vmcnt(%0)" :: "n"(N) : "memory"); }
template <int STEP> DI void dma1(rsrc_t r, int voff, int& so, int& lds) {
  asm volatile("s_mov_b32 m0, %1\n\ts_add_u32 %1, %1, 0x400\n\tbuffer_load_dwordx4 %2, %3, %0 offen lds\n\ts_add_u32 %0, %0, %4"
               : "+s"(so), "+s"(lds) : "v"(voff), "s"(r), "n"(STEP) : "memory", "scc");
}
template <int LO, int MO> DI void dmai(rsrc_t r, int voff, int lds) {
  int so;
  asm volatile("s_add_u32 m0, %1, %4\n\ts_mov_b32 %0, %5\n\tbuffer_load_dwordx4 %2, %3, %0 offen lds"
               : "=&s"(so) : "s"(lds), "v"(voff), "s"(r), "n"(LO), "n"(MO) : "memory", "scc");
}
DI void touch(rsrc_t r, int voff, int soff, LAS char* L) {
  asm volatile("s_mov_b32 m0, %0\n\ts_nop 3\n\tbuffer_load_dword %1, %2, %3 offen lds"
               :: "s"((int)(size_t)(L + L_DUMP)), "v"(voff), "s"(r), "s"(soff) : "memory");
}
template <int STEP> DI void dma64(rsrc_t r, int v0, int v1, int soff, int lds) {
  unsigned so;
  asm volatile("s_mov_b32 m0, %1\n\ts_mov_b32 %0, %5\n\tbuffer_load_dwordx4 %2, %4, %0 offen lds\n\t"
               "s_add_u32 m0, m0, 0x400\n\ts_add_u32 %0, %0, %6\n\tbuffer_load_dwordx4 %3, %4, %0 offen lds\n\t"
               "s_add_u32 m0, m0, 0x400\n\ts_add_u32 %0, %0, %6\n\tbuffer_load_dwordx4 %2, %4, %0 offen lds\n\t"
               "s_add_u32 m0, m0, 0x400\n\ts_add_u32 %0, %0, %6\n\tbuffer_load_dwordx4 %3, %4, %0 offen lds"
               : "=&s"(so) : "s"(lds), "v"(v0), "v"(v1), "s"(r), "s"(soff), "n"(STEP) : "memory", "scc");
}

#define STAMP(i) do {} while (0)

DI void publish(GAS unsigned* flag, unsigned epoch) {
  DRAIN();
  __builtin_amdgcn_s_waitcnt(0x0F70);
  bar();
  if (threadIdx.x == 0) __hip_atomic_store(flag, epoch, RLX_AGENT);
  FENCE();
}
template <int N> DI void publish_n(GAS unsigned* flag, unsigned epoch) {
  vmw<N>();
  __builtin_amdgcn_s_barrier();
  FENCE();
  if (threadIdx.x == 0) __hip_atomic_store(flag, epoch, RLX_AGENT);
  FENCE();
}
#define POLL(f, ptr) asm volatile("global_load_dword %0, %1, off sc1" : "=&v"(f) : "v"(ptr) : "memory")
#define POLLED(f, N, ptr) { \
    unsigned f2_; \
    POLL(f2_, ptr); \
    asm volatile("s_waitcnt vmcnt(%1)" : "+v"(f) : "n"((N) + 1) : "memory"); \
    if (__builtin_amdgcn_ballot_w64(f != epoch) != 0ull) { \
      asm volatile("s_waitcnt vmcnt(0)" : "+v"(f2_) :: "memory"); \
      if (__builtin_amdgcn_ballot_w64(f2_ != epoch) != 0ull) wait_flags(ptr, epoch, budget); \
    } \
    FENCE(); \
    late = f2_; }
DI void wait_flags(GAS unsigned* flag, unsigned epoch, unsigned& budget) {
  FENCE();
  while (true) {
    unsigned f = __hip_atomic_load(flag, RLX_AGENT);
    if (__builtin_amdgcn_ballot_w64(f != epoch) == 0ull) break;
    if (budget == 0u) break;
    --budget;
    if (WSLEEP) __builtin_amdgcn_s_sleep(WSLEEP);
  }
  __builtin_amdgcn_fence(__ATOMIC_ACQUIRE, "wavefront");
  FENCE();
}

DI void cv_wr(const v4u& r, int m, LAS char* stg, int wa0, int wa1) { *(LAS v4u*)(stg + ((m & 1) ? wa1 : wa0) + m * 1024) = r; }
DI v4u cv_rd(int x, LAS char* stg, int ra0, int ra1) { return trfrag(stg + (ra0 ^ (x << 5)), stg + (ra1 ^ (x << 5))); }
DI int wrap7(int v) { return v >= 7 ? v - 7 : v; }
DI int wrap14(int v) { return v >= 14 ? v - 14 : v; }
DI int swz(int row) { return ((row >> 1) & 1) | (((row >> 3) ^ (row >> 2)) & 1) << 1; }

#define FRESH() \
  int tid = threadIdx.x, bid = blockIdx.x; \
  asm volatile("" : "+v"(tid)); \
  asm volatile("" : "+s"(bid)); \
  const int lane = tid & 63, w = __builtin_amdgcn_readfirstlane(tid >> 6), bx = bid & 7, by = bid >> 3; \
  const int c = lane & 15, g = lane >> 4, q4 = (lane & 15) >> 2, p4 = lane & 3, r32 = lane & 31, hh = lane >> 5; \
  const int by7 = __builtin_amdgcn_readfirstlane((by * 37) >> 8), bym = __builtin_amdgcn_readfirstlane(by - 7 * by7); \
  const int t = 2 * bx + (by >> 4), hd = by & 15;                \
  const int st = 4 * bx + by7, dt = bym;                         \
  const int dt4 = MAP2D ? (bx & 1) * 4 + by7 : by < 21 ? bym : bym + 4 * (bx & 1), ft = 7 * (bx >> 1) + (MAP2D ? bym : by < 21 ? by7 + 4 * (bx & 1) : 3);    \
  const int rotx = hd >= 14 ? hd - 14 : wrap7(hd), rot4 = ROT4, rot5 = ROT5; \
  const int w4 = (w + G4 * 2 * ((MAP2D ? bym : by7) & 1)) & 3, pa = G5 * 2 * (by7 & 1);    \
  LAS char* stga = L + L_STG + w * 4096; \
  LAS char* ring = L + L_KVW + w * 16384; \
  LAS char* stgb = ring + 12288; \
  const int wrow = lane >> 3; \
  const int sx0 = swz(wrow) << 5, sx1 = swz(wrow + 8) << 5; \
  const int wa0 = (wrow * 128 + (lane & 7) * 16) ^ sx0, wa1 = (wrow * 128 + (lane & 7) * 16) ^ sx1; \
  auto raddr = [&](int row) { return row * 128 + (swz(row) << 5) + 8 * p4; }; \
  const int w1o = (512 * w + wrow) * (F * 2) + ft * 128 + (lane & 7) * 16, w2o = (448 * w + wrow) * (S * 2) + st * 128 + (lane & 7) * 16; \
  const int r1a = raddr(8 * g + q4), r1b = raddr(8 * g + 4 + q4), r2a = raddr(4 * g + q4), r2b = raddr(16 + 4 * g + q4); \
  LAS char* red = L + L_KVW + lane * 16; \
  (void)c; (void)g; (void)q4; (void)r32; (void)hh; (void)t; (void)hd; (void)st; (void)dt; (void)dt4; (void)ft; (void)stga; (void)stgb; (void)ring; \
  (void)sx0; (void)sx1; (void)wa0; (void)wa1; (void)rotx; (void)rot4; (void)rot5; (void)w1o; (void)w2o; (void)r1a; (void)r1b; (void)r2a; (void)r2b; \
  (void)red; (void)w4; (void)pa;
#define G4 0
#define G5 0
#define ROT4 0
#define ROT5 0

#define MNOP ""
#define MFMA32V0(d, a, b) asm volatile(MNOP "v_mfma_f32_32x32x16_bf16 %0, %1, %2, 0" : "=&v"(d) : "v"(a), "v"(b))
#define MFMA32VC(d, a, b, c) asm volatile(MNOP "v_mfma_f32_32x32x16_bf16 %0, %1, %2, %3" : "=&v"(d) : "v"(a), "v"(b), "v"(c))
#define MFMA32VA(d, a, b) asm volatile(MNOP "v_mfma_f32_32x32x16_bf16 %0, %1, %2, %0" : "+v"(d) : "v"(a), "v"(b))
#define MFMA_SETTLE(d) asm volatile("s_nop 11" : "+v"(d))
#define MFMA_SETTLE2(d, e) asm volatile("s_nop 11" : "+v"(d), "+v"(e))
DI float max3(float a, float b, float c) { float r; asm("v_max3_f32 %0, %1, %2, %3" : "=v"(r) : "v"(a), "v"(b), "v"(c)); return r; }

#define WB(cc) (L + L_WQ + (4 * hh + q4) * 64 + (g & 1) * 32 + 8 * p4 + (hd & 1) * 8 + (cc) * 4096)
#define WFRAG(wb, part, e) trfrag((wb) + (part) * 28672 + ((e) >> 1) * 2048 + ((e) & 1) * 512, (wb) + (part) * 28672 + ((e) >> 1) * 2048 + ((e) & 1) * 512 + 1024)
#define T1O ((16 * (by * 4 + w) + (lane >> 2)) * (F * 2) + (7 * (bx >> 1) + 3 * (bx & 1) + (lane & 3)) * 128)
#define T1H ((512 * (by >> 3) + 8 * ((by & 7) * 4 + w) + (lane >> 2)) * (F * 2) + (7 * (bx >> 1) + 3 * (bx & 1) + (lane & 3)) * 128)
#define CSUM(part, a) (*(LAS f32x4*)(L + L_CS + ((part) * 32 + 8 * (a) + 4 * hh) * 4))
#define WZ_RD1(n, i, sl) wz[n][i] = (n) < 16 ? trfrag(pr1[i] + (sl) * 4096, pr1[(i) ^ 2] + (sl) * 4096 + 512) : trfrag(pr2[i] + (sl) * 4096, pr2[i] + (sl) * 4096 + 2048)
#define DMA_WZ1(n, m, sl) { \
    if ((n) < 16) dmai<(sl) * 4096 + (m) * 1024, (32 * ((n) & 15) + 8 * (m)) * F * 2>(rw1, ((m) & 1) ? pw1 ^ 64 : pw1, pring); \
    else dmai<(sl) * 4096 + (m) * 1024, (32 * ((n) & 15) + 8 * (m)) * S * 2>(rw2, ((m) & 1) ? pw2 ^ 64 : pw2, pring); }
#define PIECE(L0, q) { \
    constexpr int r_ = (q) / 4, m_ = (q) % 4, n_ = (L0) + r_; \
    if constexpr (m_ == 0 && r_ >= 4) asm volatile("s_waitcnt vmcnt(8) lgkmcnt(0)" ::: "memory"); \
    else if constexpr (m_ == 0 && r_ == 3) vmw<8>(); \
    DMA_WZ1(n_, m_, r_ & 3) \
    if constexpr (r_ >= 3) WZ_RD1(n_ - 3, m_, (r_ - 3) & 3); \
    SCHED(); }
#define CMIN(a, b) ((a) < (b) ? (a) : (b))
#define DPUMP(L0, A, B) sfor<4 * ((B) - (A))>([&](auto I_) INL { PIECE(L0, 4 * ((A) - (L0)) + I_) });
#define STASH(A, B) if constexpr (layer == 1) sfor<4 * ((B) - (A))>([&](auto I_) INL { \
    constexpr int j_ = (A) + I_ / 4, m_ = I_ % 4; \
    dmai<j_ * 4096 + m_ * 1024, (32 * (14 - NS + j_) + 8 * m_) * S * 2>(rw2, (m_ & 1) ? pw2 ^ 64 : pw2, pstash); SCHED(); });
#define DTSTEP(L0, L1, sl) ((L1) - 1 - (((L1) - 1 - (L0) - (sl)) & 3))
#define DTAIL1(L0, L1, j) { if constexpr (DTSTEP(L0, L1, (j) >> 2) >= (L0) && DTSTEP(L0, L1, (j) >> 2) >= (L1) - 3) WZ_RD1(DTSTEP(L0, L1, (j) >> 2), (j) & 3, (j) >> 2); }
#define DTAIL(L0, L1) sfor<16>([&](auto J_) INL { constexpr int j_ = J_; DTAIL1(L0, L1, j_) }); SCHED();
#define REDW(a, b) *(LAS f32x4*)(red + w * 16384 + (((b) - 1) * 4 + (a)) * 1024) = acc[a][b]
#define REDR(a, b) (*(LAS f32x4*)(red + ((w + 4 - (b)) & 3) * 16384 + (((b) - 1) * 4 + (a)) * 1024))
#define COLSUM(part) { \
    LAS char* cb = L + L_WQ + (part) * 28672 + (w * 16 + 8 * hh + q4) * 64 + (g & 1) * 32 + 8 * p4; \
    v4u wc[7]; \
    _Pragma("unroll") for (int i = 0; i < 7; ++i) wc[i] = trfrag(cb + i * 4096, cb + i * 4096 + 256); \
    f32x16 cs = {}; \
    _Pragma("unroll") for (int i = 0; i < 7; ++i) cs = mfma32((v4u){0x3f803f80u, 0x3f803f80u, 0x3f803f80u, 0x3f803f80u}, wc[i], cs); \
    if (lane < 32) *(LAS float*)(L + L_CSP + (w * 96 + (part) * 32 + lane) * 4) = cs[0]; }
#define COLRED(i) { \
    LAS float* cp = (LAS float*)(L + L_CSP) + (((i) & 31) + 4 * (hd & 1) < 32 ? (i) + 4 * (hd & 1) : (i)); \
    *(LAS float*)(L + L_CS + (i) * 4) = (cp[0] + cp[96]) + (cp[192] + cp[288]); }

__global__ void __launch_bounds__(256, 1) fused_kernel(Params p) {
  extern __shared__ __attribute__((aligned(16))) char smem[];
  LAS char* L = (LAS char*)smem;
  {
    unsigned long long pc;
    int len;
    asm volatile("s_getpc_b64 %0\n.Lfk_code_from:\n\ts_mov_b32 %1, .Lfk_code_to-.Lfk_code_from" : "=s"(pc), "=s"(len));
    touch(mk((const void*)pc, len > 0 ? len : 0), (threadIdx.x & 63) < 8 ? ((blockIdx.x >> 3) * 4 + (threadIdx.x >> 6) + (threadIdx.x & 7) * 128) * 128 : OOB, 0, L);
  }
  SCHED();
  const bool on35 = (blockIdx.x >> 3) < 28;
  const bool on4 = (blockIdx.x >> 3) < (MAP2D ? ((blockIdx.x & 1) ? 21 : 28) : (blockIdx.x & 1) ? 24 : 25);
  const rsrc_t rws = mk(p.ws, WS_BYTES);
  GAS unsigned* ctrl = (GAS unsigned*)p.ws;
  GAS unsigned* flags = (GAS unsigned*)((GAS char*)p.ws + FLAG_OFF);
  STAMP(0);
  unsigned budget = 1u << 22;

  v4u xf[28];
  v4u kv[4];
  float mean = 0.f, rstd = 0.f;
  f32x4 res[4];
  unsigned epoch;
  int kvs, oo, t1o, wvo2, pw1, pw2, fo;
  unsigned nk1, nk2, pm[8];
  LAS char *kvw, *vw, *vr0, *wor[4], *pr1[4], *pr2[4], *wbq;
#define SETUP(LAYER) { \
    kvs = KV_OFF + (LAYER) * KV_SZ + (hd * 64 + 4 * t + w) * 4096 + lane * 16; \
    oo = O_OFF + (LAYER) * O_SZ + ((2 * t + (w >> 1)) * 16 + hd) * 4096 + (2 * (w & 1) + (r32 >> 4)) * 1024 + hh * 256 + c * 16; \
    t1o = MAP2D ? (lane < 56 ? (512 * (by >> 3) + 8 * ((by & 7) * 4 + w) + ((lane * 37) >> 8)) * (F * 2) + (7 * (bx >> 1) + lane - 7 * ((lane * 37) >> 8)) * 128 : OOB) : lane < 32 ? T1H : OOB;    \
    wvo2 = ((112 * w + wrow) * 896 + dt * 128 + (lane & 7) * 16) ^ sx0; \
    pw1 = w1o ^ sx0; pw2 = w2o ^ sx0; \
    const int tp = (t >= 2 && (lane & 1)) ? t - 2 : t > 0 ? t - 1 : 0; \
    fo = FLAG_OFF + ((LAYER) * 5 * 256 + ((tp & 1) * 16 + hd) * 8 + (tp >> 1)) * 128; \
    nk2 = (t < 2 && hh) ? 0xf149u : 0u; nk1 = (t < 1 && hh) ? 0xf149u : 0u;    \
    _Pragma("unroll") for (int i = 0; i < 8; ++i) { \
      const int d_ = r32 - ((2 * i & 3) + 8 * (i >> 1)) - 4 * hh; \
      pm[i] = ((unsigned)(d_ >> 31) & 0xffffu) | ((unsigned)((d_ - 1) >> 31) & 0xffff0000u); PINA(pm[i]); } \
    kvw = L + L_KVW + w * 4096 + lane * 16; \
    vw = L + L_KVW + w * 4096 + 2048 + ((lane * 16) ^ (hh << 6)); \
    vr0 = L + L_KVW + w * 4096 + 2048 + ((((g & 1) * 1024 + (p4 & 1) * 512 + hh * 64 + q4 * 16) ^ ((p4 & 1) << 6) ^ ((g & 1) << 7)) + (p4 >> 1) * 8); \
    wbq = WB(0); \
    _Pragma("unroll") for (int i = 0; i < 4; ++i) { \
      wor[i] = ring + (raddr(16 * (g >> 1) + 4 * (g & 1) + q4) ^ ((i ^ pa) << 5)); \
      pr1[i] = ring + (r1a ^ (i << 5)); pr2[i] = ring + (r2a ^ (((i + w) & 3) << 5)); \
      PINA(wor[i]); PINA(pr1[i]); PINA(pr2[i]); } \
    PINV(kvs); PINA(oo); PINA(t1o); PINA(wvo2); PINA(pw1); PINA(pw2); PINA(fo); PINA(nk1); PINA(nk2); PINA(kvw); PINA(vw); PINA(vr0); PINV(wbq); }
#define KVOUT(acc, h) { \
    _Pragma("unroll") for (int i = 0; i < 16; i += 2) kv[2 * (h) + (i >> 3)][(i >> 1) & 3] = pk(acc[i], acc[i + 1]); \
    if (hh) { kv[2 * (h) + 1][2] = (h) ? 0x00003f80u : 0u; kv[2 * (h) + 1][3] = 0u; } \
    const rsrc_t rkv = mk(p.ws, t < 15 ? WS_BYTES : 0);    \
    st16<SC1>(kv[2 * (h)], rkv, kvs + (h) * 2048, 0); \
    st16<SC1>(kv[2 * (h) + 1], rkv, kvs + (h) * 2048 + 1024, 0); \
    SCHED(); }
#define X128(p) ((LAS char*)(size_t)((unsigned)(size_t)(p) ^ 128u))
  {
    FRESH();
    const rsrc_t rwq = mk(p.wqkv, D * 3 * D * 2);
    const rsrc_t rx = mk(p.x, S * D * 2);
    const int gid = by * 4 + w, rg = (int)(size_t)ring;
    touch(rx, lane < 14 ? (t * 896 + hd * 56 + w * 14 + lane) * 128 : OOB, 0, L);
    const int xvo = (t * 128 + w * 32 + wrow) * 896 + (((lane & 7) ^ (wrow >> 1)) << 4);
#define DMA_X(cc) sfor<4>([&](auto M) INL { constexpr int m = M; dmai<((cc) & 3) * 4096 + m * 1024, m * 8 * 896 + (cc) * 128>(rx, (m & 1) ? xvo ^ 64 : xvo, rg); })
    DMA_X(0); DMA_X(1);
    touch(rwq, lane < 28 ? (2 * gid) * 2688 + 896 + lane * 128 + (lane >= 14 ? 896 : 0) : OOB, 0, L);
    DMA_X(2);
    SCHED();
    SETUP(0)
    const int tb = gid * 21 + lane, tbr = (tb * 4682) >> 16;
    int tvb = lane < 21 ? (256 + tbr) * 2688 + 896 + (tb - 14 * tbr) * 128 : OOB;
    const int tl = by * 98 + w * 25 + lane, trow = (tl * 9363) >> 16;
    int tvq = (lane < 25 && w * 25 + lane < 98) ? trow * 2688 + (tl - 7 * trow) * 128 : OOB, tvw = (lane < 25 && w * 25 + lane < 98) ? tl * 128 : OOB;
    int wvo = (w * 16 + (lane >> 2)) * 2688 + hd * 56 - (hd & 1) * 8 + (lane & 3) * 16;
    const int wlb = (int)(size_t)(L + L_WQ) + w * 1024;
#define DMA_W(part, cc) dmai<(part) * 28672 + (cc) * 4096, (cc) * 64 * 2688 + (part) * 896>(rwq, wvo, wlb)
    LAS char* xa[8];
#pragma unroll
    for (int e = 0; e < 8; ++e) { xa[e] = ring + r32 * 128 + 8 * hh + ((e ^ ((r32 >> 1) & 7)) << 4); PINV(xa[e]); }
    LAS char* cb = L + L_WQ + (w * 16 + 8 * hh + q4) * 64 + (g & 1) * 32 + 8 * p4;
    cb += 28672;
    LAS char* wb = WB(0) + 28672;
    PINV(tvb); PINV(tvq); PINV(tvw); PINV(wvo); PINV(cb); PINV(wb);
    SCHED();
    DMA_X(3);
    asm volatile("buffer_load_dword %0, %1, %2, 0 offen sc1" : "=&v"(epoch) : "v"(tid >= 1 && tid <= 10 ? FLAG_OFF + ((tid - 1) * 256 + bid) * 128 : on35 && (tid >> 4) == 1 ? S3_OFF + ((tid >> 3) & 1) * S3_SZ + (st * 8 + dt) * 1024 + (tid & 7) * 128 : 0), "s"(rws) : "memory");
    SCHED();
#define READ_X(cc) _Pragma("unroll") for (int e = 0; e < 4; ++e) { \
      const int ch = (e >> 1) * 4 + (e & 1); \
      const v2u a_ = *(LAS v2u*)(xa[ch] + ((cc) & 3) * 4096), b_ = *(LAS v2u*)(xa[ch + 2] + ((cc) & 3) * 4096); \
      xf[4 * (cc) + e] = (v4u){a_.x, a_.y, b_.x, b_.y}; }
    const v4u ones = {0x3f803f80u, 0x3f803f80u, 0x3f803f80u, 0x3f803f80u};
    f32x16 gs, ss = {};
    float s1[2] = {};
#define XSTAT(i) { if constexpr ((i) == 0) MFMA32V0(gs, xf[i], xf[i]); else MFMA32VA(gs, xf[i], xf[i]); ss = mfma32(ones, xf[i], ss); }
#define XSUM ss[0]
    STAMP(32);
    vmw<14>(); READ_X(0) LGKM0(); DMA_X(4); XSTAT(0) XSTAT(1) XSTAT(2) XSTAT(3) SCHED();
    vmw<14>(); READ_X(1) LGKM0(); DMA_X(5); touch(rwq, tvb, 0, L); XSTAT(4) XSTAT(5) XSTAT(6) XSTAT(7) SCHED();
    vmw<14>(); READ_X(2) LGKM0(); DMA_X(6); SCHED();
    asm volatile("s_waitcnt vmcnt(13)" : "+v"(epoch) :: "memory");
    epoch = __builtin_amdgcn_readfirstlane(epoch) + 1u;
    READ_X(3)
    SCHED();
    sfor<20>([&](auto I) INL {
      constexpr int i = I;
      if constexpr (i == 6) { vmw<4 + 1 + 4 + 3>(); READ_X(4) }
      if constexpr (i == 10) { vmw<1 + 4 + 5>(); READ_X(5) }
      if constexpr (i == 14) { vmw<7>(); READ_X(6) }
      if constexpr ((i & 1) == 0 && i < 16) DMA_W(1 + ((i >> 1) & 1), i >> 2);
      XSTAT(8 + i)
      SCHED();
    });
    STAMP(33);
    f32x16 ak, av;
    v4u wk[28], wv[28], wc[14];
    f32x16 csk = {}, csv = {};
    float dg = 0.f, d8[8], d4[4], pp[4];
    f32x4 cc[8];
    LAS float* pb = (LAS float*)(L + L_CSP) + 32 + 16 * w + c + 4 * (hd & 1);
    LAS float* pw = (LAS float*)(L + (hh ? L_DUMP : L_CSP + (w * 96 + 32) * 4)) + r32;
#define PICK4(o, at, a0, a1, a2, a3, b0, b1, b2, b3, m) \
    asm volatile("v_cndmask_b32 %0, %4, %8, %12\n\tv_cndmask_b32 %1, %5, %9, %12\n\tv_cndmask_b32 %2, %6, %10, %12\n\tv_cndmask_b32 %3, %7, %11, %12" \
                 : "=&v"(o[at]), "=&v"(o[(at) + 1]), "=&v"(o[(at) + 2]), "=&v"(o[(at) + 3]) \
                 : "v"(a0), "v"(a1), "v"(a2), "v"(a3), "v"(b0), "v"(b1), "v"(b2), "v"(b3), "s"(m))
    sfor<8>([&](auto R) INL {
      constexpr int r = R;
      if constexpr (r < 7) {
        vmw<(r <= 3 ? 6 : 6)>();
        if constexpr (r == 6) {
          wc[12] = trfrag(cb + r * 4096, cb + r * 4096 + 256);
          wc[13] = trfrag(cb + 28672 + r * 4096, cb + 28672 + r * 4096 + 256);
          csk = mfma32(ones, wc[12], csk); csv = mfma32(ones, wc[13], csv);
          pw[0] = csk[0]; pw[32] = csv[0];
          LGKM0();
        }
        __builtin_amdgcn_s_barrier();
        FENCE();
        SCHED();
      }
      sfor<4>([&](auto E) INL {
        constexpr int e = E, kr = 4 * r + e, km = kr - 4;
        if constexpr (r > 0) { if constexpr (km == 0) MFMA32V0(ak, wk[km], xf[km]); else MFMA32VA(ak, wk[km], xf[km]); }
        if constexpr (r < 7) wk[kr] = WFRAG(wb + r * 4096, 0, e);
        if constexpr (r < 6 && (e & 1) == 0) wc[2 * r + e / 2] = trfrag(cb + (e / 2) * 28672 + r * 4096, cb + (e / 2) * 28672 + r * 4096 + 256);
        SCHED();
        if constexpr (r > 0) { if constexpr (km == 0) MFMA32V0(av, wv[km], xf[km]); else MFMA32VA(av, wv[km], xf[km]); }
        if constexpr (r < 7) wv[kr] = WFRAG(wb + r * 4096, 1, e);
        SCHED();
        if constexpr (r < 6 && e == 1) csk = mfma32(ones, wc[2 * r], csk);
        if constexpr (r < 6 && e == 3) csv = mfma32(ones, wc[2 * r + 1], csv);
        SCHED();
        if constexpr (e == 1 || e == 3) {
          if constexpr (r < 3) DMA_W(1 + e / 2, r + 4);
          else if constexpr (r == 3) { if constexpr (e == 1) touch(rwq, tvq, 0, L); else touch(mk(p.wo, D * D * 2), tvw, 0, L); }
          else if constexpr (2 * (r - 4) + e / 2 < 7) DMA_W(0, 2 * (r - 4) + e / 2);
        }
        if constexpr (r == 1 && (e & 1) == 0) { constexpr int j = 2 * e; PICK4(d8, j, gs[j], gs[j + 1], gs[j + 2], gs[j + 3], gs[j + 8], gs[j + 9], gs[j + 10], gs[j + 11], 0xffff0000ffff0000ull); }
        if constexpr (r == 2 && e == 0) PICK4(d4, 0, d8[0], d8[1], d8[2], d8[3], d8[4], d8[5], d8[6], d8[7], 0xff00ff00ff00ff00ull);
        if constexpr (r == 2 && e == 2)
          asm volatile("v_cndmask_b32 %1, %1, %3, %5\n\tv_cndmask_b32 %2, %2, %4, %5\n\tv_cndmask_b32 %0, %1, %2, %6\n\tv_cndmask_b32 %0, 0, %0, %7"
                       : "=&v"(dg), "+v"(d4[0]), "+v"(d4[1]) : "v"(d4[2]), "v"(d4[3]), "s"(0xccccccccccccccccull), "s"(0xaaaaaaaaaaaaaaaaull), "s"(0xf0f0f0f00f0f0f0full));
        if constexpr (r == 3 && e == 0) { mean = XSUM * (1.f / D); rstd = sum32(dg) * (1.f / D) - mean * mean; PINV(mean); PINV(rstd); }
        if constexpr (r == 3 && e == 2) { rstd = __builtin_amdgcn_rsqf(fmaxf(rstd, 0.f) + EPS); PINV(rstd); }
        if constexpr (r == 4 && e == 0) { PINV(pb); PINV(pw); }
        if constexpr (r == 6 && e == 0) { pp[0] = pb[0]; pp[1] = pb[96]; pp[2] = pb[192]; pp[3] = pb[288]; }
        if constexpr (r == 6 && e == 2) *(LAS float*)(L + L_CS + (32 + 16 * w + c) * 4) = (pp[0] + pp[1]) + (pp[2] + pp[3]);
        if constexpr (r == 6 && e == 3) bar();
        if constexpr (r == 7 && e < 2) { _Pragma("unroll") for (int j = 0; j < 4; ++j) cc[4 * e + j] = CSUM(1, 4 * e + j); }
        SCHED();
      });
    });
    STAMP(34);
    MFMA_SETTLE2(ak, av);
#pragma unroll
    for (int i = 0; i < 16; ++i) ak[i] = (ak[i] - mean * cc[i >> 2][i & 3]) * rstd;
    KVOUT(ak, 0)
#pragma unroll
    for (int i = 0; i < 16; ++i) av[i] = (av[i] - mean * cc[4 + (i >> 2)][i & 3]) * rstd;
    KVOUT(av, 1)
#pragma unroll
    for (int a = 0; a < 4; ++a) res[a] = (f32x4){0.f, 0.f, 0.f, 0.f};
  }

  v4u wz[30][4];
  sfor<2>([&](auto LY) INL {
    constexpr int layer = LY;
    constexpr int Y0 = layer ? KP : 0, YE = layer ? 16 : Z_U;
    constexpr int Y1 = Y0 + (layer ? MO1 : NO1), Y2 = Y1 + (layer ? MO2 : NO2), YG = Y2 + (layer ? MO3 : NO3), YG2 = YG + (layer ? MG2 : NG2), YH = YG2 + (layer ? MG3 : NG3),
                  YH1 = YH + (layer ? MH1 : NH1), YH2 = layer ? YE : YH1 + NH2;
    static_assert(YH1 <= YH2 && YH2 <= YE && NS <= 5 && SO + SG + SH <= NS && Z_U + NU0 + NU1 <= 30 - NS, "schedule");
    GAS unsigned* fl = flags + layer * 5 * 256 * 32;
    STAMP(1 + layer * 16);
    v4u qf[2];
    v4u wo[4][4];
    v2u rr[4];
    unsigned b2r;
    v2u bb[4];
    int pring, pstash;
    constexpr int YZ = layer ? 30 - NS : 30, YU3 = layer ? YZ : 30 - NU3;
    const rsrc_t rw1 = mk(p.w1, on4 ? S * F * 2 : 0);
    const rsrc_t rw2 = mk(p.w2, on35 ? S * F * 2 : 0);
    {
      const bool hi = threadIdx.x & 32;
      FRESH();
      if (layer == 0) {
        vmw<4>();
        if (t == 15) vmw<0>();
        COLSUM(0)
        publish(fl + (0 * 256 + bid) * 32, epoch);
        if (tid < 32) COLRED(tid)
      }
      v4u wq[28];
#pragma unroll
      for (int ks = 0; ks < 4; ++ks) wq[ks] = WFRAG(wbq, 0, ks);
      f32x16 aq;
      STAMP(2 + layer * 43);
      const rsrc_t rwo = mk(p.wo, on35 ? D * D * 2 : 0);
      pring = (int)(size_t)ring;
      pstash = (int)(size_t)(L + L_WQ) + w * 20480;
      PINS(pring); PINS(pstash);
      GAS unsigned* fp = (GAS unsigned*)((GAS char*)p.ws + fo);
      unsigned pf;
      sfor<28>([&](auto I) INL {
        constexpr int ks = I;
        if constexpr (ks == 0) MFMA32V0(aq, wq[ks], xf[ks]); else MFMA32VA(aq, wq[ks], xf[ks]);
        if constexpr (ks + 4 < 28) wq[ks + 4] = WFRAG(wbq + ((ks + 4) >> 2) * 4096, 0, ks & 3);
        constexpr int wi = ks < QPUB ? ((ks & 1) ? ks / 2 : -1) : ks == QPUB ? -1 : QPUB / 2 + ks - QPUB - 1;
        if constexpr (wi >= 0 && wi < 16) dmai<wi * 1024, (28 * (wi >> 2) + 8 * (wi & 3)) * 896>(rwo, (wi & 1) ? wvo2 ^ 64 : wvo2, pring);
        if constexpr (ks == QPOLL) POLL(pf, fp);
        if constexpr (ks == QPUB) {
          if (layer != 0) publish_n<QPUB / 2>(fl + (0 * 256 + bid) * 32, epoch);
          else bar();
          STAMP(3 + layer * 16);
        }
        SCHED();
      });
      f32x4 cq[4];
#pragma unroll
      for (int a = 0; a < 4; ++a) cq[a] = CSUM(0, a);
      MFMA_SETTLE(aq);
      const float qs = rstd * (0.18898223650461362f * 1.4426950408889634f);
#pragma unroll
      for (int a = 0; a < 4; ++a) {
        qf[a >> 1][(a & 1) * 2] = pk((aq[4 * a] - mean * cq[a][0]) * qs, (aq[4 * a + 1] - mean * cq[a][1]) * qs);
        qf[a >> 1][(a & 1) * 2 + 1] = pk((aq[4 * a + 2] - mean * cq[a][2]) * qs, (aq[4 * a + 3] - mean * cq[a][3]) * qs);
      }
      if (hi) { qf[1][2] = 0x00003f80u; qf[1][3] = 0u; }
      PINV(qf[0]); PINV(qf[1]);
      f32x16 s8;
      asm volatile("s_nop 1" : "+v"(qf[0]), "+v"(qf[1]));
      MFMA32V0(s8, kv[0], qf[0]);
      MFMA32VA(s8, kv[1], qf[1]);
      SCHED();
      unsigned late;
      POLLED(pf, 0, fp)
      STAMP(4 + layer * 16);
      v4u ra[4], rb[4];
      {
        const rsrc_t r1 = mk(p.ws, t >= 1 ? WS_BYTES : 0), r2 = mk(p.ws, t >= 2 ? WS_BYTES : 0);
        const int kvo = kvs - 32768;
        rb[0] = ld16<SC1>(r2, kvo, 0); rb[1] = ld16<SC1>(r2, kvo, 1024);
        ra[0] = ld16<SC1>(r1, kvo, 16384); ra[1] = ld16<SC1>(r1, kvo, 16384 + 1024);
        SCHED();
        rb[2] = ld16<SC1>(r2, kvo, 2048); rb[3] = ld16<SC1>(r2, kvo, 3072);
        ra[2] = ld16<SC1>(r1, kvo, 16384 + 2048); ra[3] = ld16<SC1>(r1, kvo, 16384 + 3072);
        SCHED();
      }
#pragma unroll
      for (int j = 0; j < 16; ++j) wo[j >> 2][j & 3] = trfrag(wor[j & 3] + (j >> 2) * 4096, wor[(j & 3) ^ 2] + (j >> 2) * 4096 + 1024);
      SCHED();
      bar();
      LAS char* vw1 = X128(vw) + 1024;
      LAS char* vr1 = X128(vr0);
      *(LAS v4u*)(kvw + 32768) = kv[0];
      *(LAS v4u*)(kvw + 32768 + 1024) = kv[1];
      *(LAS v4u*)(vw + 32768) = kv[2];
      *(LAS v4u*)(vw1 + 32768) = kv[3];
      int rro = (64 * st + 16 * w + c) * 896 + dt * 128 + g * 8, b2o = (64 * st + 16 * w + c) * 2, b1o = ft * 128 + g * 8;
      unsigned pml[8], mk32[16], nk1l = nk1, nk2l = nk2;
      int ool = oo, t1l = t1o;
      LAS char* vr0l = vr0;
      PINV(rro); PINV(b2o); PINV(b1o); PINV(vw1); PINV(vr1); PINV(vr0l); PINV(ool); PINV(t1l); PINV(nk1l); PINV(nk2l);
#pragma unroll
      for (int i = 0; i < 8; ++i) { pml[i] = pm[i]; PINV(pml[i]); mk32[2 * i] = (unsigned)((int)(pml[i] << 16) >> 16); mk32[2 * i + 1] = (unsigned)((int)pml[i] >> 16); PINV(mk32[2 * i]); PINV(mk32[2 * i + 1]); }
      SCHED();
      f32x16 sc[8], se;
      v4u kf[8][2];
      float mx = -1e30f, mx2 = -1e30f;
#define MAX8(v, i) asm volatile("v_max3_f32 %0, %0, %2, %3\n\tv_max3_f32 %1, %1, %4, %5\n\tv_max3_f32 %0, %0, %6, %7\n\tv_max3_f32 %1, %1, %8, %9" \
        : "+v"(mx), "+v"(mx2) : "v"(v[i]), "v"(v[(i) + 1]), "v"(v[(i) + 2]), "v"(v[(i) + 3]), "v"(v[(i) + 4]), "v"(v[(i) + 5]), "v"(v[(i) + 6]), "v"(v[(i) + 7]))
      *(LAS v4u*)(kvw) = rb[0];
      MFMA32V0(sc[0], rb[0], qf[0]);
      rb[1][2] |= nk2l;
      *(LAS v4u*)(kvw + 1024) = rb[1];
      *(LAS v4u*)(kvw + 16384) = ra[0];
      SCHED();
      MFMA32VA(sc[0], rb[1], qf[1]);
      ra[1][2] |= nk1l;
      *(LAS v4u*)(kvw + 16384 + 1024) = ra[1];
      kf[4][0] = ra[0]; kf[4][1] = ra[1];
      MFMA32V0(sc[4], kf[4][0], qf[0]);
      bar();
      KEEP(late);
      STAMP(37 + layer * 4);
      MFMA32VA(sc[4], kf[4][1], qf[1]);
#pragma unroll
      for (int j = 1; j < 8; ++j) if (j != 4) { kf[j][0] = *(LAS v4u*)(kvw + j * 4096); kf[j][1] = *(LAS v4u*)(kvw + j * 4096 + 1024); }
      PINV(sc[0]);
#pragma unroll
      for (int i = 0; i < 16; ++i) se[i] = asf((asu(sc[0][i]) & mk32[i]) | (asu(s8[i]) & ~mk32[i]));
      PINV(se);
      SCHED();
      sfor<12>([&](auto N) INL {
        constexpr int n = N, jj = n >> 1, j = jj < 3 ? jj + 1 : jj + 2, hf = n & 1;
        if constexpr (hf == 0) MFMA32V0(sc[j], kf[j][0], qf[0]); else MFMA32VA(sc[j], kf[j][1], qf[1]);
        if constexpr (n < 2) MAX8(se, 8 * hf);
        else if constexpr (n < 4) MAX8(sc[4], 8 * hf);
        else if constexpr (n >= 6) MAX8(sc[jj - 2], 8 * hf);
        if constexpr (n == 4) { *(LAS v4u*)(vw) = rb[2]; *(LAS v4u*)(vw1) = rb[3]; }
        if constexpr (n == 5) { *(LAS v4u*)(vw + 16384) = ra[2]; *(LAS v4u*)(vw1 + 16384) = ra[3]; SCHED(); touch(mk(p.w1, S * F * 2), t1l, layer * KP * 32 * F * 2, L); }
        SCHED();
      });
      MAX8(sc[5], 0); MAX8(sc[5], 8); MAX8(sc[6], 0); MAX8(sc[6], 8); MAX8(sc[7], 0); MAX8(sc[7], 8);
      mx = max32(max3(mx, mx2, mx2));
      f32x16 nm;
#pragma unroll
      for (int i = 0; i < 16; ++i) nm[i] = -mx;
      PINV(nm);
      bar();
      STAMP(38 + layer * 4);
      f32x16 ao;
      v4u vf[9][2], pp[9][2];
      float ex[32][4];
#define VF(j, s) vf[j][s] = trfrag(vr0l + (j) * 4096 + (s) * 256, vr1 + (j) * 4096 + (s) * 256)
      sfor<33>([&](auto N) INL {
        constexpr int n = N, j = n >> 2, q = n & 3;
        if constexpr (j == 0) {
          if constexpr (q == 0) MFMA32VC(sc[1], kf[1][0], qf[0], nm);
          if constexpr (q == 1) MFMA32VA(sc[1], kf[1][1], qf[1]);
          if constexpr (q == 2) MFMA32VC(sc[2], kf[2][0], qf[0], nm);
          if constexpr (q == 3) MFMA32VA(sc[2], kf[2][1], qf[1]);
        } else if constexpr (j < 8) {
          if constexpr (q == 1) { if constexpr (j == 1) MFMA32V0(ao, vf[0][0], pp[0][0]); else MFMA32VA(ao, vf[j - 1][0], pp[j - 1][0]); }
          if constexpr (q == 2) MFMA32VA(ao, vf[j - 1][1], pp[j - 1][1]);
          if constexpr (j + 2 < 8) {
            if constexpr (q == 0) MFMA32VC(sc[j + 2], kf[j + 2][0], qf[0], nm);
            if constexpr (q == 3) MFMA32VA(sc[j + 2], kf[j + 2][1], qf[1]);
          } else if constexpr (j == 6) {
            if constexpr (q == 0) MFMA32VA(ao, vf[8][0], pp[8][0]);
            if constexpr (q == 3) MFMA32VA(ao, vf[8][1], pp[8][1]);
          }
        } else MFMA32VA(ao, vf[7][0], pp[7][0]);
        SCHED();
        if constexpr (n < 32) {
#pragma unroll
          for (int i = 0; i < 4; ++i) { if constexpr (j == 0) ex[n][i] = __builtin_amdgcn_exp2f(se[4 * q + i] - mx); else ex[n][i] = __builtin_amdgcn_exp2f(sc[j][4 * q + i]); }
        }
        SCHED();
        if constexpr (n > 0) {
          constexpr int jp = (n - 1) >> 2, qp = (n - 1) & 3;
#pragma unroll
          for (int i = 0; i < 2; ++i) {
            const unsigned u = pk(ex[n - 1][2 * i], ex[n - 1][2 * i + 1]);
            if constexpr (jp == 0) { pp[0][qp >> 1][(2 * qp + i) & 3] = u & pml[2 * qp + i]; pp[8][qp >> 1][(2 * qp + i) & 3] = u & ~pml[2 * qp + i]; }
            else pp[jp][qp >> 1][(2 * qp + i) & 3] = u;
          }
        }
        if constexpr (n >= 2 && n < 10 && q >= 2) rr[(n >> 2) * 2 + q - 2] = ld8<0>(mk(p.x, (on35 && layer == 0) ? S * D * 2 : 0), rro, (((n >> 2) * 2 + q - 2) ^ pa) * 32);
        if constexpr (n == 10) b2r = (unsigned)__builtin_amdgcn_raw_buffer_load_b16(mk(p.b2, S * 2), b2o, 0, 0);
        if constexpr (n >= 14 && n < 22 && q >= 2) bb[((n >> 2) - 3) * 2 + q - 2] = ld8<0>(mk(p.b1, F * 2), b1o, (((n >> 2) - 3) * 2 + q - 2) * 32);
        if constexpr (n == 31) __builtin_amdgcn_s_waitcnt(0x0F70);
        if constexpr (n == 22) KEEP(nm);
        if constexpr (j < 8 && q < 2) VF(j, q);
        if constexpr (j == 5 && q >= 2) VF(8, q - 2);
        SCHED();
      });
      asm volatile("s_nop 1" : "+v"(pp[7][1]));
      MFMA32VA(ao, vf[7][1], pp[7][1]);
      MFMA_SETTLE(ao);
      const float inv = __builtin_amdgcn_rcpf(hi32(ao[12]));
      v4u of[2];
#pragma unroll
      for (int i = 0; i < 16; i += 2) of[i >> 3][(i >> 1) & 3] = pk(ao[i] * inv, ao[i + 1] * inv);
      if (hi) { of[1][2] = 0u; of[1][3] = 0u; }
      st16<SC1>(of[0], rws, ool, 0);
      st16<SC1>(of[1], rws, ool, 512);
      SCHED();
      STAMP(39 + layer * 4);
      bar();
      DPUMP(Y0, Y0, Y1)
      publish_n<4 * (Y1 - Y0)>(fl + (1 * 256 + bid) * 32, epoch);
      STAMP(5 + layer * 16);
    }
    if (on35) {
      {
        DPUMP(Y0, Y1, CMIN(Y1 + 1, Y2))
        FRESH();
        const int tq = st >> 1;
        GAS unsigned* fp = fl + (1 * 256 + (((tq & 1) * 16 + 4 * w + (lane & 3)) * 8 + (tq >> 1))) * 32;
        int oo = O_OFF + layer * O_SZ + (st * 16 + 4 * w) * 4096 + lane * 16;
        PINV(fp); PINV(oo);
        DPUMP(Y0, CMIN(Y1 + 1, Y2), CMIN(Y1 + 2, Y2))
        int sb = S3_OFF + layer * S3_SZ + st * 8192 + (16 * w + c) * 16;
        int sw = g < 2 ? sb + dt * 1024 + g * 8 : OOB;
        int ob[4];
#pragma unroll
        for (int b = 0; b < 4; ++b) { ob[b] = ((b + w) & 3) * 1024; PINS(ob[b]); }
        PINV(sb); PINV(sw); PINV(red);
#pragma unroll
        for (int a = 0; a < 4; ++a) { res[a] += unpack4(rr[a]); PINV(res[a]); }
        unsigned pf;
        DPUMP(Y0, CMIN(Y1 + 2, Y2), Y2)
        POLL(pf, fp);
        DPUMP(Y0, Y2, YG)
        STASH(0, SO)
        unsigned late;
        POLLED(pf, 4 * (YG - Y2 + layer * SO), fp)
        STAMP(6 + layer * 16);
        v4u of[16];
#define LOAD_O(I) of[I] = ld16<SC1>(rws, oo + ((I) & 3) * 4096, ob[(((I) >> 2) + 1) & 3])
#pragma unroll
        for (int i = 0; i < 12; ++i) { LOAD_O(i); SCHED(); }
        vmw<12>();
        KEEP(late);
        STAMP(50 + layer * 4);
        f32x4 acc[4][4], part[12];
        sfor<16>([&](auto I) INL {
          constexpr int i = I, b = ((i >> 2) + 1) & 3, k = i & 3;
#pragma unroll
          for (int a = 0; a < 4; ++a) {
            acc[a][b] = mfma16(wo[k][a], of[i], k > 0 ? acc[a][b] : (f32x4){0.f, 0.f, 0.f, 0.f});
            SCHED();
            if constexpr (i < 4) { if (a == 0) LOAD_O(i + 12); }
            if constexpr (i < 12) { if (a == 0) DTAIL1(Y0, YG, i) }
            if constexpr (i >= 4 && k < 2) { if (a == 1 || a == 3) REDW(2 * k + (a >> 1), i >> 2); }
            if constexpr (i == 14) { if (a == 0) bar(); }
            if constexpr (i >= 14) { if (a < 3) { const int j = 6 * (i - 14) + 2 * a; part[j] = REDR(j / 3, j % 3 + 1); part[j + 1] = REDR((j + 1) / 3, (j + 1) % 3 + 1); } }
            SCHED();
          }
        });
        STAMP(51 + layer * 4);
#pragma unroll
        for (int a = 0; a < 4; ++a) res[a] = ((res[a] + part[3 * a]) + (part[3 * a + 1] + part[3 * a + 2])) + acc[a][0];
        f32x2 s1 = {0.f, 0.f}, s2 = {0.f, 0.f};
        stats16(res, 15, s1, s2);
        st8<SC1>((v2u){asu(sum_ab(s1[0] + s1[1], s2[0] + s2[1])), epoch}, rws, sw, 0);
        SCHED();
        DTAIL1(Y0, YG, 12) DTAIL1(Y0, YG, 13) DTAIL1(Y0, YG, 14) DTAIL1(Y0, YG, 15)
        STAMP(7 + layer * 16);
        bar();
        DPUMP(YG, YG, CMIN(YG + 1, YG2))
        int sa = sb + g * 1024, sc = g < 3 ? sb + (g + 4) * 1024 : OOB;
        int ho = HT_OFF + layer * HT_SZ + (dt * 64 + 2 * st + (w >> 1)) * 4096 + (g >> 1) * 1024 + (2 * (w & 1) + (g & 1)) * 256 + c * 16;
        PINV(sa); PINV(sc); PINV(ho);
        DPUMP(YG, CMIN(YG + 1, YG2), CMIN(YG + 2, YG2))
        LAS char* hst = L + L_HST + w * 2048;
        const int hr = 8 * (g & 1) + q4;
        int h0 = raddr(hr) ^ ((g >> 1) << 5), h1 = raddr(hr + 4) ^ ((g >> 1) << 5), hw = (c * 128 + (swz(c) << 5) + g * 8) ^ (pa << 5);
        PINV(h0); PINV(h1); PINV(hw);
        DPUMP(YG, CMIN(YG + 2, YG2), YG2)
        v4u ga, gb;
        asm volatile("buffer_load_dwordx4 %0, %2, %4, 0 offen sc1\n\tbuffer_load_dwordx4 %1, %3, %4, 0 offen sc1" : "=&v"(ga), "=&v"(gb) : "v"(sa), "v"(sc), "s"(rws) : "memory");
        DPUMP(YG, YG2, YH)
        STASH(SO, SO + SG)
        asm volatile("s_waitcnt vmcnt(%2)" : "+v"(ga), "+v"(gb) : "n"(4 * (YH - YG2 + layer * SG)) : "memory");
        while ((__builtin_amdgcn_ballot_w64((ga.y != epoch) | (ga.w != epoch)) | (__builtin_amdgcn_ballot_w64((gb.y != epoch) | (gb.w != epoch)) << 16)) != 0ull) {
          if (budget == 0u) break;
          --budget;
          asm volatile("buffer_load_dwordx4 %0, %2, %4, 0 offen sc1\n\tbuffer_load_dwordx4 %1, %3, %4, 0 offen sc1\n\ts_waitcnt vmcnt(0)"
                       : "=&v"(ga), "=&v"(gb) : "v"(sa), "v"(sc), "s"(rws) : "memory");
        }
        STAMP(8 + layer * 16);
        const unsigned t12 = asu(sum_ab(asf(ga.x) + asf(gb.x), asf(ga.z) + asf(gb.z)));
        const auto tt = __builtin_amdgcn_permlane16_swap(t12, t12, false, false);
        const float t1 = asf(tt[0]), t2 = asf(tt[1]);
        const float m = t1 * (1.f / D), var = fmaxf(t2 * (1.f / D) - m * m, 0.f);
        const float r1 = __builtin_amdgcn_rsqf(var + EPS);
        const float r = r1 * __builtin_amdgcn_rsqf(var * r1 * r1 + EPS), nr = -m * r;
#pragma unroll
        for (int a = 0; a < 4; ++a) {
          const v2u v = {pk(res[a][0] * r + nr, res[a][1] * r + nr), pk(res[a][2] * r + nr, res[a][3] * r + nr)};
          *(LAS v2u*)(hst + (hw ^ (a << 5))) = v;
        }
#pragma unroll
        for (int e = 0; e < 2; ++e) st16<SC1>(trfrag(hst + (h0 ^ (e << 6)), hst + (h1 ^ (e << 6))), rws, ho, e * 2048);
        SCHED();
        DPUMP(YG, YH, YH1)
        publish_n<4 * (YH1 - YH)>(fl + (2 * 256 + bid) * 32, epoch);
        STAMP(9 + layer * 16);
      }
      DPUMP(YG, YH1, CMIN(YH1 + 1, YH2))
      if (on4) {
        FRESH();
        const int sp = 8 * w + (lane & 7);
        GAS unsigned* fp = fl + (2 * 256 + ((sp & 3) * 7 + dt4) * 8 + (sp >> 2)) * 32;
        int hv[4];
#pragma unroll
        for (int b = 0; b < 4; ++b) { hv[b] = HT_OFF + layer * HT_SZ + (dt4 * 64 + 16 * w) * 4096 + ((b + w4) & 3) * 1024 + lane * 16; PINV(hv[b]); }
        PINV(fp);
        DPUMP(YG, CMIN(YH1 + 1, YH2), CMIN(YH1 + 2, YH2))
        int uo = U_OFF + layer * U_SZ + (dt4 * 56 + 2 * ft) * 4096 + w4 * 1024 + lane * 16;
        PINV(uo); PINV(red);
        unsigned pf;
        DPUMP(YG, CMIN(YH1 + 2, YH2), YH2)
        STASH(SO + SG, NS - SH)
        POLL(pf, fp);
        DPUMP(YG, YH2, YE)
        STASH(NS - SH, NS)
        unsigned late;
        POLLED(pf, 4 * (YE - YH2 + layer * SH), fp)
        STAMP(10 + layer * 16);
        v4u af[64];
#define LOAD_H(I) { int so_; asm volatile("s_mov_b32 %1, %4\n\tbuffer_load_dwordx4 %0, %2, %3, %1 offen sc1" \
          : "=&v"(af[I]), "=&s"(so_) : "v"(hv[(((I) >> 4) + 1) & 3]), "s"(rws), "n"(((I) & 15) * 4096) : "memory"); }
        sfor<WIN4>([&](auto I) INL { LOAD_H(I) SCHED(); });
        f32x4 acc[4][4], part[12];
        sfor<64>([&](auto I) INL {
          constexpr int i = I, b = ((i >> 4) + 1) & 3, k = i & 15;
          constexpr int np = i <= 54 ? 0 : i <= 62 ? i - 54 : 10;
          asm volatile("s_waitcnt vmcnt(%1)" : "+v"(af[i]) : "n"((i + WIN4 <= 64 ? WIN4 - 1 : 63 - i) + np) : "memory");
          if constexpr (i == 0) KEEP(late);
          SCHED();
          sfor<4>([&](auto A) INL {
            constexpr int a = A;
            acc[a][b] = mfma16(wz[k][a], af[i], k > 0 ? acc[a][b] : (f32x4){0.f, 0.f, 0.f, 0.f});
            SCHED();
            if constexpr (i + WIN4 < 64 && a == 0) LOAD_H(i + WIN4)
            if constexpr (i < 8) { if constexpr (a == 1) DTAIL1(YG, YE, 2 * i) if constexpr (a == 2) DTAIL1(YG, YE, 2 * i + 1) }
            if constexpr (i >= 16 && k < 2 && (a & 1)) REDW(2 * k + (a >> 1), i >> 4);
            if constexpr (i == 50 && a == 3) bar();
            if constexpr (i >= 51 && i < 54) part[4 * (i - 51) + a] = REDR((4 * (i - 51) + a) / 3, (4 * (i - 51) + a) % 3 + 1);
            if constexpr (i == 54 && a == 1) bar();
            if constexpr (i >= 54 && a == 2) PIECE(Z_U, np)
            if constexpr (i >= 62 && a == 3) PIECE(Z_U, np + 1)
            if constexpr (i >= 55 && i < 59 && a == 0) {
              constexpr int q = i - 55;
              part[3 * q] = (part[3 * q] + unpack4(bb[q])) + (part[3 * q + 1] + part[3 * q + 2]);
              PINV(part[3 * q]);
            }
            SCHED();
          });
        });
        STAMP(11 + layer * 16);
        v4u uf[2];
        sfor<8>([&](auto H) INL {
          constexpr int h = H, a = h >> 1, j0 = 2 * (h & 1);
          float gl[2];
#pragma unroll
          for (int j = 0; j < 2; ++j) {
            const float u = acc[a][0][j0 + j] + part[3 * a][j0 + j];
            const float e = __builtin_amdgcn_exp2f(u * (u * u * (-0.10294324f) - 2.3022082f));
            gl[j] = u * __builtin_amdgcn_rcpf(1.f + e);
          }
          uf[h >> 2][h & 3] = pk(gl[0], gl[1]);
          if constexpr (h & 1) {
            PINV(uf[h >> 2]);
            if constexpr (h == 3) st16<SC1>(uf[0], rws, uo, 0);
            if constexpr (h == 7) st16<SC1>(uf[1], rws, uo, 4096);
            SCHED();
            if constexpr (h < 7 && 3 + h / 2 < NU0) DPUMP(Z_U, Z_U + 3 + h / 2, Z_U + 4 + h / 2)
          }
        });
        STAMP(40 + layer * 4);
        DPUMP(Z_U, Z_U + NU0, Z_U + NU0 + NU1)
        publish_n<4 * NU1>(fl + (3 * 256 + bid) * 32, epoch);
      } else {
        DPUMP(YG, CMIN(YH1 + 1, YH2), YE)
        STASH(SO + SG, NS)
        DRAIN();
        DTAIL(YG, YE)
        LGKM0();
        DPUMP(Z_U, Z_U, Z_U + NU0 + NU1)
      }
      STAMP(12 + layer * 16);
      {
        DPUMP(Z_U, Z_U + NU0 + NU1, CMIN(Z_U + NU0 + NU1 + 1, YU3))
        FRESH();
        const int pi = wrap7(lane & 7);
        GAS unsigned* fp = fl + (3 * 256 + (MAP2D ? ((dt & 3) * 7 + pi) * 8 + (dt >> 2) : pi == 3 ? (21 + (dt & 3)) * 8 + (dt >> 2) : (7 * (pi & 3) + dt) * 8 + (pi >> 2)) + 2 * w) * 32;
        int uv[4];
#pragma unroll
        for (int a = 0; a < 4; a += 2) { uv[a] = U_OFF + layer * U_SZ + (dt * 56 + 14 * w) * 4096 + (a ^ pa) * 1024 + lane * 16; PINV(uv[a]); uv[a + 1] = uv[a] + 1024; }
        PINV(fp);
        DPUMP(Z_U, CMIN(Z_U + NU0 + NU1 + 1, YU3), CMIN(Z_U + NU0 + NU1 + 2, YU3))
        int ow = L_HST + w * 2048 + ((c * 128 + (swz(c) << 5) + g * 8) ^ (pa << 5)), orr = L_HST + w * 2048 + wa0;
        PINV(ow); PINV(orr);
        int xb = layer ? (OFULL ? (64 * st + 16 * w + wrow) * 896 + dt * 128 + (lane & 7) * 16 : (64 * st + 16 * w + c) * 896 + dt * 128 + g * 8)
                       : X2_OFF + (2 * st + (w >> 1)) * 28672 + (4 * dt + (g >> 1)) * 1024 + ((g & 1) * 32 + (w & 1) * 16 + c) * 16;
        int so = g < 2 ? S5_OFF + (st * 8 + dt) * 512 + (16 * w + c) * 8 + g * 4 : OOB;
#pragma unroll
        for (int a = 0; a < 4; ++a) { res[a] += bflo(b2r); PINV(res[a]); }
        PINV(xb); PINV(so); PINV(red);
        unsigned pf;
        DPUMP(Z_U, CMIN(Z_U + NU0 + NU1 + 2, YU3), YU3)
        POLL(pf, fp);
        DPUMP(Z_U, YU3, YZ)
        if constexpr (layer == 1) {
          const int dlt = L_WQ + w * 20480 - (L_KVW + w * 16384);
#pragma unroll
          for (int i = 0; i < 4; ++i) {
            LAS char* ps = pr2[i] + dlt;
#pragma unroll
            for (int j = 0; j < NS; ++j) wz[30 - NS + j][i] = trfrag(ps + j * 4096, ps + j * 4096 + 2048);
          }
          SCHED();
        }
        unsigned late;
        POLLED(pf, 4 * (YZ - YU3), fp)
        STAMP(13 + layer * 16);
        v4u af[56];
#define LOAD_U(I) af[I] = ld16<SC1>(rws, uv[(I) / 14], ((I) % 14) * 4096)
#pragma unroll
        for (int i = 0; i < WIN5; ++i) { LOAD_U(i); SCHED(); }
        vmw<WIN5>();
        KEEP(late);
        f32x4 acc[4][4], part[4][3];
        v2u xo[4];
        f32x2 s1 = {0.f, 0.f}, s2 = {0.f, 0.f};
        const rsrc_t ro = mk(p.out, S * D * 2);
        sfor<56>([&](auto I) INL {
          constexpr int i = I, a = i / 14, k = i % 14;
#pragma unroll
          for (int bq = 0; bq < 4; ++bq) {
            const int b = (bq + 1) & 3;
            if (a < 3 || b) acc[a][b] = mfma16(af[i], wz[16 + k][b], k > 0 ? acc[a][b] : (f32x4){0.f, 0.f, 0.f, 0.f});
            SCHED();
            if constexpr (i + WIN5 < 56) { if (bq == 0) LOAD_U(i + WIN5); }
            if constexpr (i < 8) { if (bq == 1) DTAIL1(Z_U, YZ, 2 * i) if (bq == 2) DTAIL1(Z_U, YZ, 2 * i + 1) }
            if constexpr (a > 0 && k < 3) { if (bq == 2) REDW(a - 1, k + 1); }
            if constexpr (i == 46) { if (bq == 2) bar(); }
            if constexpr (i >= 47 && i < 50) { if (bq < 3) part[i - 47][bq] = REDR(i - 47, bq + 1); }
            if constexpr (i >= 50 && i < 53) { if (bq == 2) { res[i - 50] = ((res[i - 50] + part[i - 50][0]) + (part[i - 50][1] + part[i - 50][2])) + acc[i - 50][0]; PINV(res[i - 50]); } }
            if constexpr (i >= 53) { if (bq == 2) { xo[i - 53] = (v2u){pk(res[i - 53][0], res[i - 53][1]), pk(res[i - 53][2], res[i - 53][3])}; stats16(res, 1 << (i - 53), s1, s2); PINV(xo[i - 53]); PINV(s1); PINV(s2); } }
            SCHED();
          }
        });
        STAMP(14 + layer * 16);
        sfor<14>([&](auto K) INL {
          constexpr int k = K;
          acc[3][0] = mfma16(af[42 + k], wz[16 + k][0], k > 0 ? acc[3][0] : (f32x4){0.f, 0.f, 0.f, 0.f});
          SCHED();
          if constexpr (k >= 1 && k < 4) REDW(3, k);
          if constexpr (k == 5) {
            if (layer == 0) st16<SC1>((v4u){xo[0].x, xo[0].y, xo[1].x, xo[1].y}, rws, xb, pa * 1024);
            else if (!OFULL) { st8<OAUX>(xo[0], ro, xb, pa * 32); st8<OAUX>(xo[1], ro, xb, pa * 32 + 32); st8<OAUX>(xo[2], ro, xb, (2 ^ pa) * 32); }
            if (OFULL) { _Pragma("unroll") for (int a = 0; a < 3; ++a) *(LAS v2u*)(L + (ow ^ (a << 5))) = xo[a]; }
          }
          if constexpr (k == 7) { bar(); part[3][0] = REDR(3, 1); part[3][1] = REDR(3, 2); part[3][2] = REDR(3, 3); }
          SCHED();
        });
        res[3] = ((res[3] + part[3][0]) + (part[3][1] + part[3][2])) + acc[3][0];
        xo[3] = (v2u){pk(res[3][0], res[3][1]), pk(res[3][2], res[3][3])};
        if (layer == 0) {
          st16<SC1>((v4u){xo[2].x, xo[2].y, xo[3].x, xo[3].y}, rws, xb, (2 ^ pa) * 1024);
          SCHED();
          stats16(res, 8, s1, s2);
          __builtin_amdgcn_raw_buffer_store_b32(asu(sum_ab(s1[0] + s1[1], s2[0] + s2[1])), rws, so, 0, SC1);
          publish(flags + (4 * 256 + bid) * 32, epoch);
        } else {
          if (!OFULL) st8<OAUX>(xo[3], ro, xb, (2 ^ pa) * 32 + 32);
          else {
            *(LAS v2u*)(L + (ow ^ (3 << 5))) = xo[3];
            const v4u r0 = *(LAS v4u*)(L + orr), r1 = *(LAS v4u*)(L + (orr ^ 64) + 1024);
            st16<OAUX>(r0, ro, xb, 0);
            st16<OAUX>(r1, ro, xb, 8 * 896);
          }
        }
      }
    }
    else {
      FRESH();
      const int rw = 4 * (by - 28) + (lane >> 4) + 16 * ((lane >> 2) & 3);
      if (MAP2D) {
        const int l7 = (lane * 37) >> 8, vo = lane < 56 ? (512 * w + 256 + 8 * (by - 28) + l7) * (F * 2) + (7 * (bx >> 1) + lane - 7 * l7) * 128 : OOB;
#pragma unroll 1
        for (int i = 0; i < 8; ++i) { touch(mk(p.w1, S * F * 2), vo, i * 32 * F * 2, L); vmw<PACE>(); }
      } else {
        const int vo = (512 * w + 256 + rw) * (F * 2) + (7 * (bx >> 1) + 3 * (bx & 1) + (lane & 3)) * 128;
#pragma unroll 1
        for (int i = layer * (KP / 2); i < 4; ++i) { touch(mk(p.w1, S * F * 2), vo, i * 64 * F * 2, L); vmw<PACE>(); }
      }
      const int vo = (448 * w + rw) * (S * 2) + bx * 512 + (lane & 3) * 128;
#pragma unroll 1
      for (int i = 0; i < (layer ? 7 - (Z_U - 16) / 2 : 7); ++i) { touch(mk(p.w2, S * F * 2), vo, (layer ? (i < 3 ? i + 4 : i - 3 + (Z_U - 16) / 2) : i) * 64 * S * 2, L); vmw<PACE>(); }
      if (ISLEEP && layer == 0) {
        while (__hip_atomic_load(fl + (3 * 256 + bx) * 32, RLX_AGENT) != epoch && budget) { --budget; __builtin_amdgcn_s_sleep(20); }
        __builtin_amdgcn_s_sleep(ISLEEP);
      }
    }
    STAMP(15 + layer * 16);
    if constexpr (layer == 0) {
      FRESH();
      const int st1 = 2 * t + (w >> 1);
      f32x16 ak, av;
      v4u wk[28], wv[28];
      LAS char* wb = WB(0) + 28672;
      PINV(wb);
      wk[0] = WFRAG(wb, 0, 0); wv[0] = WFRAG(wb, 1, 0); wk[1] = WFRAG(wb, 0, 1); wv[1] = WFRAG(wb, 1, 1);
      f32x4 ck[4], cv[4];
#pragma unroll
      for (int a = 0; a < 4; ++a) { ck[a] = CSUM(1, a); cv[a] = CSUM(2, a); PINV(ck[a]); PINV(cv[a]); }
      int xo = X2_OFF + (4 * t + w) * 28672 + lane * 16, so = S5_OFF + (st1 * 8 + hh * 4) * 512 + ((w & 1) * 32 + r32) * 8;
      int xt = lane < 14 ? X2_OFF + (4 * t + w) * 28672 + (hd * 14 + lane) * 128 : OOB;
      int so3 = hh ? OOB : so + 3 * 512;
      PINV(xo); PINV(so); PINV(so3); PINV(xt);
      kvs += KV_SZ; oo += O_SZ; fo += 5 * 256 * 128;
      PINV(kvs); PINA(oo); PINA(fo);
      GAS unsigned* fp = flags + (4 * 256 + ((st1 & 3) * 7 + wrap7(lane & 7)) * 8 + (st1 >> 2)) * 32;
      wait_flags(fp, epoch, budget);
      STAMP(18);
      v2u sp[4];
      const unsigned xtv = XTCH ? __builtin_amdgcn_raw_buffer_load_b32(rws, xt, 0, SC1) : 0u;
      SCHED();
#define LOAD_X(I) xf[I] = ld16<SC1>(rws, xo + ((I) & 3) * 1024, ((I) >> 2) * 4096)
#pragma unroll
      for (int i = 0; i < WINX; ++i) { LOAD_X(i); SCHED(); }
      sfor<28>([&](auto I) INL {
        constexpr int ks = I;
        if constexpr (ks == 0) MFMA32V0(ak, wk[ks], xf[ks]); else MFMA32VA(ak, wk[ks], xf[ks]);
        if constexpr (ks + 2 < 28) wk[ks + 2] = WFRAG(wb + ((ks + 2) >> 2) * 4096, 0, (ks + 2) & 3);
        if constexpr (ks + WINX < 28) LOAD_X(ks + WINX);
        SCHED();
        if constexpr (ks == 0) MFMA32V0(av, wv[ks], xf[ks]); else MFMA32VA(av, wv[ks], xf[ks]);
        if constexpr (ks + 2 < 28) wv[ks + 2] = WFRAG(wb + ((ks + 2) >> 2) * 4096, 1, (ks + 2) & 3);
        if constexpr (ks < 4) sp[ks] = ld8<SC1>(rws, ks == 3 ? so3 : so + ks * 512, 0);
        if constexpr (ks == 2) KEEP(xtv);
        if constexpr (ks == 18) {
          float s1 = (asf(sp[0].x) + asf(sp[1].x)) + (asf(sp[2].x) + asf(sp[3].x));
          float s2 = (asf(sp[0].y) + asf(sp[1].y)) + (asf(sp[2].y) + asf(sp[3].y));
          s1 = sum32(s1); s2 = sum32(s2);
          mean = s1 * (1.f / D);
          rstd = s2 * (1.f / D) - mean * mean;
          PINV(mean); PINV(rstd);
        }
        if constexpr (ks == 19) { rstd = __builtin_amdgcn_rsqf(fmaxf(rstd, 0.f) + EPS); PINV(rstd); }
        if constexpr (ks >= 20 && ks < 24) { ck[ks - 20] *= -mean * rstd; cv[ks - 20] *= -mean * rstd; PINV(ck[ks - 20]); PINV(cv[ks - 20]); }
        SCHED();
      });
      MFMA_SETTLE2(ak, av);
#pragma unroll
      for (int i = 0; i < 16; ++i) ak[i] = ak[i] * rstd + ck[i >> 2][i & 3];
      KVOUT(ak, 0)
#pragma unroll
      for (int i = 0; i < 16; ++i) av[i] = av[i] * rstd + cv[i >> 2][i & 3];
      KVOUT(av, 1)
    }
  });
  asm volatile(".Lfk_code_to:");
  if (blockIdx.x == 0 && threadIdx.x == 0) __hip_atomic_store(ctrl, epoch, RLX_AGENT);
}

extern "C" void kernel_launch(void* const* d_in, const int* in_sizes, int n_in, void* d_out, int out_size, void* d_ws,
                              size_t ws_size, hipStream_t stream) {
  Params p;
  p.x = d_in[0]; p.wqkv = d_in[1]; p.wo = d_in[2];
  p.w1 = d_in[4]; p.b1 = d_in[5]; p.w2 = d_in[6]; p.b2 = d_in[7];
  p.out = d_out; p.ws = d_ws;
  (void)hipFuncSetAttribute((const void*)fused_kernel, hipFuncAttributeMaxDynamicSharedMemorySize, LDS_BYTES);
  fused_kernel<<<256, 256, LDS_BYTES, stream>>>(p);
}
```

```cpp
#include <hip/hip_runtime.h>
#include <utility>

#define GAS __attribute__((address_space(1)))
#define LAS __attribute__((address_space(3)))
#define DI __device__ __forceinline__
#define INL __attribute__((always_inline))
#define RLX_AGENT __ATOMIC_RELAXED, __HIP_MEMORY_SCOPE_AGENT
typedef unsigned v4u __attribute__((ext_vector_type(4)));
typedef unsigned v2u __attribute__((ext_vector_type(2)));
typedef float f32x2 __attribute__((ext_vector_type(2)));
typedef float f32x4 __attribute__((ext_vector_type(4)));
typedef float f32x16 __attribute__((ext_vector_type(16)));
typedef short s16x4 __attribute__((ext_vector_type(4)));
typedef __bf16 bf16x2 __attribute__((ext_vector_type(2)));
typedef __bf16 bf16x8 __attribute__((ext_vector_type(8)));
typedef __amdgpu_buffer_rsrc_t rsrc_t;

constexpr int S = 2048, D = 448, F = 1792;
constexpr float EPS = 1e-5f;
constexpr int FLAG_OFF = 4096;
constexpr int KV_OFF = 512 * 1024, KV_SZ = 16 * 64 * 4096;
constexpr int O_OFF = KV_OFF + 2 * KV_SZ, O_SZ = 32 * 16 * 4096;
constexpr int S3_OFF = O_OFF + 2 * O_SZ, S3_SZ = 32 * 8 * 1024;
constexpr int HT_OFF = S3_OFF + 2 * S3_SZ, HT_SZ = 7 * 64 * 4096;
constexpr int U_OFF = HT_OFF + 2 * HT_SZ, U_SZ = 7 * 56 * 4096;
constexpr int X2_OFF = U_OFF + 2 * U_SZ, X2_SZ = 64 * 28 * 1024;
constexpr int S5_OFF = X2_OFF + X2_SZ, S5_SZ = 32 * 8 * 512;
constexpr int WS_BYTES = S5_OFF + S5_SZ;
constexpr int L_WQ = 0;
constexpr int L_KVW = 86016;
constexpr int L_CSP = L_KVW + 65536;
constexpr int L_CS = L_CSP + 1536 + 128;
constexpr int L_HST = L_CS + 384;
constexpr int L_DUMP = L_HST + 8192;
constexpr int LDS_BYTES = L_DUMP + 256;

static_assert(L_HST % 128 == 0 && LDS_BYTES <= 163840, "LDS map");
constexpr int SC1 = 16, OOB = 0x7ffff000;
constexpr int WIN = 12;
constexpr int QPUB = 10;
#define NO2 4
#define NO3 2
#define NG2 3
#define NG3 2
#define NH2 4
#define NH3 3
#define NU3 2
#define KP 5
#define MO2 4
#define MO3 1
#define MG2 3
#define MG3 0
#define NS 5
#define SG 1
#define SH 2
#define XSL 6
constexpr int Z_U = NO2 + NO3 + NG2 + NG3 + NH2 + NH3;
static_assert(Z_U >= 16 && Z_U + 3 + NU3 <= 30 && Z_U + 3 <= 30 - NS && NS <= 5 && SG + SH <= NS, "schedule");

struct Params {
  const void *x, *wqkv, *wo, *w1, *b1, *w2, *b2;
  void *out, *ws;
};

template <int... I, class Fn> DI void sfor_(std::integer_sequence<int, I...>, Fn&& f) { (f(std::integral_constant<int, I>{}), ...); }
template <int N, class Fn> DI void sfor(Fn&& f) { sfor_(std::make_integer_sequence<int, N>{}, f); }
DI float asf(unsigned u) { return __builtin_bit_cast(float, u); }
DI unsigned asu(float f) { return __builtin_bit_cast(unsigned, f); }
DI rsrc_t mk(const void* p, int bytes) { return __builtin_amdgcn_make_buffer_rsrc((void*)p, 0, bytes, 0x00020000); }
template <int AUX> DI v4u ld16(rsrc_t r, int voff, int soff) { return __builtin_amdgcn_raw_buffer_load_b128(r, voff, soff, AUX); }
template <int AUX> DI v2u ld8(rsrc_t r, int voff, int soff) { return __builtin_amdgcn_raw_buffer_load_b64(r, voff, soff, AUX); }
template <int AUX> DI void st16(v4u v, rsrc_t r, int voff, int soff) { __builtin_amdgcn_raw_buffer_store_b128(v, r, voff, soff, AUX); }
template <int AUX> DI void st8(v2u v, rsrc_t r, int voff, int soff) { __builtin_amdgcn_raw_buffer_store_b64(v, r, voff, soff, AUX); }
DI f32x4 mfma16(v4u a, v4u b, f32x4 c) {
  return __builtin_amdgcn_mfma_f32_16x16x32_bf16(__builtin_bit_cast(bf16x8, a), __builtin_bit_cast(bf16x8, b), c, 0, 0, 0);
}
DI f32x16 mfma32(v4u a, v4u b, f32x16 c) {
  return __builtin_amdgcn_mfma_f32_32x32x16_bf16(__builtin_bit_cast(bf16x8, a), __builtin_bit_cast(bf16x8, b), c, 0, 0, 0);
}
DI unsigned pk(float lo, float hi) { return __builtin_bit_cast(unsigned, __builtin_convertvector((f32x2){lo, hi}, bf16x2)); }
DI float bflo(unsigned u) { return asf(u << 16); }
DI float bfhi(unsigned u) { return asf(u & 0xffff0000u); }
DI f32x4 unpack4(v2u v) { return (f32x4){bflo(v.x), bfhi(v.x), bflo(v.y), bfhi(v.y)}; }
DI v2u trrd(LAS char* p) { return __builtin_bit_cast(v2u, __builtin_amdgcn_ds_read_tr16_b64_v4i16((LAS s16x4*)p)); }
DI v4u trfrag(LAS char* p0, LAS char* p1) { v2u a = trrd(p0), b = trrd(p1); return (v4u){a.x, a.y, b.x, b.y}; }
DI float hi32(float v) {
  unsigned u = asu(v);
  auto r = __builtin_amdgcn_permlane32_swap(u, u, false, false);
  return asf(r[1]);
}
DI float sum32(float v) {
  unsigned u = asu(v);
  auto r = __builtin_amdgcn_permlane32_swap(u, u, false, false);
  return asf(r[0]) + asf(r[1]);
}
DI float max32(float v) {
  unsigned u = asu(v);
  auto r = __builtin_amdgcn_permlane32_swap(u, u, false, false);
  return fmaxf(asf(r[0]), asf(r[1]));
}
DI float sum_ab(float a, float b) {
  auto r = __builtin_amdgcn_permlane16_swap(asu(a), asu(b), false, false);
  return sum32(asf(r[0]) + asf(r[1]));
}
DI void stats16(const f32x4 (&v)[4], int n, f32x2& s1, f32x2& s2) {
#pragma unroll
  for (int a = 0; a < 4; ++a)
    if (n >> a & 1) {
      const f32x2 lo = {v[a][0], v[a][1]}, hi = {v[a][2], v[a][3]};
      s1 += lo; s2 += lo * lo; s1 += hi; s2 += hi * hi;
    }
}
#define FENCE() asm volatile("" ::: "memory")
#define SCHED() __builtin_amdgcn_sched_barrier(0)
#define DRAIN() asm volatile("s_waitcnt vmcnt(0)" ::: "memory")
#define PINV(x) asm volatile("" : "+v"(x))
#define PINA(x) asm volatile("" : "+a"(x))
#define PINS(x) asm volatile("" : "+s"(x))
#define KEEP(x) asm volatile("" :: "v"(x))
#define LGKM0() asm volatile("s_waitcnt lgkmcnt(0)" ::: "memory")
DI void bar() { LGKM0(); __builtin_amdgcn_s_barrier(); FENCE(); }
template <int N> DI void vmw() { asm volatile("s_waitcnt vmcnt(%0)" :: "n"(N) : "memory"); }
template <int LO, int MO> DI void dmai(rsrc_t r, int voff, int lds) {
  int so;
  asm volatile("s_add_u32 m0, %1, %4\n\ts_mov_b32 %0, %5\n\tbuffer_load_dwordx4 %2, %3, %0 offen lds"
               : "=&s"(so) : "s"(lds), "v"(voff), "s"(r), "n"(LO), "n"(MO) : "memory", "scc");
}
DI void touch(rsrc_t r, int voff, int soff, LAS char* L) {
  asm volatile("s_mov_b32 m0, %0\n\ts_nop 3\n\tbuffer_load_dword %1, %2, %3 offen lds"
               :: "s"((int)(size_t)(L + L_DUMP)), "v"(voff), "s"(r), "s"(soff) : "memory");
}
DI void publish(GAS unsigned* flag, unsigned epoch) {
  DRAIN();
  __builtin_amdgcn_s_waitcnt(0x0F70);
  bar();
  if (threadIdx.x == 0) __hip_atomic_store(flag, epoch, RLX_AGENT);
  FENCE();
}
#define POLL(f, ptr) asm volatile("global_load_dword %0, %1, off sc1" : "=&v"(f) : "v"(ptr) : "memory")
#define POLLED(f, N, ptr) { \
    unsigned f2_; \
    POLL(f2_, ptr); \
    asm volatile("s_waitcnt vmcnt(%1)" : "+v"(f) : "n"((N) + 1) : "memory"); \
    if (__builtin_amdgcn_ballot_w64(f != epoch) != 0ull) { \
      asm volatile("s_waitcnt vmcnt(0)" : "+v"(f2_) :: "memory"); \
      if (__builtin_amdgcn_ballot_w64(f2_ != epoch) != 0ull) wait_flags(ptr, epoch, budget); \
    } \
    FENCE(); \
    late = f2_; }
DI void wait_flags(GAS unsigned* flag, unsigned epoch, unsigned& budget) {
  FENCE();
  while (true) {
    unsigned f = __hip_atomic_load(flag, RLX_AGENT);
    if (__builtin_amdgcn_ballot_w64(f != epoch) == 0ull) break;
    if (budget == 0u) break;
    --budget;
  }
  __builtin_amdgcn_fence(__ATOMIC_ACQUIRE, "wavefront");
  FENCE();
}

DI int oob(bool in, int off) { return off | (in ? 0 : OOB); }
DI int wrap7(int v) { return v >= 7 ? v - 7 : v; }
DI int swz(int row) { return ((row >> 1) & 1) | (((row >> 3) ^ (row >> 2)) & 1) << 1; }

#define FRESH() \
  int tid = threadIdx.x, bid = blockIdx.x; \
  asm volatile("" : "+v"(tid)); \
  asm volatile("" : "+s"(bid)); \
  const int lane = tid & 63, w = __builtin_amdgcn_readfirstlane(tid >> 6), bx = bid & 7, by = bid >> 3; \
  const int c = lane & 15, g = lane >> 4, q4 = (lane & 15) >> 2, p4 = lane & 3, r32 = lane & 31, hh = lane >> 5; \
  const int by7 = __builtin_amdgcn_readfirstlane((by * 37) >> 8), bym = __builtin_amdgcn_readfirstlane(by - 7 * by7); \
  const int t = 2 * bx + (by >> 4), hd = by & 15;                \
  const int st = 4 * bx + by7, dt = bym;                         \
  const int dt4 = by < 21 ? bym : bym + 4 * (bx & 1), ft = 7 * (bx >> 1) + (by < 21 ? by7 + 4 * (bx & 1) : 3);    \
  LAS char* ring = L + L_KVW + w * 16384; \
  const int wrow = lane >> 3, sx0 = swz(wrow) << 5; \
  auto raddr = [&](int row) { return row * 128 + (swz(row) << 5) + 8 * p4; }; \
  LAS char* red = L + L_KVW + lane * 16; \
  (void)c; (void)g; (void)q4; (void)r32; (void)hh; (void)t; (void)hd; (void)st; (void)dt; (void)dt4; (void)ft; (void)ring; (void)sx0; (void)raddr; (void)red;

#define MFMA32V0(d, a, b) asm volatile("v_mfma_f32_32x32x16_bf16 %0, %1, %2, 0" : "=&v"(d) : "v"(a), "v"(b))
#define MFMA32VC(d, a, b, c) asm volatile("v_mfma_f32_32x32x16_bf16 %0, %1, %2, %3" : "=&v"(d) : "v"(a), "v"(b), "v"(c))
#define MFMA32VA(d, a, b) asm volatile("v_mfma_f32_32x32x16_bf16 %0, %1, %2, %0" : "+v"(d) : "v"(a), "v"(b))
#define MFMA_SETTLE(d) asm volatile("s_nop 11" : "+v"(d))
#define MFMA_SETTLE2(d, e) asm volatile("s_nop 11" : "+v"(d), "+v"(e))
DI float max3(float a, float b, float c) { float r; asm("v_max3_f32 %0, %1, %2, %3" : "=v"(r) : "v"(a), "v"(b), "v"(c)); return r; }

#define WB(cc) (L + L_WQ + (4 * hh + q4) * 64 + (g & 1) * 32 + 8 * p4 + (hd & 1) * 8 + (cc) * 4096)
#define WFRAG(wb, part, e) trfrag((wb) + (part) * 28672 + ((e) >> 1) * 2048 + ((e) & 1) * 512, (wb) + (part) * 28672 + ((e) >> 1) * 2048 + ((e) & 1) * 512 + 1024)
#define CSUM(part, a) (*(LAS f32x4*)(L + L_CS + ((part) * 32 + 8 * (a) + 4 * hh) * 4))
#define WZ_RD1(n, i, sl) wz[n][i] = (n) < 16 ? trfrag(pr1[i] + (sl) * 4096, pr1[(i) ^ 2] + (sl) * 4096 + 512) : trfrag(pr2[i] + (sl) * 4096, pr2[i] + (sl) * 4096 + 2048)
#define DMA_WZ1(n, m, sl) { \
    if ((n) < 16) dmai<(sl) * 4096 + (m) * 1024, (32 * ((n) & 15) + 8 * (m)) * F * 2>(rw1, ((m) & 1) ? pw1 ^ 64 : pw1, pring); \
    else dmai<(sl) * 4096 + (m) * 1024, (32 * ((n) & 15) + 8 * (m)) * S * 2>(rw2, ((m) & 1) ? pw2 ^ 64 : pw2, pring); }
#define PIECE(L0, q) { \
    constexpr int r_ = (q) / 4, m_ = (q) % 4, n_ = (L0) + r_; \
    if constexpr (m_ == 0 && r_ >= 4) asm volatile("s_waitcnt vmcnt(8) lgkmcnt(0)" ::: "memory"); \
    else if constexpr (m_ == 0 && r_ == 3) vmw<8>(); \
    DMA_WZ1(n_, m_, r_ & 3) \
    if constexpr (r_ >= 3) WZ_RD1(n_ - 3, m_, (r_ - 3) & 3); \
    SCHED(); }
#define DPUMP(L0, A, B) sfor<4 * ((B) - (A))>([&](auto I_) INL { PIECE(L0, 4 * ((A) - (L0)) + I_) });
#define STASH(A, B) if constexpr (layer == 1) sfor<4 * ((B) - (A))>([&](auto I_) INL { \
    constexpr int j_ = (A) + I_ / 4, m_ = I_ % 4; \
    dmai<j_ * 4096 + m_ * 1024, (32 * (14 - NS + j_) + 8 * m_) * S * 2>(rw2, (m_ & 1) ? pw2 ^ 64 : pw2, pstash); SCHED(); });
#define DTSTEP(L0, L1, sl) ((L1) - 1 - (((L1) - 1 - (L0) - (sl)) & 3))
#define DTAIL1(L0, L1, j) { if constexpr (DTSTEP(L0, L1, (j) >> 2) >= (L0) && DTSTEP(L0, L1, (j) >> 2) >= (L1) - 3) WZ_RD1(DTSTEP(L0, L1, (j) >> 2), (j) & 3, (j) >> 2); }
#define DTAIL(L0, L1) sfor<16>([&](auto J_) INL { constexpr int j_ = J_; DTAIL1(L0, L1, j_) }); SCHED();
#define REDW(a, b) *(LAS f32x4*)(red + w * 16384 + (((b) - 1) * 4 + (a)) * 1024) = acc[a][b]
#define REDR(a, b) (*(LAS f32x4*)(red + ((w + 4 - (b)) & 3) * 16384 + (((b) - 1) * 4 + (a)) * 1024))
__global__ void __launch_bounds__(256, 1) fused_kernel(Params p) {
  extern __shared__ __attribute__((aligned(16))) char smem[];
  LAS char* L = (LAS char*)smem;
  {
    unsigned long long pc;
    int len;
    asm volatile("s_getpc_b64 %0\n.Lfk_code_from:\n\ts_mov_b32 %1, .Lfk_code_to-.Lfk_code_from" : "=s"(pc), "=s"(len));
    touch(mk((const void*)pc, len > 0 ? len : 0), (threadIdx.x & 63) < 8 ? ((blockIdx.x >> 3) * 4 + (threadIdx.x >> 6) + (threadIdx.x & 7) * 128) * 128 : OOB, 0, L);
  }
  SCHED();
  const bool on35 = (blockIdx.x >> 3) < 28;
  const bool on4 = (blockIdx.x >> 3) < ((blockIdx.x & 1) ? 24 : 25);
  const rsrc_t rws = mk(p.ws, WS_BYTES);
  GAS unsigned* ctrl = (GAS unsigned*)p.ws;
  GAS unsigned* flags = (GAS unsigned*)((GAS char*)p.ws + FLAG_OFF);
  unsigned budget = 1u << 22;

  v4u xf[28];
  v4u kv[4];
  float mean = 0.f, rstd = 0.f;
  f32x4 res[4];
  unsigned epoch;
  int kvs, oo, t1o, wvo2, pw1, pw2, fo;
  unsigned nk1, nk2, pm[8];
  LAS char *kvw, *vw, *vr0, *wor[4], *pr1[4], *pr2[4], *wbq;
#define KVOUT(acc, h) { \
    _Pragma("unroll") for (int i = 0; i < 16; i += 2) kv[2 * (h) + (i >> 3)][(i >> 1) & 3] = pk(acc[i], acc[i + 1]); \
    if (hh) { kv[2 * (h) + 1][2] = (h) ? 0x00003f80u : 0u; kv[2 * (h) + 1][3] = 0u; } \
    const rsrc_t rkv = mk(p.ws, t < 15 ? WS_BYTES : 0);    \
    st16<SC1>(kv[2 * (h)], rkv, kvs + (h) * 2048, 0); \
    st16<SC1>(kv[2 * (h) + 1], rkv, kvs + (h) * 2048 + 1024, 0); \
    SCHED(); }
#define X128(p) ((LAS char*)(size_t)((unsigned)(size_t)(p) ^ 128u))
  {
    FRESH();
    const rsrc_t rwq = mk(p.wqkv, D * 3 * D * 2);
    const rsrc_t rx = mk(p.x, S * D * 2);
    const int gid = by * 4 + w, rg = (int)(size_t)ring;
    touch(rx, oob(lane < 14, (t * 896 + hd * 56 + w * 14 + lane) * 128), 0, L);
    const int xvo = (t * 128 + w * 32 + wrow) * 896 + (((lane & 7) ^ (wrow >> 1)) << 4);
#define DMA_X(cc) sfor<4>([&](auto M) INL { constexpr int m = M; dmai<((cc) & 3) * 4096 + m * 1024, m * 8 * 896 + (cc) * 128>(rx, (m & 1) ? xvo ^ 64 : xvo, rg); })
    DMA_X(0); DMA_X(1);
    touch(rwq, oob(lane < 14, gid * 2688 + 896 + lane * 128), 0, L);
    DMA_X(2);
    SCHED();
    const int tb = gid * 35 + lane, tbr = (tb * 18725) >> 18;
    int tvb = oob(lane < 35, (128 + tbr) * 2688 + 896 + (tb - 14 * tbr) * 128);
    int wvo = (w * 16 + (lane >> 2)) * 2688 + hd * 56 - (hd & 1) * 8 + (lane & 3) * 16;
    const int wlb = (int)(size_t)(L + L_WQ) + w * 1024;
#define DMA_W(part, cc) dmai<(part) * 28672 + (cc) * 4096, (cc) * 64 * 2688 + (part) * 896>(rwq, wvo, wlb)
    LAS char* xa[8];
#pragma unroll
    for (int e = 0; e < 8; ++e) { xa[e] = ring + r32 * 128 + 8 * hh + ((e ^ ((r32 >> 1) & 7)) << 4); PINV(xa[e]); }
    int epo = w == 0 ? FLAG_OFF + (bid - 256) * 128 + lane * 32768 : S3_OFF + (st * 8 + dt) * 1024 - 2 * S3_SZ + (lane >> 3) * S3_SZ + (lane & 7) * 128;
    epo = (w == 0 ? lane - 1u < 10u : w == 1 && on35 && (lane >> 4) == 1) ? epo : 0;
    PINV(tvb); PINV(wvo); PINV(epo);
    SCHED();
    DMA_X(3);
    asm volatile("buffer_load_dword %0, %1, %2, 0 offen sc1" : "=&v"(epoch) : "v"(epo), "s"(rws) : "memory");
    SCHED();
    int tvq, tvw;
    LAS char *cb, *wb;
#define READ_X(cc) _Pragma("unroll") for (int e = 0; e < 4; ++e) { \
      const int ch = (e >> 1) * 4 + (e & 1); \
      const v2u a_ = *(LAS v2u*)(xa[ch] + ((cc) & 3) * 4096), b_ = *(LAS v2u*)(xa[ch + 2] + ((cc) & 3) * 4096); \
      xf[4 * (cc) + e] = (v4u){a_.x, a_.y, b_.x, b_.y}; }
    const v4u ones = {0x3f803f80u, 0x3f803f80u, 0x3f803f80u, 0x3f803f80u};
    f32x16 gs, ss = {};
#define XSTAT(i) { if constexpr ((i) == 0) MFMA32V0(gs, xf[i], xf[i]); else MFMA32VA(gs, xf[i], xf[i]); ss = mfma32(ones, xf[i], ss); }
    vmw<14>(); READ_X(0) LGKM0(); DMA_X(4); XSTAT(0) XSTAT(1) XSTAT(2) XSTAT(3) SCHED();
    vmw<14>(); READ_X(1) LGKM0(); DMA_X(5); XSTAT(4) XSTAT(5) XSTAT(6) XSTAT(7) SCHED();
    vmw<13>(); READ_X(2) LGKM0(); DMA_X(6); SCHED();
    asm volatile("s_waitcnt vmcnt(12)" : "+v"(epoch) :: "memory");
    epoch = __builtin_amdgcn_readfirstlane(epoch) + 1u;
    READ_X(3)
    SCHED();
    sfor<20>([&](auto I) INL {
      constexpr int i = I;
      if constexpr (i == 6) { vmw<8 + 3>(); READ_X(4) }
      if constexpr (i == 10) { vmw<4 + 5 + 1>(); READ_X(5) }
      if constexpr (i == 14) { vmw<7 + 1>(); READ_X(6) }
      if constexpr ((i & 1) == 0 && i < 16) DMA_W(1 + ((i >> 1) & 1), i >> 2);
      if constexpr (i == 7) touch(rwq, tvb, 0, L);
      MFMA32VA(gs, xf[8 + i], xf[8 + i]);
      if constexpr (i < 8) {
        const int d = r32 - ((2 * i & 3) + 8 * (i >> 1)) - 4 * hh;
        pm[i] = ((unsigned)(d >> 31) & 0xffffu) | ((unsigned)((d - 1) >> 31) & 0xffff0000u);
        PINA(pm[i]);
      } else if constexpr (i < 12) { wor[i - 8] = ring + (raddr(16 * (g >> 1) + 4 * (g & 1) + q4) ^ ((i - 8) << 5)); PINA(wor[i - 8]); }
      else if constexpr (i < 16) { pr1[i - 12] = ring + (raddr(8 * g + q4) ^ ((i - 12) << 5)); PINA(pr1[i - 12]); }
      else { pr2[i - 16] = ring + (raddr(4 * g + q4) ^ (((i + w) & 3) << 5)); PINA(pr2[i - 16]); }
      SCHED();
      ss = mfma32(ones, xf[8 + i], ss);
      SCHED();
      if constexpr (i == 0) { kvs = KV_OFF + (hd * 64 + 4 * t + w) * 4096 + lane * 16; PINV(kvs); }
      if constexpr (i == 1) { oo = O_OFF + ((2 * t + (w >> 1)) * 16 + hd) * 4096 + (2 * (w & 1) + (r32 >> 4)) * 1024 + hh * 256 + c * 16; PINA(oo); }
      if constexpr (i == 2) { t1o = oob(lane < 32, (512 * (by >> 3) + 8 * ((by & 7) * 4 + w) + (lane >> 2)) * (F * 2) + (7 * (bx >> 1) + 3 * (bx & 1) + (lane & 3)) * 128); PINA(t1o); }
      if constexpr (i == 3) { wvo2 = ((112 * w + wrow) * 896 + dt * 128 + (lane & 7) * 16) ^ sx0; PINA(wvo2); }
      if constexpr (i == 4) { pw1 = ((512 * w + wrow) * (F * 2) + ft * 128 + (lane & 7) * 16) ^ sx0; PINA(pw1); }
      if constexpr (i == 5) { pw2 = ((448 * w + wrow) * (S * 2) + st * 128 + (lane & 7) * 16) ^ sx0; PINA(pw2); }
      if constexpr (i == 7) {
        const int tp = (t >= 2 && (lane & 1)) ? t - 2 : t > 0 ? t - 1 : 0;
        fo = FLAG_OFF + (((tp & 1) * 16 + hd) * 8 + (tp >> 1)) * 128;
        PINA(fo);
      }
      if constexpr (i == 8) { nk2 = (t < 2 && hh) ? 0xf149u : 0u; nk1 = (t < 1 && hh) ? 0xf149u : 0u; PINA(nk1); PINA(nk2); }
      if constexpr (i == 9) { kvw = L + L_KVW + w * 4096 + lane * 16; vw = L + L_KVW + w * 4096 + 2048 + ((lane * 16) ^ (hh << 6)); PINA(kvw); PINA(vw); }
      if constexpr (i == 11) { vr0 = L + L_KVW + w * 4096 + 2048 + ((((g & 1) * 1024 + (p4 & 1) * 512 + hh * 64 + q4 * 16) ^ ((p4 & 1) << 6) ^ ((g & 1) << 7)) + (p4 >> 1) * 8); PINA(vr0); }
      if constexpr (i == 13) { wbq = WB(0); wb = wbq + 28672; PINV(wbq); PINV(wb); }
      if constexpr (i == 15) { cb = L + L_WQ + 28672 + (w * 16 + 8 * hh + q4) * 64 + (g & 1) * 32 + 8 * p4; PINV(cb); }
      if constexpr (i == 17) {
        const int tl = by * 98 + w * 25 + lane, trow = (tl * 9363) >> 16;
        tvq = oob(lane < 25 && w * 25 + lane < 98, trow * 2688 + (tl - 7 * trow) * 128); tvw = oob(lane < 25 && w * 25 + lane < 98, tl * 128);
        PINV(tvq); PINV(tvw);
      }
      SCHED();
    });
    f32x16 ak, av;
    v4u wk[28], wv[28], wc[14];
    f32x16 csk = {}, csv = {};
    float dg = 0.f, d8[8], d4[4], pp[4];
    f32x4 cc[8];
    LAS float* pb = (LAS float*)(L + L_CSP) + 32 + 16 * w + c + 4 * (hd & 1);
    LAS float* pw = (LAS float*)(L + (hh ? L_DUMP : L_CSP + (w * 96 + 32) * 4)) + r32;
#define PICK4(o, at, a0, a1, a2, a3, b0, b1, b2, b3, m) \
    asm volatile("v_cndmask_b32 %0, %4, %8, %12\n\tv_cndmask_b32 %1, %5, %9, %12\n\tv_cndmask_b32 %2, %6, %10, %12\n\tv_cndmask_b32 %3, %7, %11, %12" \
                 : "=&v"(o[at]), "=&v"(o[(at) + 1]), "=&v"(o[(at) + 2]), "=&v"(o[(at) + 3]) \
                 : "v"(a0), "v"(a1), "v"(a2), "v"(a3), "v"(b0), "v"(b1), "v"(b2), "v"(b3), "s"(m))
    sfor<8>([&](auto R) INL {
      constexpr int r = R;
      if constexpr (r < 7) {
        vmw<(r < 2 ? 7 : 6)>();
        if constexpr (r == 6) {
          wc[12] = trfrag(cb + r * 4096, cb + r * 4096 + 256);
          wc[13] = trfrag(cb + 28672 + r * 4096, cb + 28672 + r * 4096 + 256);
          csk = mfma32(ones, wc[12], csk); csv = mfma32(ones, wc[13], csv);
          pw[0] = csk[0]; pw[32] = csv[0];
          LGKM0();
        }
        __builtin_amdgcn_s_barrier();
        FENCE();
        SCHED();
      }
      sfor<4>([&](auto E) INL {
        constexpr int e = E, kr = 4 * r + e, km = kr - 4;
        if constexpr (r > 0) { if constexpr (km == 0) MFMA32V0(ak, wk[km], xf[km]); else MFMA32VA(ak, wk[km], xf[km]); }
        if constexpr (r < 7) wk[kr] = WFRAG(wb + r * 4096, 0, e);
        if constexpr (r < 6 && (e & 1) == 0) wc[2 * r + e / 2] = trfrag(cb + (e / 2) * 28672 + r * 4096, cb + (e / 2) * 28672 + r * 4096 + 256);
        SCHED();
        if constexpr (r > 0) { if constexpr (km == 0) MFMA32V0(av, wv[km], xf[km]); else MFMA32VA(av, wv[km], xf[km]); }
        if constexpr (r < 7) wv[kr] = WFRAG(wb + r * 4096, 1, e);
        SCHED();
        if constexpr (r < 6 && e == 1) csk = mfma32(ones, wc[2 * r], csk);
        if constexpr (r < 6 && e == 3) csv = mfma32(ones, wc[2 * r + 1], csv);
        SCHED();
        if constexpr (e == 1 || e == 3) {
          if constexpr (r < 3) DMA_W(1 + e / 2, r + 4);
          else if constexpr (r == 3) { if constexpr (e == 1) touch(rwq, tvq, 0, L); else touch(mk(p.wo, D * D * 2), tvw, 0, L); }
          else if constexpr (2 * (r - 4) + e / 2 < 7) DMA_W(0, 2 * (r - 4) + e / 2);
        }
        if constexpr (r == 1 && (e & 1) == 0) { constexpr int j = 2 * e; PICK4(d8, j, gs[j], gs[j + 1], gs[j + 2], gs[j + 3], gs[j + 8], gs[j + 9], gs[j + 10], gs[j + 11], 0xffff0000ffff0000ull); }
        if constexpr (r == 2 && e == 0) PICK4(d4, 0, d8[0], d8[1], d8[2], d8[3], d8[4], d8[5], d8[6], d8[7], 0xff00ff00ff00ff00ull);
        if constexpr (r == 2 && e == 2)
          asm volatile("v_cndmask_b32 %1, %1, %3, %5\n\tv_cndmask_b32 %2, %2, %4, %5\n\tv_cndmask_b32 %0, %1, %2, %6\n\tv_cndmask_b32 %0, 0, %0, %7"
                       : "=&v"(dg), "+v"(d4[0]), "+v"(d4[1]) : "v"(d4[2]), "v"(d4[3]), "s"(0xccccccccccccccccull), "s"(0xaaaaaaaaaaaaaaaaull), "s"(0xf0f0f0f00f0f0f0full));
        if constexpr (r == 3 && e == 0) { mean = ss[0] * (1.f / D); rstd = sum32(dg) * (1.f / D) - mean * mean; PINV(mean); PINV(rstd); }
        if constexpr (r == 3 && e == 2) { rstd = __builtin_amdgcn_rsqf(fmaxf(rstd, 0.f) + EPS); PINV(rstd); }
        if constexpr (r == 4 && e == 0) { PINV(pb); PINV(pw); }
        if constexpr (r == 6 && e == 0) { pp[0] = pb[0]; pp[1] = pb[96]; pp[2] = pb[192]; pp[3] = pb[288]; }
        if constexpr (r == 6 && e == 2) *(LAS float*)(L + L_CS + (32 + 16 * w + c) * 4) = (pp[0] + pp[1]) + (pp[2] + pp[3]);
        if constexpr (r == 6 && e == 3) bar();
        if constexpr (r == 7 && e < 2) { _Pragma("unroll") for (int j = 0; j < 4; ++j) cc[4 * e + j] = CSUM(1, 4 * e + j); }
        SCHED();
      });
    });
    MFMA_SETTLE2(ak, av);
#pragma unroll
    for (int i = 0; i < 16; ++i) ak[i] = (ak[i] - mean * cc[i >> 2][i & 3]) * rstd;
    KVOUT(ak, 0)
#pragma unroll
    for (int i = 0; i < 16; ++i) av[i] = (av[i] - mean * cc[4 + (i >> 2)][i & 3]) * rstd;
    KVOUT(av, 1)
  }

  unsigned b2r;
  v2u bb[4];
  v4u wz[30][4];
  sfor<2>([&](auto LY) INL {
    constexpr int layer = LY;
    constexpr int Y0 = layer ? KP : 0, Y2 = Y0 + (layer ? MO2 : NO2), YG = Y2 + (layer ? MO3 : NO3), YG2 = YG + (layer ? MG2 : NG2),
                  YH = YG2 + (layer ? MG3 : NG3), YE = layer ? 16 : Z_U, YH2 = layer ? YE : YH + NH2, YZ = layer ? 30 - NS : 30, YU3 = layer ? YZ : 30 - NU3;
    static_assert(YH <= YH2, "schedule");
    GAS unsigned* fl = flags + layer * 5 * 256 * 32;
    v4u qf[2];
    v4u wo[4][4];
    v2u rr[4];
    int pring, pstash;
    const rsrc_t rw1 = mk(p.w1, on4 ? S * F * 2 : 0);
    const rsrc_t rw2 = mk(p.w2, on35 ? S * F * 2 : 0);
    {
      const bool hi = threadIdx.x & 32;
      FRESH();
      if constexpr (layer == 0) {
        vmw<4>();
        if (t == 15) vmw<0>();
        __builtin_amdgcn_s_barrier();
        FENCE();
      }
      v4u wc[7];
      f32x16 cs = {};
      LAS char* cb = L + L_WQ + (w * 16 + 8 * hh + q4) * 64 + (g & 1) * 32 + 8 * p4;
      v4u wq[28];
#pragma unroll
      for (int ks = 0; ks < 4; ++ks) wq[ks] = WFRAG(wbq, 0, ks);
      f32x16 aq;
      const rsrc_t rwo = mk(p.wo, on35 ? D * D * 2 : 0);
      pring = (int)(size_t)ring;
      pstash = (int)(size_t)(L + L_WQ) + w * 20480;
      PINS(pring); PINS(pstash);
      GAS unsigned* fp = (GAS unsigned*)((GAS char*)p.ws + fo);
      unsigned pf;
      sfor<28>([&](auto I) INL {
        constexpr int ks = I;
        if constexpr (ks == 0) MFMA32V0(aq, wq[ks], xf[ks]); else MFMA32VA(aq, wq[ks], xf[ks]);
        if constexpr (ks + 4 < 28) wq[ks + 4] = WFRAG(wbq + ((ks + 4) >> 2) * 4096, 0, ks & 3);
        constexpr int wi = ks - QPUB - 1;
        if constexpr (wi >= 0 && wi < 16) dmai<wi * 1024, (28 * (wi >> 2) + 8 * (wi & 3)) * 896>(rwo, (wi & 1) ? wvo2 ^ 64 : wvo2, pring);
        if constexpr (layer == 0) {
          if constexpr (ks < 7) wc[ks] = trfrag(cb + ks * 4096, cb + ks * 4096 + 256);
          if constexpr (ks >= 2 && ks < 9) cs = mfma32((v4u){0x3f803f80u, 0x3f803f80u, 0x3f803f80u, 0x3f803f80u}, wc[ks - 2], cs);
          if constexpr (ks == QPUB + 1) { _Pragma("unroll") for (int q = 0; q < 4; ++q) cs[q] = ((LAS float*)(L + L_CSP) + r32 + 4 * (hd & 1))[96 * q]; }
          if constexpr (ks == QPUB + 5) *(LAS float*)(L + L_CS + r32 * 4) = (cs[0] + cs[1]) + (cs[2] + cs[3]);
        }
        if constexpr (ks == 27) POLL(pf, fp);
        if constexpr (ks == QPUB) {
          if constexpr (layer == 0) { *(LAS float*)(L + (hh ? L_DUMP : L_CSP + w * 96 * 4) + r32 * 4) = cs[0]; }
          publish(fl + (0 * 256 + bid) * 32, epoch);
        }
        SCHED();
      });
      f32x4 cq[4];
#pragma unroll
      for (int a = 0; a < 4; ++a) cq[a] = CSUM(0, a);
      MFMA_SETTLE(aq);
      const float qs = rstd * (0.18898223650461362f * 1.4426950408889634f);
#pragma unroll
      for (int a = 0; a < 4; ++a) {
        qf[a >> 1][(a & 1) * 2] = pk((aq[4 * a] - mean * cq[a][0]) * qs, (aq[4 * a + 1] - mean * cq[a][1]) * qs);
        qf[a >> 1][(a & 1) * 2 + 1] = pk((aq[4 * a + 2] - mean * cq[a][2]) * qs, (aq[4 * a + 3] - mean * cq[a][3]) * qs);
      }
      if (hi) { qf[1][2] = 0x00003f80u; qf[1][3] = 0u; }
      PINV(qf[0]); PINV(qf[1]);
      f32x16 s8;
      asm volatile("s_nop 1" : "+v"(qf[0]), "+v"(qf[1]));
      MFMA32V0(s8, kv[0], qf[0]);
      MFMA32VA(s8, kv[1], qf[1]);
      SCHED();
      unsigned late;
      POLLED(pf, 0, fp)
      v4u ra[4], rb[4];
      {
        const rsrc_t r1 = mk(p.ws, t >= 1 ? WS_BYTES : 0), r2 = mk(p.ws, t >= 2 ? WS_BYTES : 0);
        const int kvo = kvs - 32768;
        rb[0] = ld16<SC1>(r2, kvo, 0); rb[1] = ld16<SC1>(r2, kvo, 1024);
        ra[0] = ld16<SC1>(r1, kvo, 16384); ra[1] = ld16<SC1>(r1, kvo, 16384 + 1024);
        SCHED();
        rb[2] = ld16<SC1>(r2, kvo, 2048); rb[3] = ld16<SC1>(r2, kvo, 3072);
        ra[2] = ld16<SC1>(r1, kvo, 16384 + 2048); ra[3] = ld16<SC1>(r1, kvo, 16384 + 3072);
        SCHED();
      }
#pragma unroll
      for (int j = 0; j < 16; ++j) wo[j >> 2][j & 3] = trfrag(wor[j & 3] + (j >> 2) * 4096, wor[(j & 3) ^ 2] + (j >> 2) * 4096 + 1024);
      SCHED();
      bar();
      LAS char* vw1 = X128(vw) + 1024;
      LAS char* vr1 = X128(vr0);
      *(LAS v4u*)(kvw + 32768) = kv[0];
      *(LAS v4u*)(kvw + 32768 + 1024) = kv[1];
      *(LAS v4u*)(vw + 32768) = kv[2];
      *(LAS v4u*)(vw1 + 32768) = kv[3];
      int rro = (64 * st + 16 * w + c) * 896 + dt * 128 + g * 8, b2o = (64 * st + 16 * w + c) * 2, b1o = ft * 128 + g * 8;
      unsigned pml[8], mk32[16], nk1l = nk1, nk2l = nk2;
      int ool = oo, t1l = t1o;
      LAS char* vr0l = vr0;
      if constexpr (layer == 0) { PINV(rro); PINV(b2o); PINV(b1o); }
      PINV(vw1); PINV(vr1); PINV(vr0l); PINV(ool); PINV(t1l); PINV(nk1l); PINV(nk2l);
#pragma unroll
      for (int i = 0; i < 8; ++i) { pml[i] = pm[i]; PINV(pml[i]); mk32[2 * i] = (unsigned)((int)(pml[i] << 16) >> 16); mk32[2 * i + 1] = (unsigned)((int)pml[i] >> 16); PINV(mk32[2 * i]); PINV(mk32[2 * i + 1]); }
      SCHED();
      f32x16 sc[8], se;
      v4u kf[8][2];
      float mx = -1e30f, mx2 = -1e30f;
#define MAX8(v, i) asm volatile("v_max3_f32 %0, %0, %2, %3\n\tv_max3_f32 %1, %1, %4, %5\n\tv_max3_f32 %0, %0, %6, %7\n\tv_max3_f32 %1, %1, %8, %9" \
        : "+v"(mx), "+v"(mx2) : "v"(v[i]), "v"(v[(i) + 1]), "v"(v[(i) + 2]), "v"(v[(i) + 3]), "v"(v[(i) + 4]), "v"(v[(i) + 5]), "v"(v[(i) + 6]), "v"(v[(i) + 7]))
      *(LAS v4u*)(kvw) = rb[0];
      MFMA32V0(sc[0], rb[0], qf[0]);
      rb[1][2] |= nk2l;
      *(LAS v4u*)(kvw + 1024) = rb[1];
      *(LAS v4u*)(kvw + 16384) = ra[0];
      SCHED();
      MFMA32VA(sc[0], rb[1], qf[1]);
      ra[1][2] |= nk1l;
      *(LAS v4u*)(kvw + 16384 + 1024) = ra[1];
      kf[4][0] = ra[0]; kf[4][1] = ra[1];
      MFMA32V0(sc[4], kf[4][0], qf[0]);
      bar();
      KEEP(late);
      MFMA32VA(sc[4], kf[4][1], qf[1]);
#pragma unroll
      for (int j = 1; j < 8; ++j) if (j != 4) { kf[j][0] = *(LAS v4u*)(kvw + j * 4096); kf[j][1] = *(LAS v4u*)(kvw + j * 4096 + 1024); }
      PINV(sc[0]);
#pragma unroll
      for (int i = 0; i < 16; ++i) se[i] = asf((asu(sc[0][i]) & mk32[i]) | (asu(s8[i]) & ~mk32[i]));
      PINV(se);
      SCHED();
      sfor<12>([&](auto N) INL {
        constexpr int n = N, jj = n >> 1, j = jj < 3 ? jj + 1 : jj + 2, hf = n & 1;
        if constexpr (hf == 0) MFMA32V0(sc[j], kf[j][0], qf[0]); else MFMA32VA(sc[j], kf[j][1], qf[1]);
        if constexpr (n < 2) MAX8(se, 8 * hf);
        else if constexpr (n < 4) MAX8(sc[4], 8 * hf);
        else if constexpr (n >= 6) MAX8(sc[jj - 2], 8 * hf);
        if constexpr (n == 4) { *(LAS v4u*)(vw) = rb[2]; *(LAS v4u*)(vw1) = rb[3]; }
        if constexpr (n == 5) { *(LAS v4u*)(vw + 16384) = ra[2]; *(LAS v4u*)(vw1 + 16384) = ra[3]; SCHED(); touch(mk(p.w1, S * F * 2), t1l, layer * KP * 32 * F * 2, L); }
        SCHED();
      });
      MAX8(sc[5], 0); MAX8(sc[5], 8); MAX8(sc[6], 0); MAX8(sc[6], 8); MAX8(sc[7], 0); MAX8(sc[7], 8);
      mx = max32(max3(mx, mx2, mx2));
      f32x16 nm;
#pragma unroll
      for (int i = 0; i < 16; ++i) nm[i] = -mx;
      PINV(nm);
      bar();
      f32x16 ao;
      v4u vf[9][2], pp[9][2];
      float ex[32][4];
#define VF(j, s) vf[j][s] = trfrag(vr0l + (j) * 4096 + (s) * 256, vr1 + (j) * 4096 + (s) * 256)
      sfor<33>([&](auto N) INL {
        constexpr int n = N, j = n >> 2, q = n & 3;
        if constexpr (j == 0) {
          if constexpr (q == 0) MFMA32VC(sc[1], kf[1][0], qf[0], nm);
          if constexpr (q == 1) MFMA32VA(sc[1], kf[1][1], qf[1]);
          if constexpr (q == 2) MFMA32VC(sc[2], kf[2][0], qf[0], nm);
          if constexpr (q == 3) MFMA32VA(sc[2], kf[2][1], qf[1]);
        } else if constexpr (j < 8) {
          if constexpr (q == 1) { if constexpr (j == 1) MFMA32V0(ao, vf[0][0], pp[0][0]); else MFMA32VA(ao, vf[j - 1][0], pp[j - 1][0]); }
          if constexpr (q == 2) MFMA32VA(ao, vf[j - 1][1], pp[j - 1][1]);
          if constexpr (j + 2 < 8) {
            if constexpr (q == 0) MFMA32VC(sc[j + 2], kf[j + 2][0], qf[0], nm);
            if constexpr (q == 3) MFMA32VA(sc[j + 2], kf[j + 2][1], qf[1]);
          } else if constexpr (j == 6) {
            if constexpr (q == 0) MFMA32VA(ao, vf[8][0], pp[8][0]);
            if constexpr (q == 3) MFMA32VA(ao, vf[8][1], pp[8][1]);
          }
        } else MFMA32VA(ao, vf[7][0], pp[7][0]);
        SCHED();
        if constexpr (n < 32) {
#pragma unroll
          for (int i = 0; i < 4; ++i) { if constexpr (j == 0) ex[n][i] = __builtin_amdgcn_exp2f(se[4 * q + i] - mx); else ex[n][i] = __builtin_amdgcn_exp2f(sc[j][4 * q + i]); }
        }
        SCHED();
        if constexpr (n > 0) {
          constexpr int jp = (n - 1) >> 2, qp = (n - 1) & 3;
#pragma unroll
          for (int i = 0; i < 2; ++i) {
            const unsigned u = pk(ex[n - 1][2 * i], ex[n - 1][2 * i + 1]);
            if constexpr (jp == 0) { pp[0][qp >> 1][(2 * qp + i) & 3] = u & pml[2 * qp + i]; pp[8][qp >> 1][(2 * qp + i) & 3] = u & ~pml[2 * qp + i]; }
            else pp[jp][qp >> 1][(2 * qp + i) & 3] = u;
          }
        }
        if constexpr (layer == 0) {
          if constexpr (n >= 2 && n < 10 && q >= 2) rr[(n >> 2) * 2 + q - 2] = ld8<0>(mk(p.x, on35 ? S * D * 2 : 0), rro, ((n >> 2) * 2 + q - 2) * 32);
          if constexpr (n == 10) b2r = (unsigned)__builtin_amdgcn_raw_buffer_load_b16(mk(p.b2, S * 2), b2o, 0, 0);
          if constexpr (n >= 14 && n < 22 && q >= 2) bb[((n >> 2) - 3) * 2 + q - 2] = ld8<0>(mk(p.b1, F * 2), b1o, (((n >> 2) - 3) * 2 + q - 2) * 32);
        }
        if constexpr (n == 22) KEEP(nm);
        if constexpr (j < 8 && q < 2) VF(j, q);
        if constexpr (j == 5 && q >= 2) VF(8, q - 2);
        SCHED();
      });
      asm volatile("s_nop 1" : "+v"(pp[7][1]));
      MFMA32VA(ao, vf[7][1], pp[7][1]);
      MFMA_SETTLE(ao);
      const float inv = __builtin_amdgcn_rcpf(hi32(ao[12]));
      v4u of[2];
#pragma unroll
      for (int i = 0; i < 16; i += 2) of[i >> 3][(i >> 1) & 3] = pk(ao[i] * inv, ao[i + 1] * inv);
      if (hi) { of[1][2] = 0u; of[1][3] = 0u; }
      st16<SC1>(of[0], rws, ool, 0);
      st16<SC1>(of[1], rws, ool, 512);
      SCHED();
    }
    if (on35) {
      {
        FRESH();
        const int tq = st >> 1;
        GAS unsigned* fp = fl + (1 * 256 + (((tq & 1) * 16 + 4 * w + (lane & 3)) * 8 + (tq >> 1))) * 32;
        int oo = O_OFF + layer * O_SZ + (st * 16 + 4 * w) * 4096 + lane * 16;
        const int sb = S3_OFF + layer * S3_SZ + st * 8192 + (16 * w + c) * 16;
        int sw = oob(g < 2, sb + dt * 1024 + g * 8), sa = sb + g * 1024, sc = oob(g < 3, sb + (g + 4) * 1024);
        int ho = HT_OFF + layer * HT_SZ + (dt * 64 + 2 * st + (w >> 1)) * 4096 + (g >> 1) * 1024 + (2 * (w & 1) + (g & 1)) * 256 + c * 16;
        const int hr = 8 * (g & 1) + q4, hst = L_HST + w * 2048;
        int h0 = hst + (raddr(hr) ^ ((g >> 1) << 5)), h1 = hst + (raddr(hr + 4) ^ ((g >> 1) << 5)), hw = hst + c * 128 + (swz(c) << 5) + g * 8;
        int ob[4];
#pragma unroll
        for (int b = 0; b < 4; ++b) { ob[b] = ((b + w) & 3) * 1024; PINS(ob[b]); }
        PINV(fp); PINV(oo); PINV(sw); PINV(sa); PINV(sc); PINV(ho); PINV(h0); PINV(h1); PINV(hw); PINV(red);
        publish(fl + (1 * 256 + bid) * 32, epoch);
        unsigned pf;
        DPUMP(Y0, Y0, Y2)
        if constexpr (layer == 0) {
#pragma unroll
          for (int a = 0; a < 4; ++a) { res[a] = unpack4(rr[a]); PINV(res[a]); }
        }
        POLL(pf, fp);
        DPUMP(Y0, Y2, YG)
        unsigned late;
        POLLED(pf, 4 * (YG - Y2), fp)
        v4u of[16];
#define LOAD_O(I) of[I] = ld16<SC1>(rws, oo + ((I) & 3) * 4096, ob[(((I) >> 2) + 1) & 3])
#pragma unroll
        for (int i = 0; i < 12; ++i) { LOAD_O(i); SCHED(); }
        vmw<12>();
        KEEP(late);
        f32x4 acc[4][4], part[12];
        sfor<16>([&](auto I) INL {
          constexpr int i = I, b = ((i >> 2) + 1) & 3, k = i & 3;
#pragma unroll
          for (int a = 0; a < 4; ++a) {
            acc[a][b] = mfma16(wo[k][a], of[i], k > 0 ? acc[a][b] : (f32x4){0.f, 0.f, 0.f, 0.f});
            SCHED();
            if constexpr (i < 4) { if (a == 0) LOAD_O(i + 12); }
            if constexpr (i < 12) { if (a == 0) DTAIL1(Y0, YG, i) }
            if constexpr (i >= 4 && k < 2) { if (a == 1 || a == 3) REDW(2 * k + (a >> 1), i >> 2); }
            if constexpr (i == 14) { if (a == 0) bar(); }
            if constexpr (i >= 14) { if (a < 3) { const int j = 6 * (i - 14) + 2 * a; part[j] = REDR(j / 3, j % 3 + 1); part[j + 1] = REDR((j + 1) / 3, (j + 1) % 3 + 1); } }
            SCHED();
          }
        });
#pragma unroll
        for (int a = 0; a < 4; ++a) res[a] = ((res[a] + part[3 * a]) + (part[3 * a + 1] + part[3 * a + 2])) + acc[a][0];
        f32x2 s1 = {0.f, 0.f}, s2 = {0.f, 0.f};
        stats16(res, 15, s1, s2);
        st8<SC1>((v2u){asu(sum_ab(s1[0] + s1[1], s2[0] + s2[1])), epoch}, rws, sw, 0);
        SCHED();
        DTAIL1(Y0, YG, 12) DTAIL1(Y0, YG, 13) DTAIL1(Y0, YG, 14) DTAIL1(Y0, YG, 15)
        bar();
        DPUMP(YG, YG, YG2)
        v4u ga, gb;
        asm volatile("buffer_load_dwordx4 %0, %2, %4, 0 offen sc1\n\tbuffer_load_dwordx4 %1, %3, %4, 0 offen sc1" : "=&v"(ga), "=&v"(gb) : "v"(sa), "v"(sc), "s"(rws) : "memory");
        DPUMP(YG, YG2, YH)
        STASH(0, SG)
        asm volatile("s_waitcnt vmcnt(%2)" : "+v"(ga), "+v"(gb) : "n"(4 * (YH - YG2 + layer * SG)) : "memory");
        while ((__builtin_amdgcn_ballot_w64((ga.y != epoch) | (ga.w != epoch)) | (__builtin_amdgcn_ballot_w64((gb.y != epoch) | (gb.w != epoch)) << 16)) != 0ull) {
          if (budget == 0u) break;
          --budget;
          asm volatile("buffer_load_dwordx4 %0, %2, %4, 0 offen sc1\n\tbuffer_load_dwordx4 %1, %3, %4, 0 offen sc1\n\ts_waitcnt vmcnt(0)"
                       : "=&v"(ga), "=&v"(gb) : "v"(sa), "v"(sc), "s"(rws) : "memory");
        }
        const unsigned t12 = asu(sum_ab(asf(ga.x) + asf(gb.x), asf(ga.z) + asf(gb.z)));
        const auto tt = __builtin_amdgcn_permlane16_swap(t12, t12, false, false);
        const float t1 = asf(tt[0]), t2 = asf(tt[1]);
        const float m = t1 * (1.f / D), var = fmaxf(t2 * (1.f / D) - m * m, 0.f);
        const float r1 = __builtin_amdgcn_rsqf(var + EPS);
        const float r = r1 * __builtin_amdgcn_rsqf(var * r1 * r1 + EPS), nr = -m * r;
#pragma unroll
        for (int a = 0; a < 4; ++a) {
          const v2u v = {pk(res[a][0] * r + nr, res[a][1] * r + nr), pk(res[a][2] * r + nr, res[a][3] * r + nr)};
          *(LAS v2u*)(L + (hw ^ (a << 5))) = v;
        }
#pragma unroll
        for (int e = 0; e < 2; ++e) st16<SC1>(trfrag(L + (h0 ^ (e << 6)), L + (h1 ^ (e << 6))), rws, ho, e * 2048);
        SCHED();
      }
      if (on4) {
        FRESH();
        const int sp = 8 * w + (lane & 7);
        GAS unsigned* fp = fl + (2 * 256 + ((sp & 3) * 7 + dt4) * 8 + (sp >> 2)) * 32;
        int hv[4];
#pragma unroll
        for (int b = 0; b < 4; ++b) { hv[b] = HT_OFF + layer * HT_SZ + (dt4 * 64 + 16 * w) * 4096 + ((b + w) & 3) * 1024 + lane * 16; PINV(hv[b]); }
        int uo = U_OFF + layer * U_SZ + (dt4 * 56 + 2 * ft) * 4096 + w * 1024 + lane * 16;
        PINV(fp); PINV(uo); PINV(red);
        publish(fl + (2 * 256 + bid) * 32, epoch);
        unsigned pf;
        DPUMP(YG, YH, YH2)
        STASH(SG, NS - SH)
        POLL(pf, fp);
        DPUMP(YG, YH2, YE)
        STASH(NS - SH, NS)
        unsigned late;
        POLLED(pf, 4 * (YE - YH2 + layer * SH), fp)
        v4u af[64];
#define LOAD_H(I) { int so_; asm volatile("s_mov_b32 %1, %4\n\tbuffer_load_dwordx4 %0, %2, %3, %1 offen sc1" \
          : "=&v"(af[I]), "=&s"(so_) : "v"(hv[(((I) >> 4) + 1) & 3]), "s"(rws), "n"(((I) & 15) * 4096) : "memory"); }
        sfor<WIN>([&](auto I) INL { LOAD_H(I) SCHED(); });
        f32x4 acc[4][4], part[12];
        sfor<64>([&](auto I) INL {
          constexpr int i = I, b = ((i >> 4) + 1) & 3, k = i & 15;
          constexpr int np = i <= 54 ? 0 : i <= 62 ? i - 54 : 10;
          asm volatile("s_waitcnt vmcnt(%1)" : "+v"(af[i]) : "n"((i + WIN <= 64 ? WIN - 1 : 63 - i) + np) : "memory");
          if constexpr (i == 0) KEEP(late);
          SCHED();
          sfor<4>([&](auto A) INL {
            constexpr int a = A;
            acc[a][b] = mfma16(wz[k][a], af[i], k > 0 ? acc[a][b] : (f32x4){0.f, 0.f, 0.f, 0.f});
            SCHED();
            if constexpr (i + WIN < 64 && a == 0) LOAD_H(i + WIN)
            if constexpr (i < 8) { if constexpr (a == 1) DTAIL1(YG, YE, 2 * i) if constexpr (a == 2) DTAIL1(YG, YE, 2 * i + 1) }
            if constexpr (i >= 16 && k < 2 && (a & 1)) REDW(2 * k + (a >> 1), i >> 4);
            if constexpr (i == 50 && a == 3) bar();
            if constexpr (i >= 51 && i < 54) part[4 * (i - 51) + a] = REDR((4 * (i - 51) + a) / 3, (4 * (i - 51) + a) % 3 + 1);
            if constexpr (i == 54 && a == 1) bar();
            if constexpr (i >= 54 && a == 2) PIECE(Z_U, np)
            if constexpr (i >= 62 && a == 3) PIECE(Z_U, np + 1)
            if constexpr (i >= 55 && i < 59 && a == 0) {
              constexpr int q = i - 55;
              part[3 * q] = (part[3 * q] + unpack4(bb[q])) + (part[3 * q + 1] + part[3 * q + 2]);
              PINV(part[3 * q]);
            }
            SCHED();
          });
        });
        v4u uf[2];
        sfor<8>([&](auto H) INL {
          constexpr int h = H, a = h >> 1, j0 = 2 * (h & 1);
          float gl[2];
#pragma unroll
          for (int j = 0; j < 2; ++j) {
            const float u = acc[a][0][j0 + j] + part[3 * a][j0 + j];
            const float e = __builtin_amdgcn_exp2f(u * (u * u * (-0.10294324f) - 2.3022082f));
            gl[j] = u * __builtin_amdgcn_rcpf(1.f + e);
          }
          uf[h >> 2][h & 3] = pk(gl[0], gl[1]);
          if constexpr (h & 1) {
            PINV(uf[h >> 2]);
            if constexpr (h == 3) st16<SC1>(uf[0], rws, uo, 0);
            if constexpr (h == 7) st16<SC1>(uf[1], rws, uo, 4096);
            SCHED();
          }
        });
      } else {
        publish(fl + (2 * 256 + blockIdx.x) * 32, epoch);
        DPUMP(YG, YH, YE)
        STASH(SG, NS)
        DRAIN();
        DTAIL(YG, YE)
        LGKM0();
        DPUMP(Z_U, Z_U, Z_U + 3)
      }
      {
        FRESH();
        const int pi = wrap7(lane & 7);
        GAS unsigned* fp = fl + (3 * 256 + (pi == 3 ? (21 + (dt & 3)) * 8 + (dt >> 2) : (7 * (pi & 3) + dt) * 8 + (pi >> 2)) + 2 * w) * 32;
        int uv = U_OFF + layer * U_SZ + (dt * 56 + 14 * w) * 4096 + lane * 16;
        int ow = L_HST + w * 2048 + c * 128 + (swz(c) << 5) + g * 8, orr = L_HST + w * 2048 + ((wrow * 128 + (lane & 7) * 16) ^ sx0);
        int xb = layer ? (64 * st + 16 * w + wrow) * 896 + dt * 128 + (lane & 7) * 16
                       : X2_OFF + (2 * st + (w >> 1)) * 28672 + (4 * dt + (g >> 1)) * 1024 + ((g & 1) * 32 + (w & 1) * 16 + c) * 16;
        int so = oob(g < 2, S5_OFF + (st * 8 + dt) * 512 + (16 * w + c) * 8 + g * 4);
#pragma unroll
        for (int a = 0; a < 4; ++a) { res[a] += bflo(b2r); PINV(res[a]); }
        PINV(fp); PINV(uv); PINV(xb); PINV(red);
        if constexpr (layer) { PINV(ow); PINV(orr); } else PINV(so);
        if constexpr (layer == 1) {
          const int dlt = L_WQ + w * 20480 - (L_KVW + w * 16384);
#pragma unroll
          for (int i = 0; i < 4; ++i) {
            LAS char* ps = pr2[i] + dlt;
#pragma unroll
            for (int j = 0; j < NS; ++j) wz[30 - NS + j][i] = trfrag(ps + j * 4096, ps + j * 4096 + 2048);
          }
          SCHED();
        }
        if (on4) publish(fl + (3 * 256 + bid) * 32, epoch);
        unsigned pf;
        DPUMP(Z_U, Z_U + 3, YU3)
        POLL(pf, fp);
        DPUMP(Z_U, YU3, YZ)
        unsigned late;
        POLLED(pf, 4 * (YZ - YU3), fp)
        v4u af[56];
#define LOAD_U(I) af[I] = ld16<SC1>(rws, uv + ((I) / 14) * 1024, ((I) % 14) * 4096)
#pragma unroll
        for (int i = 0; i < WIN; ++i) { LOAD_U(i); SCHED(); }
        vmw<WIN>();
        KEEP(late);
        f32x4 acc[4][4], part[4][3];
        v2u xo[4];
        f32x2 s1 = {0.f, 0.f}, s2 = {0.f, 0.f};
        sfor<56>([&](auto I) INL {
          constexpr int i = I, a = i / 14, k = i % 14;
#pragma unroll
          for (int bq = 0; bq < 4; ++bq) {
            const int b = (bq + 1) & 3;
            if (a < 3 || b) acc[a][b] = mfma16(af[i], wz[16 + k][b], k > 0 ? acc[a][b] : (f32x4){0.f, 0.f, 0.f, 0.f});
            SCHED();
            if constexpr (i + WIN < 56) { if (bq == 0) LOAD_U(i + WIN); }
            if constexpr (i < 8) { if (bq == 1) DTAIL1(Z_U, YZ, 2 * i) if (bq == 2) DTAIL1(Z_U, YZ, 2 * i + 1) }
            if constexpr (a > 0 && k < 3) { if (bq == 2) REDW(a - 1, k + 1); }
            if constexpr (i == 46) { if (bq == 2) bar(); }
            if constexpr (i >= 47 && i < 50) { if (bq < 3) part[i - 47][bq] = REDR(i - 47, bq + 1); }
            if constexpr (i >= 50 && i < 53) { if (bq == 2) { res[i - 50] = ((res[i - 50] + part[i - 50][0]) + (part[i - 50][1] + part[i - 50][2])) + acc[i - 50][0]; PINV(res[i - 50]); } }
            if constexpr (i >= 53) { if (bq == 2) { xo[i - 53] = (v2u){pk(res[i - 53][0], res[i - 53][1]), pk(res[i - 53][2], res[i - 53][3])}; PINV(xo[i - 53]); if constexpr (layer == 0) { stats16(res, 1 << (i - 53), s1, s2); PINV(s1); PINV(s2); } } }
            SCHED();
          }
        });
        sfor<14>([&](auto K) INL {
          constexpr int k = K;
          acc[3][0] = mfma16(af[42 + k], wz[16 + k][0], k > 0 ? acc[3][0] : (f32x4){0.f, 0.f, 0.f, 0.f});
          SCHED();
          if constexpr (k >= 1 && k < 4) REDW(3, k);
          if constexpr (k == 5) {
            if constexpr (layer == 0) st16<SC1>((v4u){xo[0].x, xo[0].y, xo[1].x, xo[1].y}, rws, xb, 0);
            else { _Pragma("unroll") for (int a = 0; a < 3; ++a) *(LAS v2u*)(L + (ow ^ (a << 5))) = xo[a]; }
          }
          if constexpr (k == 7) { bar(); part[3][0] = REDR(3, 1); part[3][1] = REDR(3, 2); part[3][2] = REDR(3, 3); }
          SCHED();
        });
        res[3] = ((res[3] + part[3][0]) + (part[3][1] + part[3][2])) + acc[3][0];
        xo[3] = (v2u){pk(res[3][0], res[3][1]), pk(res[3][2], res[3][3])};
        if constexpr (layer == 0) {
          st16<SC1>((v4u){xo[2].x, xo[2].y, xo[3].x, xo[3].y}, rws, xb, 2048);
          SCHED();
          stats16(res, 8, s1, s2);
          __builtin_amdgcn_raw_buffer_store_b32(asu(sum_ab(s1[0] + s1[1], s2[0] + s2[1])), rws, so, 0, SC1);
          SCHED();
        } else {
          const rsrc_t ro = mk(p.out, S * D * 2);
          *(LAS v2u*)(L + (ow ^ (3 << 5))) = xo[3];
          const v4u r0 = *(LAS v4u*)(L + orr), r1 = *(LAS v4u*)(L + (orr ^ 64) + 1024);
          st16<SC1>(r0, ro, xb, 0);
          st16<SC1>(r1, ro, xb, 8 * 896);
        }
      }
    } else {
      publish(fl + (1 * 256 + blockIdx.x) * 32, epoch);
      FRESH();
      const int rw = 4 * (by - 28) + (lane >> 4) + 16 * ((lane >> 2) & 3);
      const int v1 = (512 * w + 256 + rw) * (F * 2) + (7 * (bx >> 1) + 3 * (bx & 1) + (lane & 3)) * 128;
#pragma unroll 1
      for (int i = layer * (KP / 2); i < 4; ++i) { touch(mk(p.w1, S * F * 2), v1, i * 64 * F * 2, L); vmw<1>(); }
      const int v2 = (448 * w + rw) * (S * 2) + bx * 512 + (lane & 3) * 128;
#pragma unroll 1
      for (int i = 0; i < (layer ? 7 - (Z_U - 16) / 2 : 7); ++i) { touch(mk(p.w2, S * F * 2), v2, (layer ? (i < 3 ? i + 4 : i - 3 + (Z_U - 16) / 2) : i) * 64 * S * 2, L); vmw<1>(); }
      DRAIN();
    }
    if constexpr (layer == 0) {
      FRESH();
      const int st1 = 2 * t + (w >> 1);
      f32x16 ak, av;
      v4u wk[28], wv[28];
      LAS char* wb = WB(0) + 28672;
      PINV(wb);
      wk[0] = WFRAG(wb, 0, 0); wv[0] = WFRAG(wb, 1, 0); wk[1] = WFRAG(wb, 0, 1); wv[1] = WFRAG(wb, 1, 1);
      f32x4 ck[4], cv[4];
#pragma unroll
      for (int a = 0; a < 4; ++a) { ck[a] = CSUM(1, a); cv[a] = CSUM(2, a); PINV(ck[a]); PINV(cv[a]); }
      int xo = X2_OFF + (4 * t + w) * 28672 + lane * 16, so = S5_OFF + (st1 * 8 + hh * 4) * 512 + ((w & 1) * 32 + r32) * 8;
      int so3 = oob(!hh, so + 3 * 512);
      PINV(xo); PINV(so); PINV(so3);
      kvs += KV_SZ; oo += O_SZ; fo += 5 * 256 * 128;
      PINV(kvs); PINA(oo); PINA(fo);
      GAS unsigned* fp = flags + (4 * 256 + ((st1 & 3) * 7 + wrap7(lane & 7)) * 8 + (st1 >> 2)) * 32;
      PINV(fp);
      if (on35) publish(flags + (4 * 256 + bid) * 32, epoch);
      __builtin_amdgcn_s_sleep(XSL);
      wait_flags(fp, epoch, budget);
      v2u sp[4];
#define LOAD_X(I) xf[I] = ld16<SC1>(rws, xo + ((I) & 3) * 1024, ((I) >> 2) * 4096)
#pragma unroll
      for (int i = 0; i < WIN; ++i) { LOAD_X(i); SCHED(); }
      sfor<28>([&](auto I) INL {
        constexpr int ks = I;
        if constexpr (ks == 0) MFMA32V0(ak, wk[ks], xf[ks]); else MFMA32VA(ak, wk[ks], xf[ks]);
        if constexpr (ks + 2 < 28) wk[ks + 2] = WFRAG(wb + ((ks + 2) >> 2) * 4096, 0, (ks + 2) & 3);
        if constexpr (ks + WIN < 28) LOAD_X(ks + WIN);
        SCHED();
        if constexpr (ks == 0) MFMA32V0(av, wv[ks], xf[ks]); else MFMA32VA(av, wv[ks], xf[ks]);
        if constexpr (ks + 2 < 28) wv[ks + 2] = WFRAG(wb + ((ks + 2) >> 2) * 4096, 1, (ks + 2) & 3);
        if constexpr (ks < 4) sp[ks] = ld8<SC1>(rws, ks == 3 ? so3 : so + ks * 512, 0);
        if constexpr (ks == 18) {
          float s1 = (asf(sp[0].x) + asf(sp[1].x)) + (asf(sp[2].x) + asf(sp[3].x));
          float s2 = (asf(sp[0].y) + asf(sp[1].y)) + (asf(sp[2].y) + asf(sp[3].y));
          s1 = sum32(s1); s2 = sum32(s2);
          mean = s1 * (1.f / D);
          rstd = s2 * (1.f / D) - mean * mean;
          PINV(mean); PINV(rstd);
        }
        if constexpr (ks == 19) { rstd = __builtin_amdgcn_rsqf(fmaxf(rstd, 0.f) + EPS); PINV(rstd); }
        if constexpr (ks >= 20 && ks < 24) { ck[ks - 20] *= -mean * rstd; cv[ks - 20] *= -mean * rstd; PINV(ck[ks - 20]); PINV(cv[ks - 20]); }
        SCHED();
      });
      MFMA_SETTLE2(ak, av);
#pragma unroll
      for (int i = 0; i < 16; ++i) ak[i] = ak[i] * rstd + ck[i >> 2][i & 3];
      KVOUT(ak, 0)
#pragma unroll
      for (int i = 0; i < 16; ++i) av[i] = av[i] * rstd + cv[i >> 2][i & 3];
      KVOUT(av, 1)
    }
  });
  asm volatile(".Lfk_code_to:");
  if (blockIdx.x == 0 && threadIdx.x == 0) __hip_atomic_store(ctrl, epoch, RLX_AGENT);
}

extern "C" void kernel_launch(void* const* d_in, const int* in_sizes, int n_in, void* d_out, int out_size, void* d_ws,
                              size_t ws_size, hipStream_t stream) {
  Params p;
  p.x = d_in[0]; p.wqkv = d_in[1]; p.wo = d_in[2];
  p.w1 = d_in[4]; p.b1 = d_in[5]; p.w2 = d_in[6]; p.b2 = d_in[7];
  p.out = d_out; p.ws = d_ws;
  (void)hipFuncSetAttribute((const void*)fused_kernel, hipFuncAttributeMaxDynamicSharedMemorySize, LDS_BYTES);
  fused_kernel<<<256, 256, LDS_BYTES, stream>>>(p);
}
```

```cpp
#include <hip/hip_runtime.h>
#include <utility>

#define GAS __attribute__((address_space(1)))
#define LAS __attribute__((address_space(3)))
#define DI __device__ __forceinline__
#define INL __attribute__((always_inline))
#define RLX_AGENT __ATOMIC_RELAXED, __HIP_MEMORY_SCOPE_AGENT
typedef unsigned v4u __attribute__((ext_vector_type(4)));
typedef unsigned v2u __attribute__((ext_vector_type(2)));
typedef float f32x2 __attribute__((ext_vector_type(2)));
typedef float f32x4 __attribute__((ext_vector_type(4)));
typedef float f32x16 __attribute__((ext_vector_type(16)));
typedef short s16x4 __attribute__((ext_vector_type(4)));
typedef __bf16 bf16x2 __attribute__((ext_vector_type(2)));
typedef __bf16 bf16x8 __attribute__((ext_vector_type(8)));
typedef __amdgpu_buffer_rsrc_t rsrc_t;

constexpr int S = 2048, D = 448, F = 1792;
constexpr float EPS = 1e-5f;
constexpr int FLAG_OFF = 4096;
constexpr int KV_OFF = 512 * 1024, KV_SZ = 16 * 64 * 4096;
constexpr int O_OFF = KV_OFF + 2 * KV_SZ, O_SZ = 32 * 16 * 4096;
constexpr int S3_OFF = O_OFF + 2 * O_SZ, S3_SZ = 32 * 8 * 1024;
constexpr int HT_OFF = S3_OFF + 2 * S3_SZ, HT_SZ = 7 * 64 * 4096;
constexpr int U_OFF = HT_OFF + 2 * HT_SZ, U_SZ = 7 * 56 * 4096;
constexpr int X2_OFF = U_OFF + 2 * U_SZ, X2_SZ = 64 * 28 * 1024;
constexpr int S5_OFF = X2_OFF + X2_SZ, S5_SZ = 32 * 8 * 512;
constexpr int DBG_OFF = S5_OFF + S5_SZ;
constexpr int WS_BYTES = DBG_OFF + 256 * 128 * 8;
constexpr int L_WQ = 0;
constexpr int L_KVW = 86016;
constexpr int L_STG = L_KVW + 49152;
constexpr int L_CSP = L_STG + 16384;
constexpr int L_CS = L_CSP + 1536 + 128;
constexpr int L_HST = L_CS + 384;
constexpr int L_DUMP = L_HST + 8192;
constexpr int LDS_BYTES = L_DUMP + 256;

static_assert(L_HST % 128 == 0 && LDS_BYTES <= 163840, "LDS map");
constexpr int SC1 = 16, OOB = 0x7ffff000;
#define VT_LAYOUT 1
#define WIN4 12
#define WIN5 12
#define WINX 12
#define QPUB 10
#define NO1 2
#define NO2 3
#define NO3 2
#define NG2 3
#define NG3 1
#define NH1 2
#define NH2 3
#define NH3 2
#define NU0 3
#define NU1 2
#define NU3 2
#define PACE 1
#define FTCH 1
#define XTCH 0
#define SUMDOT 0
#define OAUX 16
#define OFULL 1
#define MAP2D 0
#define WSLEEP 0
#define ISLEEP 80
#define QPOLL 24
#define QEARLY 1
#define KP 5
#define MO1 2
#define MO2 3
#define MO3 0
#define MG2 2
#define MG3 0
#define MH1 2
#define NS 5
#define SO 0
#define SG 1
#define SH 2
#define TBI 7
#define TCH0 1
#define TCH1 1
#define TCH2 1
constexpr int Z_G = NO1 + NO2 + NO3, Z_H = Z_G + NG2 + NG3, Z_U = Z_H + NH1 + NH2 + NH3;
static_assert(QPUB >= 10 && QPUB / 2 + QPOLL - QPUB >= 16 && NO1 <= 3 && NU1 <= 3 && NO2 >= 2 && NG2 >= 2 && NH2 >= 2 && Z_U >= 16 && Z_U + NU0 + NU1 + 2 + NU3 <= 30 && NU0 >= 3 && NU0 <= 6 && NO1 <= 4 && NU1 <= 4, "schedule");

struct Params {
  const void *x, *wqkv, *wo, *w1, *b1, *w2, *b2;
  void *out, *ws;
};

template <int... I, class Fn> DI void sfor_(std::integer_sequence<int, I...>, Fn&& f) { (f(std::integral_constant<int, I>{}), ...); }
template <int N, class Fn> DI void sfor(Fn&& f) { sfor_(std::make_integer_sequence<int, N>{}, f); }
DI float asf(unsigned u) { return __builtin_bit_cast(float, u); }
DI unsigned asu(float f) { return __builtin_bit_cast(unsigned, f); }
DI bf16x2 asb2(unsigned u) { return __builtin_bit_cast(bf16x2, u); }
DI rsrc_t mk(const void* p, int bytes) { return __builtin_amdgcn_make_buffer_rsrc((void*)p, 0, bytes, 0x00020000); }
template <int AUX> DI v4u ld16(rsrc_t r, int voff, int soff) { return __builtin_amdgcn_raw_buffer_load_b128(r, voff, soff, AUX); }
template <int AUX> DI v2u ld8(rsrc_t r, int voff, int soff) { return __builtin_amdgcn_raw_buffer_load_b64(r, voff, soff, AUX); }
template <int AUX> DI void st16(v4u v, rsrc_t r, int voff, int soff) { __builtin_amdgcn_raw_buffer_store_b128(v, r, voff, soff, AUX); }
template <int AUX> DI void st8(v2u v, rsrc_t r, int voff, int soff) { __builtin_amdgcn_raw_buffer_store_b64(v, r, voff, soff, AUX); }
DI f32x4 mfma16(v4u a, v4u b, f32x4 c) {
  return __builtin_amdgcn_mfma_f32_16x16x32_bf16(__builtin_bit_cast(bf16x8, a), __builtin_bit_cast(bf16x8, b), c, 0, 0, 0);
}
DI f32x16 mfma32(v4u a, v4u b, f32x16 c) {
  return __builtin_amdgcn_mfma_f32_32x32x16_bf16(__builtin_bit_cast(bf16x8, a), __builtin_bit_cast(bf16x8, b), c, 0, 0, 0);
}
DI unsigned pk(float lo, float hi) { return __builtin_bit_cast(unsigned, __builtin_convertvector((f32x2){lo, hi}, bf16x2)); }
DI float bflo(unsigned u) { return asf(u << 16); }
DI float bfhi(unsigned u) { return asf(u & 0xffff0000u); }
DI f32x4 unpack4(v2u v) { return (f32x4){bflo(v.x), bfhi(v.x), bflo(v.y), bfhi(v.y)}; }
DI v2u trrd(LAS char* p) { return __builtin_bit_cast(v2u, __builtin_amdgcn_ds_read_tr16_b64_v4i16((LAS s16x4*)p)); }
DI v4u trfrag(LAS char* p0, LAS char* p1) { v2u a = trrd(p0), b = trrd(p1); return (v4u){a.x, a.y, b.x, b.y}; }
DI float hi32(float v) {
  unsigned u = asu(v);
  auto r = __builtin_amdgcn_permlane32_swap(u, u, false, false);
  return asf(r[1]);
}
DI float sum32(float v) {
  unsigned u = asu(v);
  auto r = __builtin_amdgcn_permlane32_swap(u, u, false, false);
  return asf(r[0]) + asf(r[1]);
}
DI float max32(float v) {
  unsigned u = asu(v);
  auto r = __builtin_amdgcn_permlane32_swap(u, u, false, false);
  return fmaxf(asf(r[0]), asf(r[1]));
}
DI float sum16(float v) {
  unsigned u = asu(v);
  auto r = __builtin_amdgcn_permlane16_swap(u, u, false, false);
  return asf(r[0]) + asf(r[1]);
}
DI float sum_ab(float a, float b) {
  auto r = __builtin_amdgcn_permlane16_swap(asu(a), asu(b), false, false);
  return sum32(asf(r[0]) + asf(r[1]));
}
DI void stats16(const f32x4 (&v)[4], int n, f32x2& s1, f32x2& s2) {
#pragma unroll
  for (int a = 0; a < 4; ++a)
    if (n >> a & 1) {
      const f32x2 lo = {v[a][0], v[a][1]}, hi = {v[a][2], v[a][3]};
      s1 += lo; s2 += lo * lo; s1 += hi; s2 += hi * hi;
    }
}
#define FENCE() asm volatile("" ::: "memory")
#define SCHED() __builtin_amdgcn_sched_barrier(0)
#define DRAIN() asm volatile("s_waitcnt vmcnt(0)" ::: "memory")
#define PINV(x) asm volatile("" : "+v"(x))
#define PINA(x) asm volatile("" : "+a"(x))
#define PINS(x) asm volatile("" : "+s"(x))
#define KEEP(x) asm volatile("" :: "v"(x))
#define LGKM0() asm volatile("s_waitcnt lgkmcnt(0)" ::: "memory")
DI void bar() { LGKM0(); __builtin_amdgcn_s_barrier(); FENCE(); }
template <int N> DI void vmw() { asm volatile("s_waitcnt vmcnt(%0)" :: "n"(N) : "memory"); }
template <int STEP> DI void dma1(rsrc_t r, int voff, int& so, int& lds) {
  asm volatile("s_mov_b32 m0, %1\n\ts_add_u32 %1, %1, 0x400\n\tbuffer_load_dwordx4 %2, %3, %0 offen lds\n\ts_add_u32 %0, %0, %4"
               : "+s"(so), "+s"(lds) : "v"(voff), "s"(r), "n"(STEP) : "memory", "scc");
}
template <int LO, int MO> DI void dmai(rsrc_t r, int voff, int lds) {
  int so;
  asm volatile("s_add_u32 m0, %1, %4\n\ts_mov_b32 %0, %5\n\tbuffer_load_dwordx4 %2, %3, %0 offen lds"
               : "=&s"(so) : "s"(lds), "v"(voff), "s"(r), "n"(LO), "n"(MO) : "memory", "scc");
}
DI void touch(rsrc_t r, int voff, int soff, LAS char* L) {
  asm volatile("s_mov_b32 m0, %0\n\ts_nop 3\n\tbuffer_load_dword %1, %2, %3 offen lds"
               :: "s"((int)(size_t)(L + L_DUMP)), "v"(voff), "s"(r), "s"(soff) : "memory");
}
template <int STEP> DI void dma64(rsrc_t r, int v0, int v1, int soff, int lds) {
  unsigned so;
  asm volatile("s_mov_b32 m0, %1\n\ts_mov_b32 %0, %5\n\tbuffer_load_dwordx4 %2, %4, %0 offen lds\n\t"
               "s_add_u32 m0, m0, 0x400\n\ts_add_u32 %0, %0, %6\n\tbuffer_load_dwordx4 %3, %4, %0 offen lds\n\t"
               "s_add_u32 m0, m0, 0x400\n\ts_add_u32 %0, %0, %6\n\tbuffer_load_dwordx4 %2, %4, %0 offen lds\n\t"
               "s_add_u32 m0, m0, 0x400\n\ts_add_u32 %0, %0, %6\n\tbuffer_load_dwordx4 %3, %4, %0 offen lds"
               : "=&s"(so) : "s"(lds), "v"(v0), "v"(v1), "s"(r), "s"(soff), "n"(STEP) : "memory", "scc");
}

#define STAMP(i) do {} while (0)

DI void publish(GAS unsigned* flag, unsigned epoch) {
  DRAIN();
  __builtin_amdgcn_s_waitcnt(0x0F70);
  bar();
  if (threadIdx.x == 0) __hip_atomic_store(flag, epoch, RLX_AGENT);
  FENCE();
}
template <int N> DI void publish_n(GAS unsigned* flag, unsigned epoch) {
  vmw<N>();
  __builtin_amdgcn_s_barrier();
  FENCE();
  if (threadIdx.x == 0) __hip_atomic_store(flag, epoch, RLX_AGENT);
  FENCE();
}
#define POLL(f, ptr) asm volatile("global_load_dword %0, %1, off sc1" : "=&v"(f) : "v"(ptr) : "memory")
#define POLLED(f, N, ptr) { \
    unsigned f2_; \
    POLL(f2_, ptr); \
    asm volatile("s_waitcnt vmcnt(%1)" : "+v"(f) : "n"((N) + 1) : "memory"); \
    if (__builtin_amdgcn_ballot_w64(f != epoch) != 0ull) { \
      asm volatile("s_waitcnt vmcnt(0)" : "+v"(f2_) :: "memory"); \
      if (__builtin_amdgcn_ballot_w64(f2_ != epoch) != 0ull) wait_flags(ptr, epoch, budget); \
    } \
    FENCE(); \
    late = f2_; }
DI void wait_flags(GAS unsigned* flag, unsigned epoch, unsigned& budget) {
  FENCE();
  while (true) {
    unsigned f = __hip_atomic_load(flag, RLX_AGENT);
    if (__builtin_amdgcn_ballot_w64(f != epoch) == 0ull) break;
    if (budget == 0u) break;
    --budget;
    if (WSLEEP) __builtin_amdgcn_s_sleep(WSLEEP);
  }
  __builtin_amdgcn_fence(__ATOMIC_ACQUIRE, "wavefront");
  FENCE();
}

DI void cv_wr(const v4u& r, int m, LAS char* stg, int wa0, int wa1) { *(LAS v4u*)(stg + ((m & 1) ? wa1 : wa0) + m * 1024) = r; }
DI v4u cv_rd(int x, LAS char* stg, int ra0, int ra1) { return trfrag(stg + (ra0 ^ (x << 5)), stg + (ra1 ^ (x << 5))); }
DI int oob(bool in, int off) { return off | (in ? 0 : OOB); }
DI int wrap7(int v) { return v >= 7 ? v - 7 : v; }
DI int wrap14(int v) { return v >= 14 ? v - 14 : v; }
DI int swz(int row) { return ((row >> 1) & 1) | (((row >> 3) ^ (row >> 2)) & 1) << 1; }

#define FRESH() \
  int tid = threadIdx.x, bid = blockIdx.x; \
  asm volatile("" : "+v"(tid)); \
  asm volatile("" : "+s"(bid)); \
  const int lane = tid & 63, w = __builtin_amdgcn_readfirstlane(tid >> 6), bx = bid & 7, by = bid >> 3; \
  const int c = lane & 15, g = lane >> 4, q4 = (lane & 15) >> 2, p4 = lane & 3, r32 = lane & 31, hh = lane >> 5; \
  const int by7 = __builtin_amdgcn_readfirstlane((by * 37) >> 8), bym = __builtin_amdgcn_readfirstlane(by - 7 * by7); \
  const int t = 2 * bx + (by >> 4), hd = by & 15;                \
  const int st = 4 * bx + by7, dt = bym;                         \
  const int dt4 = MAP2D ? (bx & 1) * 4 + by7 : by < 21 ? bym : bym + 4 * (bx & 1), ft = 7 * (bx >> 1) + (MAP2D ? bym : by < 21 ? by7 + 4 * (bx & 1) : 3);    \
  const int rotx = hd >= 14 ? hd - 14 : wrap7(hd), rot4 = ROT4, rot5 = ROT5; \
  const int w4 = (w + G4 * 2 * ((MAP2D ? bym : by7) & 1)) & 3, pa = G5 * 2 * (by7 & 1);    \
  LAS char* stga = L + L_STG + w * 4096; \
  LAS char* ring = L + L_KVW + w * 16384; \
  LAS char* stgb = ring + 12288; \
  const int wrow = lane >> 3; \
  const int sx0 = swz(wrow) << 5, sx1 = swz(wrow + 8) << 5; \
  const int wa0 = (wrow * 128 + (lane & 7) * 16) ^ sx0, wa1 = (wrow * 128 + (lane & 7) * 16) ^ sx1; \
  auto raddr = [&](int row) { return row * 128 + (swz(row) << 5) + 8 * p4; }; \
  const int w1o = (512 * w + wrow) * (F * 2) + ft * 128 + (lane & 7) * 16, w2o = (448 * w + wrow) * (S * 2) + st * 128 + (lane & 7) * 16; \
  const int r1a = raddr(8 * g + q4), r1b = raddr(8 * g + 4 + q4), r2a = raddr(4 * g + q4), r2b = raddr(16 + 4 * g + q4); \
  LAS char* red = L + L_KVW + lane * 16; \
  (void)c; (void)g; (void)q4; (void)r32; (void)hh; (void)t; (void)hd; (void)st; (void)dt; (void)dt4; (void)ft; (void)stga; (void)stgb; (void)ring; \
  (void)sx0; (void)sx1; (void)wa0; (void)wa1; (void)rotx; (void)rot4; (void)rot5; (void)w1o; (void)w2o; (void)r1a; (void)r1b; (void)r2a; (void)r2b; \
  (void)red; (void)w4; (void)pa;
#define G4 0
#define G5 0
#define ROT4 0
#define ROT5 0

#define MNOP ""
#define MFMA32V0(d, a, b) asm volatile(MNOP "v_mfma_f32_32x32x16_bf16 %0, %1, %2, 0" : "=&v"(d) : "v"(a), "v"(b))
#define MFMA32VC(d, a, b, c) asm volatile(MNOP "v_mfma_f32_32x32x16_bf16 %0, %1, %2, %3" : "=&v"(d) : "v"(a), "v"(b), "v"(c))
#define MFMA32VA(d, a, b) asm volatile(MNOP "v_mfma_f32_32x32x16_bf16 %0, %1, %2, %0" : "+v"(d) : "v"(a), "v"(b))
#define MFMA_SETTLE(d) asm volatile("s_nop 11" : "+v"(d))
#define MFMA_SETTLE2(d, e) asm volatile("s_nop 11" : "+v"(d), "+v"(e))
DI float max3(float a, float b, float c) { float r; asm("v_max3_f32 %0, %1, %2, %3" : "=v"(r) : "v"(a), "v"(b), "v"(c)); return r; }

#define WB(cc) (L + L_WQ + (4 * hh + q4) * 64 + (g & 1) * 32 + 8 * p4 + (hd & 1) * 8 + (cc) * 4096)
#define WFRAG(wb, part, e) trfrag((wb) + (part) * 28672 + ((e) >> 1) * 2048 + ((e) & 1) * 512, (wb) + (part) * 28672 + ((e) >> 1) * 2048 + ((e) & 1) * 512 + 1024)
#define T1O ((16 * (by * 4 + w) + (lane >> 2)) * (F * 2) + (7 * (bx >> 1) + 3 * (bx & 1) + (lane & 3)) * 128)
#define T1H ((512 * (by >> 3) + 8 * ((by & 7) * 4 + w) + (lane >> 2)) * (F * 2) + (7 * (bx >> 1) + 3 * (bx & 1) + (lane & 3)) * 128)
#define CSUM(part, a) (*(LAS f32x4*)(L + L_CS + ((part) * 32 + 8 * (a) + 4 * hh) * 4))
#define WZ_RD1(n, i, sl) wz[n][i] = (n) < 16 ? trfrag(pr1[i] + (sl) * 4096, pr1[(i) ^ 2] + (sl) * 4096 + 512) : trfrag(pr2[i] + (sl) * 4096, pr2[i] + (sl) * 4096 + 2048)
#define DMA_WZ1(n, m, sl) { \
    if ((n) < 16) dmai<(sl) * 4096 + (m) * 1024, (32 * ((n) & 15) + 8 * (m)) * F * 2>(rw1, ((m) & 1) ? pw1 ^ 64 : pw1, pring); \
    else dmai<(sl) * 4096 + (m) * 1024, (32 * ((n) & 15) + 8 * (m)) * S * 2>(rw2, ((m) & 1) ? pw2 ^ 64 : pw2, pring); }
#define PIECE(L0, q) { \
    constexpr int r_ = (q) / 4, m_ = (q) % 4, n_ = (L0) + r_; \
    if constexpr (m_ == 0 && r_ >= 4) asm volatile("s_waitcnt vmcnt(8) lgkmcnt(0)" ::: "memory"); \
    else if constexpr (m_ == 0 && r_ == 3) vmw<8>(); \
    DMA_WZ1(n_, m_, r_ & 3) \
    if constexpr (r_ >= 3) WZ_RD1(n_ - 3, m_, (r_ - 3) & 3); \
    SCHED(); }
#define CMIN(a, b) ((a) < (b) ? (a) : (b))
#define DPUMP(L0, A, B) sfor<4 * ((B) - (A))>([&](auto I_) INL { PIECE(L0, 4 * ((A) - (L0)) + I_) });
#define STASH(A, B) if constexpr (layer == 1) sfor<4 * ((B) - (A))>([&](auto I_) INL { \
    constexpr int j_ = (A) + I_ / 4, m_ = I_ % 4; \
    dmai<j_ * 4096 + m_ * 1024, (32 * (14 - NS + j_) + 8 * m_) * S * 2>(rw2, (m_ & 1) ? pw2 ^ 64 : pw2, pstash); SCHED(); });
#define DTSTEP(L0, L1, sl) ((L1) - 1 - (((L1) - 1 - (L0) - (sl)) & 3))
#define DTAIL1(L0, L1, j) { if constexpr (DTSTEP(L0, L1, (j) >> 2) >= (L0) && DTSTEP(L0, L1, (j) >> 2) >= (L1) - 3) WZ_RD1(DTSTEP(L0, L1, (j) >> 2), (j) & 3, (j) >> 2); }
#define DTAIL(L0, L1) sfor<16>([&](auto J_) INL { constexpr int j_ = J_; DTAIL1(L0, L1, j_) }); SCHED();
#define REDW(a, b) *(LAS f32x4*)(red + w * 16384 + (((b) - 1) * 4 + (a)) * 1024) = acc[a][b]
#define REDR(a, b) (*(LAS f32x4*)(red + ((w + 4 - (b)) & 3) * 16384 + (((b) - 1) * 4 + (a)) * 1024))
#define COLSUM(part) { \
    LAS char* cb = L + L_WQ + (part) * 28672 + (w * 16 + 8 * hh + q4) * 64 + (g & 1) * 32 + 8 * p4; \
    v4u wc[7]; \
    _Pragma("unroll") for (int i = 0; i < 7; ++i) wc[i] = trfrag(cb + i * 4096, cb + i * 4096 + 256); \
    f32x16 cs = {}; \
    _Pragma("unroll") for (int i = 0; i < 7; ++i) cs = mfma32((v4u){0x3f803f80u, 0x3f803f80u, 0x3f803f80u, 0x3f803f80u}, wc[i], cs); \
    if (lane < 32) *(LAS float*)(L + L_CSP + (w * 96 + (part) * 32 + lane) * 4) = cs[0]; }
#define COLRED(i) { \
    LAS float* cp = (LAS float*)(L + L_CSP) + (((i) & 31) + 4 * (hd & 1) < 32 ? (i) + 4 * (hd & 1) : (i)); \
    *(LAS float*)(L + L_CS + (i) * 4) = (cp[0] + cp[96]) + (cp[192] + cp[288]); }

__global__ void __launch_bounds__(256, 1) fused_kernel(Params p) {
  extern __shared__ __attribute__((aligned(16))) char smem[];
  LAS char* L = (LAS char*)smem;
  {
    unsigned long long pc;
    int len;
    asm volatile("s_getpc_b64 %0\n.Lfk_code_from:\n\ts_mov_b32 %1, .Lfk_code_to-.Lfk_code_from" : "=s"(pc), "=s"(len));
    touch(mk((const void*)pc, len > 0 ? len : 0), (threadIdx.x & 63) < 8 ? ((blockIdx.x >> 3) * 4 + (threadIdx.x >> 6) + (threadIdx.x & 7) * 128) * 128 : OOB, 0, L);
  }
  SCHED();
  const bool on35 = (blockIdx.x >> 3) < 28;
  const bool on4 = (blockIdx.x >> 3) < (MAP2D ? ((blockIdx.x & 1) ? 21 : 28) : (blockIdx.x & 1) ? 24 : 25);
  const rsrc_t rws = mk(p.ws, WS_BYTES);
  GAS unsigned* ctrl = (GAS unsigned*)p.ws;
  GAS unsigned* flags = (GAS unsigned*)((GAS char*)p.ws + FLAG_OFF);
  STAMP(0);
  unsigned budget = 1u << 22;

  v4u xf[28];
  v4u kv[4];
  float mean = 0.f, rstd = 0.f;
  f32x4 res[4];
  unsigned epoch;
  int kvs, oo, t1o, wvo2, pw1, pw2, fo;
  unsigned nk1, nk2, pm[8];
  LAS char *kvw, *vw, *vr0, *wor[4], *pr1[4], *pr2[4], *wbq;
#define KVOUT(acc, h) { \
    _Pragma("unroll") for (int i = 0; i < 16; i += 2) kv[2 * (h) + (i >> 3)][(i >> 1) & 3] = pk(acc[i], acc[i + 1]); \
    if (hh) { kv[2 * (h) + 1][2] = (h) ? 0x00003f80u : 0u; kv[2 * (h) + 1][3] = 0u; } \
    const rsrc_t rkv = mk(p.ws, t < 15 ? WS_BYTES : 0);    \
    st16<SC1>(kv[2 * (h)], rkv, kvs + (h) * 2048, 0); \
    st16<SC1>(kv[2 * (h) + 1], rkv, kvs + (h) * 2048 + 1024, 0); \
    SCHED(); }
#define X128(p) ((LAS char*)(size_t)((unsigned)(size_t)(p) ^ 128u))
  {
    FRESH();
    const rsrc_t rwq = mk(p.wqkv, D * 3 * D * 2);
    const rsrc_t rx = mk(p.x, S * D * 2);
    const int gid = by * 4 + w, rg = (int)(size_t)ring;
    touch(rx, oob(lane < 14, (t * 896 + hd * 56 + w * 14 + lane) * 128), 0, L);
    const int xvo = (t * 128 + w * 32 + wrow) * 896 + (((lane & 7) ^ (wrow >> 1)) << 4);
#define DMA_X(cc) sfor<4>([&](auto M) INL { constexpr int m = M; dmai<((cc) & 3) * 4096 + m * 1024, m * 8 * 896 + (cc) * 128>(rx, (m & 1) ? xvo ^ 64 : xvo, rg); })
    DMA_X(0); DMA_X(1);
    touch(rwq, oob(lane < 14, gid * 2688 + 896 + lane * 128), 0, L);
    DMA_X(2);
    SCHED();
    const int tb = gid * 35 + lane, tbr = (tb * 18725) >> 18;
    int tvb = oob(lane < 35, (128 + tbr) * 2688 + 896 + (tb - 14 * tbr) * 128);
    int wvo = (w * 16 + (lane >> 2)) * 2688 + hd * 56 - (hd & 1) * 8 + (lane & 3) * 16;
    const int wlb = (int)(size_t)(L + L_WQ) + w * 1024;
#define DMA_W(part, cc) dmai<(part) * 28672 + (cc) * 4096, (cc) * 64 * 2688 + (part) * 896>(rwq, wvo, wlb)
    LAS char* xa[8];
#pragma unroll
    for (int e = 0; e < 8; ++e) { xa[e] = ring + r32 * 128 + 8 * hh + ((e ^ ((r32 >> 1) & 7)) << 4); PINV(xa[e]); }
    int epo = w == 0 ? FLAG_OFF + (bid - 256) * 128 + lane * 32768 : S3_OFF + (st * 8 + dt) * 1024 - 2 * S3_SZ + (lane >> 3) * S3_SZ + (lane & 7) * 128;
    epo = (w == 0 ? lane - 1u < 10u : w == 1 && on35 && (lane >> 4) == 1) ? epo : 0;
    PINV(tvb); PINV(wvo); PINV(epo);
    SCHED();
    DMA_X(3);
    asm volatile("buffer_load_dword %0, %1, %2, 0 offen sc1" : "=&v"(epoch) : "v"(epo), "s"(rws) : "memory");
    SCHED();
    int tvq, tvw;
    LAS char *cb, *wb;
#define READ_X(cc) _Pragma("unroll") for (int e = 0; e < 4; ++e) { \
      const int ch = (e >> 1) * 4 + (e & 1); \
      const v2u a_ = *(LAS v2u*)(xa[ch] + ((cc) & 3) * 4096), b_ = *(LAS v2u*)(xa[ch + 2] + ((cc) & 3) * 4096); \
      xf[4 * (cc) + e] = (v4u){a_.x, a_.y, b_.x, b_.y}; }
    const v4u ones = {0x3f803f80u, 0x3f803f80u, 0x3f803f80u, 0x3f803f80u};
    f32x16 gs, ss = {};
#define XSTAT(i) { if constexpr ((i) == 0) MFMA32V0(gs, xf[i], xf[i]); else MFMA32VA(gs, xf[i], xf[i]); ss = mfma32(ones, xf[i], ss); }
    STAMP(32);
    vmw<14>(); READ_X(0) LGKM0(); DMA_X(4); XSTAT(0) XSTAT(1) XSTAT(2) XSTAT(3) SCHED();
    vmw<14>(); READ_X(1) LGKM0(); DMA_X(5); XSTAT(4) XSTAT(5) XSTAT(6) XSTAT(7) SCHED();
    vmw<13>(); READ_X(2) LGKM0(); DMA_X(6); SCHED();
    asm volatile("s_waitcnt vmcnt(12)" : "+v"(epoch) :: "memory");
    epoch = __builtin_amdgcn_readfirstlane(epoch) + 1u;
    READ_X(3)
    SCHED();
    sfor<20>([&](auto I) INL {
      constexpr int i = I;
      if constexpr (i == 6) { vmw<8 + 3 + (TBI < 6)>(); READ_X(4) }
      if constexpr (i == 10) { vmw<4 + 5 + 1>(); READ_X(5) }
      if constexpr (i == 14) { vmw<7 + 1>(); READ_X(6) }
      if constexpr ((i & 1) == 0 && i < 16) DMA_W(1 + ((i >> 1) & 1), i >> 2);
      if constexpr (i == TBI) touch(rwq, tvb, 0, L);
      MFMA32VA(gs, xf[8 + i], xf[8 + i]);
      if constexpr (i < 8) {
        const int d = r32 - ((2 * i & 3) + 8 * (i >> 1)) - 4 * hh;
        pm[i] = ((unsigned)(d >> 31) & 0xffffu) | ((unsigned)((d - 1) >> 31) & 0xffff0000u);
        PINA(pm[i]);
      } else if constexpr (i < 12) { wor[i - 8] = ring + (raddr(16 * (g >> 1) + 4 * (g & 1) + q4) ^ ((i - 8) << 5)); PINA(wor[i - 8]); }
      else if constexpr (i < 16) { pr1[i - 12] = ring + (raddr(8 * g + q4) ^ ((i - 12) << 5)); PINA(pr1[i - 12]); }
      else { pr2[i - 16] = ring + (raddr(4 * g + q4) ^ (((i + w) & 3) << 5)); PINA(pr2[i - 16]); }
      SCHED();
      ss = mfma32(ones, xf[8 + i], ss);
      SCHED();
      if constexpr (i == 0) { kvs = KV_OFF + (hd * 64 + 4 * t + w) * 4096 + lane * 16; PINV(kvs); }
      if constexpr (i == 1) { oo = O_OFF + ((2 * t + (w >> 1)) * 16 + hd) * 4096 + (2 * (w & 1) + (r32 >> 4)) * 1024 + hh * 256 + c * 16; PINA(oo); }
      if constexpr (i == 2) { t1o = oob(lane < 32, (512 * (by >> 3) + 8 * ((by & 7) * 4 + w) + (lane >> 2)) * (F * 2) + (7 * (bx >> 1) + 3 * (bx & 1) + (lane & 3)) * 128); PINA(t1o); }
      if constexpr (i == 3) { wvo2 = ((112 * w + wrow) * 896 + dt * 128 + (lane & 7) * 16) ^ sx0; PINA(wvo2); }
      if constexpr (i == 4) { pw1 = ((512 * w + wrow) * (F * 2) + ft * 128 + (lane & 7) * 16) ^ sx0; PINA(pw1); }
      if constexpr (i == 5) { pw2 = ((448 * w + wrow) * (S * 2) + st * 128 + (lane & 7) * 16) ^ sx0; PINA(pw2); }
      if constexpr (i == 7) {
        const int tp = (t >= 2 && (lane & 1)) ? t - 2 : t > 0 ? t - 1 : 0;
        fo = FLAG_OFF + (((tp & 1) * 16 + hd) * 8 + (tp >> 1)) * 128;
        PINA(fo);
      }
      if constexpr (i == 8) { nk2 = (t < 2 && hh) ? 0xf149u : 0u; nk1 = (t < 1 && hh) ? 0xf149u : 0u; PINA(nk1); PINA(nk2); }
      if constexpr (i == 9) { kvw = L + L_KVW + w * 4096 + lane * 16; vw = L + L_KVW + w * 4096 + 2048 + ((lane * 16) ^ (hh << 6)); PINA(kvw); PINA(vw); }
      if constexpr (i == 11) { vr0 = L + L_KVW + w * 4096 + 2048 + ((((g & 1) * 1024 + (p4 & 1) * 512 + hh * 64 + q4 * 16) ^ ((p4 & 1) << 6) ^ ((g & 1) << 7)) + (p4 >> 1) * 8); PINA(vr0); }
      if constexpr (i == 13) { wbq = WB(0); wb = wbq + 28672; PINV(wbq); PINV(wb); }
      if constexpr (i == 15) { cb = L + L_WQ + 28672 + (w * 16 + 8 * hh + q4) * 64 + (g & 1) * 32 + 8 * p4; PINV(cb); }
      if constexpr (i == 17) {
        const int tl = by * 98 + w * 25 + lane, trow = (tl * 9363) >> 16;
        tvq = oob(lane < 25 && w * 25 + lane < 98, trow * 2688 + (tl - 7 * trow) * 128); tvw = oob(lane < 25 && w * 25 + lane < 98, tl * 128);
        PINV(tvq); PINV(tvw);
      }
      SCHED();
    });
    STAMP(33);
    f32x16 ak, av;
    v4u wk[28], wv[28], wc[14];
    f32x16 csk = {}, csv = {};
    float dg = 0.f, d8[8], d4[4], pp[4];
    f32x4 cc[8];
    LAS float* pb = (LAS float*)(L + L_CSP) + 32 + 16 * w + c + 4 * (hd & 1);
    LAS float* pw = (LAS float*)(L + (hh ? L_DUMP : L_CSP + (w * 96 + 32) * 4)) + r32;
#define PICK4(o, at, a0, a1, a2, a3, b0, b1, b2, b3, m) \
    asm volatile("v_cndmask_b32 %0, %4, %8, %12\n\tv_cndmask_b32 %1, %5, %9, %12\n\tv_cndmask_b32 %2, %6, %10, %12\n\tv_cndmask_b32 %3, %7, %11, %12" \
                 : "=&v"(o[at]), "=&v"(o[(at) + 1]), "=&v"(o[(at) + 2]), "=&v"(o[(at) + 3]) \
                 : "v"(a0), "v"(a1), "v"(a2), "v"(a3), "v"(b0), "v"(b1), "v"(b2), "v"(b3), "s"(m))
    sfor<8>([&](auto R) INL {
      constexpr int r = R;
      if constexpr (r < 7) {
        vmw<6 + (TBI >= 4 * r + 2)>();
        if constexpr (r == 6) {
          wc[12] = trfrag(cb + r * 4096, cb + r * 4096 + 256);
          wc[13] = trfrag(cb + 28672 + r * 4096, cb + 28672 + r * 4096 + 256);
          csk = mfma32(ones, wc[12], csk); csv = mfma32(ones, wc[13], csv);
          pw[0] = csk[0]; pw[32] = csv[0];
          LGKM0();
        }
        __builtin_amdgcn_s_barrier();
        FENCE();
        SCHED();
      }
      sfor<4>([&](auto E) INL {
        constexpr int e = E, kr = 4 * r + e, km = kr - 4;
        if constexpr (r > 0) { if constexpr (km == 0) MFMA32V0(ak, wk[km], xf[km]); else MFMA32VA(ak, wk[km], xf[km]); }
        if constexpr (r < 7) wk[kr] = WFRAG(wb + r * 4096, 0, e);
        if constexpr (r < 6 && (e & 1) == 0) wc[2 * r + e / 2] = trfrag(cb + (e / 2) * 28672 + r * 4096, cb + (e / 2) * 28672 + r * 4096 + 256);
        SCHED();
        if constexpr (r > 0) { if constexpr (km == 0) MFMA32V0(av, wv[km], xf[km]); else MFMA32VA(av, wv[km], xf[km]); }
        if constexpr (r < 7) wv[kr] = WFRAG(wb + r * 4096, 1, e);
        SCHED();
        if constexpr (r < 6 && e == 1) csk = mfma32(ones, wc[2 * r], csk);
        if constexpr (r < 6 && e == 3) csv = mfma32(ones, wc[2 * r + 1], csv);
        SCHED();
        if constexpr (e == 1 || e == 3) {
          if constexpr (r < 3) DMA_W(1 + e / 2, r + 4);
          else if constexpr (r == 3) { if constexpr (e == 1) touch(rwq, tvq, 0, L); else touch(mk(p.wo, D * D * 2), tvw, 0, L); }
          else if constexpr (2 * (r - 4) + e / 2 < 7) DMA_W(0, 2 * (r - 4) + e / 2);
        }
        if constexpr (r == 1 && (e & 1) == 0) { constexpr int j = 2 * e; PICK4(d8, j, gs[j], gs[j + 1], gs[j + 2], gs[j + 3], gs[j + 8], gs[j + 9], gs[j + 10], gs[j + 11], 0xffff0000ffff0000ull); }
        if constexpr (r == 2 && e == 0) PICK4(d4, 0, d8[0], d8[1], d8[2], d8[3], d8[4], d8[5], d8[6], d8[7], 0xff00ff00ff00ff00ull);
        if constexpr (r == 2 && e == 2)
          asm volatile("v_cndmask_b32 %1, %1, %3, %5\n\tv_cndmask_b32 %2, %2, %4, %5\n\tv_cndmask_b32 %0, %1, %2, %6\n\tv_cndmask_b32 %0, 0, %0, %7"
                       : "=&v"(dg), "+v"(d4[0]), "+v"(d4[1]) : "v"(d4[2]), "v"(d4[3]), "s"(0xccccccccccccccccull), "s"(0xaaaaaaaaaaaaaaaaull), "s"(0xf0f0f0f00f0f0f0full));
        if constexpr (r == 3 && e == 0) { mean = ss[0] * (1.f / D); rstd = sum32(dg) * (1.f / D) - mean * mean; PINV(mean); PINV(rstd); }
        if constexpr (r == 3 && e == 2) { rstd = __builtin_amdgcn_rsqf(fmaxf(rstd, 0.f) + EPS); PINV(rstd); }
        if constexpr (r == 4 && e == 0) { PINV(pb); PINV(pw); }
        if constexpr (r == 6 && e == 0) { pp[0] = pb[0]; pp[1] = pb[96]; pp[2] = pb[192]; pp[3] = pb[288]; }
        if constexpr (r == 6 && e == 2) *(LAS float*)(L + L_CS + (32 + 16 * w + c) * 4) = (pp[0] + pp[1]) + (pp[2] + pp[3]);
        if constexpr (r == 6 && e == 3) bar();
        if constexpr (r == 7 && e < 2) { _Pragma("unroll") for (int j = 0; j < 4; ++j) cc[4 * e + j] = CSUM(1, 4 * e + j); }
        SCHED();
      });
    });
    STAMP(34);
    MFMA_SETTLE2(ak, av);
#pragma unroll
    for (int i = 0; i < 16; ++i) ak[i] = (ak[i] - mean * cc[i >> 2][i & 3]) * rstd;
    KVOUT(ak, 0)
#pragma unroll
    for (int i = 0; i < 16; ++i) av[i] = (av[i] - mean * cc[4 + (i >> 2)][i & 3]) * rstd;
    KVOUT(av, 1)
#pragma unroll
    for (int a = 0; a < 4; ++a) res[a] = (f32x4){0.f, 0.f, 0.f, 0.f};
  }

  v4u wz[30][4];
  sfor<2>([&](auto LY) INL {
    constexpr int layer = LY;
    constexpr int Y0 = layer ? KP : 0, YE = layer ? 16 : Z_U;
    constexpr int Y1 = Y0 + (layer ? MO1 : NO1), Y2 = Y1 + (layer ? MO2 : NO2), YG = Y2 + (layer ? MO3 : NO3), YG2 = YG + (layer ? MG2 : NG2), YH = YG2 + (layer ? MG3 : NG3),
                  YH1 = YH + (layer ? MH1 : NH1), YH2 = layer ? YE : YH1 + NH2;
    static_assert(YH1 <= YH2 && YH2 <= YE && NS <= 5 && SO + SG + SH <= NS && Z_U + NU0 + NU1 <= 30 - NS, "schedule");
    GAS unsigned* fl = flags + layer * 5 * 256 * 32;
    STAMP(1 + layer * 16);
    v4u qf[2];
    v4u wo[4][4];
    v2u rr[4];
    unsigned b2r;
    v2u bb[4];
    int pring, pstash;
    constexpr int YZ = layer ? 30 - NS : 30, YU3 = layer ? YZ : 30 - NU3;
    const rsrc_t rw1 = mk(p.w1, on4 ? S * F * 2 : 0);
    const rsrc_t rw2 = mk(p.w2, on35 ? S * F * 2 : 0);
    {
      const bool hi = threadIdx.x & 32;
      FRESH();
      if constexpr (layer == 0) {
        vmw<4>();
        if (t == 15) vmw<0>();
        __builtin_amdgcn_s_barrier();
        FENCE();
      }
      v4u wc[7];
      f32x16 cs = {};
      LAS char* cb = L + L_WQ + (w * 16 + 8 * hh + q4) * 64 + (g & 1) * 32 + 8 * p4;
      v4u wq[28];
#pragma unroll
      for (int ks = 0; ks < 4; ++ks) wq[ks] = WFRAG(wbq, 0, ks);
      f32x16 aq;
      STAMP(2 + layer * 43);
      const rsrc_t rwo = mk(p.wo, on35 ? D * D * 2 : 0);
      pring = (int)(size_t)ring;
      pstash = (int)(size_t)(L + L_WQ) + w * 20480;
      PINS(pring); PINS(pstash);
      GAS unsigned* fp = (GAS unsigned*)((GAS char*)p.ws + fo);
      unsigned pf;
      sfor<28>([&](auto I) INL {
        constexpr int ks = I;
        if constexpr (ks == 0) MFMA32V0(aq, wq[ks], xf[ks]); else MFMA32VA(aq, wq[ks], xf[ks]);
        if constexpr (ks + 4 < 28) wq[ks + 4] = WFRAG(wbq + ((ks + 4) >> 2) * 4096, 0, ks & 3);
        constexpr int wi = ks < QPUB ? ((ks & 1) ? ks / 2 : -1) : ks == QPUB ? -1 : QPUB / 2 + ks - QPUB - 1;
        if constexpr (wi >= 0 && wi < 16) dmai<wi * 1024, (28 * (wi >> 2) + 8 * (wi & 3)) * 896>(rwo, (wi & 1) ? wvo2 ^ 64 : wvo2, pring);
        if constexpr (layer == 0) {
          if constexpr (ks < 7) wc[ks] = trfrag(cb + ks * 4096, cb + ks * 4096 + 256);
          if constexpr (ks >= 2 && ks < 9) cs = mfma32((v4u){0x3f803f80u, 0x3f803f80u, 0x3f803f80u, 0x3f803f80u}, wc[ks - 2], cs);
          if constexpr (ks == QPUB + 1) { _Pragma("unroll") for (int q = 0; q < 4; ++q) cs[q] = ((LAS float*)(L + L_CSP) + r32 + 4 * (hd & 1))[96 * q]; }
          if constexpr (ks == QPUB + 5) *(LAS float*)(L + L_CS + r32 * 4) = (cs[0] + cs[1]) + (cs[2] + cs[3]);
        }
        if constexpr (ks == QPOLL) POLL(pf, fp);
        if constexpr (ks == QPUB) {
          if constexpr (layer == 0) { *(LAS float*)(L + (hh ? L_DUMP : L_CSP + w * 96 * 4) + r32 * 4) = cs[0]; LGKM0(); }
          publish_n<QPUB / 2>(fl + (0 * 256 + bid) * 32, epoch);
          STAMP(3 + layer * 16);
        }
        SCHED();
      });
      f32x4 cq[4];
#pragma unroll
      for (int a = 0; a < 4; ++a) cq[a] = CSUM(0, a);
      MFMA_SETTLE(aq);
      const float qs = rstd * (0.18898223650461362f * 1.4426950408889634f);
#pragma unroll
      for (int a = 0; a < 4; ++a) {
        qf[a >> 1][(a & 1) * 2] = pk((aq[4 * a] - mean * cq[a][0]) * qs, (aq[4 * a + 1] - mean * cq[a][1]) * qs);
        qf[a >> 1][(a & 1) * 2 + 1] = pk((aq[4 * a + 2] - mean * cq[a][2]) * qs, (aq[4 * a + 3] - mean * cq[a][3]) * qs);
      }
      if (hi) { qf[1][2] = 0x00003f80u; qf[1][3] = 0u; }
      PINV(qf[0]); PINV(qf[1]);
      f32x16 s8;
      asm volatile("s_nop 1" : "+v"(qf[0]), "+v"(qf[1]));
      MFMA32V0(s8, kv[0], qf[0]);
      MFMA32VA(s8, kv[1], qf[1]);
      SCHED();
      unsigned late;
      POLLED(pf, 0, fp)
      STAMP(4 + layer * 16);
      v4u ra[4], rb[4];
      {
        const rsrc_t r1 = mk(p.ws, t >= 1 ? WS_BYTES : 0), r2 = mk(p.ws, t >= 2 ? WS_BYTES : 0);
        const int kvo = kvs - 32768;
        rb[0] = ld16<SC1>(r2, kvo, 0); rb[1] = ld16<SC1>(r2, kvo, 1024);
        ra[0] = ld16<SC1>(r1, kvo, 16384); ra[1] = ld16<SC1>(r1, kvo, 16384 + 1024);
        SCHED();
        rb[2] = ld16<SC1>(r2, kvo, 2048); rb[3] = ld16<SC1>(r2, kvo, 3072);
        ra[2] = ld16<SC1>(r1, kvo, 16384 + 2048); ra[3] = ld16<SC1>(r1, kvo, 16384 + 3072);
        SCHED();
      }
#pragma unroll
      for (int j = 0; j < 16; ++j) wo[j >> 2][j & 3] = trfrag(wor[j & 3] + (j >> 2) * 4096, wor[(j & 3) ^ 2] + (j >> 2) * 4096 + 1024);
      SCHED();
      bar();
      LAS char* vw1 = X128(vw) + 1024;
      LAS char* vr1 = X128(vr0);
      *(LAS v4u*)(kvw + 32768) = kv[0];
      *(LAS v4u*)(kvw + 32768 + 1024) = kv[1];
      *(LAS v4u*)(vw + 32768) = kv[2];
      *(LAS v4u*)(vw1 + 32768) = kv[3];
      int rro = (64 * st + 16 * w + c) * 896 + dt * 128 + g * 8, b2o = (64 * st + 16 * w + c) * 2, b1o = ft * 128 + g * 8;
      unsigned pml[8], mk32[16], nk1l = nk1, nk2l = nk2;
      int ool = oo, t1l = t1o;
      LAS char* vr0l = vr0;
      PINV(rro); PINV(b2o); PINV(b1o); PINV(vw1); PINV(vr1); PINV(vr0l); PINV(ool); PINV(t1l); PINV(nk1l); PINV(nk2l);
#pragma unroll
      for (int i = 0; i < 8; ++i) { pml[i] = pm[i]; PINV(pml[i]); mk32[2 * i] = (unsigned)((int)(pml[i] << 16) >> 16); mk32[2 * i + 1] = (unsigned)((int)pml[i] >> 16); PINV(mk32[2 * i]); PINV(mk32[2 * i + 1]); }
      SCHED();
      f32x16 sc[8], se;
      v4u kf[8][2];
      float mx = -1e30f, mx2 = -1e30f;
#define MAX8(v, i) asm volatile("v_max3_f32 %0, %0, %2, %3\n\tv_max3_f32 %1, %1, %4, %5\n\tv_max3_f32 %0, %0, %6, %7\n\tv_max3_f32 %1, %1, %8, %9" \
        : "+v"(mx), "+v"(mx2) : "v"(v[i]), "v"(v[(i) + 1]), "v"(v[(i) + 2]), "v"(v[(i) + 3]), "v"(v[(i) + 4]), "v"(v[(i) + 5]), "v"(v[(i) + 6]), "v"(v[(i) + 7]))
      *(LAS v4u*)(kvw) = rb[0];
      MFMA32V0(sc[0], rb[0], qf[0]);
      rb[1][2] |= nk2l;
      *(LAS v4u*)(kvw + 1024) = rb[1];
      *(LAS v4u*)(kvw + 16384) = ra[0];
      SCHED();
      MFMA32VA(sc[0], rb[1], qf[1]);
      ra[1][2] |= nk1l;
      *(LAS v4u*)(kvw + 16384 + 1024) = ra[1];
      kf[4][0] = ra[0]; kf[4][1] = ra[1];
      MFMA32V0(sc[4], kf[4][0], qf[0]);
      bar();
      KEEP(late);
      STAMP(37 + layer * 4);
      MFMA32VA(sc[4], kf[4][1], qf[1]);
#pragma unroll
      for (int j = 1; j < 8; ++j) if (j != 4) { kf[j][0] = *(LAS v4u*)(kvw + j * 4096); kf[j][1] = *(LAS v4u*)(kvw + j * 4096 + 1024); }
      PINV(sc[0]);
#pragma unroll
      for (int i = 0; i < 16; ++i) se[i] = asf((asu(sc[0][i]) & mk32[i]) | (asu(s8[i]) & ~mk32[i]));
      PINV(se);
      SCHED();
      sfor<12>([&](auto N) INL {
        constexpr int n = N, jj = n >> 1, j = jj < 3 ? jj + 1 : jj + 2, hf = n & 1;
        if constexpr (hf == 0) MFMA32V0(sc[j], kf[j][0], qf[0]); else MFMA32VA(sc[j], kf[j][1], qf[1]);
        if constexpr (n < 2) MAX8(se, 8 * hf);
        else if constexpr (n < 4) MAX8(sc[4], 8 * hf);
        else if constexpr (n >= 6) MAX8(sc[jj - 2], 8 * hf);
        if constexpr (n == 4) { *(LAS v4u*)(vw) = rb[2]; *(LAS v4u*)(vw1) = rb[3]; }
        if constexpr (n == 5) { *(LAS v4u*)(vw + 16384) = ra[2]; *(LAS v4u*)(vw1 + 16384) = ra[3]; SCHED(); touch(mk(p.w1, S * F * 2), t1l, layer * KP * 32 * F * 2, L); }
        SCHED();
      });
      MAX8(sc[5], 0); MAX8(sc[5], 8); MAX8(sc[6], 0); MAX8(sc[6], 8); MAX8(sc[7], 0); MAX8(sc[7], 8);
      mx = max32(max3(mx, mx2, mx2));
      f32x16 nm;
#pragma unroll
      for (int i = 0; i < 16; ++i) nm[i] = -mx;
      PINV(nm);
      bar();
      STAMP(38 + layer * 4);
      f32x16 ao;
      v4u vf[9][2], pp[9][2];
      float ex[32][4];
#define VF(j, s) vf[j][s] = trfrag(vr0l + (j) * 4096 + (s) * 256, vr1 + (j) * 4096 + (s) * 256)
      sfor<33>([&](auto N) INL {
        constexpr int n = N, j = n >> 2, q = n & 3;
        if constexpr (j == 0) {
          if constexpr (q == 0) MFMA32VC(sc[1], kf[1][0], qf[0], nm);
          if constexpr (q == 1) MFMA32VA(sc[1], kf[1][1], qf[1]);
          if constexpr (q == 2) MFMA32VC(sc[2], kf[2][0], qf[0], nm);
          if constexpr (q == 3) MFMA32VA(sc[2], kf[2][1], qf[1]);
        } else if constexpr (j < 8) {
          if constexpr (q == 1) { if constexpr (j == 1) MFMA32V0(ao, vf[0][0], pp[0][0]); else MFMA32VA(ao, vf[j - 1][0], pp[j - 1][0]); }
          if constexpr (q == 2) MFMA32VA(ao, vf[j - 1][1], pp[j - 1][1]);
          if constexpr (j + 2 < 8) {
            if constexpr (q == 0) MFMA32VC(sc[j + 2], kf[j + 2][0], qf[0], nm);
            if constexpr (q == 3) MFMA32VA(sc[j + 2], kf[j + 2][1], qf[1]);
          } else if constexpr (j == 6) {
            if constexpr (q == 0) MFMA32VA(ao, vf[8][0], pp[8][0]);
            if constexpr (q == 3) MFMA32VA(ao, vf[8][1], pp[8][1]);
          }
        } else MFMA32VA(ao, vf[7][0], pp[7][0]);
        SCHED();
        if constexpr (n < 32) {
#pragma unroll
          for (int i = 0; i < 4; ++i) { if constexpr (j == 0) ex[n][i] = __builtin_amdgcn_exp2f(se[4 * q + i] - mx); else ex[n][i] = __builtin_amdgcn_exp2f(sc[j][4 * q + i]); }
        }
        SCHED();
        if constexpr (n > 0) {
          constexpr int jp = (n - 1) >> 2, qp = (n - 1) & 3;
#pragma unroll
          for (int i = 0; i < 2; ++i) {
            const unsigned u = pk(ex[n - 1][2 * i], ex[n - 1][2 * i + 1]);
            if constexpr (jp == 0) { pp[0][qp >> 1][(2 * qp + i) & 3] = u & pml[2 * qp + i]; pp[8][qp >> 1][(2 * qp + i) & 3] = u & ~pml[2 * qp + i]; }
            else pp[jp][qp >> 1][(2 * qp + i) & 3] = u;
          }
        }
        if constexpr (n >= 2 && n < 10 && q >= 2) rr[(n >> 2) * 2 + q - 2] = ld8<0>(mk(p.x, (on35 && layer == 0) ? S * D * 2 : 0), rro, (((n >> 2) * 2 + q - 2) ^ pa) * 32);
        if constexpr (n == 10) b2r = (unsigned)__builtin_amdgcn_raw_buffer_load_b16(mk(p.b2, S * 2), b2o, 0, 0);
        if constexpr (n >= 14 && n < 22 && q >= 2) bb[((n >> 2) - 3) * 2 + q - 2] = ld8<0>(mk(p.b1, F * 2), b1o, (((n >> 2) - 3) * 2 + q - 2) * 32);
        if constexpr (n == 31) __builtin_amdgcn_s_waitcnt(0x0F70);
        if constexpr (n == 22) KEEP(nm);
        if constexpr (j < 8 && q < 2) VF(j, q);
        if constexpr (j == 5 && q >= 2) VF(8, q - 2);
        SCHED();
      });
      asm volatile("s_nop 1" : "+v"(pp[7][1]));
      MFMA32VA(ao, vf[7][1], pp[7][1]);
      MFMA_SETTLE(ao);
      const float inv = __builtin_amdgcn_rcpf(hi32(ao[12]));
      v4u of[2];
#pragma unroll
      for (int i = 0; i < 16; i += 2) of[i >> 3][(i >> 1) & 3] = pk(ao[i] * inv, ao[i + 1] * inv);
      if (hi) { of[1][2] = 0u; of[1][3] = 0u; }
      st16<SC1>(of[0], rws, ool, 0);
      st16<SC1>(of[1], rws, ool, 512);
      SCHED();
      STAMP(39 + layer * 4);
      bar();
      DPUMP(Y0, Y0, Y1)
      publish_n<4 * (Y1 - Y0)>(fl + (1 * 256 + bid) * 32, epoch);
      STAMP(5 + layer * 16);
    }
    if (on35) {
      {
        DPUMP(Y0, Y1, CMIN(Y1 + 1, Y2))
        FRESH();
        const int tq = st >> 1;
        GAS unsigned* fp = fl + (1 * 256 + (((tq & 1) * 16 + 4 * w + (lane & 3)) * 8 + (tq >> 1))) * 32;
        int oo = O_OFF + layer * O_SZ + (st * 16 + 4 * w) * 4096 + lane * 16;
        PINV(fp); PINV(oo);
        DPUMP(Y0, CMIN(Y1 + 1, Y2), CMIN(Y1 + 2, Y2))
        int sb = S3_OFF + layer * S3_SZ + st * 8192 + (16 * w + c) * 16;
        int sw = g < 2 ? sb + dt * 1024 + g * 8 : OOB;
        int ob[4];
#pragma unroll
        for (int b = 0; b < 4; ++b) { ob[b] = ((b + w) & 3) * 1024; PINS(ob[b]); }
        PINV(sb); PINV(sw); PINV(red);
#pragma unroll
        for (int a = 0; a < 4; ++a) { res[a] += unpack4(rr[a]); PINV(res[a]); }
        unsigned pf;
        DPUMP(Y0, CMIN(Y1 + 2, Y2), Y2)
        POLL(pf, fp);
        DPUMP(Y0, Y2, YG)
        STASH(0, SO)
        unsigned late;
        POLLED(pf, 4 * (YG - Y2 + layer * SO), fp)
        STAMP(6 + layer * 16);
        v4u of[16];
#define LOAD_O(I) of[I] = ld16<SC1>(rws, oo + ((I) & 3) * 4096, ob[(((I) >> 2) + 1) & 3])
#pragma unroll
        for (int i = 0; i < 12; ++i) { LOAD_O(i); SCHED(); }
        vmw<12>();
        KEEP(late);
        STAMP(50 + layer * 4);
        f32x4 acc[4][4], part[12];
        sfor<16>([&](auto I) INL {
          constexpr int i = I, b = ((i >> 2) + 1) & 3, k = i & 3;
#pragma unroll
          for (int a = 0; a < 4; ++a) {
            acc[a][b] = mfma16(wo[k][a], of[i], k > 0 ? acc[a][b] : (f32x4){0.f, 0.f, 0.f, 0.f});
            SCHED();
            if constexpr (i < 4) { if (a == 0) LOAD_O(i + 12); }
            if constexpr (i < 12) { if (a == 0) DTAIL1(Y0, YG, i) }
            if constexpr (i >= 4 && k < 2) { if (a == 1 || a == 3) REDW(2 * k + (a >> 1), i >> 2); }
            if constexpr (i == 14) { if (a == 0) bar(); }
            if constexpr (i >= 14) { if (a < 3) { const int j = 6 * (i - 14) + 2 * a; part[j] = REDR(j / 3, j % 3 + 1); part[j + 1] = REDR((j + 1) / 3, (j + 1) % 3 + 1); } }
            SCHED();
          }
        });
        STAMP(51 + layer * 4);
#pragma unroll
        for (int a = 0; a < 4; ++a) res[a] = ((res[a] + part[3 * a]) + (part[3 * a + 1] + part[3 * a + 2])) + acc[a][0];
        f32x2 s1 = {0.f, 0.f}, s2 = {0.f, 0.f};
        stats16(res, 15, s1, s2);
        st8<SC1>((v2u){asu(sum_ab(s1[0] + s1[1], s2[0] + s2[1])), epoch}, rws, sw, 0);
        SCHED();
        DTAIL1(Y0, YG, 12) DTAIL1(Y0, YG, 13) DTAIL1(Y0, YG, 14) DTAIL1(Y0, YG, 15)
        STAMP(7 + layer * 16);
        bar();
        DPUMP(YG, YG, CMIN(YG + 1, YG2))
        int sa = sb + g * 1024, sc = g < 3 ? sb + (g + 4) * 1024 : OOB;
        int ho = HT_OFF + layer * HT_SZ + (dt * 64 + 2 * st + (w >> 1)) * 4096 + (g >> 1) * 1024 + (2 * (w & 1) + (g & 1)) * 256 + c * 16;
        PINV(sa); PINV(sc); PINV(ho);
        DPUMP(YG, CMIN(YG + 1, YG2), CMIN(YG + 2, YG2))
        LAS char* hst = L + L_HST + w * 2048;
        const int hr = 8 * (g & 1) + q4;
        int h0 = raddr(hr) ^ ((g >> 1) << 5), h1 = raddr(hr + 4) ^ ((g >> 1) << 5), hw = (c * 128 + (swz(c) << 5) + g * 8) ^ (pa << 5);
        PINV(h0); PINV(h1); PINV(hw);
        DPUMP(YG, CMIN(YG + 2, YG2), YG2)
        v4u ga, gb;
        asm volatile("buffer_load_dwordx4 %0, %2, %4, 0 offen sc1\n\tbuffer_load_dwordx4 %1, %3, %4, 0 offen sc1" : "=&v"(ga), "=&v"(gb) : "v"(sa), "v"(sc), "s"(rws) : "memory");
        DPUMP(YG, YG2, YH)
        STASH(SO, SO + SG)
        asm volatile("s_waitcnt vmcnt(%2)" : "+v"(ga), "+v"(gb) : "n"(4 * (YH - YG2 + layer * SG)) : "memory");
        while ((__builtin_amdgcn_ballot_w64((ga.y != epoch) | (ga.w != epoch)) | (__builtin_amdgcn_ballot_w64((gb.y != epoch) | (gb.w != epoch)) << 16)) != 0ull) {
          if (budget == 0u) break;
          --budget;
          asm volatile("buffer_load_dwordx4 %0, %2, %4, 0 offen sc1\n\tbuffer_load_dwordx4 %1, %3, %4, 0 offen sc1\n\ts_waitcnt vmcnt(0)"
                       : "=&v"(ga), "=&v"(gb) : "v"(sa), "v"(sc), "s"(rws) : "memory");
        }
        STAMP(8 + layer * 16);
        const unsigned t12 = asu(sum_ab(asf(ga.x) + asf(gb.x), asf(ga.z) + asf(gb.z)));
        const auto tt = __builtin_amdgcn_permlane16_swap(t12, t12, false, false);
        const float t1 = asf(tt[0]), t2 = asf(tt[1]);
        const float m = t1 * (1.f / D), var = fmaxf(t2 * (1.f / D) - m * m, 0.f);
        const float r1 = __builtin_amdgcn_rsqf(var + EPS);
        const float r = r1 * __builtin_amdgcn_rsqf(var * r1 * r1 + EPS), nr = -m * r;
#pragma unroll
        for (int a = 0; a < 4; ++a) {
          const v2u v = {pk(res[a][0] * r + nr, res[a][1] * r + nr), pk(res[a][2] * r + nr, res[a][3] * r + nr)};
          *(LAS v2u*)(hst + (hw ^ (a << 5))) = v;
        }
#pragma unroll
        for (int e = 0; e < 2; ++e) st16<SC1>(trfrag(hst + (h0 ^ (e << 6)), hst + (h1 ^ (e << 6))), rws, ho, e * 2048);
        SCHED();
        DPUMP(YG, YH, YH1)
        publish_n<4 * (YH1 - YH)>(fl + (2 * 256 + bid) * 32, epoch);
        STAMP(9 + layer * 16);
      }
      DPUMP(YG, YH1, CMIN(YH1 + 1, YH2))
      if (on4) {
        FRESH();
        const int sp = 8 * w + (lane & 7);
        GAS unsigned* fp = fl + (2 * 256 + ((sp & 3) * 7 + dt4) * 8 + (sp >> 2)) * 32;
        int hv[4];
#pragma unroll
        for (int b = 0; b < 4; ++b) { hv[b] = HT_OFF + layer * HT_SZ + (dt4 * 64 + 16 * w) * 4096 + ((b + w4) & 3) * 1024 + lane * 16; PINV(hv[b]); }
        PINV(fp);
        DPUMP(YG, CMIN(YH1 + 1, YH2), CMIN(YH1 + 2, YH2))
        int uo = U_OFF + layer * U_SZ + (dt4 * 56 + 2 * ft) * 4096 + w4 * 1024 + lane * 16;
        PINV(uo); PINV(red);
        unsigned pf;
        DPUMP(YG, CMIN(YH1 + 2, YH2), YH2)
        STASH(SO + SG, NS - SH)
        POLL(pf, fp);
        DPUMP(YG, YH2, YE)
        STASH(NS - SH, NS)
        unsigned late;
        POLLED(pf, 4 * (YE - YH2 + layer * SH), fp)
        STAMP(10 + layer * 16);
        v4u af[64];
#define LOAD_H(I) { int so_; asm volatile("s_mov_b32 %1, %4\n\tbuffer_load_dwordx4 %0, %2, %3, %1 offen sc1" \
          : "=&v"(af[I]), "=&s"(so_) : "v"(hv[(((I) >> 4) + 1) & 3]), "s"(rws), "n"(((I) & 15) * 4096) : "memory"); }
        sfor<WIN4>([&](auto I) INL { LOAD_H(I) SCHED(); });
        f32x4 acc[4][4], part[12];
        sfor<64>([&](auto I) INL {
          constexpr int i = I, b = ((i >> 4) + 1) & 3, k = i & 15;
          constexpr int np = i <= 54 ? 0 : i <= 62 ? i - 54 : 10;
          asm volatile("s_waitcnt vmcnt(%1)" : "+v"(af[i]) : "n"((i + WIN4 <= 64 ? WIN4 - 1 : 63 - i) + np) : "memory");
          if constexpr (i == 0) KEEP(late);
          SCHED();
          sfor<4>([&](auto A) INL {
            constexpr int a = A;
            acc[a][b] = mfma16(wz[k][a], af[i], k > 0 ? acc[a][b] : (f32x4){0.f, 0.f, 0.f, 0.f});
            SCHED();
            if constexpr (i + WIN4 < 64 && a == 0) LOAD_H(i + WIN4)
            if constexpr (i < 8) { if constexpr (a == 1) DTAIL1(YG, YE, 2 * i) if constexpr (a == 2) DTAIL1(YG, YE, 2 * i + 1) }
            if constexpr (i >= 16 && k < 2 && (a & 1)) REDW(2 * k + (a >> 1), i >> 4);
            if constexpr (i == 50 && a == 3) bar();
            if constexpr (i >= 51 && i < 54) part[4 * (i - 51) + a] = REDR((4 * (i - 51) + a) / 3, (4 * (i - 51) + a) % 3 + 1);
            if constexpr (i == 54 && a == 1) bar();
            if constexpr (i >= 54 && a == 2) PIECE(Z_U, np)
            if constexpr (i >= 62 && a == 3) PIECE(Z_U, np + 1)
            if constexpr (i >= 55 && i < 59 && a == 0) {
              constexpr int q = i - 55;
              part[3 * q] = (part[3 * q] + unpack4(bb[q])) + (part[3 * q + 1] + part[3 * q + 2]);
              PINV(part[3 * q]);
            }
            SCHED();
          });
        });
        STAMP(11 + layer * 16);
        v4u uf[2];
        sfor<8>([&](auto H) INL {
          constexpr int h = H, a = h >> 1, j0 = 2 * (h & 1);
          float gl[2];
#pragma unroll
          for (int j = 0; j < 2; ++j) {
            const float u = acc[a][0][j0 + j] + part[3 * a][j0 + j];
            const float e = __builtin_amdgcn_exp2f(u * (u * u * (-0.10294324f) - 2.3022082f));
            gl[j] = u * __builtin_amdgcn_rcpf(1.f + e);
          }
          uf[h >> 2][h & 3] = pk(gl[0], gl[1]);
          if constexpr (h & 1) {
            PINV(uf[h >> 2]);
            if constexpr (h == 3) st16<SC1>(uf[0], rws, uo, 0);
            if constexpr (h == 7) st16<SC1>(uf[1], rws, uo, 4096);
            SCHED();
            if constexpr (h < 7 && 3 + h / 2 < NU0) DPUMP(Z_U, Z_U + 3 + h / 2, Z_U + 4 + h / 2)
          }
        });
        STAMP(40 + layer * 4);
        DPUMP(Z_U, Z_U + NU0, Z_U + NU0 + NU1)
        publish_n<4 * NU1>(fl + (3 * 256 + bid) * 32, epoch);
      } else {
        DPUMP(YG, CMIN(YH1 + 1, YH2), YE)
        STASH(SO + SG, NS)
        DRAIN();
        DTAIL(YG, YE)
        LGKM0();
        DPUMP(Z_U, Z_U, Z_U + NU0 + NU1)
      }
      STAMP(12 + layer * 16);
      {
        DPUMP(Z_U, Z_U + NU0 + NU1, CMIN(Z_U + NU0 + NU1 + 1, YU3))
        FRESH();
        const int pi = wrap7(lane & 7);
        GAS unsigned* fp = fl + (3 * 256 + (MAP2D ? ((dt & 3) * 7 + pi) * 8 + (dt >> 2) : pi == 3 ? (21 + (dt & 3)) * 8 + (dt >> 2) : (7 * (pi & 3) + dt) * 8 + (pi >> 2)) + 2 * w) * 32;
        int uv[4];
#pragma unroll
        for (int a = 0; a < 4; a += 2) { uv[a] = U_OFF + layer * U_SZ + (dt * 56 + 14 * w) * 4096 + (a ^ pa) * 1024 + lane * 16; PINV(uv[a]); uv[a + 1] = uv[a] + 1024; }
        PINV(fp);
        DPUMP(Z_U, CMIN(Z_U + NU0 + NU1 + 1, YU3), CMIN(Z_U + NU0 + NU1 + 2, YU3))
        int ow = L_HST + w * 2048 + ((c * 128 + (swz(c) << 5) + g * 8) ^ (pa << 5)), orr = L_HST + w * 2048 + wa0;
        PINV(ow); PINV(orr);
        int xb = layer ? (OFULL ? (64 * st + 16 * w + wrow) * 896 + dt * 128 + (lane & 7) * 16 : (64 * st + 16 * w + c) * 896 + dt * 128 + g * 8)
                       : X2_OFF + (2 * st + (w >> 1)) * 28672 + (4 * dt + (g >> 1)) * 1024 + ((g & 1) * 32 + (w & 1) * 16 + c) * 16;
        int so = g < 2 ? S5_OFF + (st * 8 + dt) * 512 + (16 * w + c) * 8 + g * 4 : OOB;
#pragma unroll
        for (int a = 0; a < 4; ++a) { res[a] += bflo(b2r); PINV(res[a]); }
        PINV(xb); PINV(so); PINV(red);
        unsigned pf;
        DPUMP(Z_U, CMIN(Z_U + NU0 + NU1 + 2, YU3), YU3)
        POLL(pf, fp);
        DPUMP(Z_U, YU3, YZ)
        if constexpr (layer == 1) {
          const int dlt = L_WQ + w * 20480 - (L_KVW + w * 16384);
#pragma unroll
          for (int i = 0; i < 4; ++i) {
            LAS char* ps = pr2[i] + dlt;
#pragma unroll
            for (int j = 0; j < NS; ++j) wz[30 - NS + j][i] = trfrag(ps + j * 4096, ps + j * 4096 + 2048);
          }
          SCHED();
        }
        unsigned late;
        POLLED(pf, 4 * (YZ - YU3), fp)
        STAMP(13 + layer * 16);
        v4u af[56];
#define LOAD_U(I) af[I] = ld16<SC1>(rws, uv[(I) / 14], ((I) % 14) * 4096)
#pragma unroll
        for (int i = 0; i < WIN5; ++i) { LOAD_U(i); SCHED(); }
        vmw<WIN5>();
        KEEP(late);
        f32x4 acc[4][4], part[4][3];
        v2u xo[4];
        f32x2 s1 = {0.f, 0.f}, s2 = {0.f, 0.f};
        const rsrc_t ro = mk(p.out, S * D * 2);
        sfor<56>([&](auto I) INL {
          constexpr int i = I, a = i / 14, k = i % 14;
#pragma unroll
          for (int bq = 0; bq < 4; ++bq) {
            const int b = (bq + 1) & 3;
            if (a < 3 || b) acc[a][b] = mfma16(af[i], wz[16 + k][b], k > 0 ? acc[a][b] : (f32x4){0.f, 0.f, 0.f, 0.f});
            SCHED();
            if constexpr (i + WIN5 < 56) { if (bq == 0) LOAD_U(i + WIN5); }
            if constexpr (i < 8) { if (bq == 1) DTAIL1(Z_U, YZ, 2 * i) if (bq == 2) DTAIL1(Z_U, YZ, 2 * i + 1) }
            if constexpr (a > 0 && k < 3) { if (bq == 2) REDW(a - 1, k + 1); }
            if constexpr (i == 46) { if (bq == 2) bar(); }
            if constexpr (i >= 47 && i < 50) { if (bq < 3) part[i - 47][bq] = REDR(i - 47, bq + 1); }
            if constexpr (i >= 50 && i < 53) { if (bq == 2) { res[i - 50] = ((res[i - 50] + part[i - 50][0]) + (part[i - 50][1] + part[i - 50][2])) + acc[i - 50][0]; PINV(res[i - 50]); } }
            if constexpr (i >= 53) { if (bq == 2) { xo[i - 53] = (v2u){pk(res[i - 53][0], res[i - 53][1]), pk(res[i - 53][2], res[i - 53][3])}; stats16(res, 1 << (i - 53), s1, s2); PINV(xo[i - 53]); PINV(s1); PINV(s2); } }
            SCHED();
          }
        });
        STAMP(14 + layer * 16);
        sfor<14>([&](auto K) INL {
          constexpr int k = K;
          acc[3][0] = mfma16(af[42 + k], wz[16 + k][0], k > 0 ? acc[3][0] : (f32x4){0.f, 0.f, 0.f, 0.f});
          SCHED();
          if constexpr (k >= 1 && k < 4) REDW(3, k);
          if constexpr (k == 5) {
            if (layer == 0) st16<SC1>((v4u){xo[0].x, xo[0].y, xo[1].x, xo[1].y}, rws, xb, pa * 1024);
            else if (!OFULL) { st8<OAUX>(xo[0], ro, xb, pa * 32); st8<OAUX>(xo[1], ro, xb, pa * 32 + 32); st8<OAUX>(xo[2], ro, xb, (2 ^ pa) * 32); }
            if (OFULL) { _Pragma("unroll") for (int a = 0; a < 3; ++a) *(LAS v2u*)(L + (ow ^ (a << 5))) = xo[a]; }
          }
          if constexpr (k == 7) { bar(); part[3][0] = REDR(3, 1); part[3][1] = REDR(3, 2); part[3][2] = REDR(3, 3); }
          SCHED();
        });
        res[3] = ((res[3] + part[3][0]) + (part[3][1] + part[3][2])) + acc[3][0];
        xo[3] = (v2u){pk(res[3][0], res[3][1]), pk(res[3][2], res[3][3])};
        if (layer == 0) {
          st16<SC1>((v4u){xo[2].x, xo[2].y, xo[3].x, xo[3].y}, rws, xb, (2 ^ pa) * 1024);
          SCHED();
          stats16(res, 8, s1, s2);
          __builtin_amdgcn_raw_buffer_store_b32(asu(sum_ab(s1[0] + s1[1], s2[0] + s2[1])), rws, so, 0, SC1);
          publish(flags + (4 * 256 + bid) * 32, epoch);
        } else {
          if (!OFULL) st8<OAUX>(xo[3], ro, xb, (2 ^ pa) * 32 + 32);
          else {
            *(LAS v2u*)(L + (ow ^ (3 << 5))) = xo[3];
            const v4u r0 = *(LAS v4u*)(L + orr), r1 = *(LAS v4u*)(L + (orr ^ 64) + 1024);
            st16<OAUX>(r0, ro, xb, 0);
            st16<OAUX>(r1, ro, xb, 8 * 896);
          }
        }
      }
    }
    else {
      FRESH();
      const int rw = 4 * (by - 28) + (lane >> 4) + 16 * ((lane >> 2) & 3);
      if (MAP2D) {
        const int l7 = (lane * 37) >> 8, vo = lane < 56 ? (512 * w + 256 + 8 * (by - 28) + l7) * (F * 2) + (7 * (bx >> 1) + lane - 7 * l7) * 128 : OOB;
#pragma unroll 1
        for (int i = 0; i < 8; ++i) { touch(mk(p.w1, S * F * 2), vo, i * 32 * F * 2, L); vmw<PACE>(); }
      } else {
        const int vo = (512 * w + 256 + rw) * (F * 2) + (7 * (bx >> 1) + 3 * (bx & 1) + (lane & 3)) * 128;
#pragma unroll 1
        for (int i = layer * (KP / 2); i < 4; ++i) { touch(mk(p.w1, S * F * 2), vo, i * 64 * F * 2, L); vmw<PACE>(); }
      }
      const int vo = (448 * w + rw) * (S * 2) + bx * 512 + (lane & 3) * 128;
#pragma unroll 1
      for (int i = 0; i < (layer ? 7 - (Z_U - 16) / 2 : 7); ++i) { touch(mk(p.w2, S * F * 2), vo, (layer ? (i < 3 ? i + 4 : i - 3 + (Z_U - 16) / 2) : i) * 64 * S * 2, L); vmw<PACE>(); }
      if (ISLEEP && layer == 0) {
        while (__hip_atomic_load(fl + (3 * 256 + bx) * 32, RLX_AGENT) != epoch && budget) { --budget; __builtin_amdgcn_s_sleep(20); }
        __builtin_amdgcn_s_sleep(ISLEEP);
      }
    }
    STAMP(15 + layer * 16);
    if constexpr (layer == 0) {
      FRESH();
      const int st1 = 2 * t + (w >> 1);
      f32x16 ak, av;
      v4u wk[28], wv[28];
      LAS char* wb = WB(0) + 28672;
      PINV(wb);
      wk[0] = WFRAG(wb, 0, 0); wv[0] = WFRAG(wb, 1, 0); wk[1] = WFRAG(wb, 0, 1); wv[1] = WFRAG(wb, 1, 1);
      f32x4 ck[4], cv[4];
#pragma unroll
      for (int a = 0; a < 4; ++a) { ck[a] = CSUM(1, a); cv[a] = CSUM(2, a); PINV(ck[a]); PINV(cv[a]); }
      int xo = X2_OFF + (4 * t + w) * 28672 + lane * 16, so = S5_OFF + (st1 * 8 + hh * 4) * 512 + ((w & 1) * 32 + r32) * 8;
      int xt = lane < 14 ? X2_OFF + (4 * t + w) * 28672 + (hd * 14 + lane) * 128 : OOB;
      int so3 = hh ? OOB : so + 3 * 512;
      PINV(xo); PINV(so); PINV(so3); PINV(xt);
      kvs += KV_SZ; oo += O_SZ; fo += 5 * 256 * 128;
      PINV(kvs); PINA(oo); PINA(fo);
      GAS unsigned* fp = flags + (4 * 256 + ((st1 & 3) * 7 + wrap7(lane & 7)) * 8 + (st1 >> 2)) * 32;
      wait_flags(fp, epoch, budget);
      STAMP(18);
      v2u sp[4];
      const unsigned xtv = XTCH ? __builtin_amdgcn_raw_buffer_load_b32(rws, xt, 0, SC1) : 0u;
      SCHED();
#define LOAD_X(I) xf[I] = ld16<SC1>(rws, xo + ((I) & 3) * 1024, ((I) >> 2) * 4096)
#pragma unroll
      for (int i = 0; i < WINX; ++i) { LOAD_X(i); SCHED(); }
      sfor<28>([&](auto I) INL {
        constexpr int ks = I;
        if constexpr (ks == 0) MFMA32V0(ak, wk[ks], xf[ks]); else MFMA32VA(ak, wk[ks], xf[ks]);
        if constexpr (ks + 2 < 28) wk[ks + 2] = WFRAG(wb + ((ks + 2) >> 2) * 4096, 0, (ks + 2) & 3);
        if constexpr (ks + WINX < 28) LOAD_X(ks + WINX);
        SCHED();
        if constexpr (ks == 0) MFMA32V0(av, wv[ks], xf[ks]); else MFMA32VA(av, wv[ks], xf[ks]);
        if constexpr (ks + 2 < 28) wv[ks + 2] = WFRAG(wb + ((ks + 2) >> 2) * 4096, 1, (ks + 2) & 3);
        if constexpr (ks < 4) sp[ks] = ld8<SC1>(rws, ks == 3 ? so3 : so + ks * 512, 0);
        if constexpr (ks == 2) KEEP(xtv);
        if constexpr (ks == 18) {
          float s1 = (asf(sp[0].x) + asf(sp[1].x)) + (asf(sp[2].x) + asf(sp[3].x));
          float s2 = (asf(sp[0].y) + asf(sp[1].y)) + (asf(sp[2].y) + asf(sp[3].y));
          s1 = sum32(s1); s2 = sum32(s2);
          mean = s1 * (1.f / D);
          rstd = s2 * (1.f / D) - mean * mean;
          PINV(mean); PINV(rstd);
        }
        if constexpr (ks == 19) { rstd = __builtin_amdgcn_rsqf(fmaxf(rstd, 0.f) + EPS); PINV(rstd); }
        if constexpr (ks >= 20 && ks < 24) { ck[ks - 20] *= -mean * rstd; cv[ks - 20] *= -mean * rstd; PINV(ck[ks - 20]); PINV(cv[ks - 20]); }
        SCHED();
      });
      MFMA_SETTLE2(ak, av);
#pragma unroll
      for (int i = 0; i < 16; ++i) ak[i] = ak[i] * rstd + ck[i >> 2][i & 3];
      KVOUT(ak, 0)
#pragma unroll
      for (int i = 0; i < 16; ++i) av[i] = av[i] * rstd + cv[i >> 2][i & 3];
      KVOUT(av, 1)
    }
  });
  asm volatile(".Lfk_code_to:");
  if (blockIdx.x == 0 && threadIdx.x == 0) __hip_atomic_store(ctrl, epoch, RLX_AGENT);
}

extern "C" void kernel_launch(void* const* d_in, const int* in_sizes, int n_in, void* d_out, int out_size, void* d_ws,
                              size_t ws_size, hipStream_t stream) {
  Params p;
  p.x = d_in[0]; p.wqkv = d_in[1]; p.wo = d_in[2];
  p.w1 = d_in[4]; p.b1 = d_in[5]; p.w2 = d_in[6]; p.b2 = d_in[7];
  p.out = d_out; p.ws = d_ws;
  (void)hipFuncSetAttribute((const void*)fused_kernel, hipFuncAttributeMaxDynamicSharedMemorySize, LDS_BYTES);
  fused_kernel<<<256, 256, LDS_BYTES, stream>>>(p);
}
```

```cpp
#include <hip/hip_runtime.h>
#include <utility>

#define GAS __attribute__((address_space(1)))
#define LAS __attribute__((address_space(3)))
#define DI __device__ __forceinline__
#define INL __attribute__((always_inline))
#define RLX_AGENT __ATOMIC_RELAXED, __HIP_MEMORY_SCOPE_AGENT
typedef unsigned v4u __attribute__((ext_vector_type(4)));
typedef unsigned v2u __attribute__((ext_vector_type(2)));
typedef float f32x2 __attribute__((ext_vector_type(2)));
typedef float f32x4 __attribute__((ext_vector_type(4)));
typedef float f32x16 __attribute__((ext_vector_type(16)));
typedef short s16x4 __attribute__((ext_vector_type(4)));
typedef __bf16 bf16x2 __attribute__((ext_vector_type(2)));
typedef __bf16 bf16x8 __attribute__((ext_vector_type(8)));
typedef __amdgpu_buffer_rsrc_t rsrc_t;

constexpr int S = 2048, D = 448, F = 1792;
constexpr float EPS = 1e-5f;
constexpr int FLAG_OFF = 4096;
constexpr int KV_OFF = 512 * 1024, KV_SZ = 16 * 64 * 4096;
constexpr int O_OFF = KV_OFF + 2 * KV_SZ, O_SZ = 32 * 16 * 4096;
constexpr int S3_OFF = O_OFF + 2 * O_SZ, S3_SZ = 32 * 8 * 1024;
constexpr int HT_OFF = S3_OFF + 2 * S3_SZ, HT_SZ = 7 * 64 * 4096;
constexpr int U_OFF = HT_OFF + 2 * HT_SZ, U_SZ = 7 * 56 * 4096;
constexpr int X2_OFF = U_OFF + 2 * U_SZ, X2_SZ = 64 * 28 * 1024;
constexpr int S5_OFF = X2_OFF + X2_SZ, S5_SZ = 32 * 8 * 512;
constexpr int WS_BYTES = S5_OFF + S5_SZ;
constexpr int L_WQ = 0;
constexpr int L_KVW = 86016;
constexpr int L_CSP = L_KVW + 65536;
constexpr int L_CS = L_CSP + 1536 + 128;
constexpr int L_HST = L_CS + 384;
constexpr int L_DUMP = L_HST + 8192;
constexpr int LDS_BYTES = L_DUMP + 256;

static_assert(L_HST % 128 == 0 && LDS_BYTES <= 163840, "LDS map");
constexpr int SC1 = 16, OOB = 0x7ffff000;
constexpr int WIN = 12;
constexpr int NO2 = 4, NO3 = 2, NG2 = 3, NG3 = 2, NH2 = 4, NH3 = 3, NU3 = 2;
constexpr int KP = 5, MO2 = 4, MO3 = 1, MG2 = 3, MG3 = 0, NS = 5, SG = 1, SH = 2;
constexpr int Z_U = NO2 + NO3 + NG2 + NG3 + NH2 + NH3;
static_assert(Z_U >= 16 && Z_U + 3 + NU3 <= 30 && Z_U + 3 <= 30 - NS && NS <= 5 && SG + SH <= NS, "schedule");

struct Params {
  const void *x, *wqkv, *wo, *w1, *b1, *w2, *b2;
  void *out, *ws;
};

template <int... I, class Fn> DI void sfor_(std::integer_sequence<int, I...>, Fn&& f) { (f(std::integral_constant<int, I>{}), ...); }
template <int N, class Fn> DI void sfor(Fn&& f) { sfor_(std::make_integer_sequence<int, N>{}, f); }
DI float asf(unsigned u) { return __builtin_bit_cast(float, u); }
DI unsigned asu(float f) { return __builtin_bit_cast(unsigned, f); }
DI rsrc_t mk(const void* p, int bytes) { return __builtin_amdgcn_make_buffer_rsrc((void*)p, 0, bytes, 0x00020000); }
template <int AUX> DI v4u ld16(rsrc_t r, int voff, int soff) { return __builtin_amdgcn_raw_buffer_load_b128(r, voff, soff, AUX); }
template <int AUX> DI v2u ld8(rsrc_t r, int voff, int soff) { return __builtin_amdgcn_raw_buffer_load_b64(r, voff, soff, AUX); }
template <int AUX> DI void st16(v4u v, rsrc_t r, int voff, int soff) { __builtin_amdgcn_raw_buffer_store_b128(v, r, voff, soff, AUX); }
template <int AUX> DI void st8(v2u v, rsrc_t r, int voff, int soff) { __builtin_amdgcn_raw_buffer_store_b64(v, r, voff, soff, AUX); }
DI f32x4 mfma16(v4u a, v4u b, f32x4 c) {
  return __builtin_amdgcn_mfma_f32_16x16x32_bf16(__builtin_bit_cast(bf16x8, a), __builtin_bit_cast(bf16x8, b), c, 0, 0, 0);
}
DI f32x16 mfma32(v4u a, v4u b, f32x16 c) {
  return __builtin_amdgcn_mfma_f32_32x32x16_bf16(__builtin_bit_cast(bf16x8, a), __builtin_bit_cast(bf16x8, b), c, 0, 0, 0);
}
DI unsigned pk(float lo, float hi) { return __builtin_bit_cast(unsigned, __builtin_convertvector((f32x2){lo, hi}, bf16x2)); }
DI float bflo(unsigned u) { return asf(u << 16); }
DI float bfhi(unsigned u) { return asf(u & 0xffff0000u); }
DI f32x4 unpack4(v2u v) { return (f32x4){bflo(v.x), bfhi(v.x), bflo(v.y), bfhi(v.y)}; }
DI v2u trrd(LAS char* p) { return __builtin_bit_cast(v2u, __builtin_amdgcn_ds_read_tr16_b64_v4i16((LAS s16x4*)p)); }
DI v4u trfrag(LAS char* p0, LAS char* p1) { v2u a = trrd(p0), b = trrd(p1); return (v4u){a.x, a.y, b.x, b.y}; }
DI float hi32(float v) {
  unsigned u = asu(v);
  auto r = __builtin_amdgcn_permlane32_swap(u, u, false, false);
  return asf(r[1]);
}
DI float sum32(float v) {
  unsigned u = asu(v);
  auto r = __builtin_amdgcn_permlane32_swap(u, u, false, false);
  return asf(r[0]) + asf(r[1]);
}
DI float max32(float v) {
  unsigned u = asu(v);
  auto r = __builtin_amdgcn_permlane32_swap(u, u, false, false);
  return fmaxf(asf(r[0]), asf(r[1]));
}
DI float sum_ab(float a, float b) {
  auto r = __builtin_amdgcn_permlane16_swap(asu(a), asu(b), false, false);
  return sum32(asf(r[0]) + asf(r[1]));
}
DI void stats16(const f32x4 (&v)[4], int n, f32x2& s1, f32x2& s2) {
#pragma unroll
  for (int a = 0; a < 4; ++a)
    if (n >> a & 1) {
      const f32x2 lo = {v[a][0], v[a][1]}, hi = {v[a][2], v[a][3]};
      s1 += lo; s2 += lo * lo; s1 += hi; s2 += hi * hi;
    }
}
#define FENCE() asm volatile("" ::: "memory")
#define SCHED() __builtin_amdgcn_sched_barrier(0)
#define DRAIN() asm volatile("s_waitcnt vmcnt(0)" ::: "memory")
#define PINV(x) asm volatile("" : "+v"(x))
#define PINA(x) asm volatile("" : "+a"(x))
#define PINS(x) asm volatile("" : "+s"(x))
#define KEEP(x) asm volatile("" :: "v"(x))
#define LGKM0() asm volatile("s_waitcnt lgkmcnt(0)" ::: "memory")
DI void bar() { LGKM0(); __builtin_amdgcn_s_barrier(); FENCE(); }
template <int N> DI void vmw() { asm volatile("s_waitcnt vmcnt(%0)" :: "n"(N) : "memory"); }
template <int LO, int MO> DI void dmai(rsrc_t r, int voff, int lds) {
  int so;
  asm volatile("s_add_u32 m0, %1, %4\n\ts_mov_b32 %0, %5\n\tbuffer_load_dwordx4 %2, %3, %0 offen lds"
               : "=&s"(so) : "s"(lds), "v"(voff), "s"(r), "n"(LO), "n"(MO) : "memory", "scc");
}
DI void touch(rsrc_t r, int voff, int soff, LAS char* L) {
  asm volatile("s_mov_b32 m0, %0\n\ts_nop 3\n\tbuffer_load_dword %1, %2, %3 offen lds"
               :: "s"((int)(size_t)(L + L_DUMP)), "v"(voff), "s"(r), "s"(soff) : "memory");
}
DI void publish(GAS unsigned* flag, unsigned epoch) {
  DRAIN();
  __builtin_amdgcn_s_waitcnt(0x0F70);
  bar();
  if (threadIdx.x == 0) __hip_atomic_store(flag, epoch, RLX_AGENT);
  FENCE();
}
#define POLL(f, ptr) asm volatile("global_load_dword %0, %1, off sc1" : "=&v"(f) : "v"(ptr) : "memory")
#define POLLED(f, N, ptr) { \
    unsigned f2_; \
    POLL(f2_, ptr); \
    asm volatile("s_waitcnt vmcnt(%1)" : "+v"(f) : "n"((N) + 1) : "memory"); \
    if (__builtin_amdgcn_ballot_w64(f != epoch) != 0ull) { \
      asm volatile("s_waitcnt vmcnt(0)" : "+v"(f2_) :: "memory"); \
      if (__builtin_amdgcn_ballot_w64(f2_ != epoch) != 0ull) wait_flags(ptr, epoch, budget); \
    } \
    FENCE(); \
    late = f2_; }
DI void wait_flags(GAS unsigned* flag, unsigned epoch, unsigned& budget) {
  FENCE();
  while (true) {
    unsigned f = __hip_atomic_load(flag, RLX_AGENT);
    if (__builtin_amdgcn_ballot_w64(f != epoch) == 0ull) break;
    if (budget == 0u) break;
    --budget;
  }
  __builtin_amdgcn_fence(__ATOMIC_ACQUIRE, "wavefront");
  FENCE();
}

DI int oob(bool in, int off) { return off | (in ? 0 : OOB); }
DI int wrap7(int v) { return v >= 7 ? v - 7 : v; }
DI int swz(int row) { return ((row >> 1) & 1) | (((row >> 3) ^ (row >> 2)) & 1) << 1; }

#define FRESH() \
  int tid = threadIdx.x, bid = blockIdx.x; \
  asm volatile("" : "+v"(tid)); \
  asm volatile("" : "+s"(bid)); \
  const int lane = tid & 63, w = __builtin_amdgcn_readfirstlane(tid >> 6), bx = bid & 7, by = bid >> 3; \
  const int c = lane & 15, g = lane >> 4, q4 = (lane & 15) >> 2, p4 = lane & 3, r32 = lane & 31, hh = lane >> 5; \
  const int by7 = __builtin_amdgcn_readfirstlane((by * 37) >> 8), bym = __builtin_amdgcn_readfirstlane(by - 7 * by7); \
  const int t = 2 * bx + (by >> 4), hd = by & 15;                \
  const int st = 4 * bx + by7, dt = bym;                         \
  const int dt4 = by < 21 ? bym : bym + 4 * (bx & 1), ft = 7 * (bx >> 1) + (by < 21 ? by7 + 4 * (bx & 1) : 3);    \
  LAS char* ring = L + L_KVW + w * 16384; \
  const int wrow = lane >> 3, sx0 = swz(wrow) << 5; \
  auto raddr = [&](int row) { return row * 128 + (swz(row) << 5) + 8 * p4; }; \
  LAS char* red = L + L_KVW + lane * 16; \
  (void)c; (void)g; (void)q4; (void)r32; (void)hh; (void)t; (void)hd; (void)st; (void)dt; (void)dt4; (void)ft; (void)ring; (void)sx0; (void)raddr; (void)red;

#define MFMA32V0(d, a, b) asm volatile("v_mfma_f32_32x32x16_bf16 %0, %1, %2, 0" : "=&v"(d) : "v"(a), "v"(b))
#define MFMA32VC(d, a, b, c) asm volatile("v_mfma_f32_32x32x16_bf16 %0, %1, %2, %3" : "=&v"(d) : "v"(a), "v"(b), "v"(c))
#define MFMA32VA(d, a, b) asm volatile("v_mfma_f32_32x32x16_bf16 %0, %1, %2, %0" : "+v"(d) : "v"(a), "v"(b))
#define MFMA_SETTLE(d) asm volatile("s_nop 11" : "+v"(d))
#define MFMA_SETTLE2(d, e) asm volatile("s_nop 11" : "+v"(d), "+v"(e))
DI float max3(float a, float b, float c) { float r; asm("v_max3_f32 %0, %1, %2, %3" : "=v"(r) : "v"(a), "v"(b), "v"(c)); return r; }

#define WB(cc) (L + L_WQ + (4 * hh + q4) * 64 + (g & 1) * 32 + 8 * p4 + (hd & 1) * 8 + (cc) * 4096)
#define WFRAG(wb, part, e) trfrag((wb) + (part) * 28672 + ((e) >> 1) * 2048 + ((e) & 1) * 512, (wb) + (part) * 28672 + ((e) >> 1) * 2048 + ((e) & 1) * 512 + 1024)
#define CSUM(part, a) (*(LAS f32x4*)(L + L_CS + ((part) * 32 + 8 * (a) + 4 * hh) * 4))
#define WZ_RD1(n, i, sl) wz[n][i] = (n) < 16 ? trfrag(pr1[i] + (sl) * 4096, pr1[(i) ^ 2] + (sl) * 4096 + 512) : trfrag(pr2[i] + (sl) * 4096, pr2[i] + (sl) * 4096 + 2048)
#define DMA_WZ1(n, m, sl) { \
    if ((n) < 16) dmai<(sl) * 4096 + (m) * 1024, (32 * ((n) & 15) + 8 * (m)) * F * 2>(rw1, ((m) & 1) ? pw1 ^ 64 : pw1, pring); \
    else dmai<(sl) * 4096 + (m) * 1024, (32 * ((n) & 15) + 8 * (m)) * S * 2>(rw2, ((m) & 1) ? pw2 ^ 64 : pw2, pring); }
#define PIECE(L0, q) { \
    constexpr int r_ = (q) / 4, m_ = (q) % 4, n_ = (L0) + r_; \
    if constexpr (m_ == 0 && r_ >= 4) asm volatile("s_waitcnt vmcnt(8) lgkmcnt(0)" ::: "memory"); \
    else if constexpr (m_ == 0 && r_ == 3) vmw<8>(); \
    DMA_WZ1(n_, m_, r_ & 3) \
    if constexpr (r_ >= 3) WZ_RD1(n_ - 3, m_, (r_ - 3) & 3); \
    SCHED(); }
#define DPUMP(L0, A, B) sfor<4 * ((B) - (A))>([&](auto I_) INL { PIECE(L0, 4 * ((A) - (L0)) + I_) });
#define STASH(A, B) if constexpr (layer == 1) sfor<4 * ((B) - (A))>([&](auto I_) INL { \
    constexpr int j_ = (A) + I_ / 4, m_ = I_ % 4; \
    dmai<j_ * 4096 + m_ * 1024, (32 * (14 - NS + j_) + 8 * m_) * S * 2>(rw2, (m_ & 1) ? pw2 ^ 64 : pw2, pstash); SCHED(); });
#define DTSTEP(L0, L1, sl) ((L1) - 1 - (((L1) - 1 - (L0) - (sl)) & 3))
#define DTAIL1(L0, L1, j) { if constexpr (DTSTEP(L0, L1, (j) >> 2) >= (L0) && DTSTEP(L0, L1, (j) >> 2) >= (L1) - 3) WZ_RD1(DTSTEP(L0, L1, (j) >> 2), (j) & 3, (j) >> 2); }
#define DTAIL(L0, L1) sfor<16>([&](auto J_) INL { constexpr int j_ = J_; DTAIL1(L0, L1, j_) }); SCHED();
#define REDW(a, b) *(LAS f32x4*)(red + w * 16384 + (((b) - 1) * 4 + (a)) * 1024) = acc[a][b]
#define REDR(a, b) (*(LAS f32x4*)(red + ((w + 4 - (b)) & 3) * 16384 + (((b) - 1) * 4 + (a)) * 1024))
__global__ void __launch_bounds__(256, 1) fused_kernel(Params p) {
  extern __shared__ __attribute__((aligned(16))) char smem[];
  LAS char* L = (LAS char*)smem;
  {
    unsigned long long pc;
    int len;
    asm volatile("s_getpc_b64 %0\n.Lfk_code_from:\n\ts_mov_b32 %1, .Lfk_code_to-.Lfk_code_from" : "=s"(pc), "=s"(len));
    touch(mk((const void*)pc, len > 0 ? len : 0), (threadIdx.x & 63) < 8 ? ((blockIdx.x >> 3) * 4 + (threadIdx.x >> 6) + (threadIdx.x & 7) * 128) * 128 : OOB, 0, L);
  }
  SCHED();
  const bool on35 = (blockIdx.x >> 3) < 28;
  const bool on4 = (blockIdx.x >> 3) < ((blockIdx.x & 1) ? 24 : 25);
  const rsrc_t rws = mk(p.ws, WS_BYTES);
  GAS unsigned* ctrl = (GAS unsigned*)p.ws;
  GAS unsigned* flags = (GAS unsigned*)((GAS char*)p.ws + FLAG_OFF);
  unsigned budget = 1u << 22;

  v4u xf[28];
  v4u kv[4];
  float mean = 0.f, rstd = 0.f;
  f32x4 res[4];
  unsigned epoch;
  int kvs, oo, t1o, wvo2, pw1, pw2, fo;
  unsigned nk1, nk2, pm[8];
  LAS char *kvw, *vw, *vr0, *wor[4], *pr1[4], *pr2[4], *wbq;
#define KVOUT(acc, h) { \
    _Pragma("unroll") for (int i = 0; i < 16; i += 2) kv[2 * (h) + (i >> 3)][(i >> 1) & 3] = pk(acc[i], acc[i + 1]); \
    if (hh) { kv[2 * (h) + 1][2] = (h) ? 0x00003f80u : 0u; kv[2 * (h) + 1][3] = 0u; } \
    const rsrc_t rkv = mk(p.ws, t < 15 ? WS_BYTES : 0);    \
    st16<SC1>(kv[2 * (h)], rkv, kvs + (h) * 2048, 0); \
    st16<SC1>(kv[2 * (h) + 1], rkv, kvs + (h) * 2048 + 1024, 0); \
    SCHED(); }
#define X128(p) ((LAS char*)(size_t)((unsigned)(size_t)(p) ^ 128u))
  {
    FRESH();
    const rsrc_t rwq = mk(p.wqkv, D * 3 * D * 2);
    const rsrc_t rx = mk(p.x, S * D * 2);
    const int gid = by * 4 + w, rg = (int)(size_t)ring;
    touch(rx, oob(lane < 14, (t * 896 + hd * 56 + w * 14 + lane) * 128), 0, L);
    const int xvo = (t * 128 + w * 32 + wrow) * 896 + (((lane & 7) ^ (wrow >> 1)) << 4);
#define DMA_X(cc) sfor<4>([&](auto M) INL { constexpr int m = M; dmai<((cc) & 3) * 4096 + m * 1024, m * 8 * 896 + (cc) * 128>(rx, (m & 1) ? xvo ^ 64 : xvo, rg); })
    DMA_X(0); DMA_X(1);
    touch(rwq, oob(lane < 14, gid * 2688 + 896 + lane * 128), 0, L);
    DMA_X(2);
    SCHED();
    const int tb = gid * 35 + lane, tbr = (tb * 18725) >> 18;
    int tvb = oob(lane < 35, (128 + tbr) * 2688 + 896 + (tb - 14 * tbr) * 128);
    int wvo = (w * 16 + (lane >> 2)) * 2688 + hd * 56 - (hd & 1) * 8 + (lane & 3) * 16;
    const int wlb = (int)(size_t)(L + L_WQ) + w * 1024;
#define DMA_W(part, cc) dmai<(part) * 28672 + (cc) * 4096, (cc) * 64 * 2688 + (part) * 896>(rwq, wvo, wlb)
    LAS char* xa[8];
#pragma unroll
    for (int e = 0; e < 8; ++e) { xa[e] = ring + r32 * 128 + 8 * hh + ((e ^ ((r32 >> 1) & 7)) << 4); PINV(xa[e]); }
    int epo = w == 0 ? FLAG_OFF + (bid - 256) * 128 + lane * 32768 : S3_OFF + (st * 8 + dt) * 1024 - 2 * S3_SZ + (lane >> 3) * S3_SZ + (lane & 7) * 128;
    epo = (w == 0 ? lane - 1u < 10u : w == 1 && on35 && (lane >> 4) == 1) ? epo : 0;
    PINV(tvb); PINV(wvo); PINV(epo);
    SCHED();
    DMA_X(3);
    asm volatile("buffer_load_dword %0, %1, %2, 0 offen sc1" : "=&v"(epoch) : "v"(epo), "s"(rws) : "memory");
    SCHED();
    int tvq, tvw;
    LAS char *cb, *wb;
#define READ_X(cc) _Pragma("unroll") for (int e = 0; e < 4; ++e) { \
      const int ch = (e >> 1) * 4 + (e & 1); \
      const v2u a_ = *(LAS v2u*)(xa[ch] + ((cc) & 3) * 4096), b_ = *(LAS v2u*)(xa[ch + 2] + ((cc) & 3) * 4096); \
      xf[4 * (cc) + e] = (v4u){a_.x, a_.y, b_.x, b_.y}; }
    const v4u ones = {0x3f803f80u, 0x3f803f80u, 0x3f803f80u, 0x3f803f80u};
    f32x16 gs, ss = {};
#define XSTAT(i) { if constexpr ((i) == 0) MFMA32V0(gs, xf[i], xf[i]); else MFMA32VA(gs, xf[i], xf[i]); ss = mfma32(ones, xf[i], ss); }
    vmw<14>(); READ_X(0) LGKM0(); DMA_X(4); XSTAT(0) XSTAT(1) XSTAT(2) XSTAT(3) SCHED();
    vmw<14>(); READ_X(1) LGKM0(); DMA_X(5); XSTAT(4) XSTAT(5) XSTAT(6) XSTAT(7) SCHED();
    vmw<13>(); READ_X(2) LGKM0(); DMA_X(6); SCHED();
    asm volatile("s_waitcnt vmcnt(12)" : "+v"(epoch) :: "memory");
    epoch = __builtin_amdgcn_readfirstlane(epoch) + 1u;
    READ_X(3)
    SCHED();
    sfor<20>([&](auto I) INL {
      constexpr int i = I;
      if constexpr (i == 6) { vmw<8 + 3>(); READ_X(4) }
      if constexpr (i == 10) { vmw<4 + 5 + 1>(); READ_X(5) }
      if constexpr (i == 14) { vmw<7 + 1>(); READ_X(6) }
      if constexpr ((i & 1) == 0 && i < 16) DMA_W(1 + ((i >> 1) & 1), i >> 2);
      if constexpr (i == 7) touch(rwq, tvb, 0, L);
      MFMA32VA(gs, xf[8 + i], xf[8 + i]);
      if constexpr (i < 8) {
        const int d = r32 - ((2 * i & 3) + 8 * (i >> 1)) - 4 * hh;
        pm[i] = ((unsigned)(d >> 31) & 0xffffu) | ((unsigned)((d - 1) >> 31) & 0xffff0000u);
        PINA(pm[i]);
      } else if constexpr (i < 12) { wor[i - 8] = ring + (raddr(16 * (g >> 1) + 4 * (g & 1) + q4) ^ ((i - 8) << 5)); PINA(wor[i - 8]); }
      else if constexpr (i < 16) { pr1[i - 12] = ring + (raddr(8 * g + q4) ^ ((i - 12) << 5)); PINA(pr1[i - 12]); }
      else { pr2[i - 16] = ring + (raddr(4 * g + q4) ^ (((i + w) & 3) << 5)); PINA(pr2[i - 16]); }
      SCHED();
      ss = mfma32(ones, xf[8 + i], ss);
      SCHED();
      if constexpr (i == 0) { kvs = KV_OFF + (hd * 64 + 4 * t + w) * 4096 + lane * 16; PINV(kvs); }
      if constexpr (i == 1) { oo = O_OFF + ((2 * t + (w >> 1)) * 16 + hd) * 4096 + (2 * (w & 1) + (r32 >> 4)) * 1024 + hh * 256 + c * 16; PINA(oo); }
      if constexpr (i == 2) { t1o = oob(lane < 32, (512 * (by >> 3) + 8 * ((by & 7) * 4 + w) + (lane >> 2)) * (F * 2) + (7 * (bx >> 1) + 3 * (bx & 1) + (lane & 3)) * 128); PINA(t1o); }
      if constexpr (i == 3) { wvo2 = ((112 * w + wrow) * 896 + dt * 128 + (lane & 7) * 16) ^ sx0; PINA(wvo2); }
      if constexpr (i == 4) { pw1 = ((512 * w + wrow) * (F * 2) + ft * 128 + (lane & 7) * 16) ^ sx0; PINA(pw1); }
      if constexpr (i == 5) { pw2 = ((448 * w + wrow) * (S * 2) + st * 128 + (lane & 7) * 16) ^ sx0; PINA(pw2); }
      if constexpr (i == 7) {
        const int tp = (t >= 2 && (lane & 1)) ? t - 2 : t > 0 ? t - 1 : 0;
        fo = FLAG_OFF + (((tp & 1) * 16 + hd) * 8 + (tp >> 1)) * 128;
        PINA(fo);
      }
      if constexpr (i == 8) { nk2 = (t < 2 && hh) ? 0xf149u : 0u; nk1 = (t < 1 && hh) ? 0xf149u : 0u; PINA(nk1); PINA(nk2); }
      if constexpr (i == 9) { kvw = L + L_KVW + w * 4096 + lane * 16; vw = L + L_KVW + w * 4096 + 2048 + ((lane * 16) ^ (hh << 6)); PINA(kvw); PINA(vw); }
      if constexpr (i == 11) { vr0 = L + L_KVW + w * 4096 + 2048 + ((((g & 1) * 1024 + (p4 & 1) * 512 + hh * 64 + q4 * 16) ^ ((p4 & 1) << 6) ^ ((g & 1) << 7)) + (p4 >> 1) * 8); PINA(vr0); }
      if constexpr (i == 13) { wbq = WB(0); wb = wbq + 28672; PINV(wbq); PINV(wb); }
      if constexpr (i == 15) { cb = L + L_WQ + 28672 + (w * 16 + 8 * hh + q4) * 64 + (g & 1) * 32 + 8 * p4; PINV(cb); }
      if constexpr (i == 17) {
        const int tl = by * 98 + w * 25 + lane, trow = (tl * 9363) >> 16;
        tvq = oob(lane < 25 && w * 25 + lane < 98, trow * 2688 + (tl - 7 * trow) * 128); tvw = oob(lane < 25 && w * 25 + lane < 98, tl * 128);
        PINV(tvq); PINV(tvw);
      }
      SCHED();
    });
    f32x16 ak, av;
    v4u wk[28], wv[28], wc[14];
    f32x16 csk = {}, csv = {};
    float dg = 0.f, d8[8], d4[4], pp[4];
    f32x4 cc[8];
    LAS float* pb = (LAS float*)(L + L_CSP) + 32 + 16 * w + c + 4 * (hd & 1);
    LAS float* pw = (LAS float*)(L + (hh ? L_DUMP : L_CSP + (w * 96 + 32) * 4)) + r32;
#define PICK4(o, at, a0, a1, a2, a3, b0, b1, b2, b3, m) \
    asm volatile("v_cndmask_b32 %0, %4, %8, %12\n\tv_cndmask_b32 %1, %5, %9, %12\n\tv_cndmask_b32 %2, %6, %10, %12\n\tv_cndmask_b32 %3, %7, %11, %12" \
                 : "=&v"(o[at]), "=&v"(o[(at) + 1]), "=&v"(o[(at) + 2]), "=&v"(o[(at) + 3]) \
                 : "v"(a0), "v"(a1), "v"(a2), "v"(a3), "v"(b0), "v"(b1), "v"(b2), "v"(b3), "s"(m))
    sfor<8>([&](auto R) INL {
      constexpr int r = R;
      if constexpr (r < 7) {
        vmw<(r < 2 ? 7 : 6)>();
        if constexpr (r == 6) {
          wc[12] = trfrag(cb + r * 4096, cb + r * 4096 + 256);
          wc[13] = trfrag(cb + 28672 + r * 4096, cb + 28672 + r * 4096 + 256);
          csk = mfma32(ones, wc[12], csk); csv = mfma32(ones, wc[13], csv);
          pw[0] = csk[0]; pw[32] = csv[0];
          LGKM0();
        }
        __builtin_amdgcn_s_barrier();
        FENCE();
        SCHED();
      }
      sfor<4>([&](auto E) INL {
        constexpr int e = E, kr = 4 * r + e, km = kr - 4;
        if constexpr (r > 0) { if constexpr (km == 0) MFMA32V0(ak, wk[km], xf[km]); else MFMA32VA(ak, wk[km], xf[km]); }
        if constexpr (r < 7) wk[kr] = WFRAG(wb + r * 4096, 0, e);
        if constexpr (r < 6 && (e & 1) == 0) wc[2 * r + e / 2] = trfrag(cb + (e / 2) * 28672 + r * 4096, cb + (e / 2) * 28672 + r * 4096 + 256);
        SCHED();
        if constexpr (r > 0) { if constexpr (km == 0) MFMA32V0(av, wv[km], xf[km]); else MFMA32VA(av, wv[km], xf[km]); }
        if constexpr (r < 7) wv[kr] = WFRAG(wb + r * 4096, 1, e);
        SCHED();
        if constexpr (r < 6 && e == 1) csk = mfma32(ones, wc[2 * r], csk);
        if constexpr (r < 6 && e == 3) csv = mfma32(ones, wc[2 * r + 1], csv);
        SCHED();
        if constexpr (e == 1 || e == 3) {
          if constexpr (r < 3) DMA_W(1 + e / 2, r + 4);
          else if constexpr (r == 3) { if constexpr (e == 1) touch(rwq, tvq, 0, L); else touch(mk(p.wo, D * D * 2), tvw, 0, L); }
          else if constexpr (2 * (r - 4) + e / 2 < 7) DMA_W(0, 2 * (r - 4) + e / 2);
        }
        if constexpr (r == 1 && (e & 1) == 0) { constexpr int j = 2 * e; PICK4(d8, j, gs[j], gs[j + 1], gs[j + 2], gs[j + 3], gs[j + 8], gs[j + 9], gs[j + 10], gs[j + 11], 0xffff0000ffff0000ull); }
        if constexpr (r == 2 && e == 0) PICK4(d4, 0, d8[0], d8[1], d8[2], d8[3], d8[4], d8[5], d8[6], d8[7], 0xff00ff00ff00ff00ull);
        if constexpr (r == 2 && e == 2)
          asm volatile("v_cndmask_b32 %1, %1, %3, %5\n\tv_cndmask_b32 %2, %2, %4, %5\n\tv_cndmask_b32 %0, %1, %2, %6\n\tv_cndmask_b32 %0, 0, %0, %7"
                       : "=&v"(dg), "+v"(d4[0]), "+v"(d4[1]) : "v"(d4[2]), "v"(d4[3]), "s"(0xccccccccccccccccull), "s"(0xaaaaaaaaaaaaaaaaull), "s"(0xf0f0f0f00f0f0f0full));
        if constexpr (r == 3 && e == 0) { mean = ss[0] * (1.f / D); rstd = sum32(dg) * (1.f / D) - mean * mean; PINV(mean); PINV(rstd); }
        if constexpr (r == 3 && e == 2) { rstd = __builtin_amdgcn_rsqf(fmaxf(rstd, 0.f) + EPS); PINV(rstd); }
        if constexpr (r == 4 && e == 0) { PINV(pb); PINV(pw); }
        if constexpr (r == 6 && e == 0) { pp[0] = pb[0]; pp[1] = pb[96]; pp[2] = pb[192]; pp[3] = pb[288]; }
        if constexpr (r == 6 && e == 2) *(LAS float*)(L + L_CS + (32 + 16 * w + c) * 4) = (pp[0] + pp[1]) + (pp[2] + pp[3]);
        if constexpr (r == 6 && e == 3) bar();
        if constexpr (r == 7 && e < 2) { _Pragma("unroll") for (int j = 0; j < 4; ++j) cc[4 * e + j] = CSUM(1, 4 * e + j); }
        SCHED();
      });
    });
    MFMA_SETTLE2(ak, av);
#pragma unroll
    for (int i = 0; i < 16; ++i) ak[i] = (ak[i] - mean * cc[i >> 2][i & 3]) * rstd;
    KVOUT(ak, 0)
#pragma unroll
    for (int i = 0; i < 16; ++i) av[i] = (av[i] - mean * cc[4 + (i >> 2)][i & 3]) * rstd;
    KVOUT(av, 1)
  }

  unsigned b2r;
  v2u bb[4];
  v4u wz[30][4];
  sfor<2>([&](auto LY) INL {
    constexpr int layer = LY;
    constexpr int Y0 = layer ? KP : 0, Y2 = Y0 + (layer ? MO2 : NO2), YG = Y2 + (layer ? MO3 : NO3), YG2 = YG + (layer ? MG2 : NG2),
                  YH = YG2 + (layer ? MG3 : NG3), YE = layer ? 16 : Z_U, YH2 = layer ? YE : YH + NH2, YZ = layer ? 30 - NS : 30, YU3 = layer ? YZ : 30 - NU3;
    static_assert(YH <= YH2, "schedule");
    constexpr int QPUB = layer ? 14 : 10;
    GAS unsigned* fl = flags + layer * 5 * 256 * 32;
    v4u qf[2];
    v4u wo[4][4];
    v2u rr[4];
    int pring, pstash;
    const rsrc_t rw1 = mk(p.w1, on4 ? S * F * 2 : 0);
    const rsrc_t rw2 = mk(p.w2, on35 ? S * F * 2 : 0);
    {
      const bool hi = threadIdx.x & 32;
      FRESH();
      if constexpr (layer == 0) {
        vmw<4>();
        if (t == 15) vmw<0>();
        __builtin_amdgcn_s_barrier();
        FENCE();
      }
      v4u wc[7];
      f32x16 cs = {};
      LAS char* cb = L + L_WQ + (w * 16 + 8 * hh + q4) * 64 + (g & 1) * 32 + 8 * p4;
      v4u wq[28];
#pragma unroll
      for (int ks = 0; ks < 4; ++ks) wq[ks] = WFRAG(wbq, 0, ks);
      f32x16 aq;
      const rsrc_t rwo = mk(p.wo, on35 ? D * D * 2 : 0);
      pring = (int)(size_t)ring;
      pstash = (int)(size_t)(L + L_WQ) + w * 20480;
      PINS(pring); PINS(pstash);
      GAS unsigned* fp = (GAS unsigned*)((GAS char*)p.ws + fo);
      unsigned pf;
      sfor<28>([&](auto I) INL {
        constexpr int ks = I;
        if constexpr (ks == 0) MFMA32V0(aq, wq[ks], xf[ks]); else MFMA32VA(aq, wq[ks], xf[ks]);
        if constexpr (ks + 4 < 28) wq[ks + 4] = WFRAG(wbq + ((ks + 4) >> 2) * 4096, 0, ks & 3);
        constexpr int wi = layer ? -1 : ks - QPUB - 1;
        if constexpr (wi >= 0 && wi < 16) dmai<wi * 1024, (28 * (wi >> 2) + 8 * (wi & 3)) * 896>(rwo, (wi & 1) ? wvo2 ^ 64 : wvo2, pring);
        if constexpr (layer == 0) {
          if constexpr (ks < 7) wc[ks] = trfrag(cb + ks * 4096, cb + ks * 4096 + 256);
          if constexpr (ks >= 2 && ks < 9) cs = mfma32((v4u){0x3f803f80u, 0x3f803f80u, 0x3f803f80u, 0x3f803f80u}, wc[ks - 2], cs);
          if constexpr (ks == QPUB + 1) { _Pragma("unroll") for (int q = 0; q < 4; ++q) cs[q] = ((LAS float*)(L + L_CSP) + r32 + 4 * (hd & 1))[96 * q]; }
          if constexpr (ks == QPUB + 5) *(LAS float*)(L + L_CS + r32 * 4) = (cs[0] + cs[1]) + (cs[2] + cs[3]);
        }
        if constexpr (ks == 27) POLL(pf, fp);
        if constexpr (ks == QPUB) {
          if constexpr (layer == 0) { *(LAS float*)(L + (hh ? L_DUMP : L_CSP + w * 96 * 4) + r32 * 4) = cs[0]; }
          publish(fl + (0 * 256 + bid) * 32, epoch);
        }
        SCHED();
      });
      f32x4 cq[4];
#pragma unroll
      for (int a = 0; a < 4; ++a) cq[a] = CSUM(0, a);
      MFMA_SETTLE(aq);
      const float qs = rstd * (0.18898223650461362f * 1.4426950408889634f);
#pragma unroll
      for (int a = 0; a < 4; ++a) {
        qf[a >> 1][(a & 1) * 2] = pk((aq[4 * a] - mean * cq[a][0]) * qs, (aq[4 * a + 1] - mean * cq[a][1]) * qs);
        qf[a >> 1][(a & 1) * 2 + 1] = pk((aq[4 * a + 2] - mean * cq[a][2]) * qs, (aq[4 * a + 3] - mean * cq[a][3]) * qs);
      }
      if (hi) { qf[1][2] = 0x00003f80u; qf[1][3] = 0u; }
      PINV(qf[0]); PINV(qf[1]);
      f32x16 s8;
      asm volatile("s_nop 1" : "+v"(qf[0]), "+v"(qf[1]));
      MFMA32V0(s8, kv[0], qf[0]);
      MFMA32VA(s8, kv[1], qf[1]);
      SCHED();
      unsigned late;
      POLLED(pf, 0, fp)
      v4u ra[4], rb[4];
      {
        const rsrc_t r1 = mk(p.ws, t >= 1 ? WS_BYTES : 0), r2 = mk(p.ws, t >= 2 ? WS_BYTES : 0);
        const int kvo = kvs - 32768;
        rb[0] = ld16<SC1>(r2, kvo, 0); rb[1] = ld16<SC1>(r2, kvo, 1024);
        ra[0] = ld16<SC1>(r1, kvo, 16384); ra[1] = ld16<SC1>(r1, kvo, 16384 + 1024);
        SCHED();
        rb[2] = ld16<SC1>(r2, kvo, 2048); rb[3] = ld16<SC1>(r2, kvo, 3072);
        ra[2] = ld16<SC1>(r1, kvo, 16384 + 2048); ra[3] = ld16<SC1>(r1, kvo, 16384 + 3072);
        SCHED();
      }
#pragma unroll
      for (int j = 0; j < 16; ++j) wo[j >> 2][j & 3] = trfrag(wor[j & 3] + (j >> 2) * 4096, wor[(j & 3) ^ 2] + (j >> 2) * 4096 + 1024);
      SCHED();
      bar();
      LAS char* vw1 = X128(vw) + 1024;
      LAS char* vr1 = X128(vr0);
      *(LAS v4u*)(kvw + 32768) = kv[0];
      *(LAS v4u*)(kvw + 32768 + 1024) = kv[1];
      *(LAS v4u*)(vw + 32768) = kv[2];
      *(LAS v4u*)(vw1 + 32768) = kv[3];
      int rro = (64 * st + 16 * w + c) * 896 + dt * 128 + g * 8, b2o = (64 * st + 16 * w + c) * 2, b1o = ft * 128 + g * 8;
      unsigned pml[8], mk32[16], nk1l = nk1, nk2l = nk2;
      int ool = oo, t1l = t1o;
      LAS char* vr0l = vr0;
      if constexpr (layer == 0) { PINV(rro); PINV(b2o); PINV(b1o); }
      PINV(vw1); PINV(vr1); PINV(vr0l); PINV(ool); PINV(t1l); PINV(nk1l); PINV(nk2l);
#pragma unroll
      for (int i = 0; i < 8; ++i) { pml[i] = pm[i]; PINV(pml[i]); mk32[2 * i] = (unsigned)((int)(pml[i] << 16) >> 16); mk32[2 * i + 1] = (unsigned)((int)pml[i] >> 16); PINV(mk32[2 * i]); PINV(mk32[2 * i + 1]); }
      SCHED();
      f32x16 sc[8], se;
      v4u kf[8][2];
      float mx = -1e30f, mx2 = -1e30f;
#define MAX8(v, i) asm volatile("v_max3_f32 %0, %0, %2, %3\n\tv_max3_f32 %1, %1, %4, %5\n\tv_max3_f32 %0, %0, %6, %7\n\tv_max3_f32 %1, %1, %8, %9" \
        : "+v"(mx), "+v"(mx2) : "v"(v[i]), "v"(v[(i) + 1]), "v"(v[(i) + 2]), "v"(v[(i) + 3]), "v"(v[(i) + 4]), "v"(v[(i) + 5]), "v"(v[(i) + 6]), "v"(v[(i) + 7]))
      *(LAS v4u*)(kvw) = rb[0];
      MFMA32V0(sc[0], rb[0], qf[0]);
      rb[1][2] |= nk2l;
      *(LAS v4u*)(kvw + 1024) = rb[1];
      *(LAS v4u*)(kvw + 16384) = ra[0];
      SCHED();
      MFMA32VA(sc[0], rb[1], qf[1]);
      ra[1][2] |= nk1l;
      *(LAS v4u*)(kvw + 16384 + 1024) = ra[1];
      kf[4][0] = ra[0]; kf[4][1] = ra[1];
      MFMA32V0(sc[4], kf[4][0], qf[0]);
      bar();
      KEEP(late);
      MFMA32VA(sc[4], kf[4][1], qf[1]);
#pragma unroll
      for (int j = 1; j < 8; ++j) if (j != 4) { kf[j][0] = *(LAS v4u*)(kvw + j * 4096); kf[j][1] = *(LAS v4u*)(kvw + j * 4096 + 1024); }
      PINV(sc[0]);
#pragma unroll
      for (int i = 0; i < 16; ++i) se[i] = asf((asu(sc[0][i]) & mk32[i]) | (asu(s8[i]) & ~mk32[i]));
      PINV(se);
      SCHED();
      sfor<12>([&](auto N) INL {
        constexpr int n = N, jj = n >> 1, j = jj < 3 ? jj + 1 : jj + 2, hf = n & 1;
        if constexpr (hf == 0) MFMA32V0(sc[j], kf[j][0], qf[0]); else MFMA32VA(sc[j], kf[j][1], qf[1]);
        if constexpr (n < 2) MAX8(se, 8 * hf);
        else if constexpr (n < 4) MAX8(sc[4], 8 * hf);
        else MAX8(sc[jj < 5 ? jj - 1 : jj], 8 * hf);
        if constexpr (n == 2) *(LAS v4u*)(vw) = rb[2];
        if constexpr (n == 3) *(LAS v4u*)(vw1) = rb[3];
        if constexpr (n == 4) *(LAS v4u*)(vw + 16384) = ra[2];
        if constexpr (n == 5) *(LAS v4u*)(vw1 + 16384) = ra[3];
        if constexpr (n == 6) touch(mk(p.w1, S * F * 2), t1l, layer * KP * 32 * F * 2, L);
        if constexpr (n >= 7) asm volatile("s_nop 0");
        SCHED();
      });
      MAX8(sc[6], 0); MAX8(sc[6], 8); MAX8(sc[7], 0); MAX8(sc[7], 8);
      mx = max32(max3(mx, mx2, mx2));
      f32x16 nm;
#pragma unroll
      for (int i = 0; i < 16; ++i) nm[i] = -mx;
      PINV(nm);
      bar();
      f32x16 ao;
      v4u vf[9][2], pp[9][2];
      float ex[32][4];
#define VF(j, s) vf[j][s] = trfrag(vr0l + (j) * 4096 + (s) * 256, vr1 + (j) * 4096 + (s) * 256)
      sfor<33>([&](auto N) INL {
        constexpr int n = N, j = n >> 2, q = n & 3;
        if constexpr (j == 0) {
          if constexpr (q == 0) MFMA32VC(sc[1], kf[1][0], qf[0], nm);
          if constexpr (q == 1) MFMA32VA(sc[1], kf[1][1], qf[1]);
          if constexpr (q == 2) MFMA32VC(sc[2], kf[2][0], qf[0], nm);
          if constexpr (q == 3) MFMA32VA(sc[2], kf[2][1], qf[1]);
        } else if constexpr (j < 8) {
          if constexpr (q == 1) { if constexpr (j == 1) MFMA32V0(ao, vf[0][0], pp[0][0]); else MFMA32VA(ao, vf[j - 1][0], pp[j - 1][0]); }
          if constexpr (q == 2) MFMA32VA(ao, vf[j - 1][1], pp[j - 1][1]);
          if constexpr (j + 2 < 8) {
            if constexpr (q == 0) MFMA32VC(sc[j + 2], kf[j + 2][0], qf[0], nm);
            if constexpr (q == 3) MFMA32VA(sc[j + 2], kf[j + 2][1], qf[1]);
          } else if constexpr (j == 6) {
            if constexpr (q == 0) MFMA32VA(ao, vf[8][0], pp[8][0]);
            if constexpr (q == 3) MFMA32VA(ao, vf[8][1], pp[8][1]);
          }
        } else MFMA32VA(ao, vf[7][0], pp[7][0]);
        SCHED();
        if constexpr (n < 32) {
#pragma unroll
          for (int i = 0; i < 4; ++i) { if constexpr (j == 0) ex[n][i] = __builtin_amdgcn_exp2f(se[4 * q + i] - mx); else ex[n][i] = __builtin_amdgcn_exp2f(sc[j][4 * q + i]); }
        }
        SCHED();
        if constexpr (n > 0) {
          constexpr int jp = (n - 1) >> 2, qp = (n - 1) & 3;
#pragma unroll
          for (int i = 0; i < 2; ++i) {
            const unsigned u = pk(ex[n - 1][2 * i], ex[n - 1][2 * i + 1]);
            if constexpr (jp == 0) { pp[0][qp >> 1][(2 * qp + i) & 3] = u & pml[2 * qp + i]; pp[8][qp >> 1][(2 * qp + i) & 3] = u & ~pml[2 * qp + i]; }
            else pp[jp][qp >> 1][(2 * qp + i) & 3] = u;
          }
        }
        if constexpr (layer == 0) {
          if constexpr (n >= 2 && n < 10 && q >= 2) rr[(n >> 2) * 2 + q - 2] = ld8<0>(mk(p.x, on35 ? S * D * 2 : 0), rro, ((n >> 2) * 2 + q - 2) * 32);
          if constexpr (n == 10) b2r = (unsigned)__builtin_amdgcn_raw_buffer_load_b16(mk(p.b2, S * 2), b2o, 0, 0);
          if constexpr (n >= 14 && n < 22 && q >= 2) bb[((n >> 2) - 3) * 2 + q - 2] = ld8<0>(mk(p.b1, F * 2), b1o, (((n >> 2) - 3) * 2 + q - 2) * 32);
        }
        if constexpr (n == 22) KEEP(nm);
        if constexpr (j < 8 && q < 2) VF(j, q);
        if constexpr (j == 5 && q >= 2) VF(8, q - 2);
        SCHED();
      });
      asm volatile("s_nop 1" : "+v"(pp[7][1]));
      MFMA32VA(ao, vf[7][1], pp[7][1]);
      MFMA_SETTLE(ao);
      const float inv = __builtin_amdgcn_rcpf(hi32(ao[12]));
      v4u of[2];
#pragma unroll
      for (int i = 0; i < 16; i += 2) of[i >> 3][(i >> 1) & 3] = pk(ao[i] * inv, ao[i + 1] * inv);
      if (hi) { of[1][2] = 0u; of[1][3] = 0u; }
      st16<SC1>(of[0], rws, ool, 0);
      st16<SC1>(of[1], rws, ool, 512);
      SCHED();
    }
    if (on35) {
      {
        FRESH();
        const int tq = st >> 1;
        GAS unsigned* fp = fl + (1 * 256 + (((tq & 1) * 16 + 4 * w + (lane & 3)) * 8 + (tq >> 1))) * 32;
        int oo = O_OFF + layer * O_SZ + (st * 16 + 4 * w) * 4096 + lane * 16;
        const int sb = S3_OFF + layer * S3_SZ + st * 8192 + (16 * w + c) * 16;
        int sw = oob(g < 2, sb + dt * 1024 + g * 8), sa = sb + g * 1024, sc = oob(g < 3, sb + (g + 4) * 1024);
        int ho = HT_OFF + layer * HT_SZ + (dt * 64 + 2 * st + (w >> 1)) * 4096 + (g >> 1) * 1024 + (2 * (w & 1) + (g & 1)) * 256 + c * 16;
        const int hr = 8 * (g & 1) + q4, hst = L_HST + w * 2048;
        int h0 = hst + (raddr(hr) ^ ((g >> 1) << 5)), h1 = hst + (raddr(hr + 4) ^ ((g >> 1) << 5)), hw = hst + c * 128 + (swz(c) << 5) + g * 8;
        int ob[4];
#pragma unroll
        for (int b = 0; b < 4; ++b) { ob[b] = ((b + w) & 3) * 1024; PINS(ob[b]); }
        PINV(fp); PINV(oo); PINV(sw); PINV(sa); PINV(sc); PINV(ho); PINV(h0); PINV(h1); PINV(hw); PINV(red);
        publish(fl + (1 * 256 + bid) * 32, epoch);
        unsigned pf;
        DPUMP(Y0, Y0, Y2)
        if constexpr (layer == 0) {
#pragma unroll
          for (int a = 0; a < 4; ++a) { res[a] = unpack4(rr[a]); PINV(res[a]); }
        }
        POLL(pf, fp);
        DPUMP(Y0, Y2, YG)
        unsigned late;
        POLLED(pf, 4 * (YG - Y2), fp)
        v4u of[16];
#define LOAD_O(I) of[I] = ld16<SC1>(rws, oo + ((I) & 3) * 4096, ob[(((I) >> 2) + 1) & 3])
#pragma unroll
        for (int i = 0; i < 12; ++i) { LOAD_O(i); SCHED(); }
        vmw<12>();
        KEEP(late);
        f32x4 acc[4][4], part[12];
        sfor<16>([&](auto I) INL {
          constexpr int i = I, b = ((i >> 2) + 1) & 3, k = i & 3;
#pragma unroll
          for (int a = 0; a < 4; ++a) {
            acc[a][b] = mfma16(wo[k][a], of[i], k > 0 ? acc[a][b] : (f32x4){0.f, 0.f, 0.f, 0.f});
            SCHED();
            if constexpr (i < 4) { if (a == 0) LOAD_O(i + 12); }
            if constexpr (i < 12) { if (a == 0) DTAIL1(Y0, YG, i) }
            if constexpr (i >= 12 && k < 2) { if (a == 1 || a == 3) REDW(2 * k + (a >> 1), 3); }
            if constexpr (i >= 4 && i < 12) { if (a == 2) REDW(k, i >> 2); }
            if constexpr (i == 14) { if (a == 0) bar(); }
            if constexpr (i >= 14) { if (a < 3) { const int j = 6 * (i - 14) + 2 * a; part[j] = REDR(j / 3, j % 3 + 1); part[j + 1] = REDR((j + 1) / 3, (j + 1) % 3 + 1); } }
            SCHED();
          }
        });
#pragma unroll
        for (int a = 0; a < 4; ++a) res[a] = ((res[a] + part[3 * a]) + (part[3 * a + 1] + part[3 * a + 2])) + acc[a][0];
        f32x2 s1 = {0.f, 0.f}, s2 = {0.f, 0.f};
        stats16(res, 15, s1, s2);
        st8<SC1>((v2u){asu(sum_ab(s1[0] + s1[1], s2[0] + s2[1])), epoch}, rws, sw, 0);
        SCHED();
        DTAIL1(Y0, YG, 12) DTAIL1(Y0, YG, 13) DTAIL1(Y0, YG, 14) DTAIL1(Y0, YG, 15)
        bar();
        DPUMP(YG, YG, YG2)
        v4u ga, gb;
        asm volatile("buffer_load_dwordx4 %0, %2, %4, 0 offen sc1\n\tbuffer_load_dwordx4 %1, %3, %4, 0 offen sc1" : "=&v"(ga), "=&v"(gb) : "v"(sa), "v"(sc), "s"(rws) : "memory");
        DPUMP(YG, YG2, YH)
        STASH(0, SG)
        asm volatile("s_waitcnt vmcnt(%2)" : "+v"(ga), "+v"(gb) : "n"(4 * (YH - YG2 + layer * SG)) : "memory");
        while ((__builtin_amdgcn_ballot_w64((ga.y != epoch) | (ga.w != epoch)) | (__builtin_amdgcn_ballot_w64((gb.y != epoch) | (gb.w != epoch)) << 16)) != 0ull) {
          if (budget == 0u) break;
          --budget;
          asm volatile("buffer_load_dwordx4 %0, %2, %4, 0 offen sc1\n\tbuffer_load_dwordx4 %1, %3, %4, 0 offen sc1\n\ts_waitcnt vmcnt(0)"
                       : "=&v"(ga), "=&v"(gb) : "v"(sa), "v"(sc), "s"(rws) : "memory");
        }
        const unsigned t12 = asu(sum_ab(asf(ga.x) + asf(gb.x), asf(ga.z) + asf(gb.z)));
        const auto tt = __builtin_amdgcn_permlane16_swap(t12, t12, false, false);
        const float t1 = asf(tt[0]), t2 = asf(tt[1]);
        const float m = t1 * (1.f / D), var = fmaxf(t2 * (1.f / D) - m * m, 0.f);
        const float r1 = __builtin_amdgcn_rsqf(var + EPS);
        const float r = r1 * __builtin_amdgcn_rsqf(var * r1 * r1 + EPS), nr = -m * r;
#pragma unroll
        for (int a = 0; a < 4; ++a) {
          const v2u v = {pk(res[a][0] * r + nr, res[a][1] * r + nr), pk(res[a][2] * r + nr, res[a][3] * r + nr)};
          *(LAS v2u*)(L + (hw ^ (a << 5))) = v;
        }
#pragma unroll
        for (int e = 0; e < 2; ++e) st16<SC1>(trfrag(L + (h0 ^ (e << 6)), L + (h1 ^ (e << 6))), rws, ho, e * 2048);
        SCHED();
      }
      if (on4) {
        FRESH();
        const int sp = 8 * w + (lane & 7);
        GAS unsigned* fp = fl + (2 * 256 + ((sp & 3) * 7 + dt4) * 8 + (sp >> 2)) * 32;
        int hv[4];
#pragma unroll
        for (int b = 0; b < 4; ++b) { hv[b] = HT_OFF + layer * HT_SZ + (dt4 * 64 + 16 * w) * 4096 + ((b + w) & 3) * 1024 + lane * 16; PINV(hv[b]); }
        int uo = U_OFF + layer * U_SZ + (dt4 * 56 + 2 * ft) * 4096 + w * 1024 + lane * 16;
        PINV(fp); PINV(uo); PINV(red);
        publish(fl + (2 * 256 + bid) * 32, epoch);
        unsigned pf;
        DPUMP(YG, YH, YH2)
        STASH(SG, NS - SH)
        POLL(pf, fp);
        DPUMP(YG, YH2, YE)
        STASH(NS - SH, NS)
        unsigned late;
        POLLED(pf, 4 * (YE - YH2 + layer * SH), fp)
        v4u af[64];
#define LOAD_H(I) { int so_; asm volatile("s_mov_b32 %1, %4\n\tbuffer_load_dwordx4 %0, %2, %3, %1 offen sc1" \
          : "=&v"(af[I]), "=&s"(so_) : "v"(hv[(((I) >> 4) + 1) & 3]), "s"(rws), "n"(((I) & 15) * 4096) : "memory"); }
        sfor<WIN>([&](auto I) INL { LOAD_H(I) SCHED(); });
        f32x4 acc[4][4], part[12];
        sfor<64>([&](auto I) INL {
          constexpr int i = I, b = ((i >> 4) + 1) & 3, k = i & 15;
          constexpr int np = i <= 54 ? 0 : i <= 62 ? i - 54 : 10;
          asm volatile("s_waitcnt vmcnt(%1)" : "+v"(af[i]) : "n"((i + WIN <= 64 ? WIN - 1 : 63 - i) + np) : "memory");
          if constexpr (i == 0) KEEP(late);
          SCHED();
          sfor<4>([&](auto A) INL {
            constexpr int a = A;
            acc[a][b] = mfma16(wz[k][a], af[i], k > 0 ? acc[a][b] : (f32x4){0.f, 0.f, 0.f, 0.f});
            SCHED();
            if constexpr (i + WIN < 64 && a == 0) LOAD_H(i + WIN)
            if constexpr (i < 8) { if constexpr (a == 1) DTAIL1(YG, YE, 2 * i) if constexpr (a == 2) DTAIL1(YG, YE, 2 * i + 1) }
            if constexpr (i >= 48 && k < 2 && (a & 1)) REDW(2 * k + (a >> 1), 3);
            if constexpr (i >= 16 && i < 48 && (k & 3) == 1 && a == 2) REDW(k >> 2, i >> 4);
            if constexpr (i == 50 && a == 3) bar();
            if constexpr (i >= 51 && i < 54) part[4 * (i - 51) + a] = REDR((4 * (i - 51) + a) / 3, (4 * (i - 51) + a) % 3 + 1);
            if constexpr (i == 54 && a == 1) bar();
            if constexpr (i >= 54 && a == 2) PIECE(Z_U, np)
            if constexpr (i >= 62 && a == 3) PIECE(Z_U, np + 1)
            if constexpr (i >= 55 && i < 59 && a == 0) {
              constexpr int q = i - 55;
              part[3 * q] = (part[3 * q] + unpack4(bb[q])) + (part[3 * q + 1] + part[3 * q + 2]);
              PINV(part[3 * q]);
            }
            SCHED();
          });
        });
        v4u uf[2];
        sfor<8>([&](auto H) INL {
          constexpr int h = H, a = h >> 1, j0 = 2 * (h & 1);
          float gl[2];
#pragma unroll
          for (int j = 0; j < 2; ++j) {
            const float u = acc[a][0][j0 + j] + part[3 * a][j0 + j];
            const float e = __builtin_amdgcn_exp2f(u * (u * u * (-0.10294324f) - 2.3022082f));
            gl[j] = u * __builtin_amdgcn_rcpf(1.f + e);
          }
          uf[h >> 2][h & 3] = pk(gl[0], gl[1]);
          if constexpr (h & 1) {
            PINV(uf[h >> 2]);
            if constexpr (h == 3) st16<SC1>(uf[0], rws, uo, 0);
            if constexpr (h == 7) st16<SC1>(uf[1], rws, uo, 4096);
            SCHED();
          }
        });
      } else {
        publish(fl + (2 * 256 + blockIdx.x) * 32, epoch);
        DPUMP(YG, YH, YE)
        STASH(SG, NS)
        DRAIN();
        DTAIL(YG, YE)
        LGKM0();
        DPUMP(Z_U, Z_U, Z_U + 3)
      }
      {
        FRESH();
        const int pi = wrap7(lane & 7);
        GAS unsigned* fp = fl + (3 * 256 + (pi == 3 ? (21 + (dt & 3)) * 8 + (dt >> 2) : (7 * (pi & 3) + dt) * 8 + (pi >> 2)) + 2 * w) * 32;
        int uv = U_OFF + layer * U_SZ + (dt * 56 + 14 * w) * 4096 + lane * 16;
        int ow = L_HST + w * 2048 + c * 128 + (swz(c) << 5) + g * 8, orr = L_HST + w * 2048 + ((wrow * 128 + (lane & 7) * 16) ^ sx0);
        int xb = layer ? (64 * st + 16 * w + wrow) * 896 + dt * 128 + (lane & 7) * 16
                       : X2_OFF + (2 * st + (w >> 1)) * 28672 + (4 * dt + (g >> 1)) * 1024 + ((g & 1) * 32 + (w & 1) * 16 + c) * 16;
        int so = oob(g < 2, S5_OFF + (st * 8 + dt) * 512 + (16 * w + c) * 8 + g * 4);
#pragma unroll
        for (int a = 0; a < 4; ++a) { res[a] += bflo(b2r); PINV(res[a]); }
        PINV(fp); PINV(uv); PINV(xb); PINV(red);
        if constexpr (layer) { PINV(ow); PINV(orr); } else PINV(so);
        if constexpr (layer == 1) {
          const int dlt = L_WQ + w * 20480 - (L_KVW + w * 16384);
#pragma unroll
          for (int i = 0; i < 4; ++i) {
            LAS char* ps = pr2[i] + dlt;
#pragma unroll
            for (int j = 0; j < NS; ++j) wz[30 - NS + j][i] = trfrag(ps + j * 4096, ps + j * 4096 + 2048);
          }
          SCHED();
        }
        if (on4) publish(fl + (3 * 256 + bid) * 32, epoch);
        unsigned pf;
        DPUMP(Z_U, Z_U + 3, YU3)
        POLL(pf, fp);
        DPUMP(Z_U, YU3, YZ)
        unsigned late;
        POLLED(pf, 4 * (YZ - YU3), fp)
        v4u af[56];
#define LOAD_U(I) af[I] = ld16<SC1>(rws, uv + ((I) / 14) * 1024, ((I) % 14) * 4096)
#pragma unroll
        for (int i = 0; i < WIN; ++i) { LOAD_U(i); SCHED(); }
        vmw<WIN>();
        KEEP(late);
        f32x4 acc[4][4], part[4][3];
        v2u xo[4];
        f32x2 s1 = {0.f, 0.f}, s2 = {0.f, 0.f};
        sfor<56>([&](auto I) INL {
          constexpr int i = I, a = i / 14, k = i % 14;
#pragma unroll
          for (int bq = 0; bq < 4; ++bq) {
            const int b = (bq + 1) & 3;
            if (a < 3 || b) acc[a][b] = mfma16(af[i], wz[16 + k][b], k > 0 ? acc[a][b] : (f32x4){0.f, 0.f, 0.f, 0.f});
            SCHED();
            if constexpr (i + WIN < 56) { if (bq == 0) LOAD_U(i + WIN); }
            if constexpr (i < 8) { if (bq == 1) DTAIL1(Z_U, YZ, 2 * i) if (bq == 2) DTAIL1(Z_U, YZ, 2 * i + 1) }
            if constexpr (a > 0 && k < 3) { if (bq == 2) REDW(a - 1, k + 1); }
            if constexpr (i == 46) { if (bq == 2) bar(); }
            if constexpr (i >= 47 && i < 50) { if (bq < 3) part[i - 47][bq] = REDR(i - 47, bq + 1); }
            if constexpr (i >= 50 && i < 53) { if (bq == 2) { res[i - 50] = ((res[i - 50] + part[i - 50][0]) + (part[i - 50][1] + part[i - 50][2])) + acc[i - 50][0]; PINV(res[i - 50]); } }
            if constexpr (i >= 53) { if (bq == 2) { xo[i - 53] = (v2u){pk(res[i - 53][0], res[i - 53][1]), pk(res[i - 53][2], res[i - 53][3])}; PINV(xo[i - 53]); if constexpr (layer == 0) { stats16(res, 1 << (i - 53), s1, s2); PINV(s1); PINV(s2); } } }
            SCHED();
          }
        });
        sfor<14>([&](auto K) INL {
          constexpr int k = K;
          acc[3][0] = mfma16(af[42 + k], wz[16 + k][0], k > 0 ? acc[3][0] : (f32x4){0.f, 0.f, 0.f, 0.f});
          SCHED();
          if constexpr (k >= 1 && k < 4) REDW(3, k);
          if constexpr (k == 5) {
            if constexpr (layer == 0) st16<SC1>((v4u){xo[0].x, xo[0].y, xo[1].x, xo[1].y}, rws, xb, 0);
            else { _Pragma("unroll") for (int a = 0; a < 3; ++a) *(LAS v2u*)(L + (ow ^ (a << 5))) = xo[a]; }
          }
          if constexpr (k == 7) { bar(); part[3][0] = REDR(3, 1); part[3][1] = REDR(3, 2); part[3][2] = REDR(3, 3); }
          SCHED();
        });
        res[3] = ((res[3] + part[3][0]) + (part[3][1] + part[3][2])) + acc[3][0];
        xo[3] = (v2u){pk(res[3][0], res[3][1]), pk(res[3][2], res[3][3])};
        if constexpr (layer == 0) {
          st16<SC1>((v4u){xo[2].x, xo[2].y, xo[3].x, xo[3].y}, rws, xb, 2048);
          SCHED();
          stats16(res, 8, s1, s2);
          __builtin_amdgcn_raw_buffer_store_b32(asu(sum_ab(s1[0] + s1[1], s2[0] + s2[1])), rws, so, 0, SC1);
          SCHED();
        } else {
          const rsrc_t ro = mk(p.out, S * D * 2);
          *(LAS v2u*)(L + (ow ^ (3 << 5))) = xo[3];
          const v4u r0 = *(LAS v4u*)(L + orr), r1 = *(LAS v4u*)(L + (orr ^ 64) + 1024);
          st16<SC1>(r0, ro, xb, 0);
          st16<SC1>(r1, ro, xb, 8 * 896);
        }
      }
    } else {
      publish(fl + (1 * 256 + blockIdx.x) * 32, epoch);
      FRESH();
      const int rw = 4 * (by - 28) + (lane >> 4) + 16 * ((lane >> 2) & 3);
      const int v1 = (512 * w + 256 + rw) * (F * 2) + (7 * (bx >> 1) + 3 * (bx & 1) + (lane & 3)) * 128;
#pragma unroll 1
      for (int i = layer * (KP / 2); i < 4; ++i) { touch(mk(p.w1, S * F * 2), v1, i * 64 * F * 2, L); vmw<1>(); }
      const int v2 = (448 * w + rw) * (S * 2) + bx * 512 + (lane & 3) * 128;
#pragma unroll 1
      for (int i = 0; i < (layer ? 7 - (Z_U - 16) / 2 : 7); ++i) { touch(mk(p.w2, S * F * 2), v2, (layer ? (i < 3 ? i + 4 : i - 3 + (Z_U - 16) / 2) : i) * 64 * S * 2, L); vmw<1>(); }
      DRAIN();
    }
    if constexpr (layer == 0) {
      FRESH();
      const int st1 = 2 * t + (w >> 1);
      f32x16 ak, av;
      v4u wk[28], wv[28];
      LAS char* wb = WB(0) + 28672;
      PINV(wb);
      wk[0] = WFRAG(wb, 0, 0); wv[0] = WFRAG(wb, 1, 0); wk[1] = WFRAG(wb, 0, 1); wv[1] = WFRAG(wb, 1, 1);
      f32x4 ck[4], cv[4];
#pragma unroll
      for (int a = 0; a < 4; ++a) { ck[a] = CSUM(1, a); cv[a] = CSUM(2, a); PINV(ck[a]); PINV(cv[a]); }
      int xo = X2_OFF + (4 * t + w) * 28672 + lane * 16, so = S5_OFF + (st1 * 8 + hh * 4) * 512 + ((w & 1) * 32 + r32) * 8;
      int so3 = oob(!hh, so + 3 * 512);
      PINV(xo); PINV(so); PINV(so3);
      kvs += KV_SZ; oo += O_SZ; fo += 5 * 256 * 128;
      PINV(kvs); PINA(oo); PINA(fo);
      GAS unsigned* fp = flags + (4 * 256 + ((st1 & 3) * 7 + wrap7(lane & 7)) * 8 + (st1 >> 2)) * 32;
      PINV(fp);
      if (on35) publish(flags + (4 * 256 + bid) * 32, epoch);
      unsigned pf, late;
      {
        const rsrc_t rwo = mk(p.wo, on35 ? D * D * 2 : 0);
        sfor<16>([&](auto I) INL {
          constexpr int wi = I;
          if constexpr (wi == 10) POLL(pf, fp);
          dmai<wi * 1024, (28 * (wi >> 2) + 8 * (wi & 3)) * 896>(rwo, (wi & 1) ? wvo2 ^ 64 : wvo2, pring);
        });
      }
      POLLED(pf, 6, fp)
      v2u sp[4];
#define LOAD_X(I) xf[I] = ld16<SC1>(rws, xo + ((I) & 3) * 1024, ((I) >> 2) * 4096)
#pragma unroll
      for (int i = 0; i < WIN; ++i) { LOAD_X(i); SCHED(); }
      sfor<28>([&](auto I) INL {
        constexpr int ks = I;
        if constexpr (ks == 0) MFMA32V0(ak, wk[ks], xf[ks]); else MFMA32VA(ak, wk[ks], xf[ks]);
        if constexpr (ks + 2 < 28) wk[ks + 2] = WFRAG(wb + ((ks + 2) >> 2) * 4096, 0, (ks + 2) & 3);
        if constexpr (ks + WIN < 28) LOAD_X(ks + WIN);
        SCHED();
        if constexpr (ks == 0) MFMA32V0(av, wv[ks], xf[ks]); else MFMA32VA(av, wv[ks], xf[ks]);
        if constexpr (ks + 2 < 28) wv[ks + 2] = WFRAG(wb + ((ks + 2) >> 2) * 4096, 1, (ks + 2) & 3);
        if constexpr (ks < 4) sp[ks] = ld8<SC1>(rws, ks == 3 ? so3 : so + ks * 512, 0);
        if constexpr (ks == 2) KEEP(late);
        if constexpr (ks == 18) {
          float s1 = (asf(sp[0].x) + asf(sp[1].x)) + (asf(sp[2].x) + asf(sp[3].x));
          float s2 = (asf(sp[0].y) + asf(sp[1].y)) + (asf(sp[2].y) + asf(sp[3].y));
          s1 = sum32(s1); s2 = sum32(s2);
          mean = s1 * (1.f / D);
          rstd = s2 * (1.f / D) - mean * mean;
          PINV(mean); PINV(rstd);
        }
        if constexpr (ks == 19) { rstd = __builtin_amdgcn_rsqf(fmaxf(rstd, 0.f) + EPS); PINV(rstd); }
        if constexpr (ks >= 20 && ks < 24) { ck[ks - 20] *= -mean * rstd; cv[ks - 20] *= -mean * rstd; PINV(ck[ks - 20]); PINV(cv[ks - 20]); }
        SCHED();
      });
      MFMA_SETTLE2(ak, av);
#pragma unroll
      for (int i = 0; i < 16; ++i) ak[i] = ak[i] * rstd + ck[i >> 2][i & 3];
      KVOUT(ak, 0)
#pragma unroll
      for (int i = 0; i < 16; ++i) av[i] = av[i] * rstd + cv[i >> 2][i & 3];
      KVOUT(av, 1)
    }
  });
  asm volatile(".Lfk_code_to:");
  if (blockIdx.x == 0 && threadIdx.x == 0) __hip_atomic_store(ctrl, epoch, RLX_AGENT);
}

extern "C" void kernel_launch(void* const* d_in, const int* in_sizes, int n_in, void* d_out, int out_size, void* d_ws,
                              size_t ws_size, hipStream_t stream) {
  Params p;
  p.x = d_in[0]; p.wqkv = d_in[1]; p.wo = d_in[2];
  p.w1 = d_in[4]; p.b1 = d_in[5]; p.w2 = d_in[6]; p.b2 = d_in[7];
  p.out = d_out; p.ws = d_ws;
  (void)hipFuncSetAttribute((const void*)fused_kernel, hipFuncAttributeMaxDynamicSharedMemorySize, LDS_BYTES);
  fused_kernel<<<256, 256, LDS_BYTES, stream>>>(p);
}
```

```cpp
#include <hip/hip_runtime.h>
#include <utility>

#define GAS __attribute__((address_space(1)))
#define LAS __attribute__((address_space(3)))
#define DI __device__ __forceinline__
#define INL __attribute__((always_inline))
#define RLX_AGENT __ATOMIC_RELAXED, __HIP_MEMORY_SCOPE_AGENT
typedef unsigned v4u __attribute__((ext_vector_type(4)));
typedef unsigned v2u __attribute__((ext_vector_type(2)));
typedef float f32x2 __attribute__((ext_vector_type(2)));
typedef float f32x4 __attribute__((ext_vector_type(4)));
typedef float f32x16 __attribute__((ext_vector_type(16)));
typedef short s16x4 __attribute__((ext_vector_type(4)));
typedef __bf16 bf16x2 __attribute__((ext_vector_type(2)));
typedef __bf16 bf16x8 __attribute__((ext_vector_type(8)));
typedef __amdgpu_buffer_rsrc_t rsrc_t;

constexpr int S = 2048, D = 448, F = 1792;
constexpr float EPS = 1e-5f;
constexpr int FLAG_OFF = 4096;
constexpr int KV_OFF = 512 * 1024, KV_SZ = 16 * 64 * 4096;
constexpr int O_OFF = KV_OFF + 2 * KV_SZ, O_SZ = 32 * 16 * 4096;
constexpr int S3_OFF = O_OFF + 2 * O_SZ, S3_SZ = 32 * 8 * 1024;
constexpr int HT_OFF = S3_OFF + 2 * S3_SZ, HT_SZ = 7 * 64 * 4096;
constexpr int U_OFF = HT_OFF + 2 * HT_SZ, U_SZ = 7 * 56 * 4096;
constexpr int X2_OFF = U_OFF + 2 * U_SZ, X2_SZ = 64 * 28 * 1024;
constexpr int S5_OFF = X2_OFF + X2_SZ, S5_SZ = 32 * 8 * 512;
constexpr int WS_BYTES = S5_OFF + S5_SZ;
constexpr int L_WQ = 0;
constexpr int L_KVW = 86016;
constexpr int L_CSP = L_KVW + 65536;
constexpr int L_CS = L_CSP + 1536 + 128;
constexpr int L_HST = L_CS + 384;
constexpr int L_DUMP = L_HST + 8192;
constexpr int LDS_BYTES = L_DUMP + 256;

static_assert(L_HST % 128 == 0 && LDS_BYTES <= 163840, "LDS map");
constexpr int SC1 = 16, OOB = 0x7ffff000;
constexpr int WIN = 12;
constexpr int NO2 = 4, NO3 = 2, NG2 = 3, NG3 = 2, NH2 = 4, NH3 = 3, NU3 = 2;
constexpr int KP = 5, MO2 = 4, MO3 = 1, MG2 = 3, MG3 = 0, NS = 5, SG = 1, SH = 2;
constexpr int W0 = 10, WINO = 9, WO1 = 9, WX0 = 9, WINX = 10;
constexpr int issued(int i, int n, int w0, int w) { const int c = w0 + i + (i < w - w0 ? i : w - w0); return c < n ? c : n; }
constexpr int Z_U = NO2 + NO3 + NG2 + NG3 + NH2 + NH3;
static_assert(Z_U >= 16 && Z_U + 3 + NU3 <= 30 && Z_U + 3 <= 30 - NS && NS <= 5 && SG + SH <= NS, "schedule");

struct Params {
  const void *x, *wqkv, *wo, *w1, *b1, *w2, *b2;
  void *out, *ws;
};

template <int... I, class Fn> DI void sfor_(std::integer_sequence<int, I...>, Fn&& f) { (f(std::integral_constant<int, I>{}), ...); }
template <int N, class Fn> DI void sfor(Fn&& f) { sfor_(std::make_integer_sequence<int, N>{}, f); }
DI float asf(unsigned u) { return __builtin_bit_cast(float, u); }
DI unsigned asu(float f) { return __builtin_bit_cast(unsigned, f); }
DI rsrc_t mk(const void* p, int bytes) { return __builtin_amdgcn_make_buffer_rsrc((void*)p, 0, bytes, 0x00020000); }
template <int AUX> DI v4u ld16(rsrc_t r, int voff, int soff) { return __builtin_amdgcn_raw_buffer_load_b128(r, voff, soff, AUX); }
template <int AUX> DI v2u ld8(rsrc_t r, int voff, int soff) { return __builtin_amdgcn_raw_buffer_load_b64(r, voff, soff, AUX); }
template <int AUX> DI void st16(v4u v, rsrc_t r, int voff, int soff) { __builtin_amdgcn_raw_buffer_store_b128(v, r, voff, soff, AUX); }
template <int AUX> DI void st8(v2u v, rsrc_t r, int voff, int soff) { __builtin_amdgcn_raw_buffer_store_b64(v, r, voff, soff, AUX); }
DI f32x4 mfma16(v4u a, v4u b, f32x4 c) {
  return __builtin_amdgcn_mfma_f32_16x16x32_bf16(__builtin_bit_cast(bf16x8, a), __builtin_bit_cast(bf16x8, b), c, 0, 0, 0);
}
DI f32x16 mfma32(v4u a, v4u b, f32x16 c) {
  return __builtin_amdgcn_mfma_f32_32x32x16_bf16(__builtin_bit_cast(bf16x8, a), __builtin_bit_cast(bf16x8, b), c, 0, 0, 0);
}
DI unsigned pk(float lo, float hi) { return __builtin_bit_cast(unsigned, __builtin_convertvector((f32x2){lo, hi}, bf16x2)); }
DI float bflo(unsigned u) { return asf(u << 16); }
DI float bfhi(unsigned u) { return asf(u & 0xffff0000u); }
DI f32x4 unpack4(v2u v) { return (f32x4){bflo(v.x), bfhi(v.x), bflo(v.y), bfhi(v.y)}; }
DI v2u trrd(LAS char* p) { return __builtin_bit_cast(v2u, __builtin_amdgcn_ds_read_tr16_b64_v4i16((LAS s16x4*)p)); }
DI v4u trfrag(LAS char* p0, LAS char* p1) { v2u a = trrd(p0), b = trrd(p1); return (v4u){a.x, a.y, b.x, b.y}; }
DI float hi32(float v) {
  unsigned u = asu(v);
  auto r = __builtin_amdgcn_permlane32_swap(u, u, false, false);
  return asf(r[1]);
}
DI float sum32(float v) {
  unsigned u = asu(v);
  auto r = __builtin_amdgcn_permlane32_swap(u, u, false, false);
  return asf(r[0]) + asf(r[1]);
}
DI float max32(float v) {
  unsigned u = asu(v);
  auto r = __builtin_amdgcn_permlane32_swap(u, u, false, false);
  return fmaxf(asf(r[0]), asf(r[1]));
}
DI float sum_ab(float a, float b) {
  auto r = __builtin_amdgcn_permlane16_swap(asu(a), asu(b), false, false);
  return sum32(asf(r[0]) + asf(r[1]));
}
DI void stats16(const f32x4 (&v)[4], int n, f32x2& s1, f32x2& s2) {
#pragma unroll
  for (int a = 0; a < 4; ++a)
    if (n >> a & 1) {
      const f32x2 lo = {v[a][0], v[a][1]}, hi = {v[a][2], v[a][3]};
      s1 += lo; s2 += lo * lo; s1 += hi; s2 += hi * hi;
    }
}
#define FENCE() asm volatile("" ::: "memory")
#define SCHED() __builtin_amdgcn_sched_barrier(0)
#define DRAIN() asm volatile("s_waitcnt vmcnt(0)" ::: "memory")
#define PINV(x) asm volatile("" : "+v"(x))
#define PINA(x) asm volatile("" : "+a"(x))
#define PINS(x) asm volatile("" : "+s"(x))
#define KEEP(x) asm volatile("" :: "v"(x))
#define LGKM0() asm volatile("s_waitcnt lgkmcnt(0)" ::: "memory")
DI void bar() { LGKM0(); __builtin_amdgcn_s_barrier(); FENCE(); }
template <int N> DI void vmw() { asm volatile("s_waitcnt vmcnt(%0)" :: "n"(N) : "memory"); }
template <int LO, int MO> DI void dmai(rsrc_t r, int voff, int lds) {
  int so;
  asm volatile("s_add_u32 m0, %1, %4\n\ts_mov_b32 %0, %5\n\tbuffer_load_dwordx4 %2, %3, %0 offen lds"
               : "=&s"(so) : "s"(lds), "v"(voff), "s"(r), "n"(LO), "n"(MO) : "memory", "scc");
}
DI void touch(rsrc_t r, int voff, int soff, LAS char* L) {
  asm volatile("s_mov_b32 m0, %0\n\ts_nop 3\n\tbuffer_load_dword %1, %2, %3 offen lds"
               :: "s"((int)(size_t)(L + L_DUMP)), "v"(voff), "s"(r), "s"(soff) : "memory");
}
DI void publish(GAS unsigned* flag, unsigned epoch) {
  DRAIN();
  __builtin_amdgcn_s_waitcnt(0x0F70);
  bar();
  if (threadIdx.x == 0) __hip_atomic_store(flag, epoch, RLX_AGENT);
  FENCE();
}
#define POLL(f, ptr) asm volatile("global_load_dword %0, %1, off sc1" : "=&v"(f) : "v"(ptr) : "memory")
#define POLLED(f, N, ptr) { \
    unsigned f2_; \
    POLL(f2_, ptr); \
    asm volatile("s_waitcnt vmcnt(%1)" : "+v"(f) : "n"((N) + 1) : "memory"); \
    if (__builtin_amdgcn_ballot_w64(f != epoch) != 0ull) { \
      asm volatile("s_waitcnt vmcnt(0)" : "+v"(f2_) :: "memory"); \
      if (__builtin_amdgcn_ballot_w64(f2_ != epoch) != 0ull) wait_flags(ptr, epoch, budget); \
    } \
    FENCE(); \
    late = f2_; }
DI void wait_flags(GAS unsigned* flag, unsigned epoch, unsigned& budget) {
  FENCE();
  while (true) {
    unsigned f = __hip_atomic_load(flag, RLX_AGENT);
    if (__builtin_amdgcn_ballot_w64(f != epoch) == 0ull) break;
    if (budget == 0u) break;
    --budget;
  }
  __builtin_amdgcn_fence(__ATOMIC_ACQUIRE, "wavefront");
  FENCE();
}

DI int oob(bool in, int off) { return off | (in ? 0 : OOB); }
DI int wrap7(int v) { return v >= 7 ? v - 7 : v; }
DI int swz(int row) { return ((row >> 1) & 1) | (((row >> 3) ^ (row >> 2)) & 1) << 1; }

#define FRESH() \
  int tid = threadIdx.x, bid = blockIdx.x; \
  asm volatile("" : "+v"(tid)); \
  asm volatile("" : "+s"(bid)); \
  const int lane = tid & 63, w = __builtin_amdgcn_readfirstlane(tid >> 6), bx = bid & 7, by = bid >> 3; \
  const int c = lane & 15, g = lane >> 4, q4 = (lane & 15) >> 2, p4 = lane & 3, r32 = lane & 31, hh = lane >> 5; \
  const int by7 = __builtin_amdgcn_readfirstlane((by * 37) >> 8), bym = __builtin_amdgcn_readfirstlane(by - 7 * by7); \
  const int t = 2 * bx + (by >> 4), hd = by & 15;                \
  const int st = 4 * bx + by7, dt = bym;                         \
  const int dt4 = by < 21 ? bym : bym + 4 * (bx & 1), ft = 7 * (bx >> 1) + (by < 21 ? by7 + 4 * (bx & 1) : 3);    \
  LAS char* ring = L + L_KVW + w * 16384; \
  const int wrow = lane >> 3, sx0 = swz(wrow) << 5; \
  auto raddr = [&](int row) { return row * 128 + (swz(row) << 5) + 8 * p4; }; \
  LAS char* red = L + L_KVW + lane * 16; \
  (void)c; (void)g; (void)q4; (void)r32; (void)hh; (void)t; (void)hd; (void)st; (void)dt; (void)dt4; (void)ft; (void)ring; (void)sx0; (void)raddr; (void)red;

#define MFMA32V0(d, a, b) asm volatile("v_mfma_f32_32x32x16_bf16 %0, %1, %2, 0" : "=&v"(d) : "v"(a), "v"(b))
#define MFMA32VC(d, a, b, c) asm volatile("v_mfma_f32_32x32x16_bf16 %0, %1, %2, %3" : "=&v"(d) : "v"(a), "v"(b), "v"(c))
#define MFMA32VA(d, a, b) asm volatile("v_mfma_f32_32x32x16_bf16 %0, %1, %2, %0" : "+v"(d) : "v"(a), "v"(b))
#define MFMA_SETTLE(d) asm volatile("s_nop 11" : "+v"(d))
#define MFMA_SETTLE2(d, e) asm volatile("s_nop 11" : "+v"(d), "+v"(e))
DI float max3(float a, float b, float c) { float r; asm("v_max3_f32 %0, %1, %2, %3" : "=v"(r) : "v"(a), "v"(b), "v"(c)); return r; }

#define WB(cc) (L + L_WQ + (4 * hh + q4) * 64 + (g & 1) * 32 + 8 * p4 + (hd & 1) * 8 + (cc) * 4096)
#define WFRAG(wb, part, e) trfrag((wb) + (part) * 28672 + ((e) >> 1) * 2048 + ((e) & 1) * 512, (wb) + (part) * 28672 + ((e) >> 1) * 2048 + ((e) & 1) * 512 + 1024)
#define CSUM(part, a) (*(LAS f32x4*)(L + L_CS + ((part) * 32 + 8 * (a) + 4 * hh) * 4))
#define WZ_RD1(n, i, sl) wz[n][i] = (n) < 16 ? trfrag(pr1[i] + (sl) * 4096, pr1[(i) ^ 2] + (sl) * 4096 + 512) : trfrag(pr2[i] + (sl) * 4096, pr2[i] + (sl) * 4096 + 2048)
#define DMA_WZ1(n, m, sl) { \
    if ((n) < 16) dmai<(sl) * 4096 + (m) * 1024, (32 * ((n) & 15) + 8 * (m)) * F * 2>(rw1, ((m) & 1) ? pw1 ^ 64 : pw1, pring); \
    else dmai<(sl) * 4096 + (m) * 1024, (32 * ((n) & 15) + 8 * (m)) * S * 2>(rw2, ((m) & 1) ? pw2 ^ 64 : pw2, pring); }
#define PIECE(L0, q) { \
    constexpr int r_ = (q) / 4, m_ = (q) % 4, n_ = (L0) + r_; \
    if constexpr (m_ == 0 && r_ >= 4) asm volatile("s_waitcnt vmcnt(8) lgkmcnt(0)" ::: "memory"); \
    else if constexpr (m_ == 0 && r_ == 3) vmw<8>(); \
    DMA_WZ1(n_, m_, r_ & 3) \
    if constexpr (r_ >= 3) WZ_RD1(n_ - 3, m_, (r_ - 3) & 3); \
    SCHED(); }
#define DPUMP(L0, A, B) sfor<4 * ((B) - (A))>([&](auto I_) INL { PIECE(L0, 4 * ((A) - (L0)) + I_) });
#define STASH(A, B) if constexpr (layer == 1) sfor<4 * ((B) - (A))>([&](auto I_) INL { \
    constexpr int j_ = (A) + I_ / 4, m_ = I_ % 4; \
    dmai<j_ * 4096 + m_ * 1024, (32 * (14 - NS + j_) + 8 * m_) * S * 2>(rw2, (m_ & 1) ? pw2 ^ 64 : pw2, pstash); SCHED(); });
#define DTSTEP(L0, L1, sl) ((L1) - 1 - (((L1) - 1 - (L0) - (sl)) & 3))
#define DTAIL1(L0, L1, j) { if constexpr (DTSTEP(L0, L1, (j) >> 2) >= (L0) && DTSTEP(L0, L1, (j) >> 2) >= (L1) - 3) WZ_RD1(DTSTEP(L0, L1, (j) >> 2), (j) & 3, (j) >> 2); }
#define DTAIL(L0, L1) sfor<16>([&](auto J_) INL { constexpr int j_ = J_; DTAIL1(L0, L1, j_) }); SCHED();
#define REDW(a, b) *(LAS f32x4*)(red + w * 16384 + (((b) - 1) * 4 + (a)) * 1024) = acc[a][b]
#define REDR(a, b) (*(LAS f32x4*)(red + ((w + 4 - (b)) & 3) * 16384 + (((b) - 1) * 4 + (a)) * 1024))
__global__ void __launch_bounds__(256, 1) fused_kernel(Params p) {
  extern __shared__ __attribute__((aligned(16))) char smem[];
  LAS char* L = (LAS char*)smem;
  {
    unsigned long long pc;
    int len;
    asm volatile("s_getpc_b64 %0\n.Lfk_code_from:\n\ts_mov_b32 %1, .Lfk_code_to-.Lfk_code_from" : "=s"(pc), "=s"(len));
    touch(mk((const void*)pc, len > 0 ? len : 0), (threadIdx.x & 63) < 8 ? ((blockIdx.x >> 3) * 4 + (threadIdx.x >> 6) + (threadIdx.x & 7) * 128) * 128 : OOB, 0, L);
  }
  SCHED();
  const bool on35 = (blockIdx.x >> 3) < 28;
  const bool on4 = (blockIdx.x >> 3) < ((blockIdx.x & 1) ? 24 : 25);
  const rsrc_t rws = mk(p.ws, WS_BYTES);
  GAS unsigned* ctrl = (GAS unsigned*)p.ws;
  GAS unsigned* flags = (GAS unsigned*)((GAS char*)p.ws + FLAG_OFF);
  unsigned budget = 1u << 22;

  v4u xf[28];
  v4u kv[4];
  float mean = 0.f, rstd = 0.f;
  f32x4 res[4];
  unsigned epoch;
  int kvs, oo, t1o, wvo2, pw1, pw2, fo;
  unsigned nk1, nk2, pm[8];
  LAS char *kvw, *vw, *vr0, *wor[4], *pr1[4], *pr2[4], *wbq;
#define KVOUT(acc, h) { \
    _Pragma("unroll") for (int i = 0; i < 16; i += 2) kv[2 * (h) + (i >> 3)][(i >> 1) & 3] = pk(acc[i], acc[i + 1]); \
    if (hh) { kv[2 * (h) + 1][2] = (h) ? 0x00003f80u : 0u; kv[2 * (h) + 1][3] = 0u; } \
    const rsrc_t rkv = mk(p.ws, t < 15 ? WS_BYTES : 0);    \
    st16<SC1>(kv[2 * (h)], rkv, kvs + (h) * 2048, 0); \
    st16<SC1>(kv[2 * (h) + 1], rkv, kvs + (h) * 2048 + 1024, 0); \
    SCHED(); }
#define X128(p) ((LAS char*)(size_t)((unsigned)(size_t)(p) ^ 128u))
  {
    FRESH();
    const rsrc_t rwq = mk(p.wqkv, D * 3 * D * 2);
    const rsrc_t rx = mk(p.x, S * D * 2);
    const int gid = by * 4 + w, rg = (int)(size_t)ring;
    touch(rx, oob(lane < 14, (t * 896 + hd * 56 + w * 14 + lane) * 128), 0, L);
    const int xvo = (t * 128 + w * 32 + wrow) * 896 + (((lane & 7) ^ (wrow >> 1)) << 4);
#define DMA_X(cc) sfor<4>([&](auto M) INL { constexpr int m = M; dmai<((cc) & 3) * 4096 + m * 1024, m * 8 * 896 + (cc) * 128>(rx, (m & 1) ? xvo ^ 64 : xvo, rg); })
    DMA_X(0); DMA_X(1);
    touch(rwq, oob(lane < 14, gid * 2688 + 896 + lane * 128), 0, L);
    DMA_X(2);
    SCHED();
    const int tb = gid * 35 + lane, tbr = (tb * 18725) >> 18;
    int tvb = oob(lane < 35, (128 + tbr) * 2688 + 896 + (tb - 14 * tbr) * 128);
    int wvo = (w * 16 + (lane >> 2)) * 2688 + hd * 56 - (hd & 1) * 8 + (lane & 3) * 16;
    const int wlb = (int)(size_t)(L + L_WQ) + w * 1024;
#define DMA_W(part, cc) dmai<(part) * 28672 + (cc) * 4096, (cc) * 64 * 2688 + (part) * 896>(rwq, wvo, wlb)
    LAS char* xa[8];
#pragma unroll
    for (int e = 0; e < 8; ++e) { xa[e] = ring + r32 * 128 + 8 * hh + ((e ^ ((r32 >> 1) & 7)) << 4); PINV(xa[e]); }
    int epo = w == 0 ? FLAG_OFF + (bid - 256) * 128 + lane * 32768 : S3_OFF + (st * 8 + dt) * 1024 - 2 * S3_SZ + (lane >> 3) * S3_SZ + (lane & 7) * 128;
    epo = (w == 0 ? lane - 1u < 10u : w == 1 && on35 && (lane >> 4) == 1) ? epo : 0;
    PINV(tvb); PINV(wvo); PINV(epo);
    SCHED();
    DMA_X(3);
    asm volatile("buffer_load_dword %0, %1, %2, 0 offen sc1" : "=&v"(epoch) : "v"(epo), "s"(rws) : "memory");
    SCHED();
    int tvq, tvw;
    LAS char *cb, *wb;
#define READ_X(cc) _Pragma("unroll") for (int e = 0; e < 4; ++e) { \
      const int ch = (e >> 1) * 4 + (e & 1); \
      const v2u a_ = *(LAS v2u*)(xa[ch] + ((cc) & 3) * 4096), b_ = *(LAS v2u*)(xa[ch + 2] + ((cc) & 3) * 4096); \
      xf[4 * (cc) + e] = (v4u){a_.x, a_.y, b_.x, b_.y}; }
    const v4u ones = {0x3f803f80u, 0x3f803f80u, 0x3f803f80u, 0x3f803f80u};
    f32x16 gs, ss = {};
#define XSTAT(i) { if constexpr ((i) == 0) MFMA32V0(gs, xf[i], xf[i]); else MFMA32VA(gs, xf[i], xf[i]); ss = mfma32(ones, xf[i], ss); }
    vmw<14>(); READ_X(0) LGKM0(); DMA_X(4); XSTAT(0) XSTAT(1) XSTAT(2) XSTAT(3) SCHED();
    vmw<14>(); READ_X(1) LGKM0(); DMA_X(5); XSTAT(4) XSTAT(5) XSTAT(6) XSTAT(7) SCHED();
    vmw<13>(); READ_X(2) LGKM0(); DMA_X(6); SCHED();
    asm volatile("s_waitcnt vmcnt(12)" : "+v"(epoch) :: "memory");
    epoch = __builtin_amdgcn_readfirstlane(epoch) + 1u;
    READ_X(3)
    SCHED();
    sfor<20>([&](auto I) INL {
      constexpr int i = I;
      if constexpr (i == 6) { vmw<8 + 3>(); READ_X(4) }
      if constexpr (i == 10) { vmw<4 + 5 + 1>(); READ_X(5) }
      if constexpr (i == 14) { vmw<7 + 1>(); READ_X(6) }
      if constexpr ((i & 1) == 0 && i < 16) DMA_W(1 + ((i >> 1) & 1), i >> 2);
      if constexpr (i == 7) touch(rwq, tvb, 0, L);
      MFMA32VA(gs, xf[8 + i], xf[8 + i]);
      if constexpr (i < 8) {
        const int d = r32 - ((2 * i & 3) + 8 * (i >> 1)) - 4 * hh;
        pm[i] = ((unsigned)(d >> 31) & 0xffffu) | ((unsigned)((d - 1) >> 31) & 0xffff0000u);
        PINA(pm[i]);
      } else if constexpr (i < 12) { wor[i - 8] = ring + (raddr(16 * (g >> 1) + 4 * (g & 1) + q4) ^ ((i - 8) << 5)); PINA(wor[i - 8]); }
      else if constexpr (i < 16) { pr1[i - 12] = ring + (raddr(8 * g + q4) ^ ((i - 12) << 5)); PINA(pr1[i - 12]); }
      else { pr2[i - 16] = ring + (raddr(4 * g + q4) ^ (((i + w) & 3) << 5)); PINA(pr2[i - 16]); }
      SCHED();
      ss = mfma32(ones, xf[8 + i], ss);
      SCHED();
      if constexpr (i == 0) { kvs = KV_OFF + (hd * 64 + 4 * t + w) * 4096 + lane * 16; PINV(kvs); }
      if constexpr (i == 1) { oo = O_OFF + ((2 * t + (w >> 1)) * 16 + hd) * 4096 + (2 * (w & 1) + (r32 >> 4)) * 1024 + hh * 256 + c * 16; PINA(oo); }
      if constexpr (i == 2) { t1o = oob(lane < 32, (512 * (by >> 3) + 8 * ((by & 7) * 4 + w) + (lane >> 2)) * (F * 2) + (7 * (bx >> 1) + 3 * (bx & 1) + (lane & 3)) * 128); PINA(t1o); }
      if constexpr (i == 3) { wvo2 = ((112 * w + wrow) * 896 + dt * 128 + (lane & 7) * 16) ^ sx0; PINA(wvo2); }
      if constexpr (i == 4) { pw1 = ((512 * w + wrow) * (F * 2) + ft * 128 + (lane & 7) * 16) ^ sx0; PINA(pw1); }
      if constexpr (i == 5) { pw2 = ((448 * w + wrow) * (S * 2) + st * 128 + (lane & 7) * 16) ^ sx0; PINA(pw2); }
      if constexpr (i == 7) {
        const int tp = (t >= 2 && (lane & 1)) ? t - 2 : t > 0 ? t - 1 : 0;
        fo = FLAG_OFF + (((tp & 1) * 16 + hd) * 8 + (tp >> 1)) * 128;
        PINA(fo);
      }
      if constexpr (i == 8) { nk2 = (t < 2 && hh) ? 0xf149u : 0u; nk1 = (t < 1 && hh) ? 0xf149u : 0u; PINA(nk1); PINA(nk2); }
      if constexpr (i == 9) { kvw = L + L_KVW + w * 4096 + lane * 16; vw = L + L_KVW + w * 4096 + 2048 + ((lane * 16) ^ (hh << 6)); PINA(kvw); PINA(vw); }
      if constexpr (i == 11) { vr0 = L + L_KVW + w * 4096 + 2048 + ((((g & 1) * 1024 + (p4 & 1) * 512 + hh * 64 + q4 * 16) ^ ((p4 & 1) << 6) ^ ((g & 1) << 7)) + (p4 >> 1) * 8); PINA(vr0); }
      if constexpr (i == 13) { wbq = WB(0); wb = wbq + 28672; PINV(wbq); PINV(wb); }
      if constexpr (i == 15) { cb = L + L_WQ + 28672 + (w * 16 + 8 * hh + q4) * 64 + (g & 1) * 32 + 8 * p4; PINV(cb); }
      if constexpr (i == 17) {
        const int tl = by * 98 + w * 25 + lane, trow = (tl * 9363) >> 16;
        tvq = oob(lane < 25 && w * 25 + lane < 98, trow * 2688 + (tl - 7 * trow) * 128); tvw = oob(lane < 25 && w * 25 + lane < 98, tl * 128);
        PINV(tvq); PINV(tvw);
      }
      SCHED();
    });
    f32x16 ak, av;
    v4u wk[28], wv[28], wc[14];
    f32x16 csk = {}, csv = {};
    float dg = 0.f, d8[8], d4[4], pp[4];
    f32x4 cc[8];
    LAS float* pb = (LAS float*)(L + L_CSP) + 32 + 16 * w + c + 4 * (hd & 1);
    LAS float* pw = (LAS float*)(L + (hh ? L_DUMP : L_CSP + (w * 96 + 32) * 4)) + r32;
#define PICK4(o, at, a0, a1, a2, a3, b0, b1, b2, b3, m) \
    asm volatile("v_cndmask_b32 %0, %4, %8, %12\n\tv_cndmask_b32 %1, %5, %9, %12\n\tv_cndmask_b32 %2, %6, %10, %12\n\tv_cndmask_b32 %3, %7, %11, %12" \
                 : "=&v"(o[at]), "=&v"(o[(at) + 1]), "=&v"(o[(at) + 2]), "=&v"(o[(at) + 3]) \
                 : "v"(a0), "v"(a1), "v"(a2), "v"(a3), "v"(b0), "v"(b1), "v"(b2), "v"(b3), "s"(m))
    sfor<8>([&](auto R) INL {
      constexpr int r = R;
      if constexpr (r == 0 || r == 2 || r == 4) {
        vmw<(r == 0 ? 5 : r == 2 ? 4 : 2)>();
        __builtin_amdgcn_s_barrier();
        FENCE();
        SCHED();
      }
      sfor<4>([&](auto E) INL {
        constexpr int e = E, kr = 4 * r + e, km = kr - 4;
        if constexpr (r > 0) { if constexpr (km == 0) MFMA32V0(ak, wk[km], xf[km]); else MFMA32VA(ak, wk[km], xf[km]); }
        if constexpr (r < 7) wk[kr] = WFRAG(wb + r * 4096, 0, e);
        constexpr int ci = r < 4 ? ((e & 1) ? -1 : 2 * r + e / 2) : r == 4 ? 8 + e : r == 5 && e < 2 ? 12 + e : -1;
        constexpr int cm = r < 4 ? ((e & 1) ? 2 * r + e / 2 : -1) : r == 4 ? (e ? 7 + e : -1) : r == 5 && e < 3 ? 11 + e : -1;
        if constexpr (ci >= 0) wc[ci] = trfrag(cb + (ci & 1) * 28672 + (ci >> 1) * 4096, cb + (ci & 1) * 28672 + (ci >> 1) * 4096 + 256);
        SCHED();
        if constexpr (r > 0) { if constexpr (km == 0) MFMA32V0(av, wv[km], xf[km]); else MFMA32VA(av, wv[km], xf[km]); }
        if constexpr (r < 7) wv[kr] = WFRAG(wb + r * 4096, 1, e);
        SCHED();
        if constexpr (cm >= 0) { if constexpr (cm & 1) csv = mfma32(ones, wc[cm], csv); else csk = mfma32(ones, wc[cm], csk); }
        SCHED();
        if constexpr (e == 1 || e == 3) {
          if constexpr (r < 3) DMA_W(1 + e / 2, r + 4);
          else if constexpr (r == 3) { if constexpr (e == 1) touch(rwq, tvq, 0, L); else touch(mk(p.wo, D * D * 2), tvw, 0, L); }
          else if constexpr (2 * (r - 4) + e / 2 < 7) DMA_W(0, 2 * (r - 4) + e / 2);
        }
        if constexpr (r == 1 && (e & 1) == 0) { constexpr int j = 2 * e; PICK4(d8, j, gs[j], gs[j + 1], gs[j + 2], gs[j + 3], gs[j + 8], gs[j + 9], gs[j + 10], gs[j + 11], 0xffff0000ffff0000ull); }
        if constexpr (r == 2 && e == 0) PICK4(d4, 0, d8[0], d8[1], d8[2], d8[3], d8[4], d8[5], d8[6], d8[7], 0xff00ff00ff00ff00ull);
        if constexpr (r == 2 && e == 2)
          asm volatile("v_cndmask_b32 %1, %1, %3, %5\n\tv_cndmask_b32 %2, %2, %4, %5\n\tv_cndmask_b32 %0, %1, %2, %6\n\tv_cndmask_b32 %0, 0, %0, %7"
                       : "=&v"(dg), "+v"(d4[0]), "+v"(d4[1]) : "v"(d4[2]), "v"(d4[3]), "s"(0xccccccccccccccccull), "s"(0xaaaaaaaaaaaaaaaaull), "s"(0xf0f0f0f00f0f0f0full));
        if constexpr (r == 3 && e == 0) { mean = ss[0] * (1.f / D); rstd = sum32(dg) * (1.f / D) - mean * mean; PINV(mean); PINV(rstd); }
        if constexpr (r == 3 && e == 2) { rstd = __builtin_amdgcn_rsqf(fmaxf(rstd, 0.f) + EPS); PINV(rstd); }
        if constexpr (r == 0 && e == 3) { PINV(pb); PINV(pw); }
        if constexpr (r == 5 && e == 3) { pw[0] = csk[0]; pw[32] = csv[0]; }
        if constexpr (r == 6 && e == 0) { bar(); pp[0] = pb[0]; pp[1] = pb[96]; pp[2] = pb[192]; pp[3] = pb[288]; }
        if constexpr (r == 6 && e == 2) *(LAS float*)(L + L_CS + (32 + 16 * w + c) * 4) = (pp[0] + pp[1]) + (pp[2] + pp[3]);
        if constexpr (r == 6 && e == 3) bar();
        if constexpr (r == 7 && e < 2) { _Pragma("unroll") for (int j = 0; j < 4; ++j) cc[4 * e + j] = CSUM(1, 4 * e + j); }
        SCHED();
      });
    });
    MFMA_SETTLE2(ak, av);
#pragma unroll
    for (int i = 0; i < 16; ++i) ak[i] = (ak[i] - mean * cc[i >> 2][i & 3]) * rstd;
    KVOUT(ak, 0)
#pragma unroll
    for (int i = 0; i < 16; ++i) av[i] = (av[i] - mean * cc[4 + (i >> 2)][i & 3]) * rstd;
    KVOUT(av, 1)
  }

  unsigned b2r;
  v2u bb[4];
  v4u wz[30][4];
  sfor<2>([&](auto LY) INL {
    constexpr int layer = LY;
    constexpr int Y0 = layer ? KP : 0, Y2 = Y0 + (layer ? MO2 : NO2), YG = Y2 + (layer ? MO3 : NO3), YG2 = YG + (layer ? MG2 : NG2),
                  YH = YG2 + (layer ? MG3 : NG3), YE = layer ? 16 : Z_U, YH2 = layer ? YE : YH + NH2, YZ = layer ? 30 - NS : 30, YU3 = layer ? YZ : 30 - NU3;
    static_assert(YH <= YH2, "schedule");
    constexpr int QPUB = layer ? 14 : 10;
    GAS unsigned* fl = flags + layer * 5 * 256 * 32;
    v4u qf[2];
    v4u wo[4][4];
    v2u rr[4];
    int pring, pstash;
    const rsrc_t rw1 = mk(p.w1, on4 ? S * F * 2 : 0);
    const rsrc_t rw2 = mk(p.w2, on35 ? S * F * 2 : 0);
    {
      const bool hi = threadIdx.x & 32;
      FRESH();
      if constexpr (layer == 0) {
        vmw<4>();
        if (t == 15) vmw<0>();
        __builtin_amdgcn_s_barrier();
        FENCE();
      }
      v4u wc[7];
      f32x16 cs = {};
      LAS char* cb = L + L_WQ + (w * 16 + 8 * hh + q4) * 64 + (g & 1) * 32 + 8 * p4;
      v4u wq[28];
#pragma unroll
      for (int ks = 0; ks < 4; ++ks) wq[ks] = WFRAG(wbq, 0, ks);
      f32x16 aq;
      const rsrc_t rwo = mk(p.wo, on35 ? D * D * 2 : 0);
      pring = (int)(size_t)ring;
      pstash = (int)(size_t)(L + L_WQ) + w * 20480;
      PINS(pring); PINS(pstash);
      GAS unsigned* fp = (GAS unsigned*)((GAS char*)p.ws + fo);
      unsigned pf;
      sfor<28>([&](auto I) INL {
        constexpr int ks = I;
        if constexpr (ks == 0) MFMA32V0(aq, wq[ks], xf[ks]); else MFMA32VA(aq, wq[ks], xf[ks]);
        if constexpr (ks + 4 < 28) wq[ks + 4] = WFRAG(wbq + ((ks + 4) >> 2) * 4096, 0, ks & 3);
        constexpr int wi = layer ? -1 : ks - QPUB - 1;
        if constexpr (wi >= 0 && wi < 16) dmai<wi * 1024, (28 * (wi >> 2) + 8 * (wi & 3)) * 896>(rwo, (wi & 1) ? wvo2 ^ 64 : wvo2, pring);
        if constexpr (layer == 0) {
          if constexpr (ks < 7) wc[ks] = trfrag(cb + ks * 4096, cb + ks * 4096 + 256);
          if constexpr (ks >= 2 && ks < 9) cs = mfma32((v4u){0x3f803f80u, 0x3f803f80u, 0x3f803f80u, 0x3f803f80u}, wc[ks - 2], cs);
          if constexpr (ks == QPUB + 1) { _Pragma("unroll") for (int q = 0; q < 4; ++q) cs[q] = ((LAS float*)(L + L_CSP) + r32 + 4 * (hd & 1))[96 * q]; }
          if constexpr (ks == QPUB + 5) *(LAS float*)(L + L_CS + r32 * 4) = (cs[0] + cs[1]) + (cs[2] + cs[3]);
        }
        if constexpr (ks == 27) POLL(pf, fp);
        if constexpr (ks == QPUB) {
          if constexpr (layer == 0) { *(LAS float*)(L + (hh ? L_DUMP : L_CSP + w * 96 * 4) + r32 * 4) = cs[0]; }
          publish(fl + (0 * 256 + bid) * 32, epoch);
        }
        SCHED();
      });
      f32x4 cq[4];
#pragma unroll
      for (int a = 0; a < 4; ++a) cq[a] = CSUM(0, a);
      MFMA_SETTLE(aq);
      const float qs = rstd * (0.18898223650461362f * 1.4426950408889634f);
#pragma unroll
      for (int a = 0; a < 4; ++a) {
        qf[a >> 1][(a & 1) * 2] = pk((aq[4 * a] - mean * cq[a][0]) * qs, (aq[4 * a + 1] - mean * cq[a][1]) * qs);
        qf[a >> 1][(a & 1) * 2 + 1] = pk((aq[4 * a + 2] - mean * cq[a][2]) * qs, (aq[4 * a + 3] - mean * cq[a][3]) * qs);
      }
      if (hi) { qf[1][2] = 0x00003f80u; qf[1][3] = 0u; }
      PINV(qf[0]); PINV(qf[1]);
      f32x16 s8;
      asm volatile("s_nop 1" : "+v"(qf[0]), "+v"(qf[1]));
      MFMA32V0(s8, kv[0], qf[0]);
      MFMA32VA(s8, kv[1], qf[1]);
      SCHED();
      unsigned late;
      POLLED(pf, 0, fp)
      v4u ra[4], rb[4];
      {
        const rsrc_t r1 = mk(p.ws, t >= 1 ? WS_BYTES : 0), r2 = mk(p.ws, t >= 2 ? WS_BYTES : 0);
        const int kvo = kvs - 32768;
        rb[0] = ld16<SC1>(r2, kvo, 0); rb[1] = ld16<SC1>(r2, kvo, 1024);
        ra[0] = ld16<SC1>(r1, kvo, 16384); ra[1] = ld16<SC1>(r1, kvo, 16384 + 1024);
        SCHED();
        rb[2] = ld16<SC1>(r2, kvo, 2048); rb[3] = ld16<SC1>(r2, kvo, 3072);
        ra[2] = ld16<SC1>(r1, kvo, 16384 + 2048); ra[3] = ld16<SC1>(r1, kvo, 16384 + 3072);
        SCHED();
      }
#pragma unroll
      for (int j = 0; j < 16; ++j) wo[j >> 2][j & 3] = trfrag(wor[j & 3] + (j >> 2) * 4096, wor[(j & 3) ^ 2] + (j >> 2) * 4096 + 1024);
      SCHED();
      bar();
      LAS char* vw1 = X128(vw) + 1024;
      LAS char* vr1 = X128(vr0);
      *(LAS v4u*)(kvw + 32768) = kv[0];
      *(LAS v4u*)(kvw + 32768 + 1024) = kv[1];
      *(LAS v4u*)(vw + 32768) = kv[2];
      *(LAS v4u*)(vw1 + 32768) = kv[3];
      int rro = (64 * st + 16 * w + c) * 896 + dt * 128 + g * 8, b2o = (64 * st + 16 * w + c) * 2, b1o = ft * 128 + g * 8;
      unsigned pml[8], mk32[16], nk1l = nk1, nk2l = nk2;
      int ool = oo, t1l = t1o;
      LAS char* vr0l = vr0;
      if constexpr (layer == 0) { PINV(rro); PINV(b2o); PINV(b1o); }
      PINV(vw1); PINV(vr1); PINV(vr0l); PINV(ool); PINV(t1l); PINV(nk1l); PINV(nk2l);
#pragma unroll
      for (int i = 0; i < 8; ++i) { pml[i] = pm[i]; PINV(pml[i]); mk32[2 * i] = (unsigned)((int)(pml[i] << 16) >> 16); mk32[2 * i + 1] = (unsigned)((int)pml[i] >> 16); PINV(mk32[2 * i]); PINV(mk32[2 * i + 1]); }
      SCHED();
      f32x16 sc[8], se;
      v4u kf[8][2];
      float mx = -1e30f, mx2 = -1e30f;
#define MAX8(v, i) asm volatile("v_max3_f32 %0, %0, %2, %3\n\tv_max3_f32 %1, %1, %4, %5\n\tv_max3_f32 %0, %0, %6, %7\n\tv_max3_f32 %1, %1, %8, %9" \
        : "+v"(mx), "+v"(mx2) : "v"(v[i]), "v"(v[(i) + 1]), "v"(v[(i) + 2]), "v"(v[(i) + 3]), "v"(v[(i) + 4]), "v"(v[(i) + 5]), "v"(v[(i) + 6]), "v"(v[(i) + 7]))
      *(LAS v4u*)(kvw) = rb[0];
      MFMA32V0(sc[0], rb[0], qf[0]);
      rb[1][2] |= nk2l;
      *(LAS v4u*)(kvw + 1024) = rb[1];
      *(LAS v4u*)(kvw + 16384) = ra[0];
      SCHED();
      MFMA32VA(sc[0], rb[1], qf[1]);
      ra[1][2] |= nk1l;
      *(LAS v4u*)(kvw + 16384 + 1024) = ra[1];
      kf[4][0] = ra[0]; kf[4][1] = ra[1];
      MFMA32V0(sc[4], kf[4][0], qf[0]);
      bar();
      KEEP(late);
      MFMA32VA(sc[4], kf[4][1], qf[1]);
#pragma unroll
      for (int j = 1; j < 8; ++j) if (j != 4) { kf[j][0] = *(LAS v4u*)(kvw + j * 4096); kf[j][1] = *(LAS v4u*)(kvw + j * 4096 + 1024); }
      PINV(sc[0]);
#pragma unroll
      for (int i = 0; i < 16; ++i) se[i] = asf((asu(sc[0][i]) & mk32[i]) | (asu(s8[i]) & ~mk32[i]));
      PINV(se);
      SCHED();
      sfor<12>([&](auto N) INL {
        constexpr int n = N, jj = n >> 1, j = jj < 3 ? jj + 1 : jj + 2, hf = n & 1;
        if constexpr (hf == 0) MFMA32V0(sc[j], kf[j][0], qf[0]); else MFMA32VA(sc[j], kf[j][1], qf[1]);
        if constexpr (n < 2) MAX8(se, 8 * hf);
        else if constexpr (n < 4) MAX8(sc[4], 8 * hf);
        else MAX8(sc[jj < 5 ? jj - 1 : jj], 8 * hf);
        if constexpr (n == 2) *(LAS v4u*)(vw) = rb[2];
        if constexpr (n == 3) *(LAS v4u*)(vw1) = rb[3];
        if constexpr (n == 4) *(LAS v4u*)(vw + 16384) = ra[2];
        if constexpr (n == 5) *(LAS v4u*)(vw1 + 16384) = ra[3];
        if constexpr (n == 6) touch(mk(p.w1, S * F * 2), t1l, layer * KP * 32 * F * 2, L);
        if constexpr (n >= 7) asm volatile("s_nop 0");
        SCHED();
      });
      MAX8(sc[6], 0); MAX8(sc[6], 8); MAX8(sc[7], 0); MAX8(sc[7], 8);
      mx = max32(max3(mx, mx2, mx2));
      f32x16 nm;
#pragma unroll
      for (int i = 0; i < 16; ++i) nm[i] = -mx;
      PINV(nm);
      bar();
      f32x16 ao;
      v4u vf[9][2], pp[9][2];
      float ex[32][4];
#define VF(j, s) vf[j][s] = trfrag(vr0l + (j) * 4096 + (s) * 256, vr1 + (j) * 4096 + (s) * 256)
      sfor<33>([&](auto N) INL {
        constexpr int n = N, j = n >> 2, q = n & 3;
        if constexpr (j == 0) {
          if constexpr (q == 0) MFMA32VC(sc[1], kf[1][0], qf[0], nm);
          if constexpr (q == 1) MFMA32VA(sc[1], kf[1][1], qf[1]);
          if constexpr (q == 2) MFMA32VC(sc[2], kf[2][0], qf[0], nm);
          if constexpr (q == 3) MFMA32VA(sc[2], kf[2][1], qf[1]);
        } else if constexpr (j < 8) {
          if constexpr (q == 1) { if constexpr (j == 1) MFMA32V0(ao, vf[0][0], pp[0][0]); else MFMA32VA(ao, vf[j - 1][0], pp[j - 1][0]); }
          if constexpr (q == 2) MFMA32VA(ao, vf[j - 1][1], pp[j - 1][1]);
          if constexpr (j + 2 < 8) {
            if constexpr (q == 0) MFMA32VC(sc[j + 2], kf[j + 2][0], qf[0], nm);
            if constexpr (q == 3) MFMA32VA(sc[j + 2], kf[j + 2][1], qf[1]);
          } else if constexpr (j == 6) {
            if constexpr (q == 0) MFMA32VA(ao, vf[8][0], pp[8][0]);
            if constexpr (q == 3) MFMA32VA(ao, vf[8][1], pp[8][1]);
          }
        } else MFMA32VA(ao, vf[7][0], pp[7][0]);
        SCHED();
        if constexpr (n < 32) {
#pragma unroll
          for (int i = 0; i < 4; ++i) { if constexpr (j == 0) ex[n][i] = __builtin_amdgcn_exp2f(se[4 * q + i] - mx); else ex[n][i] = __builtin_amdgcn_exp2f(sc[j][4 * q + i]); }
        }
        SCHED();
        if constexpr (n > 0) {
          constexpr int jp = (n - 1) >> 2, qp = (n - 1) & 3;
#pragma unroll
          for (int i = 0; i < 2; ++i) {
            const unsigned u = pk(ex[n - 1][2 * i], ex[n - 1][2 * i + 1]);
            if constexpr (jp == 0) { pp[0][qp >> 1][(2 * qp + i) & 3] = u & pml[2 * qp + i]; pp[8][qp >> 1][(2 * qp + i) & 3] = u & ~pml[2 * qp + i]; }
            else pp[jp][qp >> 1][(2 * qp + i) & 3] = u;
          }
        }
        if constexpr (layer == 0) {
          if constexpr (n >= 2 && n < 10 && q >= 2) rr[(n >> 2) * 2 + q - 2] = ld8<0>(mk(p.x, on35 ? S * D * 2 : 0), rro, ((n >> 2) * 2 + q - 2) * 32);
          if constexpr (n == 10) b2r = (unsigned)__builtin_amdgcn_raw_buffer_load_b16(mk(p.b2, S * 2), b2o, 0, 0);
          if constexpr (n >= 14 && n < 22 && q >= 2) bb[((n >> 2) - 3) * 2 + q - 2] = ld8<0>(mk(p.b1, F * 2), b1o, (((n >> 2) - 3) * 2 + q - 2) * 32);
        }
        if constexpr (n == 22) KEEP(nm);
        if constexpr (j < 8 && q < 2) VF(j, q);
        if constexpr (j == 5 && q >= 2) VF(8, q - 2);
        SCHED();
      });
      asm volatile("s_nop 1" : "+v"(pp[7][1]));
      MFMA32VA(ao, vf[7][1], pp[7][1]);
      MFMA_SETTLE(ao);
      const float inv = __builtin_amdgcn_rcpf(hi32(ao[12]));
      v4u of[2];
#pragma unroll
      for (int i = 0; i < 16; i += 2) of[i >> 3][(i >> 1) & 3] = pk(ao[i] * inv, ao[i + 1] * inv);
      if (hi) { of[1][2] = 0u; of[1][3] = 0u; }
      st16<SC1>(of[0], rws, ool, 0);
      st16<SC1>(of[1], rws, ool, 512);
      SCHED();
    }
    if (on35) {
      {
        FRESH();
        const int tq = st >> 1;
        GAS unsigned* fp = fl + (1 * 256 + (((tq & 1) * 16 + 4 * w + (lane & 3)) * 8 + (tq >> 1))) * 32;
        int oo = O_OFF + layer * O_SZ + (st * 16 + 4 * w) * 4096 + lane * 16;
        const int sb = S3_OFF + layer * S3_SZ + st * 8192 + (16 * w + c) * 16;
        int sw = oob(g < 2, sb + dt * 1024 + g * 8), sa = sb + g * 1024, sc = oob(g < 3, sb + (g + 4) * 1024);
        int ho = HT_OFF + layer * HT_SZ + (dt * 64 + 2 * st + (w >> 1)) * 4096 + (g >> 1) * 1024 + (2 * (w & 1) + (g & 1)) * 256 + c * 16;
        const int hr = 8 * (g & 1) + q4, hst = L_HST + w * 2048;
        int h0 = hst + (raddr(hr) ^ ((g >> 1) << 5)), h1 = hst + (raddr(hr + 4) ^ ((g >> 1) << 5)), hw = hst + c * 128 + (swz(c) << 5) + g * 8;
        int ob[4];
#pragma unroll
        for (int b = 0; b < 4; ++b) { ob[b] = ((b + w) & 3) * 1024; PINS(ob[b]); }
        PINV(fp); PINV(oo); PINV(sw); PINV(sa); PINV(sc); PINV(ho); PINV(h0); PINV(h1); PINV(hw); PINV(red);
        publish(fl + (1 * 256 + bid) * 32, epoch);
        unsigned pf;
        DPUMP(Y0, Y0, Y2)
        if constexpr (layer == 0) {
#pragma unroll
          for (int a = 0; a < 4; ++a) { res[a] = unpack4(rr[a]); PINV(res[a]); }
        }
        POLL(pf, fp);
        DPUMP(Y0, Y2, YG)
        unsigned late;
        POLLED(pf, 4 * (YG - Y2), fp)
        v4u of[16];
#define LOAD_O(I) of[I] = ld16<SC1>(rws, oo + ((I) & 3) * 4096, ob[(((I) >> 2) + 1) & 3])
#pragma unroll
        for (int i = 0; i < WINO; ++i) { LOAD_O(i); SCHED(); }
        vmw<WINO>();
        KEEP(late);
        f32x4 acc[4][4], part[12];
        sfor<16>([&](auto I) INL {
          constexpr int i = I, b = ((i >> 2) + 1) & 3, k = i & 3;
#pragma unroll
          for (int a = 0; a < 4; ++a) {
            acc[a][b] = mfma16(wo[k][a], of[i], k > 0 ? acc[a][b] : (f32x4){0.f, 0.f, 0.f, 0.f});
            SCHED();
            constexpr int n0 = issued(i, 16, WINO, WO1), n1 = issued(i + 1, 16, WINO, WO1);
            if constexpr (n0 < n1) { if (a == 0) LOAD_O(n0); }
            if constexpr (n0 + 1 < n1) { if (a == 3) LOAD_O(n0 + 1); }
            if constexpr (i < 12) { if (a == 1) DTAIL1(Y0, YG, i) }
            if constexpr (i >= 12 && k < 2) { if (a == 1 || a == 3) REDW(2 * k + (a >> 1), 3); }
            if constexpr (i >= 4 && i < 12) { if (a == 2) REDW(k, i >> 2); }
            if constexpr (i == 14) { if (a == 0) bar(); }
            if constexpr (i >= 14) { if (a < 3) { const int j = 6 * (i - 14) + 2 * a; part[j] = REDR(j / 3, j % 3 + 1); part[j + 1] = REDR((j + 1) / 3, (j + 1) % 3 + 1); } }
            SCHED();
          }
        });
#pragma unroll
        for (int a = 0; a < 4; ++a) res[a] = ((res[a] + part[3 * a]) + (part[3 * a + 1] + part[3 * a + 2])) + acc[a][0];
        f32x2 s1 = {0.f, 0.f}, s2 = {0.f, 0.f};
        stats16(res, 15, s1, s2);
        st8<SC1>((v2u){asu(sum_ab(s1[0] + s1[1], s2[0] + s2[1])), epoch}, rws, sw, 0);
        SCHED();
        DTAIL1(Y0, YG, 12) DTAIL1(Y0, YG, 13) DTAIL1(Y0, YG, 14) DTAIL1(Y0, YG, 15)
        bar();
        DPUMP(YG, YG, YG2)
        v4u ga, gb;
        asm volatile("buffer_load_dwordx4 %0, %2, %4, 0 offen sc1\n\tbuffer_load_dwordx4 %1, %3, %4, 0 offen sc1" : "=&v"(ga), "=&v"(gb) : "v"(sa), "v"(sc), "s"(rws) : "memory");
        DPUMP(YG, YG2, YH)
        STASH(0, SG)
        asm volatile("s_waitcnt vmcnt(%2)" : "+v"(ga), "+v"(gb) : "n"(4 * (YH - YG2 + layer * SG)) : "memory");
        while ((__builtin_amdgcn_ballot_w64((ga.y != epoch) | (ga.w != epoch)) | (__builtin_amdgcn_ballot_w64((gb.y != epoch) | (gb.w != epoch)) << 16)) != 0ull) {
          if (budget == 0u) break;
          --budget;
          asm volatile("buffer_load_dwordx4 %0, %2, %4, 0 offen sc1\n\tbuffer_load_dwordx4 %1, %3, %4, 0 offen sc1\n\ts_waitcnt vmcnt(0)"
                       : "=&v"(ga), "=&v"(gb) : "v"(sa), "v"(sc), "s"(rws) : "memory");
        }
        const unsigned t12 = asu(sum_ab(asf(ga.x) + asf(gb.x), asf(ga.z) + asf(gb.z)));
        const auto tt = __builtin_amdgcn_permlane16_swap(t12, t12, false, false);
        const float t1 = asf(tt[0]), t2 = asf(tt[1]);
        const float m = t1 * (1.f / D), var = fmaxf(t2 * (1.f / D) - m * m, 0.f);
        const float r1 = __builtin_amdgcn_rsqf(var + EPS);
        const float r = r1 * __builtin_amdgcn_rsqf(var * r1 * r1 + EPS), nr = -m * r;
#pragma unroll
        for (int a = 0; a < 4; ++a) {
          const v2u v = {pk(res[a][0] * r + nr, res[a][1] * r + nr), pk(res[a][2] * r + nr, res[a][3] * r + nr)};
          *(LAS v2u*)(L + (hw ^ (a << 5))) = v;
        }
#pragma unroll
        for (int e = 0; e < 2; ++e) st16<SC1>(trfrag(L + (h0 ^ (e << 6)), L + (h1 ^ (e << 6))), rws, ho, e * 2048);
        SCHED();
      }
      if (on4) {
        FRESH();
        const int sp = 8 * w + (lane & 7);
        GAS unsigned* fp = fl + (2 * 256 + ((sp & 3) * 7 + dt4) * 8 + (sp >> 2)) * 32;
        int hv[4];
#pragma unroll
        for (int b = 0; b < 4; ++b) { hv[b] = HT_OFF + layer * HT_SZ + (dt4 * 64 + 16 * w) * 4096 + ((b + w) & 3) * 1024 + lane * 16; PINV(hv[b]); }
        int uo = U_OFF + layer * U_SZ + (dt4 * 56 + 2 * ft) * 4096 + w * 1024 + lane * 16;
        PINV(fp); PINV(uo); PINV(red);
        publish(fl + (2 * 256 + bid) * 32, epoch);
        unsigned pf;
        DPUMP(YG, YH, YH2)
        STASH(SG, NS - SH)
        POLL(pf, fp);
        DPUMP(YG, YH2, YE)
        STASH(NS - SH, NS)
        unsigned late;
        POLLED(pf, 4 * (YE - YH2 + layer * SH), fp)
        v4u af[64];
#define LOAD_H(I) { int so_; asm volatile("s_mov_b32 %1, %4\n\tbuffer_load_dwordx4 %0, %2, %3, %1 offen sc1" \
          : "=&v"(af[I]), "=&s"(so_) : "v"(hv[(((I) >> 4) + 1) & 3]), "s"(rws), "n"(((I) & 15) * 4096) : "memory"); }
        sfor<W0>([&](auto I) INL { LOAD_H(I) SCHED(); });
        f32x4 acc[4][4], part[12];
        sfor<64>([&](auto I) INL {
          constexpr int i = I, b = ((i >> 4) + 1) & 3, k = i & 15;
          constexpr int n0 = issued(i, 64, W0, WIN), n1 = issued(i + 1, 64, W0, WIN);
          constexpr int np = i <= 54 ? 0 : i <= 62 ? i - 54 : 10;
          asm volatile("s_waitcnt vmcnt(%1)" : "+v"(af[i]) : "n"(n0 - 1 - i + np) : "memory");
          if constexpr (i == 0) KEEP(late);
          SCHED();
          sfor<4>([&](auto A) INL {
            constexpr int a = A;
            acc[a][b] = mfma16(wz[k][a], af[i], k > 0 ? acc[a][b] : (f32x4){0.f, 0.f, 0.f, 0.f});
            SCHED();
            if constexpr (n0 < n1 && a == 0) LOAD_H(n0)
            if constexpr (n0 + 1 < n1 && a == 3) LOAD_H(n0 + 1)
            if constexpr (i < 8) { if constexpr (a == 1) DTAIL1(YG, YE, 2 * i) if constexpr (a == 2) DTAIL1(YG, YE, 2 * i + 1) }
            if constexpr (i >= 48 && k < 2 && (a & 1)) REDW(2 * k + (a >> 1), 3);
            if constexpr (i >= 16 && i < 48 && (k & 3) == 1 && a == 2) REDW(k >> 2, i >> 4);
            if constexpr (i == 50 && a == 3) bar();
            if constexpr (i >= 51 && i < 54) part[4 * (i - 51) + a] = REDR((4 * (i - 51) + a) / 3, (4 * (i - 51) + a) % 3 + 1);
            if constexpr (i == 54 && a == 1) bar();
            if constexpr (i >= 54 && a == 2) PIECE(Z_U, np)
            if constexpr (i >= 62 && a == 3) PIECE(Z_U, np + 1)
            if constexpr (i >= 55 && i < 59 && a == 0) {
              constexpr int q = i - 55;
              part[3 * q] = (part[3 * q] + unpack4(bb[q])) + (part[3 * q + 1] + part[3 * q + 2]);
              PINV(part[3 * q]);
            }
            SCHED();
          });
        });
        v4u uf[2];
        sfor<8>([&](auto H) INL {
          constexpr int h = H, a = h >> 1, j0 = 2 * (h & 1);
          float gl[2];
#pragma unroll
          for (int j = 0; j < 2; ++j) {
            const float u = acc[a][0][j0 + j] + part[3 * a][j0 + j];
            const float e = __builtin_amdgcn_exp2f(u * (u * u * (-0.10294324f) - 2.3022082f));
            gl[j] = u * __builtin_amdgcn_rcpf(1.f + e);
          }
          uf[h >> 2][h & 3] = pk(gl[0], gl[1]);
          if constexpr (h & 1) {
            PINV(uf[h >> 2]);
            if constexpr (h == 3) st16<SC1>(uf[0], rws, uo, 0);
            if constexpr (h == 7) st16<SC1>(uf[1], rws, uo, 4096);
            SCHED();
          }
        });
      } else {
        publish(fl + (2 * 256 + blockIdx.x) * 32, epoch);
        DPUMP(YG, YH, YE)
        STASH(SG, NS)
        DRAIN();
        DTAIL(YG, YE)
        LGKM0();
        DPUMP(Z_U, Z_U, Z_U + 3)
      }
      {
        FRESH();
        const int pi = wrap7(lane & 7);
        GAS unsigned* fp = fl + (3 * 256 + (pi == 3 ? (21 + (dt & 3)) * 8 + (dt >> 2) : (7 * (pi & 3) + dt) * 8 + (pi >> 2)) + 2 * w) * 32;
        int uv = U_OFF + layer * U_SZ + (dt * 56 + 14 * w) * 4096 + lane * 16;
        int ow = L_HST + w * 2048 + c * 128 + (swz(c) << 5) + g * 8, orr = L_HST + w * 2048 + ((wrow * 128 + (lane & 7) * 16) ^ sx0);
        int xb = layer ? (64 * st + 16 * w + wrow) * 896 + dt * 128 + (lane & 7) * 16
                       : X2_OFF + (2 * st + (w >> 1)) * 28672 + (4 * dt + (g >> 1)) * 1024 + ((g & 1) * 32 + (w & 1) * 16 + c) * 16;
        int so = oob(g < 2, S5_OFF + (st * 8 + dt) * 512 + (16 * w + c) * 8 + g * 4);
#pragma unroll
        for (int a = 0; a < 4; ++a) { res[a] += bflo(b2r); PINV(res[a]); }
        PINV(fp); PINV(uv); PINV(xb); PINV(red);
        if constexpr (layer) { PINV(ow); PINV(orr); } else PINV(so);
        if constexpr (layer == 1) {
          const int dlt = L_WQ + w * 20480 - (L_KVW + w * 16384);
#pragma unroll
          for (int i = 0; i < 4; ++i) {
            LAS char* ps = pr2[i] + dlt;
#pragma unroll
            for (int j = 0; j < NS; ++j) wz[30 - NS + j][i] = trfrag(ps + j * 4096, ps + j * 4096 + 2048);
          }
          SCHED();
        }
        if (on4) publish(fl + (3 * 256 + bid) * 32, epoch);
        unsigned pf;
        DPUMP(Z_U, Z_U + 3, YU3)
        POLL(pf, fp);
        DPUMP(Z_U, YU3, YZ)
        unsigned late;
        POLLED(pf, 4 * (YZ - YU3), fp)
        v4u af[56];
#define LOAD_U(I) af[I] = ld16<SC1>(rws, uv + ((I) / 14) * 1024, ((I) % 14) * 4096)
#pragma unroll
        for (int i = 0; i < W0; ++i) { LOAD_U(i); SCHED(); }
        vmw<W0>();
        KEEP(late);
        f32x4 acc[4][4], part[4][3];
        v2u xo[4];
        f32x2 s1 = {0.f, 0.f}, s2 = {0.f, 0.f};
        sfor<56>([&](auto I) INL {
          constexpr int i = I, a = i / 14, k = i % 14;
#pragma unroll
          for (int bq = 0; bq < 4; ++bq) {
            const int b = (bq + 1) & 3;
            if (a < 3 || b) acc[a][b] = mfma16(af[i], wz[16 + k][b], k > 0 ? acc[a][b] : (f32x4){0.f, 0.f, 0.f, 0.f});
            SCHED();
            constexpr int n0 = issued(i, 56, W0, WIN), n1 = issued(i + 1, 56, W0, WIN);
            if constexpr (n0 < n1) { if (bq == 0) LOAD_U(n0); }
            if constexpr (n0 + 1 < n1) { if (bq == 3) LOAD_U(n0 + 1); }
            if constexpr (i < 8) { if (bq == 1) DTAIL1(Z_U, YZ, 2 * i) if (bq == 2) DTAIL1(Z_U, YZ, 2 * i + 1) }
            if constexpr (a > 0 && k < 3) { if (bq == 2) REDW(a - 1, k + 1); }
            if constexpr (i == 46) { if (bq == 2) bar(); }
            if constexpr (i >= 47 && i < 50) { if (bq < 3) part[i - 47][bq] = REDR(i - 47, bq + 1); }
            if constexpr (i >= 50 && i < 53) { if (bq == 2) { res[i - 50] = ((res[i - 50] + part[i - 50][0]) + (part[i - 50][1] + part[i - 50][2])) + acc[i - 50][0]; PINV(res[i - 50]); } }
            if constexpr (i >= 53) { if (bq == 2) { xo[i - 53] = (v2u){pk(res[i - 53][0], res[i - 53][1]), pk(res[i - 53][2], res[i - 53][3])}; PINV(xo[i - 53]); if constexpr (layer == 0) { stats16(res, 1 << (i - 53), s1, s2); PINV(s1); PINV(s2); } } }
            SCHED();
          }
        });
        sfor<14>([&](auto K) INL {
          constexpr int k = K;
          acc[3][0] = mfma16(af[42 + k], wz[16 + k][0], k > 0 ? acc[3][0] : (f32x4){0.f, 0.f, 0.f, 0.f});
          SCHED();
          if constexpr (k >= 1 && k < 4) REDW(3, k);
          if constexpr (k == 5) {
            if constexpr (layer == 0) st16<SC1>((v4u){xo[0].x, xo[0].y, xo[1].x, xo[1].y}, rws, xb, 0);
            else { _Pragma("unroll") for (int a = 0; a < 3; ++a) *(LAS v2u*)(L + (ow ^ (a << 5))) = xo[a]; }
          }
          if constexpr (k == 7) { bar(); part[3][0] = REDR(3, 1); part[3][1] = REDR(3, 2); part[3][2] = REDR(3, 3); }
          SCHED();
        });
        res[3] = ((res[3] + part[3][0]) + (part[3][1] + part[3][2])) + acc[3][0];
        xo[3] = (v2u){pk(res[3][0], res[3][1]), pk(res[3][2], res[3][3])};
        if constexpr (layer == 0) {
          st16<SC1>((v4u){xo[2].x, xo[2].y, xo[3].x, xo[3].y}, rws, xb, 2048);
          SCHED();
          stats16(res, 8, s1, s2);
          __builtin_amdgcn_raw_buffer_store_b32(asu(sum_ab(s1[0] + s1[1], s2[0] + s2[1])), rws, so, 0, SC1);
          SCHED();
        } else {
          const rsrc_t ro = mk(p.out, S * D * 2);
          *(LAS v2u*)(L + (ow ^ (3 << 5))) = xo[3];
          const v4u r0 = *(LAS v4u*)(L + orr), r1 = *(LAS v4u*)(L + (orr ^ 64) + 1024);
          st16<SC1>(r0, ro, xb, 0);
          st16<SC1>(r1, ro, xb, 8 * 896);
        }
      }
    } else {
      publish(fl + (1 * 256 + blockIdx.x) * 32, epoch);
      FRESH();
      const int rw = 4 * (by - 28) + (lane >> 4) + 16 * ((lane >> 2) & 3);
      const int v1 = (512 * w + 256 + rw) * (F * 2) + (7 * (bx >> 1) + 3 * (bx & 1) + (lane & 3)) * 128;
#pragma unroll 1
      for (int i = layer * (KP / 2); i < 4; ++i) { touch(mk(p.w1, S * F * 2), v1, i * 64 * F * 2, L); vmw<1>(); }
      const int v2 = (448 * w + rw) * (S * 2) + bx * 512 + (lane & 3) * 128;
#pragma unroll 1
      for (int i = 0; i < (layer ? 7 - (Z_U - 16) / 2 : 7); ++i) { touch(mk(p.w2, S * F * 2), v2, (layer ? (i < 3 ? i + 4 : i - 3 + (Z_U - 16) / 2) : i) * 64 * S * 2, L); vmw<1>(); }
      DRAIN();
    }
    if constexpr (layer == 0) {
      FRESH();
      const int st1 = 2 * t + (w >> 1);
      f32x16 ak, av;
      v4u wk[28], wv[28];
      LAS char* wb = WB(0) + 28672;
      PINV(wb);
      wk[0] = WFRAG(wb, 0, 0); wv[0] = WFRAG(wb, 1, 0); wk[1] = WFRAG(wb, 0, 1); wv[1] = WFRAG(wb, 1, 1);
      f32x4 ck[4], cv[4];
#pragma unroll
      for (int a = 0; a < 4; ++a) { ck[a] = CSUM(1, a); cv[a] = CSUM(2, a); PINV(ck[a]); PINV(cv[a]); }
      int xo = X2_OFF + (4 * t + w) * 28672 + lane * 16, so = S5_OFF + (st1 * 8 + hh * 4) * 512 + ((w & 1) * 32 + r32) * 8;
      int so3 = oob(!hh, so + 3 * 512);
      PINV(xo); PINV(so); PINV(so3);
      kvs += KV_SZ; oo += O_SZ; fo += 5 * 256 * 128;
      PINV(kvs); PINA(oo); PINA(fo);
      GAS unsigned* fp = flags + (4 * 256 + ((st1 & 3) * 7 + wrap7(lane & 7)) * 8 + (st1 >> 2)) * 32;
      PINV(fp);
      if (on35) publish(flags + (4 * 256 + bid) * 32, epoch);
      unsigned pf, late;
      {
        const rsrc_t rwo = mk(p.wo, on35 ? D * D * 2 : 0);
        sfor<16>([&](auto I) INL {
          constexpr int wi = I;
          if constexpr (wi == 10) POLL(pf, fp);
          dmai<wi * 1024, (28 * (wi >> 2) + 8 * (wi & 3)) * 896>(rwo, (wi & 1) ? wvo2 ^ 64 : wvo2, pring);
        });
      }
      POLLED(pf, 6, fp)
      v2u sp[4];
#define LOAD_X(I) xf[I] = ld16<SC1>(rws, xo + ((I) & 3) * 1024, ((I) >> 2) * 4096)
#pragma unroll
      for (int i = 0; i < WX0; ++i) { LOAD_X(i); SCHED(); }
      sfor<28>([&](auto I) INL {
        constexpr int ks = I;
        if constexpr (ks == 0) MFMA32V0(ak, wk[ks], xf[ks]); else MFMA32VA(ak, wk[ks], xf[ks]);
        if constexpr (ks + 2 < 28) wk[ks + 2] = WFRAG(wb + ((ks + 2) >> 2) * 4096, 0, (ks + 2) & 3);
        constexpr int n0 = issued(ks, 28, WX0, WINX), n1 = issued(ks + 1, 28, WX0, WINX);
        if constexpr (n0 < n1) LOAD_X(n0);
        SCHED();
        if constexpr (ks == 0) MFMA32V0(av, wv[ks], xf[ks]); else MFMA32VA(av, wv[ks], xf[ks]);
        if constexpr (ks + 2 < 28) wv[ks + 2] = WFRAG(wb + ((ks + 2) >> 2) * 4096, 1, (ks + 2) & 3);
        if constexpr (n0 + 1 < n1) LOAD_X(n0 + 1);
        if constexpr (ks >= 6 && ks < 10) sp[ks - 6] = ld8<SC1>(rws, ks == 9 ? so3 : so + (ks - 6) * 512, 0);
        if constexpr (ks == 2) KEEP(late);
        if constexpr (ks == 21) {
          float s1 = (asf(sp[0].x) + asf(sp[1].x)) + (asf(sp[2].x) + asf(sp[3].x));
          float s2 = (asf(sp[0].y) + asf(sp[1].y)) + (asf(sp[2].y) + asf(sp[3].y));
          s1 = sum32(s1); s2 = sum32(s2);
          mean = s1 * (1.f / D);
          rstd = s2 * (1.f / D) - mean * mean;
          PINV(mean); PINV(rstd);
        }
        if constexpr (ks == 22) { rstd = __builtin_amdgcn_rsqf(fmaxf(rstd, 0.f) + EPS); PINV(rstd); }
        if constexpr (ks >= 23 && ks < 27) { ck[ks - 23] *= -mean * rstd; cv[ks - 23] *= -mean * rstd; PINV(ck[ks - 23]); PINV(cv[ks - 23]); }
        SCHED();
      });
      MFMA_SETTLE2(ak, av);
#pragma unroll
      for (int i = 0; i < 16; ++i) ak[i] = ak[i] * rstd + ck[i >> 2][i & 3];
      KVOUT(ak, 0)
#pragma unroll
      for (int i = 0; i < 16; ++i) av[i] = av[i] * rstd + cv[i >> 2][i & 3];
      KVOUT(av, 1)
    }
  });
  asm volatile(".Lfk_code_to:");
  if (blockIdx.x == 0 && threadIdx.x == 0) __hip_atomic_store(ctrl, epoch, RLX_AGENT);
}

extern "C" void kernel_launch(void* const* d_in, const int* in_sizes, int n_in, void* d_out, int out_size, void* d_ws,
                              size_t ws_size, hipStream_t stream) {
  Params p;
  p.x = d_in[0]; p.wqkv = d_in[1]; p.wo = d_in[2];
  p.w1 = d_in[4]; p.b1 = d_in[5]; p.w2 = d_in[6]; p.b2 = d_in[7];
  p.out = d_out; p.ws = d_ws;
  (void)hipFuncSetAttribute((const void*)fused_kernel, hipFuncAttributeMaxDynamicSharedMemorySize, LDS_BYTES);
  fused_kernel<<<256, 256, LDS_BYTES, stream>>>(p);
}
```

```cpp
#include <hip/hip_runtime.h>
#include <utility>

#define GAS __attribute__((address_space(1)))
#define LAS __attribute__((address_space(3)))
#define DI __device__ __forceinline__
#define INL __attribute__((always_inline))
#define RLX_AGENT __ATOMIC_RELAXED, __HIP_MEMORY_SCOPE_AGENT
typedef unsigned v4u __attribute__((ext_vector_type(4)));
typedef unsigned v2u __attribute__((ext_vector_type(2)));
typedef float f32x2 __attribute__((ext_vector_type(2)));
typedef float f32x4 __attribute__((ext_vector_type(4)));
typedef float f32x16 __attribute__((ext_vector_type(16)));
typedef short s16x4 __attribute__((ext_vector_type(4)));
typedef __bf16 bf16x2 __attribute__((ext_vector_type(2)));
typedef __bf16 bf16x8 __attribute__((ext_vector_type(8)));
typedef __amdgpu_buffer_rsrc_t rsrc_t;

constexpr int S = 2048, D = 448, F = 1792;
constexpr float EPS = 1e-5f;
constexpr int FLAG_OFF = 4096;
constexpr int KV_OFF = 512 * 1024, KV_SZ = 16 * 64 * 4096;
constexpr int O_OFF = KV_OFF + 2 * KV_SZ, O_SZ = 32 * 16 * 4096;
constexpr int S3_OFF = O_OFF + 2 * O_SZ, S3_SZ = 32 * 8 * 1024;
constexpr int HT_OFF = S3_OFF + 2 * S3_SZ, HT_SZ = 7 * 64 * 4096;
constexpr int U_OFF = HT_OFF + 2 * HT_SZ, U_SZ = 7 * 56 * 4096;
constexpr int X2_OFF = U_OFF + 2 * U_SZ, X2_SZ = 64 * 28 * 1024;
constexpr int S5_OFF = X2_OFF + X2_SZ, S5_SZ = 32 * 8 * 512;
constexpr int DBG_OFF = S5_OFF + S5_SZ;
constexpr int WS_BYTES = DBG_OFF + 256 * 128 * 8;
constexpr int L_WQ = 0;
constexpr int L_KVW = 86016;
constexpr int L_CSP = L_KVW + 65536;
constexpr int L_CS = L_CSP + 1536 + 128;
constexpr int L_HST = L_CS + 384;
constexpr int L_DUMP = L_HST + 8192;
constexpr int LDS_BYTES = L_DUMP + 256;

static_assert(L_HST % 128 == 0 && LDS_BYTES <= 163840, "LDS map");
constexpr int SC1 = 16, OOB = 0x7ffff000;
#define VT_LAYOUT 1
constexpr int WIN = 12;
constexpr int NO2 = 4, NO3 = 2, NG2 = 3, NG3 = 2, NH2 = 4, NH3 = 3, NU3 = 2;
constexpr int KP = 5, MO2 = 4, MO3 = 1, MG2 = 3, MG3 = 0, NS = 5, SG = 1, SH = 2;
constexpr int W0 = 10, WINO = 9, WO1 = 9, WX0 = 9, WINX = 10;
constexpr int issued(int i, int n, int w0, int w) { const int c = w0 + i + (i < w - w0 ? i : w - w0); return c < n ? c : n; }
constexpr int Z_U = NO2 + NO3 + NG2 + NG3 + NH2 + NH3;
static_assert(Z_U >= 16 && Z_U + 3 + NU3 <= 30 && Z_U + 3 <= 30 - NS && NS <= 5 && SG + SH <= NS, "schedule");

struct Params {
  const void *x, *wqkv, *wo, *w1, *b1, *w2, *b2;
  void *out, *ws;
};

template <int... I, class Fn> DI void sfor_(std::integer_sequence<int, I...>, Fn&& f) { (f(std::integral_constant<int, I>{}), ...); }
template <int N, class Fn> DI void sfor(Fn&& f) { sfor_(std::make_integer_sequence<int, N>{}, f); }
DI float asf(unsigned u) { return __builtin_bit_cast(float, u); }
DI unsigned asu(float f) { return __builtin_bit_cast(unsigned, f); }
DI rsrc_t mk(const void* p, int bytes) { return __builtin_amdgcn_make_buffer_rsrc((void*)p, 0, bytes, 0x00020000); }
template <int AUX> DI v4u ld16(rsrc_t r, int voff, int soff) { return __builtin_amdgcn_raw_buffer_load_b128(r, voff, soff, AUX); }
template <int AUX> DI v2u ld8(rsrc_t r, int voff, int soff) { return __builtin_amdgcn_raw_buffer_load_b64(r, voff, soff, AUX); }
template <int AUX> DI void st16(v4u v, rsrc_t r, int voff, int soff) { __builtin_amdgcn_raw_buffer_store_b128(v, r, voff, soff, AUX); }
template <int AUX> DI void st8(v2u v, rsrc_t r, int voff, int soff) { __builtin_amdgcn_raw_buffer_store_b64(v, r, voff, soff, AUX); }
DI f32x4 mfma16(v4u a, v4u b, f32x4 c) {
  return __builtin_amdgcn_mfma_f32_16x16x32_bf16(__builtin_bit_cast(bf16x8, a), __builtin_bit_cast(bf16x8, b), c, 0, 0, 0);
}
DI f32x16 mfma32(v4u a, v4u b, f32x16 c) {
  return __builtin_amdgcn_mfma_f32_32x32x16_bf16(__builtin_bit_cast(bf16x8, a), __builtin_bit_cast(bf16x8, b), c, 0, 0, 0);
}
DI unsigned pk(float lo, float hi) { return __builtin_bit_cast(unsigned, __builtin_convertvector((f32x2){lo, hi}, bf16x2)); }
DI float bflo(unsigned u) { return asf(u << 16); }
DI float bfhi(unsigned u) { return asf(u & 0xffff0000u); }
DI f32x4 unpack4(v2u v) { return (f32x4){bflo(v.x), bfhi(v.x), bflo(v.y), bfhi(v.y)}; }
DI v2u trrd(LAS char* p) { return __builtin_bit_cast(v2u, __builtin_amdgcn_ds_read_tr16_b64_v4i16((LAS s16x4*)p)); }
DI v4u trfrag(LAS char* p0, LAS char* p1) { v2u a = trrd(p0), b = trrd(p1); return (v4u){a.x, a.y, b.x, b.y}; }
DI float hi32(float v) {
  unsigned u = asu(v);
  auto r = __builtin_amdgcn_permlane32_swap(u, u, false, false);
  return asf(r[1]);
}
DI float sum32(float v) {
  unsigned u = asu(v);
  auto r = __builtin_amdgcn_permlane32_swap(u, u, false, false);
  return asf(r[0]) + asf(r[1]);
}
DI float max32(float v) {
  unsigned u = asu(v);
  auto r = __builtin_amdgcn_permlane32_swap(u, u, false, false);
  return fmaxf(asf(r[0]), asf(r[1]));
}
DI float sum_ab(float a, float b) {
  auto r = __builtin_amdgcn_permlane16_swap(asu(a), asu(b), false, false);
  return sum32(asf(r[0]) + asf(r[1]));
}
DI void stats16(const f32x4 (&v)[4], int n, f32x2& s1, f32x2& s2) {
#pragma unroll
  for (int a = 0; a < 4; ++a)
    if (n >> a & 1) {
      const f32x2 lo = {v[a][0], v[a][1]}, hi = {v[a][2], v[a][3]};
      s1 += lo; s2 += lo * lo; s1 += hi; s2 += hi * hi;
    }
}
#define FENCE() asm volatile("" ::: "memory")
#define SCHED() __builtin_amdgcn_sched_barrier(0)
#define DRAIN() asm volatile("s_waitcnt vmcnt(0)" ::: "memory")
#define PINV(x) asm volatile("" : "+v"(x))
#define PINA(x) asm volatile("" : "+a"(x))
#define PINS(x) asm volatile("" : "+s"(x))
#define KEEP(x) asm volatile("" :: "v"(x))
#define LGKM0() asm volatile("s_waitcnt lgkmcnt(0)" ::: "memory")
DI void bar() { LGKM0(); __builtin_amdgcn_s_barrier(); FENCE(); }
template <int N> DI void vmw() { asm volatile("s_waitcnt vmcnt(%0)" :: "n"(N) : "memory"); }
template <int LO, int MO> DI void dmai(rsrc_t r, int voff, int lds) {
  int so;
  asm volatile("s_add_u32 m0, %1, %4\n\ts_mov_b32 %0, %5\n\tbuffer_load_dwordx4 %2, %3, %0 offen lds"
               : "=&s"(so) : "s"(lds), "v"(voff), "s"(r), "n"(LO), "n"(MO) : "memory", "scc");
}
DI void touch(rsrc_t r, int voff, int soff, LAS char* L) {
  asm volatile("s_mov_b32 m0, %0\n\ts_nop 3\n\tbuffer_load_dword %1, %2, %3 offen lds"
               :: "s"((int)(size_t)(L + L_DUMP)), "v"(voff), "s"(r), "s"(soff) : "memory");
}
#define STAMP(i) do {} while (0)

DI void publish(GAS unsigned* flag, unsigned epoch) {
  DRAIN();
  __builtin_amdgcn_s_waitcnt(0x0F70);
  bar();
  if (threadIdx.x == 0) __hip_atomic_store(flag, epoch, RLX_AGENT);
  FENCE();
}
#define POLL(f, ptr) asm volatile("global_load_dword %0, %1, off sc1" : "=&v"(f) : "v"(ptr) : "memory")
#define POLLED(f, N, ptr) { \
    unsigned f2_; \
    POLL(f2_, ptr); \
    asm volatile("s_waitcnt vmcnt(%1)" : "+v"(f) : "n"((N) + 1) : "memory"); \
    if (__builtin_amdgcn_ballot_w64(f != epoch) != 0ull) { \
      asm volatile("s_waitcnt vmcnt(0)" : "+v"(f2_) :: "memory"); \
      if (__builtin_amdgcn_ballot_w64(f2_ != epoch) != 0ull) wait_flags(ptr, epoch, budget); \
    } \
    FENCE(); \
    late = f2_; }
DI void wait_flags(GAS unsigned* flag, unsigned epoch, unsigned& budget) {
  FENCE();
  while (true) {
    unsigned f = __hip_atomic_load(flag, RLX_AGENT);
    if (__builtin_amdgcn_ballot_w64(f != epoch) == 0ull) break;
    if (budget == 0u) break;
    --budget;
  }
  __builtin_amdgcn_fence(__ATOMIC_ACQUIRE, "wavefront");
  FENCE();
}

DI int oob(bool in, int off) { return off | (in ? 0 : OOB); }
DI int wrap7(int v) { return v >= 7 ? v - 7 : v; }
DI int swz(int row) { return ((row >> 1) & 1) | (((row >> 3) ^ (row >> 2)) & 1) << 1; }

#define FRESH() \
  int tid = threadIdx.x, bid = blockIdx.x; \
  asm volatile("" : "+v"(tid)); \
  asm volatile("" : "+s"(bid)); \
  const int lane = tid & 63, w = __builtin_amdgcn_readfirstlane(tid >> 6), bx = bid & 7, by = bid >> 3; \
  const int c = lane & 15, g = lane >> 4, q4 = (lane & 15) >> 2, p4 = lane & 3, r32 = lane & 31, hh = lane >> 5; \
  const int by7 = __builtin_amdgcn_readfirstlane((by * 37) >> 8), bym = __builtin_amdgcn_readfirstlane(by - 7 * by7); \
  const int t = 2 * bx + (by >> 4), hd = by & 15;                \
  const int st = 4 * bx + by7, dt = bym;                         \
  const int dt4 = by < 21 ? bym : bym + 4 * (bx & 1), ft = 7 * (bx >> 1) + (by < 21 ? by7 + 4 * (bx & 1) : 3);    \
  LAS char* ring = L + L_KVW + w * 16384; \
  const int wrow = lane >> 3, sx0 = swz(wrow) << 5; \
  auto raddr = [&](int row) { return row * 128 + (swz(row) << 5) + 8 * p4; }; \
  LAS char* red = L + L_KVW + lane * 16; \
  (void)c; (void)g; (void)q4; (void)r32; (void)hh; (void)t; (void)hd; (void)st; (void)dt; (void)dt4; (void)ft; (void)ring; (void)sx0; (void)raddr; (void)red;

#define MFMA32V0(d, a, b) asm volatile("v_mfma_f32_32x32x16_bf16 %0, %1, %2, 0" : "=&v"(d) : "v"(a), "v"(b))
#define MFMA32VC(d, a, b, c) asm volatile("v_mfma_f32_32x32x16_bf16 %0, %1, %2, %3" : "=&v"(d) : "v"(a), "v"(b), "v"(c))
#define MFMA32VA(d, a, b) asm volatile("v_mfma_f32_32x32x16_bf16 %0, %1, %2, %0" : "+v"(d) : "v"(a), "v"(b))
#define MFMA_SETTLE(d) asm volatile("s_nop 11" : "+v"(d))
#define MFMA_SETTLE2(d, e) asm volatile("s_nop 11" : "+v"(d), "+v"(e))
DI float max3(float a, float b, float c) { float r; asm("v_max3_f32 %0, %1, %2, %3" : "=v"(r) : "v"(a), "v"(b), "v"(c)); return r; }

#define WB(cc) (L + L_WQ + (4 * hh + q4) * 64 + (g & 1) * 32 + 8 * p4 + (hd & 1) * 8 + (cc) * 4096)
#define WFRAG(wb, part, e) trfrag((wb) + (part) * 28672 + ((e) >> 1) * 2048 + ((e) & 1) * 512, (wb) + (part) * 28672 + ((e) >> 1) * 2048 + ((e) & 1) * 512 + 1024)
#define CSUM(part, a) (*(LAS f32x4*)(L + L_CS + ((part) * 32 + 8 * (a) + 4 * hh) * 4))
#define WZ_RD1(n, i, sl) wz[n][i] = (n) < 16 ? trfrag(pr1[i] + (sl) * 4096, pr1[(i) ^ 2] + (sl) * 4096 + 512) : trfrag(pr2[i] + (sl) * 4096, pr2[i] + (sl) * 4096 + 2048)
#define DMA_WZ1(n, m, sl) { \
    if ((n) < 16) dmai<(sl) * 4096 + (m) * 1024, (32 * ((n) & 15) + 8 * (m)) * F * 2>(rw1, ((m) & 1) ? pw1 ^ 64 : pw1, pring); \
    else dmai<(sl) * 4096 + (m) * 1024, (32 * ((n) & 15) + 8 * (m)) * S * 2>(rw2, ((m) & 1) ? pw2 ^ 64 : pw2, pring); }
#define PIECE(L0, q) { \
    constexpr int r_ = (q) / 4, m_ = (q) % 4, n_ = (L0) + r_; \
    if constexpr (m_ == 0 && r_ >= 4) asm volatile("s_waitcnt vmcnt(8) lgkmcnt(0)" ::: "memory"); \
    else if constexpr (m_ == 0 && r_ == 3) vmw<8>(); \
    DMA_WZ1(n_, m_, r_ & 3) \
    if constexpr (r_ >= 3) WZ_RD1(n_ - 3, m_, (r_ - 3) & 3); \
    SCHED(); }
#define DPUMP(L0, A, B) sfor<4 * ((B) - (A))>([&](auto I_) INL { PIECE(L0, 4 * ((A) - (L0)) + I_) });
#define STASH(A, B) if constexpr (layer == 1) sfor<4 * ((B) - (A))>([&](auto I_) INL { \
    constexpr int j_ = (A) + I_ / 4, m_ = I_ % 4; \
    dmai<j_ * 4096 + m_ * 1024, (32 * (14 - NS + j_) + 8 * m_) * S * 2>(rw2, (m_ & 1) ? pw2 ^ 64 : pw2, pstash); SCHED(); });
#define DTSTEP(L0, L1, sl) ((L1) - 1 - (((L1) - 1 - (L0) - (sl)) & 3))
#define DTAIL1(L0, L1, j) { if constexpr (DTSTEP(L0, L1, (j) >> 2) >= (L0) && DTSTEP(L0, L1, (j) >> 2) >= (L1) - 3) WZ_RD1(DTSTEP(L0, L1, (j) >> 2), (j) & 3, (j) >> 2); }
#define DTAIL(L0, L1) sfor<16>([&](auto J_) INL { constexpr int j_ = J_; DTAIL1(L0, L1, j_) }); SCHED();
#define REDW(a, b) *(LAS f32x4*)(red + w * 16384 + (((b) - 1) * 4 + (a)) * 1024) = acc[a][b]
#define REDR(a, b) (*(LAS f32x4*)(red + ((w + 4 - (b)) & 3) * 16384 + (((b) - 1) * 4 + (a)) * 1024))
__global__ void __launch_bounds__(256, 1) fused_kernel(Params p) {
  extern __shared__ __attribute__((aligned(16))) char smem[];
  LAS char* L = (LAS char*)smem;
  {
    unsigned long long pc;
    int len;
    asm volatile("s_getpc_b64 %0\n.Lfk_code_from:\n\ts_mov_b32 %1, .Lfk_code_to-.Lfk_code_from" : "=s"(pc), "=s"(len));
    touch(mk((const void*)pc, len > 0 ? len : 0), (threadIdx.x & 63) < 8 ? ((blockIdx.x >> 3) * 4 + (threadIdx.x >> 6) + (threadIdx.x & 7) * 128) * 128 : OOB, 0, L);
  }
  SCHED();
  const bool on35 = (blockIdx.x >> 3) < 28;
  const bool on4 = (blockIdx.x >> 3) < ((blockIdx.x & 1) ? 24 : 25);
  const rsrc_t rws = mk(p.ws, WS_BYTES);
  GAS unsigned* ctrl = (GAS unsigned*)p.ws;
  GAS unsigned* flags = (GAS unsigned*)((GAS char*)p.ws + FLAG_OFF);
  STAMP(0);
  unsigned budget = 1u << 22;

  v4u xf[28];
  v4u kv[4];
  float mean = 0.f, rstd = 0.f;
  f32x4 res[4];
  unsigned epoch;
  int kvs, oo, t1o, wvo2, pw1, pw2, fo;
  unsigned nk1, nk2, pm[8];
  LAS char *kvw, *vw, *vr0, *wor[4], *pr1[4], *pr2[4], *wbq;
#define KVOUT(acc, h) { \
    _Pragma("unroll") for (int i = 0; i < 16; i += 2) kv[2 * (h) + (i >> 3)][(i >> 1) & 3] = pk(acc[i], acc[i + 1]); \
    if (hh) { kv[2 * (h) + 1][2] = (h) ? 0x00003f80u : 0u; kv[2 * (h) + 1][3] = 0u; } \
    const rsrc_t rkv = mk(p.ws, t < 15 ? WS_BYTES : 0);    \
    st16<SC1>(kv[2 * (h)], rkv, kvs + (h) * 2048, 0); \
    st16<SC1>(kv[2 * (h) + 1], rkv, kvs + (h) * 2048 + 1024, 0); \
    SCHED(); }
#define X128(p) ((LAS char*)(size_t)((unsigned)(size_t)(p) ^ 128u))
  {
    FRESH();
    const rsrc_t rwq = mk(p.wqkv, D * 3 * D * 2);
    const rsrc_t rx = mk(p.x, S * D * 2);
    const int gid = by * 4 + w, rg = (int)(size_t)ring;
    touch(rx, oob(lane < 14, (t * 896 + hd * 56 + w * 14 + lane) * 128), 0, L);
    touch(rwq, oob(lane < 14, gid * 2688 + 896 + lane * 128), 0, L);
    const int xvo = (t * 128 + w * 32 + wrow) * 896 + (((lane & 7) ^ (wrow >> 1)) << 4);
    const int wvo = (w * 16 + (lane >> 2)) * 2688 + hd * 56 - (hd & 1) * 8 + (lane & 3) * 16;
    const int wlb = (int)(size_t)(L + L_WQ) + w * 1024;
#define DMA_W(part, cc) dmai<(part) * 28672 + (cc) * 4096, (cc) * 64 * 2688 + (part) * 896>(rwq, wvo, wlb)
#define DMA_C(cc, m) { if constexpr ((m) < 4) dmai<((cc) & 3) * 4096 + ((m) & 3) * 1024, ((m) & 3) * 8 * 896 + (cc) * 128>(rx, ((m) & 1) ? xvo ^ 64 : xvo, rg); \
                       else DMA_W((m) - 3, cc); }
    sfor<10>([&](auto M) INL { constexpr int m = M; DMA_C(m / 6, m % 6) });
    SCHED();
    const int tb = gid * 35 + lane, tbr = (tb * 18725) >> 18;
    int tvb = oob(lane < 35, (128 + tbr) * 2688 + 896 + (tb - 14 * tbr) * 128);
    LAS char* xa[8];
#pragma unroll
    for (int e = 0; e < 8; ++e) { xa[e] = ring + r32 * 128 + 8 * hh + ((e ^ ((r32 >> 1) & 7)) << 4); PINV(xa[e]); }
    int epo = w == 0 ? FLAG_OFF + (bid - 256) * 128 + lane * 32768 : S3_OFF + (st * 8 + dt) * 1024 - 2 * S3_SZ + (lane >> 3) * S3_SZ + (lane & 7) * 128;
    epo = (w == 0 ? lane - 1u < 10u : w == 1 && on35 && (lane >> 4) == 1) ? epo : 0;
    wbq = WB(0);
    LAS char* wb = wbq + 28672;
    LAS char* cb = L + L_WQ + 28672 + (w * 16 + 8 * hh + q4) * 64 + (g & 1) * 32 + 8 * p4;
    PINV(tvb); PINV(epo); PINV(wbq); PINV(wb); PINV(cb);
#pragma unroll
    for (int i = 0; i < 8; ++i) {
      const int d = r32 - ((2 * i & 3) + 8 * (i >> 1)) - 4 * hh;
      pm[i] = ((unsigned)(d >> 31) & 0xffffu) | ((unsigned)((d - 1) >> 31) & 0xffff0000u);
      PINA(pm[i]);
    }
#pragma unroll
    for (int i = 0; i < 4; ++i) {
      wor[i] = ring + (raddr(16 * (g >> 1) + 4 * (g & 1) + q4) ^ (i << 5));
      pr1[i] = ring + (raddr(8 * g + q4) ^ (i << 5));
      pr2[i] = ring + (raddr(4 * g + q4) ^ (((i + w) & 3) << 5));
      PINA(wor[i]); PINA(pr1[i]); PINA(pr2[i]);
    }
    SCHED();
    DMA_C(1, 4) DMA_C(1, 5)
    asm volatile("buffer_load_dword %0, %1, %2, 0 offen sc1" : "=&v"(epoch) : "v"(epo), "s"(rws) : "memory");
    touch(rwq, tvb, 0, L);
    SCHED();
    STAMP(32);
#define READ_X1(cc, e) { \
      constexpr int ch = ((e) >> 1) * 4 + ((e) & 1); \
      const v2u a_ = *(LAS v2u*)(xa[ch] + ((cc) & 3) * 4096), b_ = *(LAS v2u*)(xa[ch + 2] + ((cc) & 3) * 4096); \
      xf[4 * (cc) + (e)] = (v4u){a_.x, a_.y, b_.x, b_.y}; }
    const v4u ones = {0x3f803f80u, 0x3f803f80u, 0x3f803f80u, 0x3f803f80u};
    f32x16 ak, av, gs, ss = {}, csk = {}, csv = {};
    v4u wk[28], wv[28], wc[14];
    float dg = 0.f, d8[8], d4[4], pp[4];
    f32x4 cc[8];
    int tvq, tvw;
    LAS float *pb, *pw;
#define PICK4(o, at, a0, a1, a2, a3, b0, b1, b2, b3, m) \
    asm volatile("v_cndmask_b32 %0, %4, %8, %12\n\tv_cndmask_b32 %1, %5, %9, %12\n\tv_cndmask_b32 %2, %6, %10, %12\n\tv_cndmask_b32 %3, %7, %11, %12" \
                 : "=&v"(o[at]), "=&v"(o[(at) + 1]), "=&v"(o[(at) + 2]), "=&v"(o[(at) + 3]) \
                 : "v"(a0), "v"(a1), "v"(a2), "v"(a3), "v"(b0), "v"(b1), "v"(b2), "v"(b3), "s"(m))
    sfor<8>([&](auto R) INL {
      constexpr int r = R;
      if constexpr (r < 7) {
        if constexpr (r == 2) { asm volatile("s_waitcnt vmcnt(6)" : "+v"(epoch) :: "memory"); epoch = __builtin_amdgcn_readfirstlane(epoch) + 1u; }
        else vmw<(r < 2 || r == 5 ? 8 : r == 6 ? 4 : 6)>();
        __builtin_amdgcn_s_barrier();
        FENCE();
        SCHED();
      }
      sfor<18>([&](auto Q) INL {
        constexpr int q = Q;
        constexpr int e = q < 4 ? q >> 1 : q >= 6 && q < 8 ? 2 : q >= 12 && q < 14 ? 3 : -1;
        constexpr int sx = q >= 8 && q < 12 ? q - 8 : q >= 14 ? q - 10 : -1;
        if constexpr (e >= 0 && r > 0) {
          constexpr int km = 4 * (r - 1) + e;
          if constexpr (q & 1) { if constexpr (km == 0) MFMA32V0(av, wv[km], xf[km]); else MFMA32VA(av, wv[km], xf[km]); }
          else { if constexpr (km == 0) MFMA32V0(ak, wk[km], xf[km]); else MFMA32VA(ak, wk[km], xf[km]); }
        }
        if constexpr (r < 7) {
          if constexpr (q == 4) csk = mfma32(ones, wc[2 * r], csk);
          if constexpr (q == 5) csv = mfma32(ones, wc[2 * r + 1], csv);
          if constexpr (sx >= 0) {
            constexpr int i = 4 * r + (sx >> 1);
            if constexpr (sx & 1) ss = mfma32(ones, xf[i], ss);
            else if constexpr (i == 0) MFMA32V0(gs, xf[i], xf[i]); else MFMA32VA(gs, xf[i], xf[i]);
          }
          SCHED();
          if constexpr (q < 2) wc[2 * r + q] = trfrag(cb + q * 28672 + r * 4096, cb + q * 28672 + r * 4096 + 256);
          if constexpr (q < 4) READ_X1(r, q)
          if constexpr (q >= 4 && q < 12) { constexpr int f = q - 4; if constexpr (f & 1) wv[4 * r + f / 2] = WFRAG(wb + r * 4096, 1, f / 2); else wk[4 * r + f / 2] = WFRAG(wb + r * 4096, 0, f / 2); }
        }
        if constexpr (r < 5 && q % 3 == 2) DMA_C(r + 2, q / 3)
        if constexpr (r == 4 && q == 17) { touch(rwq, tvq, 0, L); touch(mk(p.wo, D * D * 2), tvw, 0, L); }
        if constexpr (r == 5 && (q == 11 || q == 17)) DMA_W(0, q / 17);
        if constexpr (r == 6 && q % 6 == 5) DMA_W(0, 2 + q / 6);
        if constexpr (r == 7 && (q == 1 || q == 7)) DMA_W(0, 5 + q / 7);
        if constexpr (q == 13 || q == 15) {
          constexpr int i = 2 * r + (q == 15);
          if constexpr (i == 0) { kvs = KV_OFF + (hd * 64 + 4 * t + w) * 4096 + lane * 16; PINV(kvs); }
          if constexpr (i == 1) { oo = O_OFF + ((2 * t + (w >> 1)) * 16 + hd) * 4096 + (2 * (w & 1) + (r32 >> 4)) * 1024 + hh * 256 + c * 16; PINA(oo); }
          if constexpr (i == 2) { t1o = oob(lane < 32, (512 * (by >> 3) + 8 * ((by & 7) * 4 + w) + (lane >> 2)) * (F * 2) + (7 * (bx >> 1) + 3 * (bx & 1) + (lane & 3)) * 128); PINA(t1o); }
          if constexpr (i == 3) { wvo2 = ((112 * w + wrow) * 896 + dt * 128 + (lane & 7) * 16) ^ sx0; PINA(wvo2); }
          if constexpr (i == 4) { pw1 = ((512 * w + wrow) * (F * 2) + ft * 128 + (lane & 7) * 16) ^ sx0; PINA(pw1); }
          if constexpr (i == 5) { pw2 = ((448 * w + wrow) * (S * 2) + st * 128 + (lane & 7) * 16) ^ sx0; PINA(pw2); }
          if constexpr (i == 6) {
            const int tl = by * 98 + w * 25 + lane, trow = (tl * 9363) >> 16;
            tvq = oob(lane < 25 && w * 25 + lane < 98, trow * 2688 + (tl - 7 * trow) * 128); tvw = oob(lane < 25 && w * 25 + lane < 98, tl * 128);
            PINV(tvq); PINV(tvw);
          }
          if constexpr (i == 7) {
            const int tp = (t >= 2 && (lane & 1)) ? t - 2 : t > 0 ? t - 1 : 0;
            fo = FLAG_OFF + (((tp & 1) * 16 + hd) * 8 + (tp >> 1)) * 128;
            PINA(fo);
          }
          if constexpr (i == 8) { nk2 = (t < 2 && hh) ? 0xf149u : 0u; nk1 = (t < 1 && hh) ? 0xf149u : 0u; PINA(nk1); PINA(nk2); }
          if constexpr (i == 9) { kvw = L + L_KVW + w * 4096 + lane * 16; vw = L + L_KVW + w * 4096 + 2048 + ((lane * 16) ^ (hh << 6)); PINA(kvw); PINA(vw); }
          if constexpr (i == 10) { vr0 = L + L_KVW + w * 4096 + 2048 + ((((g & 1) * 1024 + (p4 & 1) * 512 + hh * 64 + q4 * 16) ^ ((p4 & 1) << 6) ^ ((g & 1) << 7)) + (p4 >> 1) * 8); PINA(vr0); }
          if constexpr (i == 11) {
            pb = (LAS float*)(L + L_CSP) + 32 + lane + 4 * (hd & 1);
            pw = (LAS float*)(L + (hh ? L_DUMP : L_CSP + (w * 96 + 32) * 4)) + r32;
            PINV(pb); PINV(pw);
          }
        }
        if constexpr (r == 6 && q == 8) { pw[0] = csk[0]; pw[32] = csv[0]; }
        if constexpr (r == 6 && q == 12) { bar(); pp[0] = pb[0]; pp[1] = pb[96]; pp[2] = pb[192]; pp[3] = pb[288]; }
        if constexpr (r == 6 && q == 16) *(LAS float*)(L + L_CS + (32 + lane) * 4) = (pp[0] + pp[1]) + (pp[2] + pp[3]);
        if constexpr (r == 7 && q < 2) { _Pragma("unroll") for (int j = 0; j < 4; ++j) cc[4 * q + j] = CSUM(1, 4 * q + j); }
        if constexpr (r == 7 && (q == 2 || q == 3)) { constexpr int j = 4 * (q - 2); PICK4(d8, j, gs[j], gs[j + 1], gs[j + 2], gs[j + 3], gs[j + 8], gs[j + 9], gs[j + 10], gs[j + 11], 0xffff0000ffff0000ull); }
        if constexpr (r == 7 && q == 6) PICK4(d4, 0, d8[0], d8[1], d8[2], d8[3], d8[4], d8[5], d8[6], d8[7], 0xff00ff00ff00ff00ull);
        if constexpr (r == 7 && q == 7)
          asm volatile("v_cndmask_b32 %1, %1, %3, %5\n\tv_cndmask_b32 %2, %2, %4, %5\n\tv_cndmask_b32 %0, %1, %2, %6\n\tv_cndmask_b32 %0, 0, %0, %7"
                       : "=&v"(dg), "+v"(d4[0]), "+v"(d4[1]) : "v"(d4[2]), "v"(d4[3]), "s"(0xccccccccccccccccull), "s"(0xaaaaaaaaaaaaaaaaull), "s"(0xf0f0f0f00f0f0f0full));
        if constexpr (r == 7 && q == 12) { mean = ss[0] * (1.f / D); rstd = sum32(dg) * (1.f / D) - mean * mean; PINV(mean); PINV(rstd); }
        if constexpr (r == 7 && q == 13) { rstd = __builtin_amdgcn_rsqf(fmaxf(rstd, 0.f) + EPS); PINV(rstd); }
        SCHED();
      });
    });
    STAMP(34);
    MFMA_SETTLE2(ak, av);
#pragma unroll
    for (int i = 0; i < 16; ++i) ak[i] = (ak[i] - mean * cc[i >> 2][i & 3]) * rstd;
    KVOUT(ak, 0)
#pragma unroll
    for (int i = 0; i < 16; ++i) av[i] = (av[i] - mean * cc[4 + (i >> 2)][i & 3]) * rstd;
    KVOUT(av, 1)
  }

  unsigned b2r;
  v2u bb[4];
  v4u wz[30][4];
  sfor<2>([&](auto LY) INL {
    constexpr int layer = LY;
    constexpr int Y0 = layer ? KP : 0, Y2 = Y0 + (layer ? MO2 : NO2), YG = Y2 + (layer ? MO3 : NO3), YG2 = YG + (layer ? MG2 : NG2),
                  YH = YG2 + (layer ? MG3 : NG3), YE = layer ? 16 : Z_U, YH2 = layer ? YE : YH + NH2, YZ = layer ? 30 - NS : 30, YU3 = layer ? YZ : 30 - NU3;
    static_assert(YH <= YH2, "schedule");
    constexpr int QPUB = layer ? 14 : 10;
    GAS unsigned* fl = flags + layer * 5 * 256 * 32;
    STAMP(1 + layer * 16);
    v4u qf[2];
    v4u wo[4][4];
    v2u rr[4];
    int pring, pstash;
    const rsrc_t rw1 = mk(p.w1, on4 ? S * F * 2 : 0);
    const rsrc_t rw2 = mk(p.w2, on35 ? S * F * 2 : 0);
    {
      const bool hi = threadIdx.x & 32;
      FRESH();
      if constexpr (layer == 0) {
        vmw<4>();
        if (t == 15) vmw<0>();
        __builtin_amdgcn_s_barrier();
        FENCE();
      }
      v4u wc[7];
      f32x16 cs = {};
      LAS char* cb = L + L_WQ + (w * 16 + 8 * hh + q4) * 64 + (g & 1) * 32 + 8 * p4;
      v4u wq[28];
#pragma unroll
      for (int ks = 0; ks < 4; ++ks) wq[ks] = WFRAG(wbq, 0, ks);
      f32x16 aq;
      STAMP(2 + layer * 43);
      const rsrc_t rwo = mk(p.wo, on35 ? D * D * 2 : 0);
      pring = (int)(size_t)ring;
      pstash = (int)(size_t)(L + L_WQ) + w * 20480;
      PINS(pring); PINS(pstash);
      GAS unsigned* fp = (GAS unsigned*)((GAS char*)p.ws + fo);
      unsigned pf;
      sfor<28>([&](auto I) INL {
        constexpr int ks = I;
        if constexpr (ks == 0) MFMA32V0(aq, wq[ks], xf[ks]); else MFMA32VA(aq, wq[ks], xf[ks]);
        if constexpr (ks + 4 < 28) wq[ks + 4] = WFRAG(wbq + ((ks + 4) >> 2) * 4096, 0, ks & 3);
        constexpr int wi = layer ? -1 : ks - QPUB - 1;
        if constexpr (wi >= 0 && wi < 16) dmai<wi * 1024, (28 * (wi >> 2) + 8 * (wi & 3)) * 896>(rwo, (wi & 1) ? wvo2 ^ 64 : wvo2, pring);
        if constexpr (layer == 0) {
          if constexpr (ks < 7) wc[ks] = trfrag(cb + ks * 4096, cb + ks * 4096 + 256);
          if constexpr (ks >= 2 && ks < 9) cs = mfma32((v4u){0x3f803f80u, 0x3f803f80u, 0x3f803f80u, 0x3f803f80u}, wc[ks - 2], cs);
          if constexpr (ks == QPUB + 1) { _Pragma("unroll") for (int q = 0; q < 4; ++q) cs[q] = ((LAS float*)(L + L_CSP) + r32 + 4 * (hd & 1))[96 * q]; }
          if constexpr (ks == QPUB + 5) *(LAS float*)(L + L_CS + r32 * 4) = (cs[0] + cs[1]) + (cs[2] + cs[3]);
        }
        if constexpr (ks == 27) POLL(pf, fp);
        if constexpr (ks == QPUB) {
          if constexpr (layer == 0) { *(LAS float*)(L + (hh ? L_DUMP : L_CSP + w * 96 * 4) + r32 * 4) = cs[0]; }
          publish(fl + (0 * 256 + bid) * 32, epoch);
          STAMP(3 + layer * 16);
        }
        SCHED();
      });
      f32x4 cq[4];
#pragma unroll
      for (int a = 0; a < 4; ++a) cq[a] = CSUM(0, a);
      MFMA_SETTLE(aq);
      const float qs = rstd * (0.18898223650461362f * 1.4426950408889634f);
#pragma unroll
      for (int a = 0; a < 4; ++a) {
        qf[a >> 1][(a & 1) * 2] = pk((aq[4 * a] - mean * cq[a][0]) * qs, (aq[4 * a + 1] - mean * cq[a][1]) * qs);
        qf[a >> 1][(a & 1) * 2 + 1] = pk((aq[4 * a + 2] - mean * cq[a][2]) * qs, (aq[4 * a + 3] - mean * cq[a][3]) * qs);
      }
      if (hi) { qf[1][2] = 0x00003f80u; qf[1][3] = 0u; }
      PINV(qf[0]); PINV(qf[1]);
      f32x16 s8;
      asm volatile("s_nop 1" : "+v"(qf[0]), "+v"(qf[1]));
      MFMA32V0(s8, kv[0], qf[0]);
      MFMA32VA(s8, kv[1], qf[1]);
      SCHED();
      unsigned late;
      POLLED(pf, 0, fp)
      STAMP(4 + layer * 16);
      v4u ra[4], rb[4];
      {
        const rsrc_t r1 = mk(p.ws, t >= 1 ? WS_BYTES : 0), r2 = mk(p.ws, t >= 2 ? WS_BYTES : 0);
        const int kvo = kvs - 32768;
        rb[0] = ld16<SC1>(r2, kvo, 0); rb[1] = ld16<SC1>(r2, kvo, 1024);
        ra[0] = ld16<SC1>(r1, kvo, 16384); ra[1] = ld16<SC1>(r1, kvo, 16384 + 1024);
        SCHED();
        rb[2] = ld16<SC1>(r2, kvo, 2048); rb[3] = ld16<SC1>(r2, kvo, 3072);
        ra[2] = ld16<SC1>(r1, kvo, 16384 + 2048); ra[3] = ld16<SC1>(r1, kvo, 16384 + 3072);
        SCHED();
      }
#pragma unroll
      for (int j = 0; j < 16; ++j) wo[j >> 2][j & 3] = trfrag(wor[j & 3] + (j >> 2) * 4096, wor[(j & 3) ^ 2] + (j >> 2) * 4096 + 1024);
      SCHED();
      bar();
      LAS char* vw1 = X128(vw) + 1024;
      LAS char* vr1 = X128(vr0);
      *(LAS v4u*)(kvw + 32768) = kv[0];
      *(LAS v4u*)(kvw + 32768 + 1024) = kv[1];
      *(LAS v4u*)(vw + 32768) = kv[2];
      *(LAS v4u*)(vw1 + 32768) = kv[3];
      int rro = (64 * st + 16 * w + c) * 896 + dt * 128 + g * 8, b2o = (64 * st + 16 * w + c) * 2, b1o = ft * 128 + g * 8;
      unsigned pml[8], mk32[16], nk1l = nk1, nk2l = nk2;
      int ool = oo, t1l = t1o;
      LAS char* vr0l = vr0;
      if constexpr (layer == 0) { PINV(rro); PINV(b2o); PINV(b1o); }
      PINV(vw1); PINV(vr1); PINV(vr0l); PINV(ool); PINV(t1l); PINV(nk1l); PINV(nk2l);
#pragma unroll
      for (int i = 0; i < 8; ++i) { pml[i] = pm[i]; PINV(pml[i]); mk32[2 * i] = (unsigned)((int)(pml[i] << 16) >> 16); mk32[2 * i + 1] = (unsigned)((int)pml[i] >> 16); PINV(mk32[2 * i]); PINV(mk32[2 * i + 1]); }
      SCHED();
      f32x16 sc[8], se;
      v4u kf[8][2];
      float mx = -1e30f, mx2 = -1e30f;
#define MAX8(v, i) asm volatile("v_max3_f32 %0, %0, %2, %3\n\tv_max3_f32 %1, %1, %4, %5\n\tv_max3_f32 %0, %0, %6, %7\n\tv_max3_f32 %1, %1, %8, %9" \
        : "+v"(mx), "+v"(mx2) : "v"(v[i]), "v"(v[(i) + 1]), "v"(v[(i) + 2]), "v"(v[(i) + 3]), "v"(v[(i) + 4]), "v"(v[(i) + 5]), "v"(v[(i) + 6]), "v"(v[(i) + 7]))
      *(LAS v4u*)(kvw) = rb[0];
      MFMA32V0(sc[0], rb[0], qf[0]);
      rb[1][2] |= nk2l;
      *(LAS v4u*)(kvw + 1024) = rb[1];
      *(LAS v4u*)(kvw + 16384) = ra[0];
      SCHED();
      MFMA32VA(sc[0], rb[1], qf[1]);
      ra[1][2] |= nk1l;
      *(LAS v4u*)(kvw + 16384 + 1024) = ra[1];
      kf[4][0] = ra[0]; kf[4][1] = ra[1];
      MFMA32V0(sc[4], kf[4][0], qf[0]);
      bar();
      KEEP(late);
      STAMP(37 + layer * 4);
      MFMA32VA(sc[4], kf[4][1], qf[1]);
#pragma unroll
      for (int j = 1; j < 8; ++j) if (j != 4) { kf[j][0] = *(LAS v4u*)(kvw + j * 4096); kf[j][1] = *(LAS v4u*)(kvw + j * 4096 + 1024); }
      PINV(sc[0]);
#pragma unroll
      for (int i = 0; i < 16; ++i) se[i] = asf((asu(sc[0][i]) & mk32[i]) | (asu(s8[i]) & ~mk32[i]));
      PINV(se);
      SCHED();
      sfor<12>([&](auto N) INL {
        constexpr int n = N, jj = n >> 1, j = jj < 3 ? jj + 1 : jj + 2, hf = n & 1;
        if constexpr (hf == 0) MFMA32V0(sc[j], kf[j][0], qf[0]); else MFMA32VA(sc[j], kf[j][1], qf[1]);
        if constexpr (n < 2) MAX8(se, 8 * hf);
        else if constexpr (n < 4) MAX8(sc[4], 8 * hf);
        else MAX8(sc[jj < 5 ? jj - 1 : jj], 8 * hf);
        if constexpr (n == 2) *(LAS v4u*)(vw) = rb[2];
        if constexpr (n == 3) *(LAS v4u*)(vw1) = rb[3];
        if constexpr (n == 4) *(LAS v4u*)(vw + 16384) = ra[2];
        if constexpr (n == 5) *(LAS v4u*)(vw1 + 16384) = ra[3];
        if constexpr (n == 6) touch(mk(p.w1, S * F * 2), t1l, layer * KP * 32 * F * 2, L);
        if constexpr (n >= 7) asm volatile("s_nop 0");
        SCHED();
      });
      MAX8(sc[6], 0); MAX8(sc[6], 8); MAX8(sc[7], 0); MAX8(sc[7], 8);
      mx = max32(max3(mx, mx2, mx2));
      f32x16 nm;
#pragma unroll
      for (int i = 0; i < 16; ++i) nm[i] = -mx;
      PINV(nm);
      bar();
      STAMP(38 + layer * 4);
      f32x16 ao;
      v4u vf[9][2], pp[9][2];
      float ex[32][4];
#define VF(j, s) vf[j][s] = trfrag(vr0l + (j) * 4096 + (s) * 256, vr1 + (j) * 4096 + (s) * 256)
      sfor<33>([&](auto N) INL {
        constexpr int n = N, j = n >> 2, q = n & 3;
        if constexpr (j == 0) {
          if constexpr (q == 0) MFMA32VC(sc[1], kf[1][0], qf[0], nm);
          if constexpr (q == 1) MFMA32VA(sc[1], kf[1][1], qf[1]);
          if constexpr (q == 2) MFMA32VC(sc[2], kf[2][0], qf[0], nm);
          if constexpr (q == 3) MFMA32VA(sc[2], kf[2][1], qf[1]);
        } else if constexpr (j < 8) {
          if constexpr (q == 1) { if constexpr (j == 1) MFMA32V0(ao, vf[0][0], pp[0][0]); else MFMA32VA(ao, vf[j - 1][0], pp[j - 1][0]); }
          if constexpr (q == 2) MFMA32VA(ao, vf[j - 1][1], pp[j - 1][1]);
          if constexpr (j + 2 < 8) {
            if constexpr (q == 0) MFMA32VC(sc[j + 2], kf[j + 2][0], qf[0], nm);
            if constexpr (q == 3) MFMA32VA(sc[j + 2], kf[j + 2][1], qf[1]);
          } else if constexpr (j == 6) {
            if constexpr (q == 0) MFMA32VA(ao, vf[8][0], pp[8][0]);
            if constexpr (q == 3) MFMA32VA(ao, vf[8][1], pp[8][1]);
          }
        } else MFMA32VA(ao, vf[7][0], pp[7][0]);
        SCHED();
        if constexpr (n < 32) {
#pragma unroll
          for (int i = 0; i < 4; ++i) { if constexpr (j == 0) ex[n][i] = __builtin_amdgcn_exp2f(se[4 * q + i] - mx); else ex[n][i] = __builtin_amdgcn_exp2f(sc[j][4 * q + i]); }
        }
        SCHED();
        if constexpr (n > 0) {
          constexpr int jp = (n - 1) >> 2, qp = (n - 1) & 3;
#pragma unroll
          for (int i = 0; i < 2; ++i) {
            const unsigned u = pk(ex[n - 1][2 * i], ex[n - 1][2 * i + 1]);
            if constexpr (jp == 0) { pp[0][qp >> 1][(2 * qp + i) & 3] = u & pml[2 * qp + i]; pp[8][qp >> 1][(2 * qp + i) & 3] = u & ~pml[2 * qp + i]; }
            else pp[jp][qp >> 1][(2 * qp + i) & 3] = u;
          }
        }
        if constexpr (layer == 0) {
          if constexpr (n >= 2 && n < 10 && q >= 2) rr[(n >> 2) * 2 + q - 2] = ld8<0>(mk(p.x, on35 ? S * D * 2 : 0), rro, ((n >> 2) * 2 + q - 2) * 32);
          if constexpr (n == 10) b2r = (unsigned)__builtin_amdgcn_raw_buffer_load_b16(mk(p.b2, S * 2), b2o, 0, 0);
          if constexpr (n >= 14 && n < 22 && q >= 2) bb[((n >> 2) - 3) * 2 + q - 2] = ld8<0>(mk(p.b1, F * 2), b1o, (((n >> 2) - 3) * 2 + q - 2) * 32);
        }
        if constexpr (n == 22) KEEP(nm);
        if constexpr (j < 8 && q < 2) VF(j, q);
        if constexpr (j == 5 && q >= 2) VF(8, q - 2);
        SCHED();
      });
      asm volatile("s_nop 1" : "+v"(pp[7][1]));
      MFMA32VA(ao, vf[7][1], pp[7][1]);
      MFMA_SETTLE(ao);
      const float inv = __builtin_amdgcn_rcpf(hi32(ao[12]));
      v4u of[2];
#pragma unroll
      for (int i = 0; i < 16; i += 2) of[i >> 3][(i >> 1) & 3] = pk(ao[i] * inv, ao[i + 1] * inv);
      if (hi) { of[1][2] = 0u; of[1][3] = 0u; }
      st16<SC1>(of[0], rws, ool, 0);
      st16<SC1>(of[1], rws, ool, 512);
      SCHED();
      STAMP(39 + layer * 4);
    }
    if (on35) {
      {
        FRESH();
        const int tq = st >> 1;
        GAS unsigned* fp = fl + (1 * 256 + (((tq & 1) * 16 + 4 * w + (lane & 3)) * 8 + (tq >> 1))) * 32;
        int oo = O_OFF + layer * O_SZ + (st * 16 + 4 * w) * 4096 + lane * 16;
        const int sb = S3_OFF + layer * S3_SZ + st * 8192 + (16 * w + c) * 16;
        int sw = oob(g < 2, sb + dt * 1024 + g * 8), sa = sb + g * 1024, sc = oob(g < 3, sb + (g + 4) * 1024);
        int ho = HT_OFF + layer * HT_SZ + (dt * 64 + 2 * st + (w >> 1)) * 4096 + (g >> 1) * 1024 + (2 * (w & 1) + (g & 1)) * 256 + c * 16;
        const int hr = 8 * (g & 1) + q4, hst = L_HST + w * 2048;
        int h0 = hst + (raddr(hr) ^ ((g >> 1) << 5)), h1 = hst + (raddr(hr + 4) ^ ((g >> 1) << 5)), hw = hst + c * 128 + (swz(c) << 5) + g * 8;
        int ob[4];
#pragma unroll
        for (int b = 0; b < 4; ++b) { ob[b] = ((b + w) & 3) * 1024; PINS(ob[b]); }
        PINV(fp); PINV(oo); PINV(sw); PINV(sa); PINV(sc); PINV(ho); PINV(h0); PINV(h1); PINV(hw); PINV(red);
        publish(fl + (1 * 256 + bid) * 32, epoch);
        STAMP(5 + layer * 16);
        unsigned pf;
        DPUMP(Y0, Y0, Y2)
        if constexpr (layer == 0) {
#pragma unroll
          for (int a = 0; a < 4; ++a) { res[a] = unpack4(rr[a]); PINV(res[a]); }
        }
        POLL(pf, fp);
        DPUMP(Y0, Y2, YG)
        unsigned late;
        POLLED(pf, 4 * (YG - Y2), fp)
        STAMP(6 + layer * 16);
        v4u of[16];
#define LOAD_O(I) of[I] = ld16<SC1>(rws, oo + ((I) & 3) * 4096, ob[(((I) >> 2) + 1) & 3])
#pragma unroll
        for (int i = 0; i < WINO; ++i) { LOAD_O(i); SCHED(); }
        vmw<WINO>();
        KEEP(late);
        STAMP(50 + layer * 4);
        f32x4 acc[4][4], part[12];
        sfor<16>([&](auto I) INL {
          constexpr int i = I, b = ((i >> 2) + 1) & 3, k = i & 3;
#pragma unroll
          for (int a = 0; a < 4; ++a) {
            acc[a][b] = mfma16(wo[k][a], of[i], k > 0 ? acc[a][b] : (f32x4){0.f, 0.f, 0.f, 0.f});
            SCHED();
            constexpr int n0 = issued(i, 16, WINO, WO1), n1 = issued(i + 1, 16, WINO, WO1);
            if constexpr (n0 < n1) { if (a == 0) LOAD_O(n0); }
            if constexpr (n0 + 1 < n1) { if (a == 3) LOAD_O(n0 + 1); }
            if constexpr (i < 12) { if (a == 1) DTAIL1(Y0, YG, i) }
            if constexpr (i >= 12 && k < 2) { if (a == 1 || a == 3) REDW(2 * k + (a >> 1), 3); }
            if constexpr (i >= 4 && i < 12) { if (a == 2) REDW(k, i >> 2); }
            if constexpr (i == 14) { if (a == 0) bar(); }
            if constexpr (i >= 14) { if (a < 3) { const int j = 6 * (i - 14) + 2 * a; part[j] = REDR(j / 3, j % 3 + 1); part[j + 1] = REDR((j + 1) / 3, (j + 1) % 3 + 1); } }
            SCHED();
          }
        });
        STAMP(51 + layer * 4);
#pragma unroll
        for (int a = 0; a < 4; ++a) res[a] = ((res[a] + part[3 * a]) + (part[3 * a + 1] + part[3 * a + 2])) + acc[a][0];
        f32x2 s1 = {0.f, 0.f}, s2 = {0.f, 0.f};
        stats16(res, 15, s1, s2);
        st8<SC1>((v2u){asu(sum_ab(s1[0] + s1[1], s2[0] + s2[1])), epoch}, rws, sw, 0);
        SCHED();
        DTAIL1(Y0, YG, 12) DTAIL1(Y0, YG, 13) DTAIL1(Y0, YG, 14) DTAIL1(Y0, YG, 15)
        STAMP(7 + layer * 16);
        bar();
        DPUMP(YG, YG, YG2)
        v4u ga, gb;
        asm volatile("buffer_load_dwordx4 %0, %2, %4, 0 offen sc1\n\tbuffer_load_dwordx4 %1, %3, %4, 0 offen sc1" : "=&v"(ga), "=&v"(gb) : "v"(sa), "v"(sc), "s"(rws) : "memory");
        DPUMP(YG, YG2, YH)
        STASH(0, SG)
        asm volatile("s_waitcnt vmcnt(%2)" : "+v"(ga), "+v"(gb) : "n"(4 * (YH - YG2 + layer * SG)) : "memory");
        while ((__builtin_amdgcn_ballot_w64((ga.y != epoch) | (ga.w != epoch)) | (__builtin_amdgcn_ballot_w64((gb.y != epoch) | (gb.w != epoch)) << 16)) != 0ull) {
          if (budget == 0u) break;
          --budget;
          asm volatile("buffer_load_dwordx4 %0, %2, %4, 0 offen sc1\n\tbuffer_load_dwordx4 %1, %3, %4, 0 offen sc1\n\ts_waitcnt vmcnt(0)"
                       : "=&v"(ga), "=&v"(gb) : "v"(sa), "v"(sc), "s"(rws) : "memory");
        }
        STAMP(8 + layer * 16);
        const unsigned t12 = asu(sum_ab(asf(ga.x) + asf(gb.x), asf(ga.z) + asf(gb.z)));
        const auto tt = __builtin_amdgcn_permlane16_swap(t12, t12, false, false);
        const float t1 = asf(tt[0]), t2 = asf(tt[1]);
        const float m = t1 * (1.f / D), var = fmaxf(t2 * (1.f / D) - m * m, 0.f);
        const float r1 = __builtin_amdgcn_rsqf(var + EPS);
        const float r = r1 * __builtin_amdgcn_rsqf(var * r1 * r1 + EPS), nr = -m * r;
#pragma unroll
        for (int a = 0; a < 4; ++a) {
          const v2u v = {pk(res[a][0] * r + nr, res[a][1] * r + nr), pk(res[a][2] * r + nr, res[a][3] * r + nr)};
          *(LAS v2u*)(L + (hw ^ (a << 5))) = v;
        }
#pragma unroll
        for (int e = 0; e < 2; ++e) st16<SC1>(trfrag(L + (h0 ^ (e << 6)), L + (h1 ^ (e << 6))), rws, ho, e * 2048);
        SCHED();
      }
      if (on4) {
        FRESH();
        const int sp = 8 * w + (lane & 7);
        GAS unsigned* fp = fl + (2 * 256 + ((sp & 3) * 7 + dt4) * 8 + (sp >> 2)) * 32;
        int hv[4];
#pragma unroll
        for (int b = 0; b < 4; ++b) { hv[b] = HT_OFF + layer * HT_SZ + (dt4 * 64 + 16 * w) * 4096 + ((b + w) & 3) * 1024 + lane * 16; PINV(hv[b]); }
        int uo = U_OFF + layer * U_SZ + (dt4 * 56 + 2 * ft) * 4096 + w * 1024 + lane * 16;
        PINV(fp); PINV(uo); PINV(red);
        publish(fl + (2 * 256 + bid) * 32, epoch);
        STAMP(9 + layer * 16);
        unsigned pf;
        DPUMP(YG, YH, YH2)
        STASH(SG, NS - SH)
        POLL(pf, fp);
        DPUMP(YG, YH2, YE)
        STASH(NS - SH, NS)
        unsigned late;
        POLLED(pf, 4 * (YE - YH2 + layer * SH), fp)
        STAMP(10 + layer * 16);
        v4u af[64];
#define LOAD_H(I) { int so_; asm volatile("s_mov_b32 %1, %4\n\tbuffer_load_dwordx4 %0, %2, %3, %1 offen sc1" \
          : "=&v"(af[I]), "=&s"(so_) : "v"(hv[(((I) >> 4) + 1) & 3]), "s"(rws), "n"(((I) & 15) * 4096) : "memory"); }
        sfor<W0>([&](auto I) INL { LOAD_H(I) SCHED(); });
        f32x4 acc[4][4], part[12];
        sfor<64>([&](auto I) INL {
          constexpr int i = I, b = ((i >> 4) + 1) & 3, k = i & 15;
          constexpr int n0 = issued(i, 64, W0, WIN), n1 = issued(i + 1, 64, W0, WIN);
          constexpr int np = i <= 54 ? 0 : i <= 62 ? i - 54 : 10;
          asm volatile("s_waitcnt vmcnt(%1)" : "+v"(af[i]) : "n"(n0 - 1 - i + np) : "memory");
          if constexpr (i == 0) KEEP(late);
          SCHED();
          sfor<4>([&](auto A) INL {
            constexpr int a = A;
            acc[a][b] = mfma16(wz[k][a], af[i], k > 0 ? acc[a][b] : (f32x4){0.f, 0.f, 0.f, 0.f});
            SCHED();
            if constexpr (n0 < n1 && a == 0) LOAD_H(n0)
            if constexpr (n0 + 1 < n1 && a == 3) LOAD_H(n0 + 1)
            if constexpr (i < 8) { if constexpr (a == 1) DTAIL1(YG, YE, 2 * i) if constexpr (a == 2) DTAIL1(YG, YE, 2 * i + 1) }
            if constexpr (i >= 48 && k < 2 && (a & 1)) REDW(2 * k + (a >> 1), 3);
            if constexpr (i >= 16 && i < 48 && (k & 3) == 1 && a == 2) REDW(k >> 2, i >> 4);
            if constexpr (i == 50 && a == 3) bar();
            if constexpr (i >= 51 && i < 54) part[4 * (i - 51) + a] = REDR((4 * (i - 51) + a) / 3, (4 * (i - 51) + a) % 3 + 1);
            if constexpr (i == 54 && a == 1) bar();
            if constexpr (i >= 54 && a == 2) PIECE(Z_U, np)
            if constexpr (i >= 62 && a == 3) PIECE(Z_U, np + 1)
            if constexpr (i >= 55 && i < 59 && a == 0) {
              constexpr int q = i - 55;
              part[3 * q] = (part[3 * q] + unpack4(bb[q])) + (part[3 * q + 1] + part[3 * q + 2]);
              PINV(part[3 * q]);
            }
            SCHED();
          });
        });
        STAMP(11 + layer * 16);
        v4u uf[2];
        sfor<8>([&](auto H) INL {
          constexpr int h = H, a = h >> 1, j0 = 2 * (h & 1);
          float gl[2];
#pragma unroll
          for (int j = 0; j < 2; ++j) {
            const float u = acc[a][0][j0 + j] + part[3 * a][j0 + j];
            const float e = __builtin_amdgcn_exp2f(u * (u * u * (-0.10294324f) - 2.3022082f));
            gl[j] = u * __builtin_amdgcn_rcpf(1.f + e);
          }
          uf[h >> 2][h & 3] = pk(gl[0], gl[1]);
          if constexpr (h & 1) {
            PINV(uf[h >> 2]);
            if constexpr (h == 3) st16<SC1>(uf[0], rws, uo, 0);
            if constexpr (h == 7) st16<SC1>(uf[1], rws, uo, 4096);
            SCHED();
          }
        });
        STAMP(40 + layer * 4);
      } else {
        publish(fl + (2 * 256 + blockIdx.x) * 32, epoch);
        DPUMP(YG, YH, YE)
        STASH(SG, NS)
        DRAIN();
        DTAIL(YG, YE)
        LGKM0();
        DPUMP(Z_U, Z_U, Z_U + 3)
      }
      {
        FRESH();
        const int pi = wrap7(lane & 7);
        GAS unsigned* fp = fl + (3 * 256 + (pi == 3 ? (21 + (dt & 3)) * 8 + (dt >> 2) : (7 * (pi & 3) + dt) * 8 + (pi >> 2)) + 2 * w) * 32;
        int uv = U_OFF + layer * U_SZ + (dt * 56 + 14 * w) * 4096 + lane * 16;
        int ow = L_HST + w * 2048 + c * 128 + (swz(c) << 5) + g * 8, orr = L_HST + w * 2048 + ((wrow * 128 + (lane & 7) * 16) ^ sx0);
        int xb = layer ? (64 * st + 16 * w + wrow) * 896 + dt * 128 + (lane & 7) * 16
                       : X2_OFF + (2 * st + (w >> 1)) * 28672 + (4 * dt + (g >> 1)) * 1024 + ((g & 1) * 32 + (w & 1) * 16 + c) * 16;
        int so = oob(g < 2, S5_OFF + (st * 8 + dt) * 512 + (16 * w + c) * 8 + g * 4);
#pragma unroll
        for (int a = 0; a < 4; ++a) { res[a] += bflo(b2r); PINV(res[a]); }
        PINV(fp); PINV(uv); PINV(xb); PINV(red);
        if constexpr (layer) { PINV(ow); PINV(orr); } else PINV(so);
        if constexpr (layer == 1) {
          const int dlt = L_WQ + w * 20480 - (L_KVW + w * 16384);
#pragma unroll
          for (int i = 0; i < 4; ++i) {
            LAS char* ps = pr2[i] + dlt;
#pragma unroll
            for (int j = 0; j < NS; ++j) wz[30 - NS + j][i] = trfrag(ps + j * 4096, ps + j * 4096 + 2048);
          }
          SCHED();
        }
        if (on4) publish(fl + (3 * 256 + bid) * 32, epoch);
        STAMP(12 + layer * 16);
        unsigned pf;
        DPUMP(Z_U, Z_U + 3, YU3)
        POLL(pf, fp);
        DPUMP(Z_U, YU3, YZ)
        unsigned late;
        POLLED(pf, 4 * (YZ - YU3), fp)
        STAMP(13 + layer * 16);
        v4u af[56];
#define LOAD_U(I) af[I] = ld16<SC1>(rws, uv + ((I) / 14) * 1024, ((I) % 14) * 4096)
#pragma unroll
        for (int i = 0; i < W0; ++i) { LOAD_U(i); SCHED(); }
        vmw<W0>();
        KEEP(late);
        f32x4 acc[4][4], part[4][3];
        v2u xo[4];
        f32x2 s1 = {0.f, 0.f}, s2 = {0.f, 0.f};
        sfor<56>([&](auto I) INL {
          constexpr int i = I, a = i / 14, k = i % 14;
#pragma unroll
          for (int bq = 0; bq < 4; ++bq) {
            const int b = (bq + 1) & 3;
            if (a < 3 || b) acc[a][b] = mfma16(af[i], wz[16 + k][b], k > 0 ? acc[a][b] : (f32x4){0.f, 0.f, 0.f, 0.f});
            SCHED();
            constexpr int n0 = issued(i, 56, W0, WIN), n1 = issued(i + 1, 56, W0, WIN);
            if constexpr (n0 < n1) { if (bq == 0) LOAD_U(n0); }
            if constexpr (n0 + 1 < n1) { if (bq == 3) LOAD_U(n0 + 1); }
            if constexpr (i < 8) { if (bq == 1) DTAIL1(Z_U, YZ, 2 * i) if (bq == 2) DTAIL1(Z_U, YZ, 2 * i + 1) }
            if constexpr (a > 0 && k < 3) { if (bq == 2) REDW(a - 1, k + 1); }
            if constexpr (i == 46) { if (bq == 2) bar(); }
            if constexpr (i >= 47 && i < 50) { if (bq < 3) part[i - 47][bq] = REDR(i - 47, bq + 1); }
            if constexpr (i >= 50 && i < 53) { if (bq == 2) { res[i - 50] = ((res[i - 50] + part[i - 50][0]) + (part[i - 50][1] + part[i - 50][2])) + acc[i - 50][0]; PINV(res[i - 50]); } }
            if constexpr (i >= 53) { if (bq == 2) { xo[i - 53] = (v2u){pk(res[i - 53][0], res[i - 53][1]), pk(res[i - 53][2], res[i - 53][3])}; PINV(xo[i - 53]); if constexpr (layer == 0) { stats16(res, 1 << (i - 53), s1, s2); PINV(s1); PINV(s2); } } }
            SCHED();
          }
        });
        STAMP(14 + layer * 16);
        sfor<14>([&](auto K) INL {
          constexpr int k = K;
          acc[3][0] = mfma16(af[42 + k], wz[16 + k][0], k > 0 ? acc[3][0] : (f32x4){0.f, 0.f, 0.f, 0.f});
          SCHED();
          if constexpr (k >= 1 && k < 4) REDW(3, k);
          if constexpr (k == 5) {
            if constexpr (layer == 0) st16<SC1>((v4u){xo[0].x, xo[0].y, xo[1].x, xo[1].y}, rws, xb, 0);
            else { _Pragma("unroll") for (int a = 0; a < 3; ++a) *(LAS v2u*)(L + (ow ^ (a << 5))) = xo[a]; }
          }
          if constexpr (k == 7) { bar(); part[3][0] = REDR(3, 1); part[3][1] = REDR(3, 2); part[3][2] = REDR(3, 3); }
          SCHED();
        });
        res[3] = ((res[3] + part[3][0]) + (part[3][1] + part[3][2])) + acc[3][0];
        xo[3] = (v2u){pk(res[3][0], res[3][1]), pk(res[3][2], res[3][3])};
        if constexpr (layer == 0) {
          st16<SC1>((v4u){xo[2].x, xo[2].y, xo[3].x, xo[3].y}, rws, xb, 2048);
          SCHED();
          stats16(res, 8, s1, s2);
          __builtin_amdgcn_raw_buffer_store_b32(asu(sum_ab(s1[0] + s1[1], s2[0] + s2[1])), rws, so, 0, SC1);
          SCHED();
        } else {
          const rsrc_t ro = mk(p.out, S * D * 2);
          *(LAS v2u*)(L + (ow ^ (3 << 5))) = xo[3];
          const v4u r0 = *(LAS v4u*)(L + orr), r1 = *(LAS v4u*)(L + (orr ^ 64) + 1024);
          st16<SC1>(r0, ro, xb, 0);
          st16<SC1>(r1, ro, xb, 8 * 896);
        }
      }
    } else {
      publish(fl + (1 * 256 + blockIdx.x) * 32, epoch);
      FRESH();
      const int rw = 4 * (by - 28) + (lane >> 4) + 16 * ((lane >> 2) & 3);
      const int v1 = (512 * w + 256 + rw) * (F * 2) + (7 * (bx >> 1) + 3 * (bx & 1) + (lane & 3)) * 128;
#pragma unroll 1
      for (int i = layer * (KP / 2); i < 4; ++i) { touch(mk(p.w1, S * F * 2), v1, i * 64 * F * 2, L); vmw<1>(); }
      const int v2 = (448 * w + rw) * (S * 2) + bx * 512 + (lane & 3) * 128;
#pragma unroll 1
      for (int i = 0; i < (layer ? 7 - (Z_U - 16) / 2 : 7); ++i) { touch(mk(p.w2, S * F * 2), v2, (layer ? (i < 3 ? i + 4 : i - 3 + (Z_U - 16) / 2) : i) * 64 * S * 2, L); vmw<1>(); }
      DRAIN();
    }
    if constexpr (layer == 1) STAMP(31);
    if constexpr (layer == 0) {
      FRESH();
      const int st1 = 2 * t + (w >> 1);
      f32x16 ak, av;
      v4u wk[28], wv[28];
      LAS char* wb = WB(0) + 28672;
      PINV(wb);
      wk[0] = WFRAG(wb, 0, 0); wv[0] = WFRAG(wb, 1, 0); wk[1] = WFRAG(wb, 0, 1); wv[1] = WFRAG(wb, 1, 1);
      f32x4 ck[4], cv[4];
#pragma unroll
      for (int a = 0; a < 4; ++a) { ck[a] = CSUM(1, a); cv[a] = CSUM(2, a); PINV(ck[a]); PINV(cv[a]); }
      int xo = X2_OFF + (4 * t + w) * 28672 + lane * 16, so = S5_OFF + (st1 * 8 + hh * 4) * 512 + ((w & 1) * 32 + r32) * 8;
      int so3 = oob(!hh, so + 3 * 512);
      PINV(xo); PINV(so); PINV(so3);
      kvs += KV_SZ; oo += O_SZ; fo += 5 * 256 * 128;
      PINV(kvs); PINA(oo); PINA(fo);
      GAS unsigned* fp = flags + (4 * 256 + ((st1 & 3) * 7 + wrap7(lane & 7)) * 8 + (st1 >> 2)) * 32;
      PINV(fp);
      if (on35) publish(flags + (4 * 256 + bid) * 32, epoch);
      STAMP(15);
      unsigned pf, late;
      {
        const rsrc_t rwo = mk(p.wo, on35 ? D * D * 2 : 0);
        sfor<16>([&](auto I) INL {
          constexpr int wi = I;
          if constexpr (wi == 10) POLL(pf, fp);
          dmai<wi * 1024, (28 * (wi >> 2) + 8 * (wi & 3)) * 896>(rwo, (wi & 1) ? wvo2 ^ 64 : wvo2, pring);
        });
      }
      POLLED(pf, 6, fp)
      STAMP(18);
      v2u sp[4];
#define LOAD_X(I) xf[I] = ld16<SC1>(rws, xo + ((I) & 3) * 1024, ((I) >> 2) * 4096)
#pragma unroll
      for (int i = 0; i < WX0; ++i) { LOAD_X(i); SCHED(); }
      sfor<28>([&](auto I) INL {
        constexpr int ks = I;
        if constexpr (ks == 0) MFMA32V0(ak, wk[ks], xf[ks]); else MFMA32VA(ak, wk[ks], xf[ks]);
        if constexpr (ks + 2 < 28) wk[ks + 2] = WFRAG(wb + ((ks + 2) >> 2) * 4096, 0, (ks + 2) & 3);
        constexpr int n0 = issued(ks, 28, WX0, WINX), n1 = issued(ks + 1, 28, WX0, WINX);
        if constexpr (n0 < n1) LOAD_X(n0);
        SCHED();
        if constexpr (ks == 0) MFMA32V0(av, wv[ks], xf[ks]); else MFMA32VA(av, wv[ks], xf[ks]);
        if constexpr (ks + 2 < 28) wv[ks + 2] = WFRAG(wb + ((ks + 2) >> 2) * 4096, 1, (ks + 2) & 3);
        if constexpr (n0 + 1 < n1) LOAD_X(n0 + 1);
        if constexpr (ks >= 6 && ks < 10) sp[ks - 6] = ld8<SC1>(rws, ks == 9 ? so3 : so + (ks - 6) * 512, 0);
        if constexpr (ks == 2) KEEP(late);
        if constexpr (ks == 21) {
          float s1 = (asf(sp[0].x) + asf(sp[1].x)) + (asf(sp[2].x) + asf(sp[3].x));
          float s2 = (asf(sp[0].y) + asf(sp[1].y)) + (asf(sp[2].y) + asf(sp[3].y));
          s1 = sum32(s1); s2 = sum32(s2);
          mean = s1 * (1.f / D);
          rstd = s2 * (1.f / D) - mean * mean;
          PINV(mean); PINV(rstd);
        }
        if constexpr (ks == 22) { rstd = __builtin_amdgcn_rsqf(fmaxf(rstd, 0.f) + EPS); PINV(rstd); }
        if constexpr (ks >= 23 && ks < 27) { ck[ks - 23] *= -mean * rstd; cv[ks - 23] *= -mean * rstd; PINV(ck[ks - 23]); PINV(cv[ks - 23]); }
        SCHED();
      });
      MFMA_SETTLE2(ak, av);
#pragma unroll
      for (int i = 0; i < 16; ++i) ak[i] = ak[i] * rstd + ck[i >> 2][i & 3];
      KVOUT(ak, 0)
#pragma unroll
      for (int i = 0; i < 16; ++i) av[i] = av[i] * rstd + cv[i >> 2][i & 3];
      KVOUT(av, 1)
    }
  });
  asm volatile(".Lfk_code_to:");
  if (blockIdx.x == 0 && threadIdx.x == 0) __hip_atomic_store(ctrl, epoch, RLX_AGENT);
}

extern "C" void kernel_launch(void* const* d_in, const int* in_sizes, int n_in, void* d_out, int out_size, void* d_ws,
                              size_t ws_size, hipStream_t stream) {
  Params p;
  p.x = d_in[0]; p.wqkv = d_in[1]; p.wo = d_in[2];
  p.w1 = d_in[4]; p.b1 = d_in[5]; p.w2 = d_in[6]; p.b2 = d_in[7];
  p.out = d_out; p.ws = d_ws;
  (void)hipFuncSetAttribute((const void*)fused_kernel, hipFuncAttributeMaxDynamicSharedMemorySize, LDS_BYTES);
  fused_kernel<<<256, 256, LDS_BYTES, stream>>>(p);
}
```

```cpp
#include <hip/hip_runtime.h>
#include <utility>

#define GAS __attribute__((address_space(1)))
#define LAS __attribute__((address_space(3)))
#define DI __device__ __forceinline__
#define INL __attribute__((always_inline))
#define RLX_AGENT __ATOMIC_RELAXED, __HIP_MEMORY_SCOPE_AGENT
typedef unsigned v4u __attribute__((ext_vector_type(4)));
typedef unsigned v2u __attribute__((ext_vector_type(2)));
typedef float f32x2 __attribute__((ext_vector_type(2)));
typedef float f32x4 __attribute__((ext_vector_type(4)));
typedef float f32x16 __attribute__((ext_vector_type(16)));
typedef short s16x4 __attribute__((ext_vector_type(4)));
typedef __bf16 bf16x2 __attribute__((ext_vector_type(2)));
typedef __bf16 bf16x8 __attribute__((ext_vector_type(8)));
typedef __amdgpu_buffer_rsrc_t rsrc_t;

constexpr int S = 2048, D = 448, F = 1792;
constexpr float EPS = 1e-5f;
constexpr int FLAG_OFF = 4096;
constexpr int KV_OFF = 512 * 1024, KV_SZ = 16 * 64 * 4096;
constexpr int O_OFF = KV_OFF + 2 * KV_SZ, O_SZ = 32 * 16 * 4096;
constexpr int S3_OFF = O_OFF + 2 * O_SZ, S3_SZ = 32 * 8 * 1024;
constexpr int HT_OFF = S3_OFF + 2 * S3_SZ, HT_SZ = 7 * 64 * 4096;
constexpr int U_OFF = HT_OFF + 2 * HT_SZ, U_SZ = 7 * 56 * 4096;
constexpr int X2_OFF = U_OFF + 2 * U_SZ, X2_SZ = 64 * 28 * 1024;
constexpr int S5_OFF = X2_OFF + X2_SZ, S5_SZ = 32 * 8 * 512;
constexpr int S0_OFF = S5_OFF + S5_SZ, S0_SZ = S * 16;
constexpr int WS_BYTES = S0_OFF + S0_SZ;
constexpr int L_WQ = 0;
constexpr int L_KVW = 86016;
constexpr int L_CSP = L_KVW + 65536;
constexpr int L_CS = L_CSP + 1536 + 128;
constexpr int L_HST = L_CS + 384;
constexpr int L_DUMP = L_HST + 8192;
constexpr int LDS_BYTES = L_DUMP + 256;

static_assert(L_HST % 128 == 0 && LDS_BYTES <= 163840, "LDS map");
constexpr int SC1 = 16, OOB = 0x7ffff000;
constexpr int WIN = 12;
constexpr int NO2 = 4, NO3 = 2, NG2 = 3, NG3 = 2, NH2 = 4, NH3 = 3, NU3 = 2;
constexpr int KP = 4, MO2 = 4, MO3 = 1, MG2 = 3, MG3 = 1, NS = 5, SG = 1, SH = 2;
constexpr int W0 = 10, WINO = 9, WO1 = 9, WX0 = 9, WINX = 10;
constexpr int issued(int i, int n, int w0, int w) { const int c = w0 + i + (i < w - w0 ? i : w - w0); return c < n ? c : n; }
constexpr int Z_U = NO2 + NO3 + NG2 + NG3 + NH2 + NH3;
static_assert(Z_U >= 16 && Z_U + 3 + NU3 <= 30 && Z_U + 3 <= 30 - NS && NS <= 5 && SG + SH <= NS, "schedule");

struct Params {
  const void *x, *wqkv, *wo, *w1, *b1, *w2, *b2;
  void *out, *ws;
};

template <int... I, class Fn> DI void sfor_(std::integer_sequence<int, I...>, Fn&& f) { (f(std::integral_constant<int, I>{}), ...); }
template <int N, class Fn> DI void sfor(Fn&& f) { sfor_(std::make_integer_sequence<int, N>{}, f); }
DI float asf(unsigned u) { return __builtin_bit_cast(float, u); }
DI unsigned asu(float f) { return __builtin_bit_cast(unsigned, f); }
DI rsrc_t mk(const void* p, int bytes) { return __builtin_amdgcn_make_buffer_rsrc((void*)p, 0, bytes, 0x00020000); }
template <int AUX> DI v4u ld16(rsrc_t r, int voff, int soff) { return __builtin_amdgcn_raw_buffer_load_b128(r, voff, soff, AUX); }
template <int AUX> DI v2u ld8(rsrc_t r, int voff, int soff) { return __builtin_amdgcn_raw_buffer_load_b64(r, voff, soff, AUX); }
template <int AUX> DI void st16(v4u v, rsrc_t r, int voff, int soff) { __builtin_amdgcn_raw_buffer_store_b128(v, r, voff, soff, AUX); }
template <int AUX> DI void st8(v2u v, rsrc_t r, int voff, int soff) { __builtin_amdgcn_raw_buffer_store_b64(v, r, voff, soff, AUX); }
DI f32x4 mfma16(v4u a, v4u b, f32x4 c) {
  return __builtin_amdgcn_mfma_f32_16x16x32_bf16(__builtin_bit_cast(bf16x8, a), __builtin_bit_cast(bf16x8, b), c, 0, 0, 0);
}
DI f32x16 mfma32(v4u a, v4u b, f32x16 c) {
  return __builtin_amdgcn_mfma_f32_32x32x16_bf16(__builtin_bit_cast(bf16x8, a), __builtin_bit_cast(bf16x8, b), c, 0, 0, 0);
}
DI unsigned pk(float lo, float hi) { return __builtin_bit_cast(unsigned, __builtin_convertvector((f32x2){lo, hi}, bf16x2)); }
DI float bflo(unsigned u) { return asf(u << 16); }
DI float bfhi(unsigned u) { return asf(u & 0xffff0000u); }
DI f32x4 unpack4(v2u v) { return (f32x4){bflo(v.x), bfhi(v.x), bflo(v.y), bfhi(v.y)}; }
DI v2u trrd(LAS char* p) { return __builtin_bit_cast(v2u, __builtin_amdgcn_ds_read_tr16_b64_v4i16((LAS s16x4*)p)); }
DI v4u trfrag(LAS char* p0, LAS char* p1) { v2u a = trrd(p0), b = trrd(p1); return (v4u){a.x, a.y, b.x, b.y}; }
DI float hi32(float v) {
  unsigned u = asu(v);
  auto r = __builtin_amdgcn_permlane32_swap(u, u, false, false);
  return asf(r[1]);
}
DI float sum32(float v) {
  unsigned u = asu(v);
  auto r = __builtin_amdgcn_permlane32_swap(u, u, false, false);
  return asf(r[0]) + asf(r[1]);
}
DI float max32(float v) {
  unsigned u = asu(v);
  auto r = __builtin_amdgcn_permlane32_swap(u, u, false, false);
  return fmaxf(asf(r[0]), asf(r[1]));
}
DI float sum_ab(float a, float b) {
  auto r = __builtin_amdgcn_permlane16_swap(asu(a), asu(b), false, false);
  return sum32(asf(r[0]) + asf(r[1]));
}
template <int CTRL> DI float dpp_add(float v) { return v + asf((unsigned)__builtin_amdgcn_update_dpp(0, (int)asu(v), CTRL, 0xf, 0xf, true)); }
DI float sum_row(float v) { return dpp_add<0x140>(dpp_add<0x141>(dpp_add<0x4e>(dpp_add<0xb1>(v)))); }
DI float dot2(unsigned a, unsigned b, float c) { return __builtin_amdgcn_fdot2_f32_bf16(__builtin_bit_cast(bf16x2, a), __builtin_bit_cast(bf16x2, b), c, false); }
DI void stats16(const f32x4 (&v)[4], int n, f32x2& s1, f32x2& s2) {
#pragma unroll
  for (int a = 0; a < 4; ++a)
    if (n >> a & 1) {
      const f32x2 lo = {v[a][0], v[a][1]}, hi = {v[a][2], v[a][3]};
      s1 += lo; s2 += lo * lo; s1 += hi; s2 += hi * hi;
    }
}
#define FENCE() asm volatile("" ::: "memory")
#define SCHED() __builtin_amdgcn_sched_barrier(0)
#define DRAIN() asm volatile("s_waitcnt vmcnt(0)" ::: "memory")
#define PINV(x) asm volatile("" : "+v"(x))
#define PINA(x) asm volatile("" : "+a"(x))
#define PINS(x) asm volatile("" : "+s"(x))
#define KEEP(x) asm volatile("" :: "v"(x))
#define LGKM0() asm volatile("s_waitcnt lgkmcnt(0)" ::: "memory")
DI void bar() { LGKM0(); __builtin_amdgcn_s_barrier(); FENCE(); }
template <int N> DI void vmw() { asm volatile("s_waitcnt vmcnt(%0)" :: "n"(N) : "memory"); }
template <int LO, int MO> DI void dmai(rsrc_t r, int voff, int lds) {
  int so;
  asm volatile("s_add_u32 m0, %1, %4\n\ts_mov_b32 %0, %5\n\tbuffer_load_dwordx4 %2, %3, %0 offen lds"
               : "=&s"(so) : "s"(lds), "v"(voff), "s"(r), "n"(LO), "n"(MO) : "memory", "scc");
}
DI void touch(rsrc_t r, int voff, int soff, LAS char* L) {
  asm volatile("s_mov_b32 m0, %0\n\ts_nop 3\n\tbuffer_load_dword %1, %2, %3 offen lds"
               :: "s"((int)(size_t)(L + L_DUMP)), "v"(voff), "s"(r), "s"(soff) : "memory");
}
DI void publish(GAS unsigned* flag, unsigned epoch) {
  DRAIN();
  __builtin_amdgcn_s_waitcnt(0x0F70);
  bar();
  if (threadIdx.x == 0) __hip_atomic_store(flag, epoch, RLX_AGENT);
  FENCE();
}
#define POLL(f, ptr) asm volatile("global_load_dword %0, %1, off sc1" : "=&v"(f) : "v"(ptr) : "memory")
#define POLLED(f, N, ptr) { \
    unsigned f2_; \
    POLL(f2_, ptr); \
    asm volatile("s_waitcnt vmcnt(%1)" : "+v"(f) : "n"((N) + 1) : "memory"); \
    if (__builtin_amdgcn_ballot_w64(f != epoch) != 0ull) { \
      asm volatile("s_waitcnt vmcnt(0)" : "+v"(f2_) :: "memory"); \
      if (__builtin_amdgcn_ballot_w64(f2_ != epoch) != 0ull) wait_flags(ptr, epoch, budget); \
    } \
    FENCE(); \
    late = f2_; }
DI void wait_flags(GAS unsigned* flag, unsigned epoch, unsigned& budget) {
  FENCE();
  while (true) {
    unsigned f = __hip_atomic_load(flag, RLX_AGENT);
    if (__builtin_amdgcn_ballot_w64(f != epoch) == 0ull) break;
    if (budget == 0u) break;
    --budget;
  }
  __builtin_amdgcn_fence(__ATOMIC_ACQUIRE, "wavefront");
  FENCE();
}

DI int oob(bool in, int off) { return off | (in ? 0 : OOB); }
DI int wrap7(int v) { return v >= 7 ? v - 7 : v; }
DI int swz(int row) { return ((row >> 1) & 1) | (((row >> 3) ^ (row >> 2)) & 1) << 1; }

#define FRESH() \
  int tid = threadIdx.x, bid = blockIdx.x; \
  asm volatile("" : "+v"(tid)); \
  asm volatile("" : "+s"(bid)); \
  const int lane = tid & 63, w = __builtin_amdgcn_readfirstlane(tid >> 6), bx = bid & 7, by = bid >> 3; \
  const int c = lane & 15, g = lane >> 4, q4 = (lane & 15) >> 2, p4 = lane & 3, r32 = lane & 31, hh = lane >> 5; \
  const int by7 = __builtin_amdgcn_readfirstlane((by * 37) >> 8), bym = __builtin_amdgcn_readfirstlane(by - 7 * by7); \
  const int t = 2 * bx + (by >> 4), hd = by & 15;                \
  const int st = 4 * bx + by7, dt = bym;                         \
  const int dt4 = by < 21 ? bym : bym + 4 * (bx & 1), ft = 7 * (bx >> 1) + (by < 21 ? by7 + 4 * (bx & 1) : 3);    \
  LAS char* ring = L + L_KVW + w * 16384; \
  const int wrow = lane >> 3, sx0 = swz(wrow) << 5; \
  auto raddr = [&](int row) { return row * 128 + (swz(row) << 5) + 8 * p4; }; \
  LAS char* red = L + L_KVW + lane * 16; \
  (void)c; (void)g; (void)q4; (void)r32; (void)hh; (void)t; (void)hd; (void)st; (void)dt; (void)dt4; (void)ft; (void)ring; (void)sx0; (void)raddr; (void)red;

#define MFMA32V0(d, a, b) asm volatile("v_mfma_f32_32x32x16_bf16 %0, %1, %2, 0" : "=&v"(d) : "v"(a), "v"(b))
#define MFMA32VC(d, a, b, c) asm volatile("v_mfma_f32_32x32x16_bf16 %0, %1, %2, %3" : "=&v"(d) : "v"(a), "v"(b), "v"(c))
#define MFMA32VA(d, a, b) asm volatile("v_mfma_f32_32x32x16_bf16 %0, %1, %2, %0" : "+v"(d) : "v"(a), "v"(b))
#define MFMA_SETTLE(d) asm volatile("s_nop 11" : "+v"(d))
#define MFMA_SETTLE2(d, e) asm volatile("s_nop 11" : "+v"(d), "+v"(e))
DI float max3(float a, float b, float c) { float r; asm("v_max3_f32 %0, %1, %2, %3" : "=v"(r) : "v"(a), "v"(b), "v"(c)); return r; }

#define WB(cc) (L + L_WQ + (4 * hh + q4) * 64 + (g & 1) * 32 + 8 * p4 + (hd & 1) * 8 + (cc) * 4096)
#define WFRAG(wb, part, e) trfrag((wb) + (part) * 28672 + ((e) >> 1) * 2048 + ((e) & 1) * 512, (wb) + (part) * 28672 + ((e) >> 1) * 2048 + ((e) & 1) * 512 + 1024)
#define CSUM(part, a) (*(LAS f32x4*)(L + L_CS + ((part) * 32 + 8 * (a) + 4 * hh) * 4))
#define WZ_RD1(n, i, sl) wz[n][i] = (n) < 16 ? trfrag(pr1[i] + (sl) * 4096, pr1[(i) ^ 2] + (sl) * 4096 + 512) : trfrag(pr2[i] + (sl) * 4096, pr2[i] + (sl) * 4096 + 2048)
#define DMA_WZ1(n, m, sl) { \
    if ((n) < 16) dmai<(sl) * 4096 + (m) * 1024, (32 * ((n) & 15) + 8 * (m)) * F * 2>(rw1, ((m) & 1) ? pw1 ^ 64 : pw1, pring); \
    else dmai<(sl) * 4096 + (m) * 1024, (32 * ((n) & 15) + 8 * (m)) * S * 2>(rw2, ((m) & 1) ? pw2 ^ 64 : pw2, pring); }
#define PIECE(L0, q) { \
    constexpr int r_ = (q) / 4, m_ = (q) % 4, n_ = (L0) + r_; \
    if constexpr (m_ == 0 && r_ >= 4) asm volatile("s_waitcnt vmcnt(8) lgkmcnt(0)" ::: "memory"); \
    else if constexpr (m_ == 0 && r_ == 3) vmw<8>(); \
    DMA_WZ1(n_, m_, r_ & 3) \
    if constexpr (r_ >= 3) WZ_RD1(n_ - 3, m_, (r_ - 3) & 3); \
    SCHED(); }
#define DPUMP(L0, A, B) sfor<4 * ((B) - (A))>([&](auto I_) INL { PIECE(L0, 4 * ((A) - (L0)) + I_) });
#define STASH(A, B) if constexpr (layer == 1) sfor<4 * ((B) - (A))>([&](auto I_) INL { \
    constexpr int j_ = (A) + I_ / 4, m_ = I_ % 4; \
    dmai<j_ * 4096 + m_ * 1024, (32 * (14 - NS + j_) + 8 * m_) * S * 2>(rw2, (m_ & 1) ? pw2 ^ 64 : pw2, pstash); SCHED(); });
#define DTSTEP(L0, L1, sl) ((L1) - 1 - (((L1) - 1 - (L0) - (sl)) & 3))
#define DTAIL1(L0, L1, j) { if constexpr (DTSTEP(L0, L1, (j) >> 2) >= (L0) && DTSTEP(L0, L1, (j) >> 2) >= (L1) - 3) WZ_RD1(DTSTEP(L0, L1, (j) >> 2), (j) & 3, (j) >> 2); }
#define DTAIL(L0, L1) sfor<16>([&](auto J_) INL { constexpr int j_ = J_; DTAIL1(L0, L1, j_) }); SCHED();
#define REDW(a, b) *(LAS f32x4*)(red + w * 16384 + (((b) - 1) * 4 + (a)) * 1024) = acc[a][b]
#define REDR(a, b) (*(LAS f32x4*)(red + ((w + 4 - (b)) & 3) * 16384 + (((b) - 1) * 4 + (a)) * 1024))
__global__ void __launch_bounds__(256, 1) fused_kernel(Params p) {
  extern __shared__ __attribute__((aligned(16))) char smem[];
  LAS char* L = (LAS char*)smem;
  {
    unsigned long long pc;
    int len;
    asm volatile("s_getpc_b64 %0\n.Lfk_code_from:\n\ts_mov_b32 %1, .Lfk_code_to-.Lfk_code_from" : "=s"(pc), "=s"(len));
    touch(mk((const void*)pc, len > 0 ? len : 0), (threadIdx.x & 63) < 8 ? ((blockIdx.x >> 3) * 4 + (threadIdx.x >> 6) + (threadIdx.x & 7) * 128) * 128 : OOB, 0, L);
  }
  SCHED();
  const bool on35 = (blockIdx.x >> 3) < 28;
  const bool on4 = (blockIdx.x >> 3) < ((blockIdx.x & 1) ? 24 : 25);
  const rsrc_t rws = mk(p.ws, WS_BYTES);
  GAS unsigned* ctrl = (GAS unsigned*)p.ws;
  GAS unsigned* flags = (GAS unsigned*)((GAS char*)p.ws + FLAG_OFF);
  unsigned budget = 1u << 22;

  v4u xf[28];
  v4u kv[4];
  float mean = 0.f, rstd = 0.f;
  f32x4 res[4];
  unsigned epoch;
  int kvs, oo, t1o, wvo2, pw1, pw2, fo;
  unsigned nk1, nk2, pm[8];
  LAS char *kvw, *vw, *vr0, *wor[4], *pr1[4], *pr2[4], *wbq;
#define KVOUT(acc, h) { \
    _Pragma("unroll") for (int i = 0; i < 16; i += 2) kv[2 * (h) + (i >> 3)][(i >> 1) & 3] = pk(acc[i], acc[i + 1]); \
    if (hh) { kv[2 * (h) + 1][2] = (h) ? 0x00003f80u : 0u; kv[2 * (h) + 1][3] = 0u; } \
    const rsrc_t rkv = mk(p.ws, t < 15 ? WS_BYTES : 0);    \
    st16<SC1>(kv[2 * (h)], rkv, kvs + (h) * 2048, 0); \
    st16<SC1>(kv[2 * (h) + 1], rkv, kvs + (h) * 2048 + 1024, 0); \
    SCHED(); }
#define X128(p) ((LAS char*)(size_t)((unsigned)(size_t)(p) ^ 128u))
  {
    FRESH();
    const rsrc_t rwq = mk(p.wqkv, D * 3 * D * 2);
    const rsrc_t rx = mk(p.x, S * D * 2);
    const int gid = by * 4 + w, rg = (int)(size_t)ring;
    v4u xr0, xr1;
    asm volatile("s_nop 4\n\tbuffer_load_dword %2, %5, %6, 0 offen sc1\n\tbuffer_load_dwordx4 %0, %3, %4, 0 offen nt\n\tbuffer_load_dwordx4 %1, %3, %4, 0 offen offset:896 nt"
                 : "=&v"(xr0), "=&v"(xr1), "=&v"(epoch) : "v"(oob(lane < 56, (t * 128 + hd * 8 + 2 * w) * 896 + lane * 16)), "s"(rx), "v"(0), "s"(rws) : "memory");
    touch(rwq, oob(lane < 14, gid * 2688 + 896 + lane * 128), 0, L);
    const int xvo = (t * 128 + w * 32 + wrow) * 896 + (((lane & 7) ^ (wrow >> 1)) << 4);
    const int wvo = (w * 16 + (lane >> 2)) * 2688 + hd * 56 - (hd & 1) * 8 + (lane & 3) * 16;
    const int wlb = (int)(size_t)(L + L_WQ) + w * 1024;
#define DMA_W(part, cc) dmai<(part) * 28672 + (cc) * 4096, (cc) * 64 * 2688 + (part) * 896>(rwq, wvo, wlb)
#define DMA_C(cc, m) { if constexpr ((m) < 4) dmai<((cc) & 3) * 4096 + ((m) & 3) * 1024, ((m) & 3) * 8 * 896 + (cc) * 128>(rx, ((m) & 1) ? xvo ^ 64 : xvo, rg); \
                       else DMA_W((m) - 3, cc); }
    const int tb = gid * 35 + lane, tbr = (tb * 18725) >> 18;
    int tvb = oob(lane < 35, (128 + tbr) * 2688 + 896 + (tb - 14 * tbr) * 128);
    sfor<6>([&](auto M) INL { constexpr int m = M; DMA_C(0, m) });
    SCHED();
    LAS char* xa[8];
#pragma unroll
    for (int e = 0; e < 8; ++e) { xa[e] = ring + r32 * 128 + 8 * hh + ((e ^ ((r32 >> 1) & 7)) << 4); PINV(xa[e]); }
    int epo = w == 0 ? FLAG_OFF + (bid - 256) * 128 + lane * 32768 : S3_OFF + (st * 8 + dt) * 1024 - 2 * S3_SZ + (lane >> 3) * S3_SZ + (lane & 7) * 128;
    epo = (w == 0 ? lane - 1u < 10u : w == 1 && on35 && (lane >> 4) == 1) ? epo : 0;
    int s0o = oob((lane & 15) == 0, S0_OFF + (t * 128 + hd * 8 + 2 * w) * 16 + g * 8), s0i = S0_OFF + (t * 128 + w * 32 + r32) * 16;
    PINV(s0o); PINV(s0i);
    wbq = WB(0);
    LAS char* wb = wbq + 28672;
    LAS char* cb = L + L_WQ + 28672 + (w * 16 + 8 * hh + q4) * 64 + (g & 1) * 32 + 8 * p4;
    PINV(tvb); PINV(epo); PINV(wbq); PINV(wb); PINV(cb);
#pragma unroll
    for (int i = 0; i < 8; ++i) {
      const int d = r32 - ((2 * i & 3) + 8 * (i >> 1)) - 4 * hh;
      pm[i] = ((unsigned)(d >> 31) & 0xffffu) | ((unsigned)((d - 1) >> 31) & 0xffff0000u);
      PINA(pm[i]);
    }
#pragma unroll
    for (int i = 0; i < 4; ++i) {
      wor[i] = ring + (raddr(16 * (g >> 1) + 4 * (g & 1) + q4) ^ (i << 5));
      pr1[i] = ring + (raddr(8 * g + q4) ^ (i << 5));
      pr2[i] = ring + (raddr(4 * g + q4) ^ (((i + w) & 3) << 5));
      PINA(wor[i]); PINA(pr1[i]); PINA(pr2[i]);
    }
    SCHED();
    sfor<6>([&](auto M) INL { constexpr int m = M; DMA_C(1, m) });
    asm volatile("s_mov_b32 m0, %0\n\ts_nop 3\n\tbuffer_load_dword %1, %2, 0 offen sc1 lds" :: "s"((int)(size_t)(L + L_DUMP)), "v"(epo), "s"(rws) : "memory");
    touch(rwq, tvb, 0, L);
    SCHED();
    {
      asm volatile("s_waitcnt vmcnt(15)" : "+v"(xr0), "+v"(xr1), "+v"(epoch) :: "memory");
      epoch = __builtin_amdgcn_readfirstlane(epoch) + 1u;
      float a1 = 0.f, a2 = 0.f, b1 = 0.f, b2 = 0.f;
#pragma unroll
      for (int j = 0; j < 4; ++j) { a1 = dot2(xr0[j], 0x3f803f80u, a1); a2 = dot2(xr0[j], xr0[j], a2); b1 = dot2(xr1[j], 0x3f803f80u, b1); b2 = dot2(xr1[j], xr1[j], b2); }
      const auto ra = __builtin_amdgcn_permlane16_swap(asu(a1), asu(a2), false, false), rb = __builtin_amdgcn_permlane16_swap(asu(b1), asu(b2), false, false);
      const auto rc = __builtin_amdgcn_permlane32_swap(asu(asf(ra[0]) + asf(ra[1])), asu(asf(rb[0]) + asf(rb[1])), false, false);
      st8<SC1>((v2u){asu(sum_row(asf(rc[0]) + asf(rc[1]))), epoch}, rws, s0o, 0);
      SCHED();
    }
#define READ_X1(cc, e) { \
      constexpr int ch = ((e) >> 1) * 4 + ((e) & 1); \
      const v2u a_ = *(LAS v2u*)(xa[ch] + ((cc) & 3) * 4096), b_ = *(LAS v2u*)(xa[ch + 2] + ((cc) & 3) * 4096); \
      xf[4 * (cc) + (e)] = (v4u){a_.x, a_.y, b_.x, b_.y}; }
    const v4u ones = {0x3f803f80u, 0x3f803f80u, 0x3f803f80u, 0x3f803f80u};
    f32x16 ak, av, csk = {}, csv = {};
    v4u wk[28], wv[28], wc[14], gx;
    float pp[4];
    f32x4 cc[8];
    int tvq, tvw;
    LAS float *pb, *pw;
    sfor<8>([&](auto R) INL {
      constexpr int r = R;
      if constexpr (r < 7) {
        vmw<(r < 2 ? 9 : r == 5 ? 8 : r == 6 ? 4 : 6)>();
        __builtin_amdgcn_s_barrier();
        FENCE();
        SCHED();
      }
      sfor<10>([&](auto Q) INL {
        constexpr int q = Q, e = q < 4 ? q >> 1 : q >= 6 ? (q >> 1) - 1 : -1;
        if constexpr (e >= 0 && r > 0) {
          constexpr int km = 4 * (r - 1) + e;
          if constexpr (q & 1) { if constexpr (km == 0) MFMA32V0(av, wv[km], xf[km]); else MFMA32VA(av, wv[km], xf[km]); }
          else { if constexpr (km == 0) MFMA32V0(ak, wk[km], xf[km]); else MFMA32VA(ak, wk[km], xf[km]); }
        }
        if constexpr (r < 7) {
          if constexpr (q == 4) csk = mfma32(ones, wc[2 * r], csk);
          if constexpr (q == 5) csv = mfma32(ones, wc[2 * r + 1], csv);
          SCHED();
          if constexpr (q < 2) wc[2 * r + q] = trfrag(cb + q * 28672 + r * 4096, cb + q * 28672 + r * 4096 + 256);
          if constexpr (q >= 2) { constexpr int f = q - 2; if constexpr (f & 1) wv[4 * r + f / 2] = WFRAG(wb + r * 4096, 1, f / 2); else wk[4 * r + f / 2] = WFRAG(wb + r * 4096, 0, f / 2); }
          if constexpr (q == 0 || q == 3 || q == 5 || q == 8) READ_X1(r, (q == 0 ? 0 : q == 3 ? 1 : q == 5 ? 2 : 3))
        }
        constexpr int dm = q == 1 ? 0 : q == 2 ? 1 : q == 4 ? 2 : q == 6 ? 3 : q == 7 ? 4 : q == 9 ? 5 : -1;
        if constexpr (r < 5 && dm >= 0) DMA_C(r + 2, dm)
        if constexpr (r == 4 && q == 9) { touch(rwq, tvq, 0, L); touch(mk(p.wo, D * D * 2), tvw, 0, L); }
        if constexpr (r == 6 && q == 0) asm volatile("buffer_load_dwordx4 %0, %1, %2, 0 offen sc1" : "=&v"(gx) : "v"(s0i), "s"(rws) : "memory");
        if constexpr (r == 5 && (q == 6 || q == 9)) DMA_W(0, q / 9);
        if constexpr (r == 6 && q % 3 == 2) DMA_W(0, 2 + q / 3);
        if constexpr (r == 7 && (q == 1 || q == 7)) DMA_W(0, 5 + q / 7);
        if constexpr (q == 3 || q == 8) {
          constexpr int i = 2 * r + (q == 8);
          if constexpr (i == 0) { kvs = KV_OFF + (hd * 64 + 4 * t + w) * 4096 + lane * 16; PINV(kvs); }
          if constexpr (i == 1) { oo = O_OFF + ((2 * t + (w >> 1)) * 16 + hd) * 4096 + (2 * (w & 1) + (r32 >> 4)) * 1024 + hh * 256 + c * 16; PINA(oo); }
          if constexpr (i == 2) { t1o = oob(lane < 32, (512 * (by >> 3) + 8 * ((by & 7) * 4 + w) + (lane >> 2)) * (F * 2) + (7 * (bx >> 1) + 3 * (bx & 1) + (lane & 3)) * 128); PINA(t1o); }
          if constexpr (i == 3) { wvo2 = ((112 * w + wrow) * 896 + dt * 128 + (lane & 7) * 16) ^ sx0; PINA(wvo2); }
          if constexpr (i == 4) { pw1 = ((512 * w + wrow) * (F * 2) + ft * 128 + (lane & 7) * 16) ^ sx0; PINA(pw1); }
          if constexpr (i == 5) { pw2 = ((448 * w + wrow) * (S * 2) + st * 128 + (lane & 7) * 16) ^ sx0; PINA(pw2); }
          if constexpr (i == 6) {
            const int tl = by * 98 + w * 25 + lane, trow = (tl * 9363) >> 16;
            tvq = oob(lane < 25 && w * 25 + lane < 98, trow * 2688 + (tl - 7 * trow) * 128); tvw = oob(lane < 25 && w * 25 + lane < 98, tl * 128);
            PINV(tvq); PINV(tvw);
          }
          if constexpr (i == 7) {
            const int tp = (t >= 2 && (lane & 1)) ? t - 2 : t > 0 ? t - 1 : 0;
            fo = FLAG_OFF + (((tp & 1) * 16 + hd) * 8 + (tp >> 1)) * 128;
            PINA(fo);
          }
          if constexpr (i == 8) { nk2 = (t < 2 && hh) ? 0xf149u : 0u; nk1 = (t < 1 && hh) ? 0xf149u : 0u; PINA(nk1); PINA(nk2); }
          if constexpr (i == 9) { kvw = L + L_KVW + w * 4096 + lane * 16; vw = L + L_KVW + w * 4096 + 2048 + ((lane * 16) ^ (hh << 6)); PINA(kvw); PINA(vw); }
          if constexpr (i == 10) { vr0 = L + L_KVW + w * 4096 + 2048 + ((((g & 1) * 1024 + (p4 & 1) * 512 + hh * 64 + q4 * 16) ^ ((p4 & 1) << 6) ^ ((g & 1) << 7)) + (p4 >> 1) * 8); PINA(vr0); }
          if constexpr (i == 11) {
            pb = (LAS float*)(L + L_CSP) + 32 + lane + 4 * (hd & 1);
            pw = (LAS float*)(L + (hh ? L_DUMP : L_CSP + (w * 96 + 32) * 4)) + r32;
            PINV(pb); PINV(pw);
          }
        }
        if constexpr (r == 6 && q == 7) { pw[0] = csk[0]; pw[32] = csv[0]; }
        if constexpr (r == 6 && q == 9) { bar(); pp[0] = pb[0]; pp[1] = pb[96]; pp[2] = pb[192]; pp[3] = pb[288]; }
        if constexpr (r == 7 && q == 2) *(LAS float*)(L + L_CS + (32 + lane) * 4) = (pp[0] + pp[1]) + (pp[2] + pp[3]);
        if constexpr (r == 7 && (q == 3 || q == 6)) { _Pragma("unroll") for (int j = 0; j < 4; ++j) cc[4 * (q / 6) + j] = CSUM(1, 4 * (q / 6) + j); }
        SCHED();
      });
    });
    asm volatile("s_waitcnt vmcnt(5)" : "+v"(gx) :: "memory");
    while (__builtin_amdgcn_ballot_w64((gx.y != epoch) | (gx.w != epoch)) != 0ull) {
      if (budget == 0u) break;
      --budget;
      asm volatile("buffer_load_dwordx4 %0, %1, %2, 0 offen sc1\n\ts_waitcnt vmcnt(0)" : "=&v"(gx) : "v"(s0i), "s"(rws) : "memory");
    }
    mean = asf(gx.x) * (1.f / D);
    rstd = __builtin_amdgcn_rsqf(fmaxf(asf(gx.z) * (1.f / D) - mean * mean, 0.f) + EPS);
    MFMA_SETTLE2(ak, av);
#pragma unroll
    for (int i = 0; i < 16; ++i) ak[i] = (ak[i] - mean * cc[i >> 2][i & 3]) * rstd;
    KVOUT(ak, 0)
#pragma unroll
    for (int i = 0; i < 16; ++i) av[i] = (av[i] - mean * cc[4 + (i >> 2)][i & 3]) * rstd;
    KVOUT(av, 1)
  }

  unsigned b2r;
  v2u bb[4];
  v4u wz[30][4];
  sfor<2>([&](auto LY) INL {
    constexpr int layer = LY;
    constexpr int Y0 = layer ? KP : 0, Y2 = Y0 + (layer ? MO2 : NO2), YG = Y2 + (layer ? MO3 : NO3), YG2 = YG + (layer ? MG2 : NG2),
                  YH = YG2 + (layer ? MG3 : NG3), YE = layer ? 16 : Z_U, YH2 = layer ? YE : YH + NH2, YZ = layer ? 30 - NS : 30, YU3 = layer ? YZ : 30 - NU3;
    static_assert(YH <= YH2, "schedule");
    constexpr int QPUB = layer ? 14 : 10;
    GAS unsigned* fl = flags + layer * 5 * 256 * 32;
    v4u qf[2];
    v4u wo[4][4];
    v2u rr[4];
    int pring, pstash;
    const rsrc_t rw1 = mk(p.w1, on4 ? S * F * 2 : 0);
    const rsrc_t rw2 = mk(p.w2, on35 ? S * F * 2 : 0);
    {
      const bool hi = threadIdx.x & 32;
      FRESH();
      if constexpr (layer == 0) {
        vmw<4>();
        if (t == 15) vmw<0>();
        __builtin_amdgcn_s_barrier();
        FENCE();
      }
      v4u wc[7];
      f32x16 cs = {};
      LAS char* cb = L + L_WQ + (w * 16 + 8 * hh + q4) * 64 + (g & 1) * 32 + 8 * p4;
      v4u wq[28];
#pragma unroll
      for (int ks = 0; ks < 4; ++ks) wq[ks] = WFRAG(wbq, 0, ks);
      f32x16 aq;
      const rsrc_t rwo = mk(p.wo, on35 ? D * D * 2 : 0);
      pring = (int)(size_t)ring;
      pstash = (int)(size_t)(L + L_WQ) + w * 20480;
      PINS(pring); PINS(pstash);
      GAS unsigned* fp = (GAS unsigned*)((GAS char*)p.ws + fo);
      unsigned pf;
      sfor<28>([&](auto I) INL {
        constexpr int ks = I;
        if constexpr (ks == 0) MFMA32V0(aq, wq[ks], xf[ks]); else MFMA32VA(aq, wq[ks], xf[ks]);
        if constexpr (ks + 4 < 28) wq[ks + 4] = WFRAG(wbq + ((ks + 4) >> 2) * 4096, 0, ks & 3);
        constexpr int wi = layer ? -1 : ks - QPUB - 1;
        if constexpr (wi >= 0 && wi < 16) dmai<wi * 1024, (28 * (wi >> 2) + 8 * (wi & 3)) * 896>(rwo, (wi & 1) ? wvo2 ^ 64 : wvo2, pring);
        if constexpr (layer == 0) {
          if constexpr (ks < 7) wc[ks] = trfrag(cb + ks * 4096, cb + ks * 4096 + 256);
          if constexpr (ks >= 2 && ks < 9) cs = mfma32((v4u){0x3f803f80u, 0x3f803f80u, 0x3f803f80u, 0x3f803f80u}, wc[ks - 2], cs);
          if constexpr (ks == QPUB + 1) { _Pragma("unroll") for (int q = 0; q < 4; ++q) cs[q] = ((LAS float*)(L + L_CSP) + r32 + 4 * (hd & 1))[96 * q]; }
          if constexpr (ks == QPUB + 5) *(LAS float*)(L + L_CS + r32 * 4) = (cs[0] + cs[1]) + (cs[2] + cs[3]);
        }
        if constexpr (ks == 27) POLL(pf, fp);
        if constexpr (ks == QPUB) {
          if constexpr (layer == 0) { *(LAS float*)(L + (hh ? L_DUMP : L_CSP + w * 96 * 4) + r32 * 4) = cs[0]; }
          publish(fl + (0 * 256 + bid) * 32, epoch);
        }
        SCHED();
      });
      f32x4 cq[4];
#pragma unroll
      for (int a = 0; a < 4; ++a) cq[a] = CSUM(0, a);
      MFMA_SETTLE(aq);
      const float qs = rstd * (0.18898223650461362f * 1.4426950408889634f);
#pragma unroll
      for (int a = 0; a < 4; ++a) {
        qf[a >> 1][(a & 1) * 2] = pk((aq[4 * a] - mean * cq[a][0]) * qs, (aq[4 * a + 1] - mean * cq[a][1]) * qs);
        qf[a >> 1][(a & 1) * 2 + 1] = pk((aq[4 * a + 2] - mean * cq[a][2]) * qs, (aq[4 * a + 3] - mean * cq[a][3]) * qs);
      }
      if (hi) { qf[1][2] = 0x00003f80u; qf[1][3] = 0u; }
      PINV(qf[0]); PINV(qf[1]);
      f32x16 s8;
      asm volatile("s_nop 1" : "+v"(qf[0]), "+v"(qf[1]));
      MFMA32V0(s8, kv[0], qf[0]);
      MFMA32VA(s8, kv[1], qf[1]);
      SCHED();
      unsigned late;
      POLLED(pf, 0, fp)
      v4u ra[4], rb[4];
      {
        const rsrc_t r1 = mk(p.ws, t >= 1 ? WS_BYTES : 0), r2 = mk(p.ws, t >= 2 ? WS_BYTES : 0);
        const int kvo = kvs - 32768;
        rb[0] = ld16<SC1>(r2, kvo, 0); rb[1] = ld16<SC1>(r2, kvo, 1024);
        ra[0] = ld16<SC1>(r1, kvo, 16384); ra[1] = ld16<SC1>(r1, kvo, 16384 + 1024);
        SCHED();
        rb[2] = ld16<SC1>(r2, kvo, 2048); rb[3] = ld16<SC1>(r2, kvo, 3072);
        ra[2] = ld16<SC1>(r1, kvo, 16384 + 2048); ra[3] = ld16<SC1>(r1, kvo, 16384 + 3072);
        SCHED();
      }
#pragma unroll
      for (int j = 0; j < 16; ++j) wo[j >> 2][j & 3] = trfrag(wor[j & 3] + (j >> 2) * 4096, wor[(j & 3) ^ 2] + (j >> 2) * 4096 + 1024);
      SCHED();
      bar();
      LAS char* vw1 = X128(vw) + 1024;
      LAS char* vr1 = X128(vr0);
      *(LAS v4u*)(kvw + 32768) = kv[0];
      *(LAS v4u*)(kvw + 32768 + 1024) = kv[1];
      *(LAS v4u*)(vw + 32768) = kv[2];
      *(LAS v4u*)(vw1 + 32768) = kv[3];
      int rro = (64 * st + 16 * w + c) * 896 + dt * 128 + g * 8, b2o = (64 * st + 16 * w + c) * 2, b1o = ft * 128 + g * 8;
      unsigned pml[8], mk32[16], nk1l = nk1, nk2l = nk2;
      int ool = oo, t1l = t1o;
      LAS char* vr0l = vr0;
      if constexpr (layer == 0) { PINV(rro); PINV(b2o); PINV(b1o); }
      PINV(vw1); PINV(vr1); PINV(vr0l); PINV(ool); PINV(t1l); PINV(nk1l); PINV(nk2l);
#pragma unroll
      for (int i = 0; i < 8; ++i) { pml[i] = pm[i]; PINV(pml[i]); mk32[2 * i] = (unsigned)((int)(pml[i] << 16) >> 16); mk32[2 * i + 1] = (unsigned)((int)pml[i] >> 16); PINV(mk32[2 * i]); PINV(mk32[2 * i + 1]); }
      SCHED();
      f32x16 sc[8], se;
      v4u kf[8][2];
      float mx = -1e30f, mx2 = -1e30f;
#define MAX8(v, i) asm volatile("v_max3_f32 %0, %0, %2, %3\n\tv_max3_f32 %1, %1, %4, %5\n\tv_max3_f32 %0, %0, %6, %7\n\tv_max3_f32 %1, %1, %8, %9" \
        : "+v"(mx), "+v"(mx2) : "v"(v[i]), "v"(v[(i) + 1]), "v"(v[(i) + 2]), "v"(v[(i) + 3]), "v"(v[(i) + 4]), "v"(v[(i) + 5]), "v"(v[(i) + 6]), "v"(v[(i) + 7]))
      *(LAS v4u*)(kvw) = rb[0];
      MFMA32V0(sc[0], rb[0], qf[0]);
      rb[1][2] |= nk2l;
      *(LAS v4u*)(kvw + 1024) = rb[1];
      *(LAS v4u*)(kvw + 16384) = ra[0];
      SCHED();
      MFMA32VA(sc[0], rb[1], qf[1]);
      ra[1][2] |= nk1l;
      *(LAS v4u*)(kvw + 16384 + 1024) = ra[1];
      kf[4][0] = ra[0]; kf[4][1] = ra[1];
      MFMA32V0(sc[4], kf[4][0], qf[0]);
      bar();
      KEEP(late);
      MFMA32VA(sc[4], kf[4][1], qf[1]);
#pragma unroll
      for (int j = 1; j < 8; ++j) if (j != 4) { kf[j][0] = *(LAS v4u*)(kvw + j * 4096); kf[j][1] = *(LAS v4u*)(kvw + j * 4096 + 1024); }
      PINV(sc[0]);
#pragma unroll
      for (int i = 0; i < 16; ++i) se[i] = asf((asu(sc[0][i]) & mk32[i]) | (asu(s8[i]) & ~mk32[i]));
      PINV(se);
      SCHED();
      sfor<12>([&](auto N) INL {
        constexpr int n = N, jj = n >> 1, j = jj < 3 ? jj + 1 : jj + 2, hf = n & 1;
        if constexpr (hf == 0) MFMA32V0(sc[j], kf[j][0], qf[0]); else MFMA32VA(sc[j], kf[j][1], qf[1]);
        if constexpr (n < 2) MAX8(se, 8 * hf);
        else if constexpr (n < 4) MAX8(sc[4], 8 * hf);
        else MAX8(sc[jj < 5 ? jj - 1 : jj], 8 * hf);
        if constexpr (n == 2) *(LAS v4u*)(vw) = rb[2];
        if constexpr (n == 3) *(LAS v4u*)(vw1) = rb[3];
        if constexpr (n == 4) *(LAS v4u*)(vw + 16384) = ra[2];
        if constexpr (n == 5) *(LAS v4u*)(vw1 + 16384) = ra[3];
        if constexpr (n == 6) touch(mk(p.w1, S * F * 2), t1l, layer * KP * 32 * F * 2, L);
        if constexpr (n >= 7) asm volatile("s_nop 0");
        SCHED();
      });
      MAX8(sc[6], 0); MAX8(sc[6], 8); MAX8(sc[7], 0); MAX8(sc[7], 8);
      mx = max32(max3(mx, mx2, mx2));
      f32x16 nm;
#pragma unroll
      for (int i = 0; i < 16; ++i) nm[i] = -mx;
      PINV(nm);
      bar();
      f32x16 ao;
      v4u vf[9][2], pp[9][2];
      float ex[32][4];
#define VF(j, s) vf[j][s] = trfrag(vr0l + (j) * 4096 + (s) * 256, vr1 + (j) * 4096 + (s) * 256)
      sfor<33>([&](auto N) INL {
        constexpr int n = N, j = n >> 2, q = n & 3;
        if constexpr (j == 0) {
          if constexpr (q == 0) MFMA32VC(sc[1], kf[1][0], qf[0], nm);
          if constexpr (q == 1) MFMA32VA(sc[1], kf[1][1], qf[1]);
          if constexpr (q == 2) MFMA32VC(sc[2], kf[2][0], qf[0], nm);
          if constexpr (q == 3) MFMA32VA(sc[2], kf[2][1], qf[1]);
        } else if constexpr (j < 8) {
          if constexpr (q == 1) { if constexpr (j == 1) MFMA32V0(ao, vf[0][0], pp[0][0]); else MFMA32VA(ao, vf[j - 1][0], pp[j - 1][0]); }
          if constexpr (q == 2) MFMA32VA(ao, vf[j - 1][1], pp[j - 1][1]);
          if constexpr (j + 2 < 8) {
            if constexpr (q == 0) MFMA32VC(sc[j + 2], kf[j + 2][0], qf[0], nm);
            if constexpr (q == 3) MFMA32VA(sc[j + 2], kf[j + 2][1], qf[1]);
          } else if constexpr (j == 6) {
            if constexpr (q == 0) MFMA32VA(ao, vf[8][0], pp[8][0]);
            if constexpr (q == 3) MFMA32VA(ao, vf[8][1], pp[8][1]);
          }
        } else MFMA32VA(ao, vf[7][0], pp[7][0]);
        SCHED();
        if constexpr (n < 32) {
#pragma unroll
          for (int i = 0; i < 4; ++i) { if constexpr (j == 0) ex[n][i] = __builtin_amdgcn_exp2f(se[4 * q + i] - mx); else ex[n][i] = __builtin_amdgcn_exp2f(sc[j][4 * q + i]); }
        }
        SCHED();
        if constexpr (n > 0) {
          constexpr int jp = (n - 1) >> 2, qp = (n - 1) & 3;
#pragma unroll
          for (int i = 0; i < 2; ++i) {
            const unsigned u = pk(ex[n - 1][2 * i], ex[n - 1][2 * i + 1]);
            if constexpr (jp == 0) { pp[0][qp >> 1][(2 * qp + i) & 3] = u & pml[2 * qp + i]; pp[8][qp >> 1][(2 * qp + i) & 3] = u & ~pml[2 * qp + i]; }
            else pp[jp][qp >> 1][(2 * qp + i) & 3] = u;
          }
        }
        if constexpr (layer == 0) {
          if constexpr (n >= 2 && n < 10 && q >= 2) rr[(n >> 2) * 2 + q - 2] = ld8<0>(mk(p.x, on35 ? S * D * 2 : 0), rro, ((n >> 2) * 2 + q - 2) * 32);
          if constexpr (n == 10) b2r = (unsigned)__builtin_amdgcn_raw_buffer_load_b16(mk(p.b2, S * 2), b2o, 0, 0);
          if constexpr (n >= 14 && n < 22 && q >= 2) bb[((n >> 2) - 3) * 2 + q - 2] = ld8<0>(mk(p.b1, F * 2), b1o, (((n >> 2) - 3) * 2 + q - 2) * 32);
        }
        if constexpr (n == 22) KEEP(nm);
        if constexpr (j < 8 && q < 2) VF(j, q);
        if constexpr (j == 5 && q >= 2) VF(8, q - 2);
        SCHED();
      });
      asm volatile("s_nop 1" : "+v"(pp[7][1]));
      MFMA32VA(ao, vf[7][1], pp[7][1]);
      MFMA_SETTLE(ao);
      const float inv = __builtin_amdgcn_rcpf(hi32(ao[12]));
      v4u of[2];
#pragma unroll
      for (int i = 0; i < 16; i += 2) of[i >> 3][(i >> 1) & 3] = pk(ao[i] * inv, ao[i + 1] * inv);
      if (hi) { of[1][2] = 0u; of[1][3] = 0u; }
      st16<SC1>(of[0], rws, ool, 0);
      st16<SC1>(of[1], rws, ool, 512);
      SCHED();
    }
    if (on35) {
      {
        FRESH();
        const int tq = st >> 1;
        GAS unsigned* fp = fl + (1 * 256 + (((tq & 1) * 16 + 4 * w + (lane & 3)) * 8 + (tq >> 1))) * 32;
        int oo = O_OFF + layer * O_SZ + (st * 16 + 4 * w) * 4096 + lane * 16;
        const int sb = S3_OFF + layer * S3_SZ + st * 8192 + (16 * w + c) * 16;
        int sw = oob(g < 2, sb + dt * 1024 + g * 8), sa = sb + g * 1024, sc = oob(g < 3, sb + (g + 4) * 1024);
        int ho = HT_OFF + layer * HT_SZ + (dt * 64 + 2 * st + (w >> 1)) * 4096 + (g >> 1) * 1024 + (2 * (w & 1) + (g & 1)) * 256 + c * 16;
        const int hr = 8 * (g & 1) + q4, hst = L_HST + w * 2048;
        int h0 = hst + (raddr(hr) ^ ((g >> 1) << 5)), h1 = hst + (raddr(hr + 4) ^ ((g >> 1) << 5)), hw = hst + c * 128 + (swz(c) << 5) + g * 8;
        int ob[4];
#pragma unroll
        for (int b = 0; b < 4; ++b) { ob[b] = ((b + w) & 3) * 1024; PINS(ob[b]); }
        PINV(fp); PINV(oo); PINV(sw); PINV(sa); PINV(sc); PINV(ho); PINV(h0); PINV(h1); PINV(hw); PINV(red);
        publish(fl + (1 * 256 + bid) * 32, epoch);
        unsigned pf;
        DPUMP(Y0, Y0, Y2)
        if constexpr (layer == 0) {
#pragma unroll
          for (int a = 0; a < 4; ++a) { res[a] = unpack4(rr[a]); PINV(res[a]); }
        }
        POLL(pf, fp);
        DPUMP(Y0, Y2, YG)
        unsigned late;
        POLLED(pf, 4 * (YG - Y2), fp)
        v4u of[16];
#define LOAD_O(I) of[I] = ld16<SC1>(rws, oo + ((I) & 3) * 4096, ob[(((I) >> 2) + 1) & 3])
#pragma unroll
        for (int i = 0; i < WINO; ++i) { LOAD_O(i); SCHED(); }
        vmw<WINO>();
        KEEP(late);
        f32x4 acc[4][4], part[12];
        sfor<16>([&](auto I) INL {
          constexpr int i = I, b = ((i >> 2) + 1) & 3, k = i & 3;
#pragma unroll
          for (int a = 0; a < 4; ++a) {
            acc[a][b] = mfma16(wo[k][a], of[i], k > 0 ? acc[a][b] : (f32x4){0.f, 0.f, 0.f, 0.f});
            SCHED();
            constexpr int n0 = issued(i, 16, WINO, WO1), n1 = issued(i + 1, 16, WINO, WO1);
            if constexpr (n0 < n1) { if (a == 0) LOAD_O(n0); }
            if constexpr (n0 + 1 < n1) { if (a == 3) LOAD_O(n0 + 1); }
            if constexpr (i < 12) { if (a == 1) DTAIL1(Y0, YG, i) }
            if constexpr (i >= 12 && k < 2) { if (a == 1 || a == 3) REDW(2 * k + (a >> 1), 3); }
            if constexpr (i >= 4 && i < 12) { if (a == 2) REDW(k, i >> 2); }
            if constexpr (i == 14) { if (a == 0) bar(); }
            if constexpr (i >= 14) { if (a < 3) { const int j = 6 * (i - 14) + 2 * a; part[j] = REDR(j / 3, j % 3 + 1); part[j + 1] = REDR((j + 1) / 3, (j + 1) % 3 + 1); } }
            SCHED();
          }
        });
#pragma unroll
        for (int a = 0; a < 4; ++a) res[a] = ((res[a] + part[3 * a]) + (part[3 * a + 1] + part[3 * a + 2])) + acc[a][0];
        f32x2 s1 = {0.f, 0.f}, s2 = {0.f, 0.f};
        stats16(res, 15, s1, s2);
        st8<SC1>((v2u){asu(sum_ab(s1[0] + s1[1], s2[0] + s2[1])), epoch}, rws, sw, 0);
        SCHED();
        DTAIL1(Y0, YG, 12) DTAIL1(Y0, YG, 13) DTAIL1(Y0, YG, 14) DTAIL1(Y0, YG, 15)
        bar();
        DPUMP(YG, YG, YG2)
        v4u ga, gb;
        asm volatile("buffer_load_dwordx4 %0, %2, %4, 0 offen sc1\n\tbuffer_load_dwordx4 %1, %3, %4, 0 offen sc1" : "=&v"(ga), "=&v"(gb) : "v"(sa), "v"(sc), "s"(rws) : "memory");
        DPUMP(YG, YG2, YH)
        STASH(0, SG)
        asm volatile("s_waitcnt vmcnt(%2)" : "+v"(ga), "+v"(gb) : "n"(4 * (YH - YG2 + layer * SG)) : "memory");
        while ((__builtin_amdgcn_ballot_w64((ga.y != epoch) | (ga.w != epoch)) | (__builtin_amdgcn_ballot_w64((gb.y != epoch) | (gb.w != epoch)) << 16)) != 0ull) {
          if (budget == 0u) break;
          --budget;
          asm volatile("buffer_load_dwordx4 %0, %2, %4, 0 offen sc1\n\tbuffer_load_dwordx4 %1, %3, %4, 0 offen sc1\n\ts_waitcnt vmcnt(0)"
                       : "=&v"(ga), "=&v"(gb) : "v"(sa), "v"(sc), "s"(rws) : "memory");
        }
        const unsigned t12 = asu(sum_ab(asf(ga.x) + asf(gb.x), asf(ga.z) + asf(gb.z)));
        const auto tt = __builtin_amdgcn_permlane16_swap(t12, t12, false, false);
        const float t1 = asf(tt[0]), t2 = asf(tt[1]);
        const float m = t1 * (1.f / D), var = fmaxf(t2 * (1.f / D) - m * m, 0.f);
        const float r1 = __builtin_amdgcn_rsqf(var + EPS);
        const float r = r1 * __builtin_amdgcn_rsqf(var * r1 * r1 + EPS), nr = -m * r;
#pragma unroll
        for (int a = 0; a < 4; ++a) {
          const v2u v = {pk(res[a][0] * r + nr, res[a][1] * r + nr), pk(res[a][2] * r + nr, res[a][3] * r + nr)};
          *(LAS v2u*)(L + (hw ^ (a << 5))) = v;
        }
#pragma unroll
        for (int e = 0; e < 2; ++e) st16<SC1>(trfrag(L + (h0 ^ (e << 6)), L + (h1 ^ (e << 6))), rws, ho, e * 2048);
        SCHED();
      }
      if (on4) {
        FRESH();
        const int sp = 8 * w + (lane & 7);
        GAS unsigned* fp = fl + (2 * 256 + ((sp & 3) * 7 + dt4) * 8 + (sp >> 2)) * 32;
        int hv[4];
#pragma unroll
        for (int b = 0; b < 4; ++b) { hv[b] = HT_OFF + layer * HT_SZ + (dt4 * 64 + 16 * w) * 4096 + ((b + w) & 3) * 1024 + lane * 16; PINV(hv[b]); }
        int uo = U_OFF + layer * U_SZ + (dt4 * 56 + 2 * ft) * 4096 + w * 1024 + lane * 16;
        PINV(fp); PINV(uo); PINV(red);
        publish(fl + (2 * 256 + bid) * 32, epoch);
        unsigned pf;
        DPUMP(YG, YH, YH2)
        STASH(SG, NS - SH)
        POLL(pf, fp);
        DPUMP(YG, YH2, YE)
        STASH(NS - SH, NS)
        unsigned late;
        POLLED(pf, 4 * (YE - YH2 + layer * SH), fp)
        v4u af[64];
#define LOAD_H(I) { int so_; asm volatile("s_mov_b32 %1, %4\n\tbuffer_load_dwordx4 %0, %2, %3, %1 offen sc1" \
          : "=&v"(af[I]), "=&s"(so_) : "v"(hv[(((I) >> 4) + 1) & 3]), "s"(rws), "n"(((I) & 15) * 4096) : "memory"); }
        sfor<W0>([&](auto I) INL { LOAD_H(I) SCHED(); });
        f32x4 acc[4][4], part[12];
        sfor<64>([&](auto I) INL {
          constexpr int i = I, b = ((i >> 4) + 1) & 3, k = i & 15;
          constexpr int n0 = issued(i, 64, W0, WIN), n1 = issued(i + 1, 64, W0, WIN);
          constexpr int np = i <= 54 ? 0 : i <= 62 ? i - 54 : 10;
          asm volatile("s_waitcnt vmcnt(%1)" : "+v"(af[i]) : "n"(n0 - 1 - i + np) : "memory");
          if constexpr (i == 0) KEEP(late);
          SCHED();
          sfor<4>([&](auto A) INL {
            constexpr int a = A;
            acc[a][b] = mfma16(wz[k][a], af[i], k > 0 ? acc[a][b] : (f32x4){0.f, 0.f, 0.f, 0.f});
            SCHED();
            if constexpr (n0 < n1 && a == 0) LOAD_H(n0)
            if constexpr (n0 + 1 < n1 && a == 3) LOAD_H(n0 + 1)
            if constexpr (i < 8) { if constexpr (a == 1) DTAIL1(YG, YE, 2 * i) if constexpr (a == 2) DTAIL1(YG, YE, 2 * i + 1) }
            if constexpr (i >= 48 && k < 2 && (a & 1)) REDW(2 * k + (a >> 1), 3);
            if constexpr (i >= 16 && i < 48 && (k & 3) == 1 && a == 2) REDW(k >> 2, i >> 4);
            if constexpr (i == 50 && a == 3) bar();
            if constexpr (i >= 51 && i < 54) part[4 * (i - 51) + a] = REDR((4 * (i - 51) + a) / 3, (4 * (i - 51) + a) % 3 + 1);
            if constexpr (i == 54 && a == 1) bar();
            if constexpr (i >= 54 && a == 2) PIECE(Z_U, np)
            if constexpr (i >= 62 && a == 3) PIECE(Z_U, np + 1)
            if constexpr (i >= 55 && i < 59 && a == 0) {
              constexpr int q = i - 55;
              part[3 * q] = (part[3 * q] + unpack4(bb[q])) + (part[3 * q + 1] + part[3 * q + 2]);
              PINV(part[3 * q]);
            }
            SCHED();
          });
        });
        v4u uf[2];
        sfor<8>([&](auto H) INL {
          constexpr int h = H, a = h >> 1, j0 = 2 * (h & 1);
          float gl[2];
#pragma unroll
          for (int j = 0; j < 2; ++j) {
            const float u = acc[a][0][j0 + j] + part[3 * a][j0 + j];
            const float e = __builtin_amdgcn_exp2f(u * (u * u * (-0.10294324f) - 2.3022082f));
            gl[j] = u * __builtin_amdgcn_rcpf(1.f + e);
          }
          uf[h >> 2][h & 3] = pk(gl[0], gl[1]);
          if constexpr (h & 1) {
            PINV(uf[h >> 2]);
            if constexpr (h == 3) st16<SC1>(uf[0], rws, uo, 0);
            if constexpr (h == 7) st16<SC1>(uf[1], rws, uo, 4096);
            SCHED();
          }
        });
      } else {
        publish(fl + (2 * 256 + blockIdx.x) * 32, epoch);
        DPUMP(YG, YH, YE)
        STASH(SG, NS)
        DRAIN();
        DTAIL(YG, YE)
        LGKM0();
        DPUMP(Z_U, Z_U, Z_U + 3)
      }
      {
        FRESH();
        const int pi = wrap7(lane & 7);
        GAS unsigned* fp = fl + (3 * 256 + (pi == 3 ? (21 + (dt & 3)) * 8 + (dt >> 2) : (7 * (pi & 3) + dt) * 8 + (pi >> 2)) + 2 * w) * 32;
        int uv = U_OFF + layer * U_SZ + (dt * 56 + 14 * w) * 4096 + lane * 16;
        int ow = L_HST + w * 2048 + c * 128 + (swz(c) << 5) + g * 8, orr = L_HST + w * 2048 + ((wrow * 128 + (lane & 7) * 16) ^ sx0);
        int xb = layer ? (64 * st + 16 * w + wrow) * 896 + dt * 128 + (lane & 7) * 16
                       : X2_OFF + (2 * st + (w >> 1)) * 28672 + (4 * dt + (g >> 1)) * 1024 + ((g & 1) * 32 + (w & 1) * 16 + c) * 16;
        int so = oob(g < 2, S5_OFF + (st * 8 + dt) * 512 + (16 * w + c) * 8 + g * 4);
#pragma unroll
        for (int a = 0; a < 4; ++a) { res[a] += bflo(b2r); PINV(res[a]); }
        PINV(fp); PINV(uv); PINV(xb); PINV(red);
        if constexpr (layer) { PINV(ow); PINV(orr); } else PINV(so);
        if constexpr (layer == 1) {
          const int dlt = L_WQ + w * 20480 - (L_KVW + w * 16384);
#pragma unroll
          for (int i = 0; i < 4; ++i) {
            LAS char* ps = pr2[i] + dlt;
#pragma unroll
            for (int j = 0; j < NS; ++j) wz[30 - NS + j][i] = trfrag(ps + j * 4096, ps + j * 4096 + 2048);
          }
          SCHED();
        }
        if (on4) publish(fl + (3 * 256 + bid) * 32, epoch);
        unsigned pf;
        DPUMP(Z_U, Z_U + 3, YU3)
        POLL(pf, fp);
        DPUMP(Z_U, YU3, YZ)
        unsigned late;
        POLLED(pf, 4 * (YZ - YU3), fp)
        v4u af[56];
#define LOAD_U(I) af[I] = ld16<SC1>(rws, uv + ((I) / 14) * 1024, ((I) % 14) * 4096)
#pragma unroll
        for (int i = 0; i < W0; ++i) { LOAD_U(i); SCHED(); }
        vmw<W0>();
        KEEP(late);
        f32x4 acc[4][4], part[4][3];
        v2u xo[4];
        f32x2 s1 = {0.f, 0.f}, s2 = {0.f, 0.f};
        sfor<56>([&](auto I) INL {
          constexpr int i = I, a = i / 14, k = i % 14;
#pragma unroll
          for (int bq = 0; bq < 4; ++bq) {
            const int b = (bq + 1) & 3;
            if (a < 3 || b) acc[a][b] = mfma16(af[i], wz[16 + k][b], k > 0 ? acc[a][b] : (f32x4){0.f, 0.f, 0.f, 0.f});
            SCHED();
            constexpr int n0 = issued(i, 56, W0, WIN), n1 = issued(i + 1, 56, W0, WIN);
            if constexpr (n0 < n1) { if (bq == 0) LOAD_U(n0); }
            if constexpr (n0 + 1 < n1) { if (bq == 3) LOAD_U(n0 + 1); }
            if constexpr (i < 8) { if (bq == 1) DTAIL1(Z_U, YZ, 2 * i) if (bq == 2) DTAIL1(Z_U, YZ, 2 * i + 1) }
            if constexpr (a > 0 && k < 3) { if (bq == 2) REDW(a - 1, k + 1); }
            if constexpr (i == 46) { if (bq == 2) bar(); }
            if constexpr (i >= 47 && i < 50) { if (bq < 3) part[i - 47][bq] = REDR(i - 47, bq + 1); }
            if constexpr (i >= 50 && i < 53) { if (bq == 2) { res[i - 50] = ((res[i - 50] + part[i - 50][0]) + (part[i - 50][1] + part[i - 50][2])) + acc[i - 50][0]; PINV(res[i - 50]); } }
            if constexpr (i >= 53) { if (bq == 2) { xo[i - 53] = (v2u){pk(res[i - 53][0], res[i - 53][1]), pk(res[i - 53][2], res[i - 53][3])}; PINV(xo[i - 53]); if constexpr (layer == 0) { stats16(res, 1 << (i - 53), s1, s2); PINV(s1); PINV(s2); } } }
            SCHED();
          }
        });
        sfor<14>([&](auto K) INL {
          constexpr int k = K;
          acc[3][0] = mfma16(af[42 + k], wz[16 + k][0], k > 0 ? acc[3][0] : (f32x4){0.f, 0.f, 0.f, 0.f});
          SCHED();
          if constexpr (k >= 1 && k < 4) REDW(3, k);
          if constexpr (k == 5) {
            if constexpr (layer == 0) st16<SC1>((v4u){xo[0].x, xo[0].y, xo[1].x, xo[1].y}, rws, xb, 0);
            else { _Pragma("unroll") for (int a = 0; a < 3; ++a) *(LAS v2u*)(L + (ow ^ (a << 5))) = xo[a]; }
          }
          if constexpr (k == 7) { bar(); part[3][0] = REDR(3, 1); part[3][1] = REDR(3, 2); part[3][2] = REDR(3, 3); }
          SCHED();
        });
        res[3] = ((res[3] + part[3][0]) + (part[3][1] + part[3][2])) + acc[3][0];
        xo[3] = (v2u){pk(res[3][0], res[3][1]), pk(res[3][2], res[3][3])};
        if constexpr (layer == 0) {
          st16<SC1>((v4u){xo[2].x, xo[2].y, xo[3].x, xo[3].y}, rws, xb, 2048);
          SCHED();
          stats16(res, 8, s1, s2);
          __builtin_amdgcn_raw_buffer_store_b32(asu(sum_ab(s1[0] + s1[1], s2[0] + s2[1])), rws, so, 0, SC1);
          SCHED();
        } else {
          const rsrc_t ro = mk(p.out, S * D * 2);
          *(LAS v2u*)(L + (ow ^ (3 << 5))) = xo[3];
          const v4u r0 = *(LAS v4u*)(L + orr), r1 = *(LAS v4u*)(L + (orr ^ 64) + 1024);
          st16<SC1>(r0, ro, xb, 0);
          st16<SC1>(r1, ro, xb, 8 * 896);
        }
      }
    } else {
      publish(fl + (1 * 256 + blockIdx.x) * 32, epoch);
      FRESH();
      const int rw = 4 * (by - 28) + (lane >> 4) + 16 * ((lane >> 2) & 3);
      const int v1 = (512 * w + 256 + rw) * (F * 2) + (7 * (bx >> 1) + 3 * (bx & 1) + (lane & 3)) * 128;
#pragma unroll 1
      for (int i = layer * (KP / 2); i < 4; ++i) { touch(mk(p.w1, S * F * 2), v1, i * 64 * F * 2, L); vmw<1>(); }
      const int v2 = (448 * w + rw) * (S * 2) + bx * 512 + (lane & 3) * 128;
#pragma unroll 1
      for (int i = 0; i < (layer ? 7 - (Z_U - 16) / 2 : 7); ++i) { touch(mk(p.w2, S * F * 2), v2, (layer ? (i < 3 ? i + 4 : i - 3 + (Z_U - 16) / 2) : i) * 64 * S * 2, L); vmw<1>(); }
      DRAIN();
    }
    if constexpr (layer == 0) {
      FRESH();
      const int st1 = 2 * t + (w >> 1);
      f32x16 ak, av;
      v4u wk[28], wv[28];
      LAS char* wb = WB(0) + 28672;
      PINV(wb);
      wk[0] = WFRAG(wb, 0, 0); wv[0] = WFRAG(wb, 1, 0); wk[1] = WFRAG(wb, 0, 1); wv[1] = WFRAG(wb, 1, 1);
      f32x4 ck[4], cv[4];
#pragma unroll
      for (int a = 0; a < 4; ++a) { ck[a] = CSUM(1, a); cv[a] = CSUM(2, a); PINV(ck[a]); PINV(cv[a]); }
      int xo = X2_OFF + (4 * t + w) * 28672 + lane * 16, so = S5_OFF + (st1 * 8 + hh * 4) * 512 + ((w & 1) * 32 + r32) * 8;
      int so3 = oob(!hh, so + 3 * 512);
      PINV(xo); PINV(so); PINV(so3);
      kvs += KV_SZ; oo += O_SZ; fo += 5 * 256 * 128;
      PINV(kvs); PINA(oo); PINA(fo);
      GAS unsigned* fp = flags + (4 * 256 + ((st1 & 3) * 7 + wrap7(lane & 7)) * 8 + (st1 >> 2)) * 32;
      PINV(fp);
      if (on35) publish(flags + (4 * 256 + bid) * 32, epoch);
      unsigned pf, late;
      {
        const rsrc_t rwo = mk(p.wo, on35 ? D * D * 2 : 0);
        sfor<16>([&](auto I) INL {
          constexpr int wi = I;
          if constexpr (wi == 10) POLL(pf, fp);
          dmai<wi * 1024, (28 * (wi >> 2) + 8 * (wi & 3)) * 896>(rwo, (wi & 1) ? wvo2 ^ 64 : wvo2, pring);
        });
      }
      POLLED(pf, 6, fp)
      v2u sp[4];
#define LOAD_X(I) xf[I] = ld16<SC1>(rws, xo + ((I) & 3) * 1024, ((I) >> 2) * 4096)
#pragma unroll
      for (int i = 0; i < WX0; ++i) { LOAD_X(i); SCHED(); }
      sfor<28>([&](auto I) INL {
        constexpr int ks = I;
        if constexpr (ks == 0) MFMA32V0(ak, wk[ks], xf[ks]); else MFMA32VA(ak, wk[ks], xf[ks]);
        if constexpr (ks + 2 < 28) wk[ks + 2] = WFRAG(wb + ((ks + 2) >> 2) * 4096, 0, (ks + 2) & 3);
        constexpr int n0 = issued(ks, 28, WX0, WINX), n1 = issued(ks + 1, 28, WX0, WINX);
        if constexpr (n0 < n1) LOAD_X(n0);
        SCHED();
        if constexpr (ks == 0) MFMA32V0(av, wv[ks], xf[ks]); else MFMA32VA(av, wv[ks], xf[ks]);
        if constexpr (ks + 2 < 28) wv[ks + 2] = WFRAG(wb + ((ks + 2) >> 2) * 4096, 1, (ks + 2) & 3);
        if constexpr (n0 + 1 < n1) LOAD_X(n0 + 1);
        if constexpr (ks >= 6 && ks < 10) sp[ks - 6] = ld8<SC1>(rws, ks == 9 ? so3 : so + (ks - 6) * 512, 0);
        if constexpr (ks == 2) KEEP(late);
        if constexpr (ks == 21) {
          float s1 = (asf(sp[0].x) + asf(sp[1].x)) + (asf(sp[2].x) + asf(sp[3].x));
          float s2 = (asf(sp[0].y) + asf(sp[1].y)) + (asf(sp[2].y) + asf(sp[3].y));
          s1 = sum32(s1); s2 = sum32(s2);
          mean = s1 * (1.f / D);
          rstd = s2 * (1.f / D) - mean * mean;
          PINV(mean); PINV(rstd);
        }
        if constexpr (ks == 22) { rstd = __builtin_amdgcn_rsqf(fmaxf(rstd, 0.f) + EPS); PINV(rstd); }
        if constexpr (ks >= 23 && ks < 27) { ck[ks - 23] *= -mean * rstd; cv[ks - 23] *= -mean * rstd; PINV(ck[ks - 23]); PINV(cv[ks - 23]); }
        SCHED();
      });
      MFMA_SETTLE2(ak, av);
#pragma unroll
      for (int i = 0; i < 16; ++i) ak[i] = ak[i] * rstd + ck[i >> 2][i & 3];
      KVOUT(ak, 0)
#pragma unroll
      for (int i = 0; i < 16; ++i) av[i] = av[i] * rstd + cv[i >> 2][i & 3];
      KVOUT(av, 1)
    }
  });
  asm volatile(".Lfk_code_to:");
  if (blockIdx.x == 0 && threadIdx.x == 0) __hip_atomic_store(ctrl, epoch, RLX_AGENT);
}

extern "C" void kernel_launch(void* const* d_in, const int* in_sizes, int n_in, void* d_out, int out_size, void* d_ws,
                              size_t ws_size, hipStream_t stream) {
  Params p;
  p.x = d_in[0]; p.wqkv = d_in[1]; p.wo = d_in[2];
  p.w1 = d_in[4]; p.b1 = d_in[5]; p.w2 = d_in[6]; p.b2 = d_in[7];
  p.out = d_out; p.ws = d_ws;
  (void)hipFuncSetAttribute((const void*)fused_kernel, hipFuncAttributeMaxDynamicSharedMemorySize, LDS_BYTES);
  fused_kernel<<<256, 256, LDS_BYTES, stream>>>(p);
}
```

```cpp
#define PADI 34
#define PADT 1000
#include <hip/hip_runtime.h>
#include <utility>

#define GAS __attribute__((address_space(1)))
#define LAS __attribute__((address_space(3)))
#define DI __device__ __forceinline__
#define INL __attribute__((always_inline))
#define RLX_AGENT __ATOMIC_RELAXED, __HIP_MEMORY_SCOPE_AGENT
typedef unsigned v4u __attribute__((ext_vector_type(4)));
typedef unsigned v2u __attribute__((ext_vector_type(2)));
typedef float f32x2 __attribute__((ext_vector_type(2)));
typedef float f32x4 __attribute__((ext_vector_type(4)));
typedef float f32x16 __attribute__((ext_vector_type(16)));
typedef short s16x4 __attribute__((ext_vector_type(4)));
typedef __bf16 bf16x2 __attribute__((ext_vector_type(2)));
typedef __bf16 bf16x8 __attribute__((ext_vector_type(8)));
typedef __amdgpu_buffer_rsrc_t rsrc_t;

constexpr int S = 2048, D = 448, F = 1792;
constexpr float EPS = 1e-5f;
constexpr int FLAG_OFF = 4096;
constexpr int KV_OFF = 512 * 1024, KV_SZ = 16 * 64 * 4096;
constexpr int O_OFF = KV_OFF + 2 * KV_SZ, O_SZ = 32 * 16 * 4096;
constexpr int S3_OFF = O_OFF + 2 * O_SZ, S3_SZ = 32 * 8 * 1024;
constexpr int HT_OFF = S3_OFF + 2 * S3_SZ, HT_SZ = 7 * 64 * 4096;
constexpr int U_OFF = HT_OFF + 2 * HT_SZ, U_SZ = 7 * 56 * 4096;
constexpr int X2_OFF = U_OFF + 2 * U_SZ, X2_SZ = 64 * 28 * 1024;
constexpr int S5_OFF = X2_OFF + X2_SZ, S5_SZ = 32 * 8 * 512;
constexpr int S0_OFF = S5_OFF + S5_SZ, S0_SZ = S * 16;
constexpr int DBG_OFF = S0_OFF + S0_SZ;
constexpr int WS_BYTES = DBG_OFF + 256 * 128 * 8;
constexpr int L_WQ = 0;
constexpr int L_KVW = 86016;
constexpr int L_CSP = L_KVW + 65536;
constexpr int L_CS = L_CSP + 1536 + 128;
constexpr int L_HST = L_CS + 384;
constexpr int L_DUMP = L_HST + 8192;
constexpr int LDS_BYTES = L_DUMP + 256;

static_assert(L_HST % 128 == 0 && LDS_BYTES <= 163840, "LDS map");
constexpr int SC1 = 16, OOB = 0x7ffff000;
#define VT_LAYOUT 1
constexpr int WIN = 12;
constexpr int NO2 = 4, NO3 = 2, NG2 = 3, NG3 = 2, NH2 = 4, NH3 = 3, NU3 = 2;
constexpr int KP = 4, MO2 = 4, MO3 = 1, MG2 = 3, MG3 = 1, NS = 5, SG = 1, SH = 2;
constexpr int W0 = 10, WINO = 9, WO1 = 9, WX0 = 9, WINX = 10;
constexpr int issued(int i, int n, int w0, int w) { const int c = w0 + i + (i < w - w0 ? i : w - w0); return c < n ? c : n; }
constexpr int Z_U = NO2 + NO3 + NG2 + NG3 + NH2 + NH3;
static_assert(Z_U >= 16 && Z_U + 3 + NU3 <= 30 && Z_U + 3 <= 30 - NS && NS <= 5 && SG + SH <= NS, "schedule");

struct Params {
  const void *x, *wqkv, *wo, *w1, *b1, *w2, *b2;
  void *out, *ws;
};

template <int... I, class Fn> DI void sfor_(std::integer_sequence<int, I...>, Fn&& f) { (f(std::integral_constant<int, I>{}), ...); }
template <int N, class Fn> DI void sfor(Fn&& f) { sfor_(std::make_integer_sequence<int, N>{}, f); }
DI float asf(unsigned u) { return __builtin_bit_cast(float, u); }
DI unsigned asu(float f) { return __builtin_bit_cast(unsigned, f); }
DI rsrc_t mk(const void* p, int bytes) { return __builtin_amdgcn_make_buffer_rsrc((void*)p, 0, bytes, 0x00020000); }
template <int AUX> DI v4u ld16(rsrc_t r, int voff, int soff) { return __builtin_amdgcn_raw_buffer_load_b128(r, voff, soff, AUX); }
template <int AUX> DI v2u ld8(rsrc_t r, int voff, int soff) { return __builtin_amdgcn_raw_buffer_load_b64(r, voff, soff, AUX); }
template <int AUX> DI void st16(v4u v, rsrc_t r, int voff, int soff) { __builtin_amdgcn_raw_buffer_store_b128(v, r, voff, soff, AUX); }
template <int AUX> DI void st8(v2u v, rsrc_t r, int voff, int soff) { __builtin_amdgcn_raw_buffer_store_b64(v, r, voff, soff, AUX); }
DI f32x4 mfma16(v4u a, v4u b, f32x4 c) {
  return __builtin_amdgcn_mfma_f32_16x16x32_bf16(__builtin_bit_cast(bf16x8, a), __builtin_bit_cast(bf16x8, b), c, 0, 0, 0);
}
DI f32x16 mfma32(v4u a, v4u b, f32x16 c) {
  return __builtin_amdgcn_mfma_f32_32x32x16_bf16(__builtin_bit_cast(bf16x8, a), __builtin_bit_cast(bf16x8, b), c, 0, 0, 0);
}
DI unsigned pk(float lo, float hi) { return __builtin_bit_cast(unsigned, __builtin_convertvector((f32x2){lo, hi}, bf16x2)); }
DI float bflo(unsigned u) { return asf(u << 16); }
DI float bfhi(unsigned u) { return asf(u & 0xffff0000u); }
DI f32x4 unpack4(v2u v) { return (f32x4){bflo(v.x), bfhi(v.x), bflo(v.y), bfhi(v.y)}; }
DI v2u trrd(LAS char* p) { return __builtin_bit_cast(v2u, __builtin_amdgcn_ds_read_tr16_b64_v4i16((LAS s16x4*)p)); }
DI v4u trfrag(LAS char* p0, LAS char* p1) { v2u a = trrd(p0), b = trrd(p1); return (v4u){a.x, a.y, b.x, b.y}; }
DI float hi32(float v) {
  unsigned u = asu(v);
  auto r = __builtin_amdgcn_permlane32_swap(u, u, false, false);
  return asf(r[1]);
}
DI float sum32(float v) {
  unsigned u = asu(v);
  auto r = __builtin_amdgcn_permlane32_swap(u, u, false, false);
  return asf(r[0]) + asf(r[1]);
}
DI float max32(float v) {
  unsigned u = asu(v);
  auto r = __builtin_amdgcn_permlane32_swap(u, u, false, false);
  return fmaxf(asf(r[0]), asf(r[1]));
}
DI float sum_ab(float a, float b) {
  auto r = __builtin_amdgcn_permlane16_swap(asu(a), asu(b), false, false);
  return sum32(asf(r[0]) + asf(r[1]));
}
template <int CTRL> DI float dpp_add(float v) { return v + asf((unsigned)__builtin_amdgcn_update_dpp(0, (int)asu(v), CTRL, 0xf, 0xf, true)); }
DI float sum_row(float v) { return dpp_add<0x140>(dpp_add<0x141>(dpp_add<0x4e>(dpp_add<0xb1>(v)))); }
DI float dot2(unsigned a, unsigned b, float c) { return __builtin_amdgcn_fdot2_f32_bf16(__builtin_bit_cast(bf16x2, a), __builtin_bit_cast(bf16x2, b), c, false); }
DI void stats16(const f32x4 (&v)[4], int n, f32x2& s1, f32x2& s2) {
#pragma unroll
  for (int a = 0; a < 4; ++a)
    if (n >> a & 1) {
      const f32x2 lo = {v[a][0], v[a][1]}, hi = {v[a][2], v[a][3]};
      s1 += lo; s2 += lo * lo; s1 += hi; s2 += hi * hi;
    }
}
#define FENCE() asm volatile("" ::: "memory")
#define SCHED() __builtin_amdgcn_sched_barrier(0)
#define DRAIN() asm volatile("s_waitcnt vmcnt(0)" ::: "memory")
#define PINV(x) asm volatile("" : "+v"(x))
#define PINA(x) asm volatile("" : "+a"(x))
#define PINS(x) asm volatile("" : "+s"(x))
#define KEEP(x) asm volatile("" :: "v"(x))
#define LGKM0() asm volatile("s_waitcnt lgkmcnt(0)" ::: "memory")
DI void bar() { LGKM0(); __builtin_amdgcn_s_barrier(); FENCE(); }
template <int N> DI void vmw() { asm volatile("s_waitcnt vmcnt(%0)" :: "n"(N) : "memory"); }
template <int LO, int MO> DI void dmai(rsrc_t r, int voff, int lds) {
  int so;
  asm volatile("s_add_u32 m0, %1, %4\n\ts_mov_b32 %0, %5\n\tbuffer_load_dwordx4 %2, %3, %0 offen lds"
               : "=&s"(so) : "s"(lds), "v"(voff), "s"(r), "n"(LO), "n"(MO) : "memory", "scc");
}
DI void touch(rsrc_t r, int voff, int soff, LAS char* L) {
  asm volatile("s_mov_b32 m0, %0\n\ts_nop 3\n\tbuffer_load_dword %1, %2, %3 offen lds"
               :: "s"((int)(size_t)(L + L_DUMP)), "v"(voff), "s"(r), "s"(soff) : "memory");
}
#define STAMP(i) do { if ((i) == PADI) { while (__builtin_amdgcn_s_memrealtime() - tstart < PADT) __builtin_amdgcn_s_sleep(1); } } while (0)

DI void publish(GAS unsigned* flag, unsigned epoch) {
  DRAIN();
  __builtin_amdgcn_s_waitcnt(0x0F70);
  bar();
  if (threadIdx.x == 0) __hip_atomic_store(flag, epoch, RLX_AGENT);
  FENCE();
}
#define POLL(f, ptr) asm volatile("global_load_dword %0, %1, off sc1" : "=&v"(f) : "v"(ptr) : "memory")
#define POLLED(f, N, ptr) { \
    unsigned f2_; \
    POLL(f2_, ptr); \
    asm volatile("s_waitcnt vmcnt(%1)" : "+v"(f) : "n"((N) + 1) : "memory"); \
    if (__builtin_amdgcn_ballot_w64(f != epoch) != 0ull) { \
      asm volatile("s_waitcnt vmcnt(0)" : "+v"(f2_) :: "memory"); \
      if (__builtin_amdgcn_ballot_w64(f2_ != epoch) != 0ull) wait_flags(ptr, epoch, budget); \
    } \
    FENCE(); \
    late = f2_; }
DI void wait_flags(GAS unsigned* flag, unsigned epoch, unsigned& budget) {
  FENCE();
  while (true) {
    unsigned f = __hip_atomic_load(flag, RLX_AGENT);
    if (__builtin_amdgcn_ballot_w64(f != epoch) == 0ull) break;
    if (budget == 0u) break;
    --budget;
  }
  __builtin_amdgcn_fence(__ATOMIC_ACQUIRE, "wavefront");
  FENCE();
}

DI int oob(bool in, int off) { return off | (in ? 0 : OOB); }
DI int wrap7(int v) { return v >= 7 ? v - 7 : v; }
DI int swz(int row) { return ((row >> 1) & 1) | (((row >> 3) ^ (row >> 2)) & 1) << 1; }

#define FRESH() \
  int tid = threadIdx.x, bid = blockIdx.x; \
  asm volatile("" : "+v"(tid)); \
  asm volatile("" : "+s"(bid)); \
  const int lane = tid & 63, w = __builtin_amdgcn_readfirstlane(tid >> 6), bx = bid & 7, by = bid >> 3; \
  const int c = lane & 15, g = lane >> 4, q4 = (lane & 15) >> 2, p4 = lane & 3, r32 = lane & 31, hh = lane >> 5; \
  const int by7 = __builtin_amdgcn_readfirstlane((by * 37) >> 8), bym = __builtin_amdgcn_readfirstlane(by - 7 * by7); \
  const int t = 2 * bx + (by >> 4), hd = by & 15;                \
  const int st = 4 * bx + by7, dt = bym;                         \
  const int dt4 = by < 21 ? bym : bym + 4 * (bx & 1), ft = 7 * (bx >> 1) + (by < 21 ? by7 + 4 * (bx & 1) : 3);    \
  LAS char* ring = L + L_KVW + w * 16384; \
  const int wrow = lane >> 3, sx0 = swz(wrow) << 5; \
  auto raddr = [&](int row) { return row * 128 + (swz(row) << 5) + 8 * p4; }; \
  LAS char* red = L + L_KVW + lane * 16; \
  (void)c; (void)g; (void)q4; (void)r32; (void)hh; (void)t; (void)hd; (void)st; (void)dt; (void)dt4; (void)ft; (void)ring; (void)sx0; (void)raddr; (void)red;

#define MFMA32V0(d, a, b) asm volatile("v_mfma_f32_32x32x16_bf16 %0, %1, %2, 0" : "=&v"(d) : "v"(a), "v"(b))
#define MFMA32VC(d, a, b, c) asm volatile("v_mfma_f32_32x32x16_bf16 %0, %1, %2, %3" : "=&v"(d) : "v"(a), "v"(b), "v"(c))
#define MFMA32VA(d, a, b) asm volatile("v_mfma_f32_32x32x16_bf16 %0, %1, %2, %0" : "+v"(d) : "v"(a), "v"(b))
#define MFMA_SETTLE(d) asm volatile("s_nop 11" : "+v"(d))
#define MFMA_SETTLE2(d, e) asm volatile("s_nop 11" : "+v"(d), "+v"(e))
DI float max3(float a, float b, float c) { float r; asm("v_max3_f32 %0, %1, %2, %3" : "=v"(r) : "v"(a), "v"(b), "v"(c)); return r; }

#define WB(cc) (L + L_WQ + (4 * hh + q4) * 64 + (g & 1) * 32 + 8 * p4 + (hd & 1) * 8 + (cc) * 4096)
#define WFRAG(wb, part, e) trfrag((wb) + (part) * 28672 + ((e) >> 1) * 2048 + ((e) & 1) * 512, (wb) + (part) * 28672 + ((e) >> 1) * 2048 + ((e) & 1) * 512 + 1024)
#define CSUM(part, a) (*(LAS f32x4*)(L + L_CS + ((part) * 32 + 8 * (a) + 4 * hh) * 4))
#define WZ_RD1(n, i, sl) wz[n][i] = (n) < 16 ? trfrag(pr1[i] + (sl) * 4096, pr1[(i) ^ 2] + (sl) * 4096 + 512) : trfrag(pr2[i] + (sl) * 4096, pr2[i] + (sl) * 4096 + 2048)
#define DMA_WZ1(n, m, sl) { \
    if ((n) < 16) dmai<(sl) * 4096 + (m) * 1024, (32 * ((n) & 15) + 8 * (m)) * F * 2>(rw1, ((m) & 1) ? pw1 ^ 64 : pw1, pring); \
    else dmai<(sl) * 4096 + (m) * 1024, (32 * ((n) & 15) + 8 * (m)) * S * 2>(rw2, ((m) & 1) ? pw2 ^ 64 : pw2, pring); }
#define PIECE(L0, q) { \
    constexpr int r_ = (q) / 4, m_ = (q) % 4, n_ = (L0) + r_; \
    if constexpr (m_ == 0 && r_ >= 4) asm volatile("s_waitcnt vmcnt(8) lgkmcnt(0)" ::: "memory"); \
    else if constexpr (m_ == 0 && r_ == 3) vmw<8>(); \
    DMA_WZ1(n_, m_, r_ & 3) \
    if constexpr (r_ >= 3) WZ_RD1(n_ - 3, m_, (r_ - 3) & 3); \
    SCHED(); }
#define DPUMP(L0, A, B) sfor<4 * ((B) - (A))>([&](auto I_) INL { PIECE(L0, 4 * ((A) - (L0)) + I_) });
#define STASH(A, B) if constexpr (layer == 1) sfor<4 * ((B) - (A))>([&](auto I_) INL { \
    constexpr int j_ = (A) + I_ / 4, m_ = I_ % 4; \
    dmai<j_ * 4096 + m_ * 1024, (32 * (14 - NS + j_) + 8 * m_) * S * 2>(rw2, (m_ & 1) ? pw2 ^ 64 : pw2, pstash); SCHED(); });
#define DTSTEP(L0, L1, sl) ((L1) - 1 - (((L1) - 1 - (L0) - (sl)) & 3))
#define DTAIL1(L0, L1, j) { if constexpr (DTSTEP(L0, L1, (j) >> 2) >= (L0) && DTSTEP(L0, L1, (j) >> 2) >= (L1) - 3) WZ_RD1(DTSTEP(L0, L1, (j) >> 2), (j) & 3, (j) >> 2); }
#define DTAIL(L0, L1) sfor<16>([&](auto J_) INL { constexpr int j_ = J_; DTAIL1(L0, L1, j_) }); SCHED();
#define REDW(a, b) *(LAS f32x4*)(red + w * 16384 + (((b) - 1) * 4 + (a)) * 1024) = acc[a][b]
#define REDR(a, b) (*(LAS f32x4*)(red + ((w + 4 - (b)) & 3) * 16384 + (((b) - 1) * 4 + (a)) * 1024))
__global__ void __launch_bounds__(256, 1) fused_kernel(Params p) {
  extern __shared__ __attribute__((aligned(16))) char smem[];
  LAS char* L = (LAS char*)smem;
  const unsigned long long tstart = __builtin_amdgcn_s_memrealtime();
  {
    unsigned long long pc;
    int len;
    asm volatile("s_getpc_b64 %0\n.Lfk_code_from:\n\ts_mov_b32 %1, .Lfk_code_to-.Lfk_code_from" : "=s"(pc), "=s"(len));
    touch(mk((const void*)pc, len > 0 ? len : 0), (threadIdx.x & 63) < 8 ? ((blockIdx.x >> 3) * 4 + (threadIdx.x >> 6) + (threadIdx.x & 7) * 128) * 128 : OOB, 0, L);
  }
  SCHED();
  const bool on35 = (blockIdx.x >> 3) < 28;
  const bool on4 = (blockIdx.x >> 3) < ((blockIdx.x & 1) ? 24 : 25);
  const rsrc_t rws = mk(p.ws, WS_BYTES);
  GAS unsigned* ctrl = (GAS unsigned*)p.ws;
  GAS unsigned* flags = (GAS unsigned*)((GAS char*)p.ws + FLAG_OFF);
  STAMP(0);
  unsigned budget = 1u << 22;

  v4u xf[28];
  v4u kv[4];
  float mean = 0.f, rstd = 0.f;
  f32x4 res[4];
  unsigned epoch;
  int kvs, oo, t1o, wvo2, pw1, pw2, fo;
  unsigned nk1, nk2, pm[8];
  LAS char *kvw, *vw, *vr0, *wor[4], *pr1[4], *pr2[4], *wbq;
#define KVOUT(acc, h) { \
    _Pragma("unroll") for (int i = 0; i < 16; i += 2) kv[2 * (h) + (i >> 3)][(i >> 1) & 3] = pk(acc[i], acc[i + 1]); \
    if (hh) { kv[2 * (h) + 1][2] = (h) ? 0x00003f80u : 0u; kv[2 * (h) + 1][3] = 0u; } \
    const rsrc_t rkv = mk(p.ws, t < 15 ? WS_BYTES : 0);    \
    st16<SC1>(kv[2 * (h)], rkv, kvs + (h) * 2048, 0); \
    st16<SC1>(kv[2 * (h) + 1], rkv, kvs + (h) * 2048 + 1024, 0); \
    SCHED(); }
#define X128(p) ((LAS char*)(size_t)((unsigned)(size_t)(p) ^ 128u))
  {
    FRESH();
    const rsrc_t rwq = mk(p.wqkv, D * 3 * D * 2);
    const rsrc_t rx = mk(p.x, S * D * 2);
    const int gid = by * 4 + w, rg = (int)(size_t)ring;
    v4u xr0, xr1;
    asm volatile("s_nop 4\n\tbuffer_load_dword %2, %5, %6, 0 offen sc1\n\tbuffer_load_dwordx4 %0, %3, %4, 0 offen\n\tbuffer_load_dwordx4 %1, %3, %4, 0 offen offset:896"
                 : "=&v"(xr0), "=&v"(xr1), "=&v"(epoch) : "v"(oob(lane < 56, (t * 128 + hd * 8 + 2 * w) * 896 + lane * 16)), "s"(rx), "v"(0), "s"(rws) : "memory");
    touch(rwq, oob(lane < 14, gid * 2688 + 896 + lane * 128), 0, L);
    const int xvo = (t * 128 + w * 32 + wrow) * 896 + (((lane & 7) ^ (wrow >> 1)) << 4);
    const int wvo = (w * 16 + (lane >> 2)) * 2688 + hd * 56 - (hd & 1) * 8 + (lane & 3) * 16;
    const int wlb = (int)(size_t)(L + L_WQ) + w * 1024;
#define DMA_W(part, cc) dmai<(part) * 28672 + (cc) * 4096, (cc) * 64 * 2688 + (part) * 896>(rwq, wvo, wlb)
#define DMA_C(cc, m) { if constexpr ((m) < 4) dmai<((cc) & 3) * 4096 + ((m) & 3) * 1024, ((m) & 3) * 8 * 896 + (cc) * 128>(rx, ((m) & 1) ? xvo ^ 64 : xvo, rg); \
                       else DMA_W((m) - 3, cc); }
    const int tb = gid * 35 + lane, tbr = (tb * 18725) >> 18;
    int tvb = oob(lane < 35, (128 + tbr) * 2688 + 896 + (tb - 14 * tbr) * 128);
    sfor<6>([&](auto M) INL { constexpr int m = M; DMA_C(0, m) });
    SCHED();
    LAS char* xa[8];
#pragma unroll
    for (int e = 0; e < 8; ++e) { xa[e] = ring + r32 * 128 + 8 * hh + ((e ^ ((r32 >> 1) & 7)) << 4); PINV(xa[e]); }
    int epo = w == 0 ? FLAG_OFF + (bid - 256) * 128 + lane * 32768 : S3_OFF + (st * 8 + dt) * 1024 - 2 * S3_SZ + (lane >> 3) * S3_SZ + (lane & 7) * 128;
    epo = (w == 0 ? lane - 1u < 10u : w == 1 && on35 && (lane >> 4) == 1) ? epo : 0;
    int s0o = oob((lane & 15) == 0, S0_OFF + (t * 128 + hd * 8 + 2 * w) * 16 + g * 8), s0i = S0_OFF + (t * 128 + w * 32 + r32) * 16;
    PINV(s0o); PINV(s0i);
    wbq = WB(0);
    LAS char* wb = wbq + 28672;
    LAS char* cb = L + L_WQ + 28672 + (w * 16 + 8 * hh + q4) * 64 + (g & 1) * 32 + 8 * p4;
    PINV(tvb); PINV(epo); PINV(wbq); PINV(wb); PINV(cb);
#pragma unroll
    for (int i = 0; i < 8; ++i) {
      const int d = r32 - ((2 * i & 3) + 8 * (i >> 1)) - 4 * hh;
      pm[i] = ((unsigned)(d >> 31) & 0xffffu) | ((unsigned)((d - 1) >> 31) & 0xffff0000u);
      PINA(pm[i]);
    }
#pragma unroll
    for (int i = 0; i < 4; ++i) {
      wor[i] = ring + (raddr(16 * (g >> 1) + 4 * (g & 1) + q4) ^ (i << 5));
      pr1[i] = ring + (raddr(8 * g + q4) ^ (i << 5));
      pr2[i] = ring + (raddr(4 * g + q4) ^ (((i + w) & 3) << 5));
      PINA(wor[i]); PINA(pr1[i]); PINA(pr2[i]);
    }
    SCHED();
    sfor<6>([&](auto M) INL { constexpr int m = M; DMA_C(1, m) });
    asm volatile("s_mov_b32 m0, %0\n\ts_nop 3\n\tbuffer_load_dword %1, %2, 0 offen sc1 lds" :: "s"((int)(size_t)(L + L_DUMP)), "v"(epo), "s"(rws) : "memory");
    touch(rwq, tvb, 0, L);
    SCHED();
    {
      asm volatile("s_waitcnt vmcnt(15)" : "+v"(xr0), "+v"(xr1), "+v"(epoch) :: "memory");
      epoch = __builtin_amdgcn_readfirstlane(epoch) + 1u;
      float a1 = 0.f, a2 = 0.f, b1 = 0.f, b2 = 0.f;
#pragma unroll
      for (int j = 0; j < 4; ++j) { a1 = dot2(xr0[j], 0x3f803f80u, a1); a2 = dot2(xr0[j], xr0[j], a2); b1 = dot2(xr1[j], 0x3f803f80u, b1); b2 = dot2(xr1[j], xr1[j], b2); }
      const auto ra = __builtin_amdgcn_permlane16_swap(asu(a1), asu(a2), false, false), rb = __builtin_amdgcn_permlane16_swap(asu(b1), asu(b2), false, false);
      const auto rc = __builtin_amdgcn_permlane32_swap(asu(asf(ra[0]) + asf(ra[1])), asu(asf(rb[0]) + asf(rb[1])), false, false);
      st8<SC1>((v2u){asu(sum_row(asf(rc[0]) + asf(rc[1]))), epoch}, rws, s0o, 0);
      SCHED();
    }
    STAMP(32);
#define READ_X1(cc, e) { \
      constexpr int ch = ((e) >> 1) * 4 + ((e) & 1); \
      const v2u a_ = *(LAS v2u*)(xa[ch] + ((cc) & 3) * 4096), b_ = *(LAS v2u*)(xa[ch + 2] + ((cc) & 3) * 4096); \
      xf[4 * (cc) + (e)] = (v4u){a_.x, a_.y, b_.x, b_.y}; }
    const v4u ones = {0x3f803f80u, 0x3f803f80u, 0x3f803f80u, 0x3f803f80u};
    f32x16 ak, av, csk = {}, csv = {};
    v4u wk[28], wv[28], wc[14], gx;
    float pp[4];
    f32x4 cc[8];
    int tvq, tvw;
    LAS float *pb, *pw;
    sfor<8>([&](auto R) INL {
      constexpr int r = R;
      if constexpr (r < 7) {
        vmw<(r < 2 ? 9 : r == 5 ? 8 : r == 6 ? 4 : 6)>();
        __builtin_amdgcn_s_barrier();
        FENCE();
        SCHED();
      }
      sfor<10>([&](auto Q) INL {
        constexpr int q = Q, e = q < 4 ? q >> 1 : q >= 6 ? (q >> 1) - 1 : -1;
        if constexpr (e >= 0 && r > 0) {
          constexpr int km = 4 * (r - 1) + e;
          if constexpr (q & 1) { if constexpr (km == 0) MFMA32V0(av, wv[km], xf[km]); else MFMA32VA(av, wv[km], xf[km]); }
          else { if constexpr (km == 0) MFMA32V0(ak, wk[km], xf[km]); else MFMA32VA(ak, wk[km], xf[km]); }
        }
        if constexpr (r < 7) {
          if constexpr (q == 4) csk = mfma32(ones, wc[2 * r], csk);
          if constexpr (q == 5) csv = mfma32(ones, wc[2 * r + 1], csv);
          SCHED();
          if constexpr (q < 2) wc[2 * r + q] = trfrag(cb + q * 28672 + r * 4096, cb + q * 28672 + r * 4096 + 256);
          if constexpr (q >= 2) { constexpr int f = q - 2; if constexpr (f & 1) wv[4 * r + f / 2] = WFRAG(wb + r * 4096, 1, f / 2); else wk[4 * r + f / 2] = WFRAG(wb + r * 4096, 0, f / 2); }
          if constexpr (q == 0 || q == 3 || q == 5 || q == 8) READ_X1(r, (q == 0 ? 0 : q == 3 ? 1 : q == 5 ? 2 : 3))
        }
        constexpr int dm = q == 1 ? 0 : q == 2 ? 1 : q == 4 ? 2 : q == 6 ? 3 : q == 7 ? 4 : q == 9 ? 5 : -1;
        if constexpr (r < 5 && dm >= 0) DMA_C(r + 2, dm)
        if constexpr (r == 4 && q == 9) { touch(rwq, tvq, 0, L); touch(mk(p.wo, D * D * 2), tvw, 0, L); }
        if constexpr (r == 6 && q == 0) asm volatile("buffer_load_dwordx4 %0, %1, %2, 0 offen sc1" : "=&v"(gx) : "v"(s0i), "s"(rws) : "memory");
        if constexpr (r == 5 && (q == 6 || q == 9)) DMA_W(0, q / 9);
        if constexpr (r == 6 && q % 3 == 2) DMA_W(0, 2 + q / 3);
        if constexpr (r == 7 && (q == 1 || q == 7)) DMA_W(0, 5 + q / 7);
        if constexpr (q == 3 || q == 8) {
          constexpr int i = 2 * r + (q == 8);
          if constexpr (i == 0) { kvs = KV_OFF + (hd * 64 + 4 * t + w) * 4096 + lane * 16; PINV(kvs); }
          if constexpr (i == 1) { oo = O_OFF + ((2 * t + (w >> 1)) * 16 + hd) * 4096 + (2 * (w & 1) + (r32 >> 4)) * 1024 + hh * 256 + c * 16; PINA(oo); }
          if constexpr (i == 2) { t1o = oob(lane < 32, (512 * (by >> 3) + 8 * ((by & 7) * 4 + w) + (lane >> 2)) * (F * 2) + (7 * (bx >> 1) + 3 * (bx & 1) + (lane & 3)) * 128); PINA(t1o); }
          if constexpr (i == 3) { wvo2 = ((112 * w + wrow) * 896 + dt * 128 + (lane & 7) * 16) ^ sx0; PINA(wvo2); }
          if constexpr (i == 4) { pw1 = ((512 * w + wrow) * (F * 2) + ft * 128 + (lane & 7) * 16) ^ sx0; PINA(pw1); }
          if constexpr (i == 5) { pw2 = ((448 * w + wrow) * (S * 2) + st * 128 + (lane & 7) * 16) ^ sx0; PINA(pw2); }
          if constexpr (i == 6) {
            const int tl = by * 98 + w * 25 + lane, trow = (tl * 9363) >> 16;
            tvq = oob(lane < 25 && w * 25 + lane < 98, trow * 2688 + (tl - 7 * trow) * 128); tvw = oob(lane < 25 && w * 25 + lane < 98, tl * 128);
            PINV(tvq); PINV(tvw);
          }
          if constexpr (i == 7) {
            const int tp = (t >= 2 && (lane & 1)) ? t - 2 : t > 0 ? t - 1 : 0;
            fo = FLAG_OFF + (((tp & 1) * 16 + hd) * 8 + (tp >> 1)) * 128;
            PINA(fo);
          }
          if constexpr (i == 8) { nk2 = (t < 2 && hh) ? 0xf149u : 0u; nk1 = (t < 1 && hh) ? 0xf149u : 0u; PINA(nk1); PINA(nk2); }
          if constexpr (i == 9) { kvw = L + L_KVW + w * 4096 + lane * 16; vw = L + L_KVW + w * 4096 + 2048 + ((lane * 16) ^ (hh << 6)); PINA(kvw); PINA(vw); }
          if constexpr (i == 10) { vr0 = L + L_KVW + w * 4096 + 2048 + ((((g & 1) * 1024 + (p4 & 1) * 512 + hh * 64 + q4 * 16) ^ ((p4 & 1) << 6) ^ ((g & 1) << 7)) + (p4 >> 1) * 8); PINA(vr0); }
          if constexpr (i == 11) {
            pb = (LAS float*)(L + L_CSP) + 32 + lane + 4 * (hd & 1);
            pw = (LAS float*)(L + (hh ? L_DUMP : L_CSP + (w * 96 + 32) * 4)) + r32;
            PINV(pb); PINV(pw);
          }
        }
        if constexpr (r == 6 && q == 7) { pw[0] = csk[0]; pw[32] = csv[0]; }
        if constexpr (r == 6 && q == 9) { bar(); pp[0] = pb[0]; pp[1] = pb[96]; pp[2] = pb[192]; pp[3] = pb[288]; }
        if constexpr (r == 7 && q == 2) *(LAS float*)(L + L_CS + (32 + lane) * 4) = (pp[0] + pp[1]) + (pp[2] + pp[3]);
        if constexpr (r == 7 && (q == 3 || q == 6)) { _Pragma("unroll") for (int j = 0; j < 4; ++j) cc[4 * (q / 6) + j] = CSUM(1, 4 * (q / 6) + j); }
        SCHED();
      });
    });
    asm volatile("s_waitcnt vmcnt(5)" : "+v"(gx) :: "memory");
    while (__builtin_amdgcn_ballot_w64((gx.y != epoch) | (gx.w != epoch)) != 0ull) {
      if (budget == 0u) break;
      --budget;
      asm volatile("buffer_load_dwordx4 %0, %1, %2, 0 offen sc1\n\ts_waitcnt vmcnt(0)" : "=&v"(gx) : "v"(s0i), "s"(rws) : "memory");
    }
    mean = asf(gx.x) * (1.f / D);
    rstd = __builtin_amdgcn_rsqf(fmaxf(asf(gx.z) * (1.f / D) - mean * mean, 0.f) + EPS);
    STAMP(34);
    MFMA_SETTLE2(ak, av);
#pragma unroll
    for (int i = 0; i < 16; ++i) ak[i] = (ak[i] - mean * cc[i >> 2][i & 3]) * rstd;
    KVOUT(ak, 0)
#pragma unroll
    for (int i = 0; i < 16; ++i) av[i] = (av[i] - mean * cc[4 + (i >> 2)][i & 3]) * rstd;
    KVOUT(av, 1)
  }

  unsigned b2r;
  v2u bb[4];
  v4u wz[30][4];
  sfor<2>([&](auto LY) INL {
    constexpr int layer = LY;
    constexpr int Y0 = layer ? KP : 0, Y2 = Y0 + (layer ? MO2 : NO2), YG = Y2 + (layer ? MO3 : NO3), YG2 = YG + (layer ? MG2 : NG2),
                  YH = YG2 + (layer ? MG3 : NG3), YE = layer ? 16 : Z_U, YH2 = layer ? YE : YH + NH2, YZ = layer ? 30 - NS : 30, YU3 = layer ? YZ : 30 - NU3;
    static_assert(YH <= YH2, "schedule");
    constexpr int QPUB = layer ? 14 : 10;
    GAS unsigned* fl = flags + layer * 5 * 256 * 32;
    STAMP(1 + layer * 16);
    v4u qf[2];
    v4u wo[4][4];
    v2u rr[4];
    int pring, pstash;
    const rsrc_t rw1 = mk(p.w1, on4 ? S * F * 2 : 0);
    const rsrc_t rw2 = mk(p.w2, on35 ? S * F * 2 : 0);
    {
      const bool hi = threadIdx.x & 32;
      FRESH();
      if constexpr (layer == 0) {
        vmw<4>();
        if (t == 15) vmw<0>();
        __builtin_amdgcn_s_barrier();
        FENCE();
      }
      v4u wc[7];
      f32x16 cs = {};
      LAS char* cb = L + L_WQ + (w * 16 + 8 * hh + q4) * 64 + (g & 1) * 32 + 8 * p4;
      v4u wq[28];
#pragma unroll
      for (int ks = 0; ks < 4; ++ks) wq[ks] = WFRAG(wbq, 0, ks);
      f32x16 aq;
      STAMP(2 + layer * 43);
      const rsrc_t rwo = mk(p.wo, on35 ? D * D * 2 : 0);
      pring = (int)(size_t)ring;
      pstash = (int)(size_t)(L + L_WQ) + w * 20480;
      PINS(pring); PINS(pstash);
      GAS unsigned* fp = (GAS unsigned*)((GAS char*)p.ws + fo);
      unsigned pf;
      sfor<28>([&](auto I) INL {
        constexpr int ks = I;
        if constexpr (ks == 0) MFMA32V0(aq, wq[ks], xf[ks]); else MFMA32VA(aq, wq[ks], xf[ks]);
        if constexpr (ks + 4 < 28) wq[ks + 4] = WFRAG(wbq + ((ks + 4) >> 2) * 4096, 0, ks & 3);
        constexpr int wi = layer ? -1 : ks - QPUB - 1;
        if constexpr (wi >= 0 && wi < 16) dmai<wi * 1024, (28 * (wi >> 2) + 8 * (wi & 3)) * 896>(rwo, (wi & 1) ? wvo2 ^ 64 : wvo2, pring);
        if constexpr (layer == 0) {
          if constexpr (ks < 7) wc[ks] = trfrag(cb + ks * 4096, cb + ks * 4096 + 256);
          if constexpr (ks >= 2 && ks < 9) cs = mfma32((v4u){0x3f803f80u, 0x3f803f80u, 0x3f803f80u, 0x3f803f80u}, wc[ks - 2], cs);
          if constexpr (ks == QPUB + 1) { _Pragma("unroll") for (int q = 0; q < 4; ++q) cs[q] = ((LAS float*)(L + L_CSP) + r32 + 4 * (hd & 1))[96 * q]; }
          if constexpr (ks == QPUB + 5) *(LAS float*)(L + L_CS + r32 * 4) = (cs[0] + cs[1]) + (cs[2] + cs[3]);
        }
        if constexpr (ks == 27) POLL(pf, fp);
        if constexpr (ks == QPUB) {
          if constexpr (layer == 0) { *(LAS float*)(L + (hh ? L_DUMP : L_CSP + w * 96 * 4) + r32 * 4) = cs[0]; }
          publish(fl + (0 * 256 + bid) * 32, epoch);
          STAMP(3 + layer * 16);
        }
        SCHED();
      });
      f32x4 cq[4];
#pragma unroll
      for (int a = 0; a < 4; ++a) cq[a] = CSUM(0, a);
      MFMA_SETTLE(aq);
      const float qs = rstd * (0.18898223650461362f * 1.4426950408889634f);
#pragma unroll
      for (int a = 0; a < 4; ++a) {
        qf[a >> 1][(a & 1) * 2] = pk((aq[4 * a] - mean * cq[a][0]) * qs, (aq[4 * a + 1] - mean * cq[a][1]) * qs);
        qf[a >> 1][(a & 1) * 2 + 1] = pk((aq[4 * a + 2] - mean * cq[a][2]) * qs, (aq[4 * a + 3] - mean * cq[a][3]) * qs);
      }
      if (hi) { qf[1][2] = 0x00003f80u; qf[1][3] = 0u; }
      PINV(qf[0]); PINV(qf[1]);
      f32x16 s8;
      asm volatile("s_nop 1" : "+v"(qf[0]), "+v"(qf[1]));
      MFMA32V0(s8, kv[0], qf[0]);
      MFMA32VA(s8, kv[1], qf[1]);
      SCHED();
      unsigned late;
      POLLED(pf, 0, fp)
      STAMP(4 + layer * 16);
      v4u ra[4], rb[4];
      {
        const rsrc_t r1 = mk(p.ws, t >= 1 ? WS_BYTES : 0), r2 = mk(p.ws, t >= 2 ? WS_BYTES : 0);
        const int kvo = kvs - 32768;
        rb[0] = ld16<SC1>(r2, kvo, 0); rb[1] = ld16<SC1>(r2, kvo, 1024);
        ra[0] = ld16<SC1>(r1, kvo, 16384); ra[1] = ld16<SC1>(r1, kvo, 16384 + 1024);
        SCHED();
        rb[2] = ld16<SC1>(r2, kvo, 2048); rb[3] = ld16<SC1>(r2, kvo, 3072);
        ra[2] = ld16<SC1>(r1, kvo, 16384 + 2048); ra[3] = ld16<SC1>(r1, kvo, 16384 + 3072);
        SCHED();
      }
#pragma unroll
      for (int j = 0; j < 16; ++j) wo[j >> 2][j & 3] = trfrag(wor[j & 3] + (j >> 2) * 4096, wor[(j & 3) ^ 2] + (j >> 2) * 4096 + 1024);
      SCHED();
      bar();
      LAS char* vw1 = X128(vw) + 1024;
      LAS char* vr1 = X128(vr0);
      *(LAS v4u*)(kvw + 32768) = kv[0];
      *(LAS v4u*)(kvw + 32768 + 1024) = kv[1];
      *(LAS v4u*)(vw + 32768) = kv[2];
      *(LAS v4u*)(vw1 + 32768) = kv[3];
      int rro = (64 * st + 16 * w + c) * 896 + dt * 128 + g * 8, b2o = (64 * st + 16 * w + c) * 2, b1o = ft * 128 + g * 8;
      unsigned pml[8], mk32[16], nk1l = nk1, nk2l = nk2;
      int ool = oo, t1l = t1o;
      LAS char* vr0l = vr0;
      if constexpr (layer == 0) { PINV(rro); PINV(b2o); PINV(b1o); }
      PINV(vw1); PINV(vr1); PINV(vr0l); PINV(ool); PINV(t1l); PINV(nk1l); PINV(nk2l);
#pragma unroll
      for (int i = 0; i < 8; ++i) { pml[i] = pm[i]; PINV(pml[i]); mk32[2 * i] = (unsigned)((int)(pml[i] << 16) >> 16); mk32[2 * i + 1] = (unsigned)((int)pml[i] >> 16); PINV(mk32[2 * i]); PINV(mk32[2 * i + 1]); }
      SCHED();
      f32x16 sc[8], se;
      v4u kf[8][2];
      float mx = -1e30f, mx2 = -1e30f;
#define MAX8(v, i) asm volatile("v_max3_f32 %0, %0, %2, %3\n\tv_max3_f32 %1, %1, %4, %5\n\tv_max3_f32 %0, %0, %6, %7\n\tv_max3_f32 %1, %1, %8, %9" \
        : "+v"(mx), "+v"(mx2) : "v"(v[i]), "v"(v[(i) + 1]), "v"(v[(i) + 2]), "v"(v[(i) + 3]), "v"(v[(i) + 4]), "v"(v[(i) + 5]), "v"(v[(i) + 6]), "v"(v[(i) + 7]))
      *(LAS v4u*)(kvw) = rb[0];
      MFMA32V0(sc[0], rb[0], qf[0]);
      rb[1][2] |= nk2l;
      *(LAS v4u*)(kvw + 1024) = rb[1];
      *(LAS v4u*)(kvw + 16384) = ra[0];
      SCHED();
      MFMA32VA(sc[0], rb[1], qf[1]);
      ra[1][2] |= nk1l;
      *(LAS v4u*)(kvw + 16384 + 1024) = ra[1];
      kf[4][0] = ra[0]; kf[4][1] = ra[1];
      MFMA32V0(sc[4], kf[4][0], qf[0]);
      bar();
      KEEP(late);
      STAMP(37 + layer * 4);
      MFMA32VA(sc[4], kf[4][1], qf[1]);
#pragma unroll
      for (int j = 1; j < 8; ++j) if (j != 4) { kf[j][0] = *(LAS v4u*)(kvw + j * 4096); kf[j][1] = *(LAS v4u*)(kvw + j * 4096 + 1024); }
      PINV(sc[0]);
#pragma unroll
      for (int i = 0; i < 16; ++i) se[i] = asf((asu(sc[0][i]) & mk32[i]) | (asu(s8[i]) & ~mk32[i]));
      PINV(se);
      SCHED();
      sfor<12>([&](auto N) INL {
        constexpr int n = N, jj = n >> 1, j = jj < 3 ? jj + 1 : jj + 2, hf = n & 1;
        if constexpr (hf == 0) MFMA32V0(sc[j], kf[j][0], qf[0]); else MFMA32VA(sc[j], kf[j][1], qf[1]);
        if constexpr (n < 2) MAX8(se, 8 * hf);
        else if constexpr (n < 4) MAX8(sc[4], 8 * hf);
        else MAX8(sc[jj < 5 ? jj - 1 : jj], 8 * hf);
        if constexpr (n == 2) *(LAS v4u*)(vw) = rb[2];
        if constexpr (n == 3) *(LAS v4u*)(vw1) = rb[3];
        if constexpr (n == 4) *(LAS v4u*)(vw + 16384) = ra[2];
        if constexpr (n == 5) *(LAS v4u*)(vw1 + 16384) = ra[3];
        if constexpr (n == 6) touch(mk(p.w1, S * F * 2), t1l, layer * KP * 32 * F * 2, L);
        if constexpr (n >= 7) asm volatile("s_nop 0");
        SCHED();
      });
      MAX8(sc[6], 0); MAX8(sc[6], 8); MAX8(sc[7], 0); MAX8(sc[7], 8);
      mx = max32(max3(mx, mx2, mx2));
      f32x16 nm;
#pragma unroll
      for (int i = 0; i < 16; ++i) nm[i] = -mx;
      PINV(nm);
      bar();
      STAMP(38 + layer * 4);
      f32x16 ao;
      v4u vf[9][2], pp[9][2];
      float ex[32][4];
#define VF(j, s) vf[j][s] = trfrag(vr0l + (j) * 4096 + (s) * 256, vr1 + (j) * 4096 + (s) * 256)
      sfor<33>([&](auto N) INL {
        constexpr int n = N, j = n >> 2, q = n & 3;
        if constexpr (j == 0) {
          if constexpr (q == 0) MFMA32VC(sc[1], kf[1][0], qf[0], nm);
          if constexpr (q == 1) MFMA32VA(sc[1], kf[1][1], qf[1]);
          if constexpr (q == 2) MFMA32VC(sc[2], kf[2][0], qf[0], nm);
          if constexpr (q == 3) MFMA32VA(sc[2], kf[2][1], qf[1]);
        } else if constexpr (j < 8) {
          if constexpr (q == 1) { if constexpr (j == 1) MFMA32V0(ao, vf[0][0], pp[0][0]); else MFMA32VA(ao, vf[j - 1][0], pp[j - 1][0]); }
          if constexpr (q == 2) MFMA32VA(ao, vf[j - 1][1], pp[j - 1][1]);
          if constexpr (j + 2 < 8) {
            if constexpr (q == 0) MFMA32VC(sc[j + 2], kf[j + 2][0], qf[0], nm);
            if constexpr (q == 3) MFMA32VA(sc[j + 2], kf[j + 2][1], qf[1]);
          } else if constexpr (j == 6) {
            if constexpr (q == 0) MFMA32VA(ao, vf[8][0], pp[8][0]);
            if constexpr (q == 3) MFMA32VA(ao, vf[8][1], pp[8][1]);
          }
        } else MFMA32VA(ao, vf[7][0], pp[7][0]);
        SCHED();
        if constexpr (n < 32) {
#pragma unroll
          for (int i = 0; i < 4; ++i) { if constexpr (j == 0) ex[n][i] = __builtin_amdgcn_exp2f(se[4 * q + i] - mx); else ex[n][i] = __builtin_amdgcn_exp2f(sc[j][4 * q + i]); }
        }
        SCHED();
        if constexpr (n > 0) {
          constexpr int jp = (n - 1) >> 2, qp = (n - 1) & 3;
#pragma unroll
          for (int i = 0; i < 2; ++i) {
            const unsigned u = pk(ex[n - 1][2 * i], ex[n - 1][2 * i + 1]);
            if constexpr (jp == 0) { pp[0][qp >> 1][(2 * qp + i) & 3] = u & pml[2 * qp + i]; pp[8][qp >> 1][(2 * qp + i) & 3] = u & ~pml[2 * qp + i]; }
            else pp[jp][qp >> 1][(2 * qp + i) & 3] = u;
          }
        }
        if constexpr (layer == 0) {
          if constexpr (n >= 2 && n < 10 && q >= 2) rr[(n >> 2) * 2 + q - 2] = ld8<0>(mk(p.x, on35 ? S * D * 2 : 0), rro, ((n >> 2) * 2 + q - 2) * 32);
          if constexpr (n == 10) b2r = (unsigned)__builtin_amdgcn_raw_buffer_load_b16(mk(p.b2, S * 2), b2o, 0, 0);
          if constexpr (n >= 14 && n < 22 && q >= 2) bb[((n >> 2) - 3) * 2 + q - 2] = ld8<0>(mk(p.b1, F * 2), b1o, (((n >> 2) - 3) * 2 + q - 2) * 32);
        }
        if constexpr (n == 22) KEEP(nm);
        if constexpr (j < 8 && q < 2) VF(j, q);
        if constexpr (j == 5 && q >= 2) VF(8, q - 2);
        SCHED();
      });
      asm volatile("s_nop 1" : "+v"(pp[7][1]));
      MFMA32VA(ao, vf[7][1], pp[7][1]);
      MFMA_SETTLE(ao);
      const float inv = __builtin_amdgcn_rcpf(hi32(ao[12]));
      v4u of[2];
#pragma unroll
      for (int i = 0; i < 16; i += 2) of[i >> 3][(i >> 1) & 3] = pk(ao[i] * inv, ao[i + 1] * inv);
      if (hi) { of[1][2] = 0u; of[1][3] = 0u; }
      st16<SC1>(of[0], rws, ool, 0);
      st16<SC1>(of[1], rws, ool, 512);
      SCHED();
      STAMP(39 + layer * 4);
    }
    if (on35) {
      {
        FRESH();
        const int tq = st >> 1;
        GAS unsigned* fp = fl + (1 * 256 + (((tq & 1) * 16 + 4 * w + (lane & 3)) * 8 + (tq >> 1))) * 32;
        int oo = O_OFF + layer * O_SZ + (st * 16 + 4 * w) * 4096 + lane * 16;
        const int sb = S3_OFF + layer * S3_SZ + st * 8192 + (16 * w + c) * 16;
        int sw = oob(g < 2, sb + dt * 1024 + g * 8), sa = sb + g * 1024, sc = oob(g < 3, sb + (g + 4) * 1024);
        int ho = HT_OFF + layer * HT_SZ + (dt * 64 + 2 * st + (w >> 1)) * 4096 + (g >> 1) * 1024 + (2 * (w & 1) + (g & 1)) * 256 + c * 16;
        const int hr = 8 * (g & 1) + q4, hst = L_HST + w * 2048;
        int h0 = hst + (raddr(hr) ^ ((g >> 1) << 5)), h1 = hst + (raddr(hr + 4) ^ ((g >> 1) << 5)), hw = hst + c * 128 + (swz(c) << 5) + g * 8;
        int ob[4];
#pragma unroll
        for (int b = 0; b < 4; ++b) { ob[b] = ((b + w) & 3) * 1024; PINS(ob[b]); }
        PINV(fp); PINV(oo); PINV(sw); PINV(sa); PINV(sc); PINV(ho); PINV(h0); PINV(h1); PINV(hw); PINV(red);
        publish(fl + (1 * 256 + bid) * 32, epoch);
        STAMP(5 + layer * 16);
        unsigned pf;
        DPUMP(Y0, Y0, Y2)
        if constexpr (layer == 0) {
#pragma unroll
          for (int a = 0; a < 4; ++a) { res[a] = unpack4(rr[a]); PINV(res[a]); }
        }
        POLL(pf, fp);
        DPUMP(Y0, Y2, YG)
        unsigned late;
        POLLED(pf, 4 * (YG - Y2), fp)
        STAMP(6 + layer * 16);
        v4u of[16];
#define LOAD_O(I) of[I] = ld16<SC1>(rws, oo + ((I) & 3) * 4096, ob[(((I) >> 2) + 1) & 3])
#pragma unroll
        for (int i = 0; i < WINO; ++i) { LOAD_O(i); SCHED(); }
        vmw<WINO>();
        KEEP(late);
        STAMP(50 + layer * 4);
        f32x4 acc[4][4], part[12];
        sfor<16>([&](auto I) INL {
          constexpr int i = I, b = ((i >> 2) + 1) & 3, k = i & 3;
#pragma unroll
          for (int a = 0; a < 4; ++a) {
            acc[a][b] = mfma16(wo[k][a], of[i], k > 0 ? acc[a][b] : (f32x4){0.f, 0.f, 0.f, 0.f});
            SCHED();
            constexpr int n0 = issued(i, 16, WINO, WO1), n1 = issued(i + 1, 16, WINO, WO1);
            if constexpr (n0 < n1) { if (a == 0) LOAD_O(n0); }
            if constexpr (n0 + 1 < n1) { if (a == 3) LOAD_O(n0 + 1); }
            if constexpr (i < 12) { if (a == 1) DTAIL1(Y0, YG, i) }
            if constexpr (i >= 12 && k < 2) { if (a == 1 || a == 3) REDW(2 * k + (a >> 1), 3); }
            if constexpr (i >= 4 && i < 12) { if (a == 2) REDW(k, i >> 2); }
            if constexpr (i == 14) { if (a == 0) bar(); }
            if constexpr (i >= 14) { if (a < 3) { const int j = 6 * (i - 14) + 2 * a; part[j] = REDR(j / 3, j % 3 + 1); part[j + 1] = REDR((j + 1) / 3, (j + 1) % 3 + 1); } }
            SCHED();
          }
        });
        STAMP(51 + layer * 4);
#pragma unroll
        for (int a = 0; a < 4; ++a) res[a] = ((res[a] + part[3 * a]) + (part[3 * a + 1] + part[3 * a + 2])) + acc[a][0];
        f32x2 s1 = {0.f, 0.f}, s2 = {0.f, 0.f};
        stats16(res, 15, s1, s2);
        st8<SC1>((v2u){asu(sum_ab(s1[0] + s1[1], s2[0] + s2[1])), epoch}, rws, sw, 0);
        SCHED();
        DTAIL1(Y0, YG, 12) DTAIL1(Y0, YG, 13) DTAIL1(Y0, YG, 14) DTAIL1(Y0, YG, 15)
        STAMP(7 + layer * 16);
        bar();
        DPUMP(YG, YG, YG2)
        v4u ga, gb;
        asm volatile("buffer_load_dwordx4 %0, %2, %4, 0 offen sc1\n\tbuffer_load_dwordx4 %1, %3, %4, 0 offen sc1" : "=&v"(ga), "=&v"(gb) : "v"(sa), "v"(sc), "s"(rws) : "memory");
        DPUMP(YG, YG2, YH)
        STASH(0, SG)
        asm volatile("s_waitcnt vmcnt(%2)" : "+v"(ga), "+v"(gb) : "n"(4 * (YH - YG2 + layer * SG)) : "memory");
        while ((__builtin_amdgcn_ballot_w64((ga.y != epoch) | (ga.w != epoch)) | (__builtin_amdgcn_ballot_w64((gb.y != epoch) | (gb.w != epoch)) << 16)) != 0ull) {
          if (budget == 0u) break;
          --budget;
          asm volatile("buffer_load_dwordx4 %0, %2, %4, 0 offen sc1\n\tbuffer_load_dwordx4 %1, %3, %4, 0 offen sc1\n\ts_waitcnt vmcnt(0)"
                       : "=&v"(ga), "=&v"(gb) : "v"(sa), "v"(sc), "s"(rws) : "memory");
        }
        STAMP(8 + layer * 16);
        const unsigned t12 = asu(sum_ab(asf(ga.x) + asf(gb.x), asf(ga.z) + asf(gb.z)));
        const auto tt = __builtin_amdgcn_permlane16_swap(t12, t12, false, false);
        const float t1 = asf(tt[0]), t2 = asf(tt[1]);
        const float m = t1 * (1.f / D), var = fmaxf(t2 * (1.f / D) - m * m, 0.f);
        const float r1 = __builtin_amdgcn_rsqf(var + EPS);
        const float r = r1 * __builtin_amdgcn_rsqf(var * r1 * r1 + EPS), nr = -m * r;
#pragma unroll
        for (int a = 0; a < 4; ++a) {
          const v2u v = {pk(res[a][0] * r + nr, res[a][1] * r + nr), pk(res[a][2] * r + nr, res[a][3] * r + nr)};
          *(LAS v2u*)(L + (hw ^ (a << 5))) = v;
        }
#pragma unroll
        for (int e = 0; e < 2; ++e) st16<SC1>(trfrag(L + (h0 ^ (e << 6)), L + (h1 ^ (e << 6))), rws, ho, e * 2048);
        SCHED();
      }
      if (on4) {
        FRESH();
        const int sp = 8 * w + (lane & 7);
        GAS unsigned* fp = fl + (2 * 256 + ((sp & 3) * 7 + dt4) * 8 + (sp >> 2)) * 32;
        int hv[4];
#pragma unroll
        for (int b = 0; b < 4; ++b) { hv[b] = HT_OFF + layer * HT_SZ + (dt4 * 64 + 16 * w) * 4096 + ((b + w) & 3) * 1024 + lane * 16; PINV(hv[b]); }
        int uo = U_OFF + layer * U_SZ + (dt4 * 56 + 2 * ft) * 4096 + w * 1024 + lane * 16;
        PINV(fp); PINV(uo); PINV(red);
        publish(fl + (2 * 256 + bid) * 32, epoch);
        STAMP(9 + layer * 16);
        unsigned pf;
        DPUMP(YG, YH, YH2)
        STASH(SG, NS - SH)
        POLL(pf, fp);
        DPUMP(YG, YH2, YE)
        STASH(NS - SH, NS)
        unsigned late;
        POLLED(pf, 4 * (YE - YH2 + layer * SH), fp)
        STAMP(10 + layer * 16);
        v4u af[64];
#define LOAD_H(I) { int so_; asm volatile("s_mov_b32 %1, %4\n\tbuffer_load_dwordx4 %0, %2, %3, %1 offen sc1" \
          : "=&v"(af[I]), "=&s"(so_) : "v"(hv[(((I) >> 4) + 1) & 3]), "s"(rws), "n"(((I) & 15) * 4096) : "memory"); }
        sfor<W0>([&](auto I) INL { LOAD_H(I) SCHED(); });
        f32x4 acc[4][4], part[12];
        sfor<64>([&](auto I) INL {
          constexpr int i = I, b = ((i >> 4) + 1) & 3, k = i & 15;
          constexpr int n0 = issued(i, 64, W0, WIN), n1 = issued(i + 1, 64, W0, WIN);
          constexpr int np = i <= 54 ? 0 : i <= 62 ? i - 54 : 10;
          asm volatile("s_waitcnt vmcnt(%1)" : "+v"(af[i]) : "n"(n0 - 1 - i + np) : "memory");
          if constexpr (i == 0) KEEP(late);
          SCHED();
          sfor<4>([&](auto A) INL {
            constexpr int a = A;
            acc[a][b] = mfma16(wz[k][a], af[i], k > 0 ? acc[a][b] : (f32x4){0.f, 0.f, 0.f, 0.f});
            SCHED();
            if constexpr (n0 < n1 && a == 0) LOAD_H(n0)
            if constexpr (n0 + 1 < n1 && a == 3) LOAD_H(n0 + 1)
            if constexpr (i < 8) { if constexpr (a == 1) DTAIL1(YG, YE, 2 * i) if constexpr (a == 2) DTAIL1(YG, YE, 2 * i + 1) }
            if constexpr (i >= 48 && k < 2 && (a & 1)) REDW(2 * k + (a >> 1), 3);
            if constexpr (i >= 16 && i < 48 && (k & 3) == 1 && a == 2) REDW(k >> 2, i >> 4);
            if constexpr (i == 50 && a == 3) bar();
            if constexpr (i >= 51 && i < 54) part[4 * (i - 51) + a] = REDR((4 * (i - 51) + a) / 3, (4 * (i - 51) + a) % 3 + 1);
            if constexpr (i == 54 && a == 1) bar();
            if constexpr (i >= 54 && a == 2) PIECE(Z_U, np)
            if constexpr (i >= 62 && a == 3) PIECE(Z_U, np + 1)
            if constexpr (i >= 55 && i < 59 && a == 0) {
              constexpr int q = i - 55;
              part[3 * q] = (part[3 * q] + unpack4(bb[q])) + (part[3 * q + 1] + part[3 * q + 2]);
              PINV(part[3 * q]);
            }
            SCHED();
          });
        });
        STAMP(11 + layer * 16);
        v4u uf[2];
        sfor<8>([&](auto H) INL {
          constexpr int h = H, a = h >> 1, j0 = 2 * (h & 1);
          float gl[2];
#pragma unroll
          for (int j = 0; j < 2; ++j) {
            const float u = acc[a][0][j0 + j] + part[3 * a][j0 + j];
            const float e = __builtin_amdgcn_exp2f(u * (u * u * (-0.10294324f) - 2.3022082f));
            gl[j] = u * __builtin_amdgcn_rcpf(1.f + e);
          }
          uf[h >> 2][h & 3] = pk(gl[0], gl[1]);
          if constexpr (h & 1) {
            PINV(uf[h >> 2]);
            if constexpr (h == 3) st16<SC1>(uf[0], rws, uo, 0);
            if constexpr (h == 7) st16<SC1>(uf[1], rws, uo, 4096);
            SCHED();
          }
        });
        STAMP(40 + layer * 4);
      } else {
        publish(fl + (2 * 256 + blockIdx.x) * 32, epoch);
        DPUMP(YG, YH, YE)
        STASH(SG, NS)
        DRAIN();
        DTAIL(YG, YE)
        LGKM0();
        DPUMP(Z_U, Z_U, Z_U + 3)
      }
      {
        FRESH();
        const int pi = wrap7(lane & 7);
        GAS unsigned* fp = fl + (3 * 256 + (pi == 3 ? (21 + (dt & 3)) * 8 + (dt >> 2) : (7 * (pi & 3) + dt) * 8 + (pi >> 2)) + 2 * w) * 32;
        int uv = U_OFF + layer * U_SZ + (dt * 56 + 14 * w) * 4096 + lane * 16;
        int ow = L_HST + w * 2048 + c * 128 + (swz(c) << 5) + g * 8, orr = L_HST + w * 2048 + ((wrow * 128 + (lane & 7) * 16) ^ sx0);
        int xb = layer ? (64 * st + 16 * w + wrow) * 896 + dt * 128 + (lane & 7) * 16
                       : X2_OFF + (2 * st + (w >> 1)) * 28672 + (4 * dt + (g >> 1)) * 1024 + ((g & 1) * 32 + (w & 1) * 16 + c) * 16;
        int so = oob(g < 2, S5_OFF + (st * 8 + dt) * 512 + (16 * w + c) * 8 + g * 4);
#pragma unroll
        for (int a = 0; a < 4; ++a) { res[a] += bflo(b2r); PINV(res[a]); }
        PINV(fp); PINV(uv); PINV(xb); PINV(red);
        if constexpr (layer) { PINV(ow); PINV(orr); } else PINV(so);
        if constexpr (layer == 1) {
          const int dlt = L_WQ + w * 20480 - (L_KVW + w * 16384);
#pragma unroll
          for (int i = 0; i < 4; ++i) {
            LAS char* ps = pr2[i] + dlt;
#pragma unroll
            for (int j = 0; j < NS; ++j) wz[30 - NS + j][i] = trfrag(ps + j * 4096, ps + j * 4096 + 2048);
          }
          SCHED();
        }
        if (on4) publish(fl + (3 * 256 + bid) * 32, epoch);
        STAMP(12 + layer * 16);
        unsigned pf;
        DPUMP(Z_U, Z_U + 3, YU3)
        POLL(pf, fp);
        DPUMP(Z_U, YU3, YZ)
        unsigned late;
        POLLED(pf, 4 * (YZ - YU3), fp)
        STAMP(13 + layer * 16);
        v4u af[56];
#define LOAD_U(I) af[I] = ld16<SC1>(rws, uv + ((I) / 14) * 1024, ((I) % 14) * 4096)
#pragma unroll
        for (int i = 0; i < W0; ++i) { LOAD_U(i); SCHED(); }
        vmw<W0>();
        KEEP(late);
        f32x4 acc[4][4], part[4][3];
        v2u xo[4];
        f32x2 s1 = {0.f, 0.f}, s2 = {0.f, 0.f};
        sfor<56>([&](auto I) INL {
          constexpr int i = I, a = i / 14, k = i % 14;
#pragma unroll
          for (int bq = 0; bq < 4; ++bq) {
            const int b = (bq + 1) & 3;
            if (a < 3 || b) acc[a][b] = mfma16(af[i], wz[16 + k][b], k > 0 ? acc[a][b] : (f32x4){0.f, 0.f, 0.f, 0.f});
            SCHED();
            constexpr int n0 = issued(i, 56, W0, WIN), n1 = issued(i + 1, 56, W0, WIN);
            if constexpr (n0 < n1) { if (bq == 0) LOAD_U(n0); }
            if constexpr (n0 + 1 < n1) { if (bq == 3) LOAD_U(n0 + 1); }
            if constexpr (i < 8) { if (bq == 1) DTAIL1(Z_U, YZ, 2 * i) if (bq == 2) DTAIL1(Z_U, YZ, 2 * i + 1) }
            if constexpr (a > 0 && k < 3) { if (bq == 2) REDW(a - 1, k + 1); }
            if constexpr (i == 46) { if (bq == 2) bar(); }
            if constexpr (i >= 47 && i < 50) { if (bq < 3) part[i - 47][bq] = REDR(i - 47, bq + 1); }
            if constexpr (i >= 50 && i < 53) { if (bq == 2) { res[i - 50] = ((res[i - 50] + part[i - 50][0]) + (part[i - 50][1] + part[i - 50][2])) + acc[i - 50][0]; PINV(res[i - 50]); } }
            if constexpr (i >= 53) { if (bq == 2) { xo[i - 53] = (v2u){pk(res[i - 53][0], res[i - 53][1]), pk(res[i - 53][2], res[i - 53][3])}; PINV(xo[i - 53]); if constexpr (layer == 0) { stats16(res, 1 << (i - 53), s1, s2); PINV(s1); PINV(s2); } } }
            SCHED();
          }
        });
        STAMP(14 + layer * 16);
        sfor<14>([&](auto K) INL {
          constexpr int k = K;
          acc[3][0] = mfma16(af[42 + k], wz[16 + k][0], k > 0 ? acc[3][0] : (f32x4){0.f, 0.f, 0.f, 0.f});
          SCHED();
          if constexpr (k >= 1 && k < 4) REDW(3, k);
          if constexpr (k == 5) {
            if constexpr (layer == 0) st16<SC1>((v4u){xo[0].x, xo[0].y, xo[1].x, xo[1].y}, rws, xb, 0);
            else { _Pragma("unroll") for (int a = 0; a < 3; ++a) *(LAS v2u*)(L + (ow ^ (a << 5))) = xo[a]; }
          }
          if constexpr (k == 7) { bar(); part[3][0] = REDR(3, 1); part[3][1] = REDR(3, 2); part[3][2] = REDR(3, 3); }
          SCHED();
        });
        res[3] = ((res[3] + part[3][0]) + (part[3][1] + part[3][2])) + acc[3][0];
        xo[3] = (v2u){pk(res[3][0], res[3][1]), pk(res[3][2], res[3][3])};
        if constexpr (layer == 0) {
          st16<SC1>((v4u){xo[2].x, xo[2].y, xo[3].x, xo[3].y}, rws, xb, 2048);
          SCHED();
          stats16(res, 8, s1, s2);
          __builtin_amdgcn_raw_buffer_store_b32(asu(sum_ab(s1[0] + s1[1], s2[0] + s2[1])), rws, so, 0, SC1);
          SCHED();
        } else {
          const rsrc_t ro = mk(p.out, S * D * 2);
          *(LAS v2u*)(L + (ow ^ (3 << 5))) = xo[3];
          const v4u r0 = *(LAS v4u*)(L + orr), r1 = *(LAS v4u*)(L + (orr ^ 64) + 1024);
          st16<SC1>(r0, ro, xb, 0);
          st16<SC1>(r1, ro, xb, 8 * 896);
        }
      }
    } else {
      publish(fl + (1 * 256 + blockIdx.x) * 32, epoch);
      FRESH();
      const int rw = 4 * (by - 28) + (lane >> 4) + 16 * ((lane >> 2) & 3);
      const int v1 = (512 * w + 256 + rw) * (F * 2) + (7 * (bx >> 1) + 3 * (bx & 1) + (lane & 3)) * 128;
#pragma unroll 1
      for (int i = layer * (KP / 2); i < 4; ++i) { touch(mk(p.w1, S * F * 2), v1, i * 64 * F * 2, L); vmw<1>(); }
      const int v2 = (448 * w + rw) * (S * 2) + bx * 512 + (lane & 3) * 128;
#pragma unroll 1
      for (int i = 0; i < (layer ? 7 - (Z_U - 16) / 2 : 7); ++i) { touch(mk(p.w2, S * F * 2), v2, (layer ? (i < 3 ? i + 4 : i - 3 + (Z_U - 16) / 2) : i) * 64 * S * 2, L); vmw<1>(); }
      DRAIN();
    }
    if constexpr (layer == 1) STAMP(31);
    if constexpr (layer == 0) {
      FRESH();
      const int st1 = 2 * t + (w >> 1);
      f32x16 ak, av;
      v4u wk[28], wv[28];
      LAS char* wb = WB(0) + 28672;
      PINV(wb);
      wk[0] = WFRAG(wb, 0, 0); wv[0] = WFRAG(wb, 1, 0); wk[1] = WFRAG(wb, 0, 1); wv[1] = WFRAG(wb, 1, 1);
      f32x4 ck[4], cv[4];
#pragma unroll
      for (int a = 0; a < 4; ++a) { ck[a] = CSUM(1, a); cv[a] = CSUM(2, a); PINV(ck[a]); PINV(cv[a]); }
      int xo = X2_OFF + (4 * t + w) * 28672 + lane * 16, so = S5_OFF + (st1 * 8 + hh * 4) * 512 + ((w & 1) * 32 + r32) * 8;
      int so3 = oob(!hh, so + 3 * 512);
      PINV(xo); PINV(so); PINV(so3);
      kvs += KV_SZ; oo += O_SZ; fo += 5 * 256 * 128;
      PINV(kvs); PINA(oo); PINA(fo);
      GAS unsigned* fp = flags + (4 * 256 + ((st1 & 3) * 7 + wrap7(lane & 7)) * 8 + (st1 >> 2)) * 32;
      PINV(fp);
      if (on35) publish(flags + (4 * 256 + bid) * 32, epoch);
      STAMP(15);
      unsigned pf, late;
      {
        const rsrc_t rwo = mk(p.wo, on35 ? D * D * 2 : 0);
        sfor<16>([&](auto I) INL {
          constexpr int wi = I;
          if constexpr (wi == 10) POLL(pf, fp);
          dmai<wi * 1024, (28 * (wi >> 2) + 8 * (wi & 3)) * 896>(rwo, (wi & 1) ? wvo2 ^ 64 : wvo2, pring);
        });
      }
      POLLED(pf, 6, fp)
      STAMP(18);
      v2u sp[4];
#define LOAD_X(I) xf[I] = ld16<SC1>(rws, xo + ((I) & 3) * 1024, ((I) >> 2) * 4096)
#pragma unroll
      for (int i = 0; i < WX0; ++i) { LOAD_X(i); SCHED(); }
      sfor<28>([&](auto I) INL {
        constexpr int ks = I;
        if constexpr (ks == 0) MFMA32V0(ak, wk[ks], xf[ks]); else MFMA32VA(ak, wk[ks], xf[ks]);
        if constexpr (ks + 2 < 28) wk[ks + 2] = WFRAG(wb + ((ks + 2) >> 2) * 4096, 0, (ks + 2) & 3);
        constexpr int n0 = issued(ks, 28, WX0, WINX), n1 = issued(ks + 1, 28, WX0, WINX);
        if constexpr (n0 < n1) LOAD_X(n0);
        SCHED();
        if constexpr (ks == 0) MFMA32V0(av, wv[ks], xf[ks]); else MFMA32VA(av, wv[ks], xf[ks]);
        if constexpr (ks + 2 < 28) wv[ks + 2] = WFRAG(wb + ((ks + 2) >> 2) * 4096, 1, (ks + 2) & 3);
        if constexpr (n0 + 1 < n1) LOAD_X(n0 + 1);
        if constexpr (ks >= 6 && ks < 10) sp[ks - 6] = ld8<SC1>(rws, ks == 9 ? so3 : so + (ks - 6) * 512, 0);
        if constexpr (ks == 2) KEEP(late);
        if constexpr (ks == 21) {
          float s1 = (asf(sp[0].x) + asf(sp[1].x)) + (asf(sp[2].x) + asf(sp[3].x));
          float s2 = (asf(sp[0].y) + asf(sp[1].y)) + (asf(sp[2].y) + asf(sp[3].y));
          s1 = sum32(s1); s2 = sum32(s2);
          mean = s1 * (1.f / D);
          rstd = s2 * (1.f / D) - mean * mean;
          PINV(mean); PINV(rstd);
        }
        if constexpr (ks == 22) { rstd = __builtin_amdgcn_rsqf(fmaxf(rstd, 0.f) + EPS); PINV(rstd); }
        if constexpr (ks >= 23 && ks < 27) { ck[ks - 23] *= -mean * rstd; cv[ks - 23] *= -mean * rstd; PINV(ck[ks - 23]); PINV(cv[ks - 23]); }
        SCHED();
      });
      MFMA_SETTLE2(ak, av);
#pragma unroll
      for (int i = 0; i < 16; ++i) ak[i] = ak[i] * rstd + ck[i >> 2][i & 3];
      KVOUT(ak, 0)
#pragma unroll
      for (int i = 0; i < 16; ++i) av[i] = av[i] * rstd + cv[i >> 2][i & 3];
      KVOUT(av, 1)
    }
  });
  asm volatile(".Lfk_code_to:");
  if (blockIdx.x == 0 && threadIdx.x == 0) __hip_atomic_store(ctrl, epoch, RLX_AGENT);
}

extern "C" void kernel_launch(void* const* d_in, const int* in_sizes, int n_in, void* d_out, int out_size, void* d_ws,
                              size_t ws_size, hipStream_t stream) {
  Params p;
  p.x = d_in[0]; p.wqkv = d_in[1]; p.wo = d_in[2];
  p.w1 = d_in[4]; p.b1 = d_in[5]; p.w2 = d_in[6]; p.b2 = d_in[7];
  p.out = d_out; p.ws = d_ws;
  (void)hipFuncSetAttribute((const void*)fused_kernel, hipFuncAttributeMaxDynamicSharedMemorySize, LDS_BYTES);
  fused_kernel<<<256, 256, LDS_BYTES, stream>>>(p);
}
```

```cpp
#define LA 2
#define TBE 0
#include <hip/hip_runtime.h>
#include <utility>

#define GAS __attribute__((address_space(1)))
#define LAS __attribute__((address_space(3)))
#define DI __device__ __forceinline__
#define INL __attribute__((always_inline))
#define RLX_AGENT __ATOMIC_RELAXED, __HIP_MEMORY_SCOPE_AGENT
typedef unsigned v4u __attribute__((ext_vector_type(4)));
typedef unsigned v2u __attribute__((ext_vector_type(2)));
typedef float f32x2 __attribute__((ext_vector_type(2)));
typedef float f32x4 __attribute__((ext_vector_type(4)));
typedef float f32x16 __attribute__((ext_vector_type(16)));
typedef short s16x4 __attribute__((ext_vector_type(4)));
typedef __bf16 bf16x2 __attribute__((ext_vector_type(2)));
typedef __bf16 bf16x8 __attribute__((ext_vector_type(8)));
typedef __amdgpu_buffer_rsrc_t rsrc_t;

constexpr int S = 2048, D = 448, F = 1792;
constexpr float EPS = 1e-5f;
constexpr int FLAG_OFF = 4096;
constexpr int KV_OFF = 512 * 1024, KV_SZ = 16 * 64 * 4096;
constexpr int O_OFF = KV_OFF + 2 * KV_SZ, O_SZ = 32 * 16 * 4096;
constexpr int S3_OFF = O_OFF + 2 * O_SZ, S3_SZ = 32 * 8 * 1024;
constexpr int HT_OFF = S3_OFF + 2 * S3_SZ, HT_SZ = 7 * 64 * 4096;
constexpr int U_OFF = HT_OFF + 2 * HT_SZ, U_SZ = 7 * 56 * 4096;
constexpr int X2_OFF = U_OFF + 2 * U_SZ, X2_SZ = 64 * 28 * 1024;
constexpr int S5_OFF = X2_OFF + X2_SZ, S5_SZ = 32 * 8 * 512;
constexpr int S0_OFF = S5_OFF + S5_SZ, S0_SZ = S * 16;
constexpr int DBG_OFF = S0_OFF + S0_SZ;
constexpr int WS_BYTES = DBG_OFF + 256 * 128 * 8;
constexpr int L_WQ = 0;
constexpr int L_KVW = 86016;
constexpr int L_CSP = L_KVW + 65536;
constexpr int L_CS = L_CSP + 1536 + 128;
constexpr int L_HST = L_CS + 384;
constexpr int L_DUMP = L_HST + 8192;
constexpr int LDS_BYTES = L_DUMP + 256;

static_assert(L_HST % 128 == 0 && LDS_BYTES <= 163840, "LDS map");
constexpr int SC1 = 16, OOB = 0x7ffff000;
#define VT_LAYOUT 1
constexpr int WIN = 12;
constexpr int NO2 = 4, NO3 = 2, NG2 = 3, NG3 = 2, NH2 = 4, NH3 = 3, NU3 = 2;
constexpr int KP = 4, MO2 = 4, MO3 = 1, MG2 = 3, MG3 = 1, NS = 5, SG = 1, SH = 2;
constexpr int W0 = 10, WINO = 9, WO1 = 9, WX0 = 9, WINX = 10;
constexpr int issued(int i, int n, int w0, int w) { const int c = w0 + i + (i < w - w0 ? i : w - w0); return c < n ? c : n; }
#define NF1 6
constexpr int Z_U = NO2 + NO3 + NG2 + NG3 + NH2 + NH3;
static_assert(Z_U >= 16 && Z_U + 3 + NU3 <= 30 && Z_U + 3 <= 30 - NS && NS <= 5 && SG + SH <= NS, "schedule");

struct Params {
  const void *x, *wqkv, *wo, *w1, *b1, *w2, *b2;
  void *out, *ws;
};

template <int... I, class Fn> DI void sfor_(std::integer_sequence<int, I...>, Fn&& f) { (f(std::integral_constant<int, I>{}), ...); }
template <int N, class Fn> DI void sfor(Fn&& f) { sfor_(std::make_integer_sequence<int, N>{}, f); }
DI float asf(unsigned u) { return __builtin_bit_cast(float, u); }
DI unsigned asu(float f) { return __builtin_bit_cast(unsigned, f); }
DI rsrc_t mk(const void* p, int bytes) { return __builtin_amdgcn_make_buffer_rsrc((void*)p, 0, bytes, 0x00020000); }
template <int AUX> DI v4u ld16(rsrc_t r, int voff, int soff) { return __builtin_amdgcn_raw_buffer_load_b128(r, voff, soff, AUX); }
template <int AUX> DI v2u ld8(rsrc_t r, int voff, int soff) { return __builtin_amdgcn_raw_buffer_load_b64(r, voff, soff, AUX); }
template <int AUX> DI void st16(v4u v, rsrc_t r, int voff, int soff) { __builtin_amdgcn_raw_buffer_store_b128(v, r, voff, soff, AUX); }
template <int AUX> DI void st8(v2u v, rsrc_t r, int voff, int soff) { __builtin_amdgcn_raw_buffer_store_b64(v, r, voff, soff, AUX); }
DI f32x4 mfma16(v4u a, v4u b, f32x4 c) {
  return __builtin_amdgcn_mfma_f32_16x16x32_bf16(__builtin_bit_cast(bf16x8, a), __builtin_bit_cast(bf16x8, b), c, 0, 0, 0);
}
DI f32x16 mfma32(v4u a, v4u b, f32x16 c) {
  return __builtin_amdgcn_mfma_f32_32x32x16_bf16(__builtin_bit_cast(bf16x8, a), __builtin_bit_cast(bf16x8, b), c, 0, 0, 0);
}
DI unsigned pk(float lo, float hi) { return __builtin_bit_cast(unsigned, __builtin_convertvector((f32x2){lo, hi}, bf16x2)); }
DI float bflo(unsigned u) { return asf(u << 16); }
DI float bfhi(unsigned u) { return asf(u & 0xffff0000u); }
DI f32x4 unpack4(v2u v) { return (f32x4){bflo(v.x), bfhi(v.x), bflo(v.y), bfhi(v.y)}; }
DI v2u trrd(LAS char* p) { return __builtin_bit_cast(v2u, __builtin_amdgcn_ds_read_tr16_b64_v4i16((LAS s16x4*)p)); }
DI v4u trfrag(LAS char* p0, LAS char* p1) { v2u a = trrd(p0), b = trrd(p1); return (v4u){a.x, a.y, b.x, b.y}; }
DI float hi32(float v) {
  unsigned u = asu(v);
  auto r = __builtin_amdgcn_permlane32_swap(u, u, false, false);
  return asf(r[1]);
}
DI float sum32(float v) {
  unsigned u = asu(v);
  auto r = __builtin_amdgcn_permlane32_swap(u, u, false, false);
  return asf(r[0]) + asf(r[1]);
}
DI float max32(float v) {
  unsigned u = asu(v);
  auto r = __builtin_amdgcn_permlane32_swap(u, u, false, false);
  return fmaxf(asf(r[0]), asf(r[1]));
}
DI float sum_ab(float a, float b) {
  auto r = __builtin_amdgcn_permlane16_swap(asu(a), asu(b), false, false);
  return sum32(asf(r[0]) + asf(r[1]));
}
template <int CTRL> DI float dpp_add(float v) { return v + asf((unsigned)__builtin_amdgcn_update_dpp(0, (int)asu(v), CTRL, 0xf, 0xf, true)); }
DI float sum_row(float v) { return dpp_add<0x140>(dpp_add<0x141>(dpp_add<0x4e>(dpp_add<0xb1>(v)))); }
DI float dot2(unsigned a, unsigned b, float c) { return __builtin_amdgcn_fdot2_f32_bf16(__builtin_bit_cast(bf16x2, a), __builtin_bit_cast(bf16x2, b), c, false); }
DI void stats16(const f32x4 (&v)[4], int n, f32x2& s1, f32x2& s2) {
#pragma unroll
  for (int a = 0; a < 4; ++a)
    if (n >> a & 1) {
      const f32x2 lo = {v[a][0], v[a][1]}, hi = {v[a][2], v[a][3]};
      s1 += lo; s2 += lo * lo; s1 += hi; s2 += hi * hi;
    }
}
#define FENCE() asm volatile("" ::: "memory")
#define SCHED() __builtin_amdgcn_sched_barrier(0)
#define DRAIN() asm volatile("s_waitcnt vmcnt(0)" ::: "memory")
#define PINV(x) asm volatile("" : "+v"(x))
#define PINA(x) asm volatile("" : "+a"(x))
#define PINS(x) asm volatile("" : "+s"(x))
#define KEEP(x) asm volatile("" :: "v"(x))
#define LGKM0() asm volatile("s_waitcnt lgkmcnt(0)" ::: "memory")
DI void bar() { LGKM0(); __builtin_amdgcn_s_barrier(); FENCE(); }
template <int N> DI void vmw() { asm volatile("s_waitcnt vmcnt(%0)" :: "n"(N) : "memory"); }
template <int LO, int MO> DI void dmai(rsrc_t r, int voff, int lds) {
  int so;
  asm volatile("s_add_u32 m0, %1, %4\n\ts_mov_b32 %0, %5\n\tbuffer_load_dwordx4 %2, %3, %0 offen lds"
               : "=&s"(so) : "s"(lds), "v"(voff), "s"(r), "n"(LO), "n"(MO) : "memory", "scc");
}
DI void touch(rsrc_t r, int voff, int soff, LAS char* L) {
  asm volatile("s_mov_b32 m0, %0\n\ts_nop 3\n\tbuffer_load_dword %1, %2, %3 offen lds"
               :: "s"((int)(size_t)(L + L_DUMP)), "v"(voff), "s"(r), "s"(soff) : "memory");
}
#define STAMP(i) do {} while (0)

DI void publish(GAS unsigned* flag, unsigned epoch) {
  DRAIN();
  __builtin_amdgcn_s_waitcnt(0x0F70);
  bar();
  if (threadIdx.x == 0) __hip_atomic_store(flag, epoch, RLX_AGENT);
  FENCE();
}
#define POLL(f, ptr) asm volatile("global_load_dword %0, %1, off sc1" : "=&v"(f) : "v"(ptr) : "memory")
#define POLLED(f, N, ptr) { \
    unsigned f2_; \
    POLL(f2_, ptr); \
    asm volatile("s_waitcnt vmcnt(%1)" : "+v"(f) : "n"((N) + 1) : "memory"); \
    if (__builtin_amdgcn_ballot_w64(f != epoch) != 0ull) { \
      asm volatile("s_waitcnt vmcnt(0)" : "+v"(f2_) :: "memory"); \
      if (__builtin_amdgcn_ballot_w64(f2_ != epoch) != 0ull) wait_flags(ptr, epoch, budget); \
    } \
    FENCE(); \
    late = f2_; }
DI void wait_flags(GAS unsigned* flag, unsigned epoch, unsigned& budget) {
  FENCE();
  while (true) {
    unsigned f = __hip_atomic_load(flag, RLX_AGENT);
    if (__builtin_amdgcn_ballot_w64(f != epoch) == 0ull) break;
    if (budget == 0u) break;
    --budget;
  }
  __builtin_amdgcn_fence(__ATOMIC_ACQUIRE, "wavefront");
  FENCE();
}

DI int oob(bool in, int off) { return off | (in ? 0 : OOB); }
DI int wrap7(int v) { return v >= 7 ? v - 7 : v; }
DI int swz(int row) { return ((row >> 1) & 1) | (((row >> 3) ^ (row >> 2)) & 1) << 1; }

#define FRESH() \
  int tid = threadIdx.x, bid = blockIdx.x; \
  asm volatile("" : "+v"(tid)); \
  asm volatile("" : "+s"(bid)); \
  const int lane = tid & 63, w = __builtin_amdgcn_readfirstlane(tid >> 6), bx = bid & 7, by = bid >> 3; \
  const int c = lane & 15, g = lane >> 4, q4 = (lane & 15) >> 2, p4 = lane & 3, r32 = lane & 31, hh = lane >> 5; \
  const int by7 = __builtin_amdgcn_readfirstlane((by * 37) >> 8), bym = __builtin_amdgcn_readfirstlane(by - 7 * by7); \
  const int t = 2 * bx + (by >> 4), hd = by & 15;                \
  const int st = 4 * bx + by7, dt = bym;                         \
  const int dt4 = by < 21 ? bym : bym + 4 * (bx & 1), ft = 7 * (bx >> 1) + (by < 21 ? by7 + 4 * (bx & 1) : 3);    \
  LAS char* ring = L + L_KVW + w * 16384; \
  const int wrow = lane >> 3, sx0 = swz(wrow) << 5; \
  auto raddr = [&](int row) { return row * 128 + (swz(row) << 5) + 8 * p4; }; \
  LAS char* red = L + L_KVW + lane * 16; \
  (void)c; (void)g; (void)q4; (void)r32; (void)hh; (void)t; (void)hd; (void)st; (void)dt; (void)dt4; (void)ft; (void)ring; (void)sx0; (void)raddr; (void)red;

#define MFMA32V0(d, a, b) asm volatile("v_mfma_f32_32x32x16_bf16 %0, %1, %2, 0" : "=&v"(d) : "v"(a), "v"(b))
#define MFMA32VC(d, a, b, c) asm volatile("v_mfma_f32_32x32x16_bf16 %0, %1, %2, %3" : "=&v"(d) : "v"(a), "v"(b), "v"(c))
#define MFMA32VA(d, a, b) asm volatile("v_mfma_f32_32x32x16_bf16 %0, %1, %2, %0" : "+v"(d) : "v"(a), "v"(b))
#define MFMA_SETTLE(d) asm volatile("s_nop 11" : "+v"(d))
#define MFMA_SETTLE2(d, e) asm volatile("s_nop 11" : "+v"(d), "+v"(e))
DI float max3(float a, float b, float c) { float r; asm("v_max3_f32 %0, %1, %2, %3" : "=v"(r) : "v"(a), "v"(b), "v"(c)); return r; }

#define WB(cc) (L + L_WQ + (4 * hh + q4) * 64 + (g & 1) * 32 + 8 * p4 + (hd & 1) * 8 + (cc) * 4096)
#define WFRAG(wb, part, e) trfrag((wb) + (part) * 28672 + ((e) >> 1) * 2048 + ((e) & 1) * 512, (wb) + (part) * 28672 + ((e) >> 1) * 2048 + ((e) & 1) * 512 + 1024)
#define CSUM(part, a) (*(LAS f32x4*)(L + L_CS + ((part) * 32 + 8 * (a) + 4 * hh) * 4))
#define WZ_RD1(n, i, sl) wz[n][i] = (n) < 16 ? trfrag(pr1[i] + (sl) * 4096, pr1[(i) ^ 2] + (sl) * 4096 + 512) : trfrag(pr2[i] + (sl) * 4096, pr2[i] + (sl) * 4096 + 2048)
#define DMA_WZ1(n, m, sl) { \
    if ((n) < 16) dmai<(sl) * 4096 + (m) * 1024, (32 * ((n) & 15) + 8 * (m)) * F * 2>(rw1, ((m) & 1) ? pw1 ^ 64 : pw1, pring); \
    else dmai<(sl) * 4096 + (m) * 1024, (32 * ((n) & 15) + 8 * (m)) * S * 2>(rw2, ((m) & 1) ? pw2 ^ 64 : pw2, pring); }
#define PIECE(L0, q) { \
    constexpr int r_ = (q) / 4, m_ = (q) % 4, n_ = (L0) + r_; \
    if constexpr (m_ == 0 && r_ >= 4) asm volatile("s_waitcnt vmcnt(8) lgkmcnt(0)" ::: "memory"); \
    else if constexpr (m_ == 0 && r_ == 3) vmw<8>(); \
    DMA_WZ1(n_, m_, r_ & 3) \
    if constexpr (r_ >= 3) WZ_RD1(n_ - 3, m_, (r_ - 3) & 3); \
    SCHED(); }
#define DPUMP(L0, A, B) sfor<4 * ((B) - (A))>([&](auto I_) INL { PIECE(L0, 4 * ((A) - (L0)) + I_) });
#define STASH(A, B) if constexpr (layer == 1) sfor<4 * ((B) - (A))>([&](auto I_) INL { \
    constexpr int j_ = (A) + I_ / 4, m_ = I_ % 4; \
    dmai<j_ * 4096 + m_ * 1024, (32 * (14 - NS + j_) + 8 * m_) * S * 2>(rw2, (m_ & 1) ? pw2 ^ 64 : pw2, pstash); SCHED(); });
#define DTSTEP(L0, L1, sl) ((L1) - 1 - (((L1) - 1 - (L0) - (sl)) & 3))
#define DTAIL1(L0, L1, j) { if constexpr (DTSTEP(L0, L1, (j) >> 2) >= (L0) && DTSTEP(L0, L1, (j) >> 2) >= (L1) - 3) WZ_RD1(DTSTEP(L0, L1, (j) >> 2), (j) & 3, (j) >> 2); }
#define DTAIL(L0, L1) sfor<16>([&](auto J_) INL { constexpr int j_ = J_; DTAIL1(L0, L1, j_) }); SCHED();
#define REDW(a, b) *(LAS f32x4*)(red + w * 16384 + (((b) - 1) * 4 + (a)) * 1024) = acc[a][b]
#define REDR(a, b) (*(LAS f32x4*)(red + ((w + 4 - (b)) & 3) * 16384 + (((b) - 1) * 4 + (a)) * 1024))
__global__ void __launch_bounds__(256, 1) fused_kernel(Params p) {
  extern __shared__ __attribute__((aligned(16))) char smem[];
  LAS char* L = (LAS char*)smem;
  {
    unsigned long long pc;
    int len;
    asm volatile("s_getpc_b64 %0\n.Lfk_code_from:\n\ts_mov_b32 %1, .Lfk_code_to-.Lfk_code_from" : "=s"(pc), "=s"(len));
    touch(mk((const void*)pc, len > 0 ? len : 0), (threadIdx.x & 63) < 8 ? ((blockIdx.x >> 3) * 4 + (threadIdx.x >> 6) + (threadIdx.x & 7) * 128) * 128 : OOB, 0, L);
  }
  SCHED();
  const bool on35 = (blockIdx.x >> 3) < 28;
  const bool on4 = (blockIdx.x >> 3) < ((blockIdx.x & 1) ? 24 : 25);
  const rsrc_t rws = mk(p.ws, WS_BYTES);
  GAS unsigned* ctrl = (GAS unsigned*)p.ws;
  GAS unsigned* flags = (GAS unsigned*)((GAS char*)p.ws + FLAG_OFF);
  STAMP(0);
  unsigned budget = 1u << 22;

  v4u xf[28];
  v4u kv[4];
  float mean = 0.f, rstd = 0.f;
  f32x4 res[4];
  unsigned epoch;
  int kvs, oo, t1o, wvo2, pw1, pw2, fo;
  unsigned nk1, nk2, pm[8];
  LAS char *kvw, *vw, *vr0, *wor[4], *pr1[4], *pr2[4], *wbq;
#define KVOUT(acc, h) { \
    _Pragma("unroll") for (int i = 0; i < 16; i += 2) kv[2 * (h) + (i >> 3)][(i >> 1) & 3] = pk(acc[i], acc[i + 1]); \
    if (hh) { kv[2 * (h) + 1][2] = (h) ? 0x00003f80u : 0u; kv[2 * (h) + 1][3] = 0u; } \
    const rsrc_t rkv = mk(p.ws, t < 15 ? WS_BYTES : 0);    \
    st16<SC1>(kv[2 * (h)], rkv, kvs + (h) * 2048, 0); \
    st16<SC1>(kv[2 * (h) + 1], rkv, kvs + (h) * 2048 + 1024, 0); \
    SCHED(); }
#define X128(p) ((LAS char*)(size_t)((unsigned)(size_t)(p) ^ 128u))
  {
    FRESH();
    const rsrc_t rwq = mk(p.wqkv, D * 3 * D * 2);
    const rsrc_t rx = mk(p.x, S * D * 2);
    const int gid = by * 4 + w, rg = (int)(size_t)ring;
    v4u xr0, xr1;
    asm volatile("s_nop 4\n\tbuffer_load_dword %2, %5, %6, 0 offen sc1\n\tbuffer_load_dwordx4 %0, %3, %4, 0 offen\n\tbuffer_load_dwordx4 %1, %3, %4, 0 offen offset:896"
                 : "=&v"(xr0), "=&v"(xr1), "=&v"(epoch) : "v"(oob(lane < 56, (t * 128 + hd * 8 + 2 * w) * 896 + lane * 16)), "s"(rx), "v"(0), "s"(rws) : "memory");
    touch(rwq, oob(lane < 14, gid * 2688 + 896 + lane * 128), 0, L);
    const int xvo = (t * 128 + w * 32 + wrow) * 896 + (((lane & 7) ^ (wrow >> 1)) << 4);
    const int wvo = (w * 16 + (lane >> 2)) * 2688 + hd * 56 - (hd & 1) * 8 + (lane & 3) * 16;
    const int wlb = (int)(size_t)(L + L_WQ) + w * 1024;
#define DMA_W(part, cc) dmai<(part) * 28672 + (cc) * 4096, (cc) * 64 * 2688 + (part) * 896>(rwq, wvo, wlb)
#define DMA_C(cc, m) { if constexpr ((m) < 4) dmai<((cc) & 3) * 4096 + ((m) & 3) * 1024, ((m) & 3) * 8 * 896 + (cc) * 128>(rx, ((m) & 1) ? xvo ^ 64 : xvo, rg); \
                       else DMA_W((m) - 3, cc); }
    const int tb = gid * 35 + lane, tbr = (tb * 18725) >> 18;
    int tvb = oob(lane < 35, (128 + tbr) * 2688 + 896 + (tb - 14 * tbr) * 128);
    if (TBE == 1) touch(rwq, tvb, 0, L);
    sfor<NF1>([&](auto M) INL { constexpr int m = M; DMA_C(m / 6, m % 6) if (TBE == 2 && m == 5) touch(rwq, tvb, 0, L); });
    SCHED();
    LAS char* xa[8];
#pragma unroll
    for (int e = 0; e < 8; ++e) { xa[e] = ring + r32 * 128 + 8 * hh + ((e ^ ((r32 >> 1) & 7)) << 4); PINV(xa[e]); }
    int epo = w == 0 ? FLAG_OFF + (bid - 256) * 128 + lane * 32768 : S3_OFF + (st * 8 + dt) * 1024 - 2 * S3_SZ + (lane >> 3) * S3_SZ + (lane & 7) * 128;
    epo = (w == 0 ? lane - 1u < 10u : w == 1 && on35 && (lane >> 4) == 1) ? epo : 0;
    int s0o = oob((lane & 15) == 0, S0_OFF + (t * 128 + hd * 8 + 2 * w) * 16 + g * 8), s0i = S0_OFF + (t * 128 + w * 32 + r32) * 16;
    PINV(s0o); PINV(s0i);
    wbq = WB(0);
    LAS char* wb = wbq + 28672;
    LAS char* cb = L + L_WQ + 28672 + (w * 16 + 8 * hh + q4) * 64 + (g & 1) * 32 + 8 * p4;
    PINV(tvb); PINV(epo); PINV(wbq); PINV(wb); PINV(cb);
#pragma unroll
    for (int i = 0; i < 8; ++i) {
      const int d = r32 - ((2 * i & 3) + 8 * (i >> 1)) - 4 * hh;
      pm[i] = ((unsigned)(d >> 31) & 0xffffu) | ((unsigned)((d - 1) >> 31) & 0xffff0000u);
      PINA(pm[i]);
    }
#pragma unroll
    for (int i = 0; i < 4; ++i) {
      wor[i] = ring + (raddr(16 * (g >> 1) + 4 * (g & 1) + q4) ^ (i << 5));
      pr1[i] = ring + (raddr(8 * g + q4) ^ (i << 5));
      pr2[i] = ring + (raddr(4 * g + q4) ^ (((i + w) & 3) << 5));
      PINA(wor[i]); PINA(pr1[i]); PINA(pr2[i]);
    }
    SCHED();
    sfor<12 - NF1>([&](auto M) INL { constexpr int m = M + NF1; DMA_C(m / 6, m % 6) });
    touch(rws, epo, 0, L);
    if (TBE == 0) touch(rwq, tvb, 0, L);
    SCHED();
    {
      asm volatile("s_waitcnt vmcnt(15)" : "+v"(xr0), "+v"(xr1), "+v"(epoch) :: "memory");
      epoch = __builtin_amdgcn_readfirstlane(epoch) + 1u;
      float a1 = 0.f, a2 = 0.f, b1 = 0.f, b2 = 0.f;
#pragma unroll
      for (int j = 0; j < 4; ++j) { a1 = dot2(xr0[j], 0x3f803f80u, a1); a2 = dot2(xr0[j], xr0[j], a2); b1 = dot2(xr1[j], 0x3f803f80u, b1); b2 = dot2(xr1[j], xr1[j], b2); }
      const auto ra = __builtin_amdgcn_permlane16_swap(asu(a1), asu(a2), false, false), rb = __builtin_amdgcn_permlane16_swap(asu(b1), asu(b2), false, false);
      const auto rc = __builtin_amdgcn_permlane32_swap(asu(asf(ra[0]) + asf(ra[1])), asu(asf(rb[0]) + asf(rb[1])), false, false);
      st8<SC1>((v2u){asu(sum_row(asf(rc[0]) + asf(rc[1]))), epoch}, rws, s0o, 0);
      SCHED();
    }
    if constexpr (LA == 3) sfor<6>([&](auto M) INL { constexpr int m = M; DMA_C(2, m) });
    STAMP(32);
#define READ_X1(cc, e) { \
      constexpr int ch = ((e) >> 1) * 4 + ((e) & 1); \
      const v2u a_ = *(LAS v2u*)(xa[ch] + ((cc) & 3) * 4096), b_ = *(LAS v2u*)(xa[ch + 2] + ((cc) & 3) * 4096); \
      xf[4 * (cc) + (e)] = (v4u){a_.x, a_.y, b_.x, b_.y}; }
    const v4u ones = {0x3f803f80u, 0x3f803f80u, 0x3f803f80u, 0x3f803f80u};
    f32x16 ak, av, csk = {}, csv = {};
    v4u wk[28], wv[28], wc[14], gx;
    float pp[4];
    f32x4 cc[8];
    int tvq, tvw;
    LAS float *pb, *pw;
    sfor<8>([&](auto R) INL {
      constexpr int r = R;
      if constexpr (r < 7) {
        vmw<(LA == 2 ? (r == 0 ? 9 - (TBE == 1) : r == 1 ? 9 - (TBE > 0) : r == 5 ? 8 : r == 6 ? 4 : 6) : (r == 0 ? 15 - (TBE == 1) : r == 1 ? 15 - (TBE > 0) : r <= 4 ? 12 : r == 5 ? 6 + 2 : 2 + 2))>();
        if constexpr (LA == 3 && r > 0) LGKM0();
        __builtin_amdgcn_s_barrier();
        FENCE();
        SCHED();
      }
      sfor<10>([&](auto Q) INL {
        constexpr int q = Q, e = q < 4 ? q >> 1 : q >= 6 ? (q >> 1) - 1 : -1;
        if constexpr (e >= 0 && r > 0) {
          constexpr int km = 4 * (r - 1) + e;
          if constexpr (q & 1) { if constexpr (km == 0) MFMA32V0(av, wv[km], xf[km]); else MFMA32VA(av, wv[km], xf[km]); }
          else { if constexpr (km == 0) MFMA32V0(ak, wk[km], xf[km]); else MFMA32VA(ak, wk[km], xf[km]); }
        }
        if constexpr (r < 7) {
          if constexpr (q == 4) csk = mfma32(ones, wc[2 * r], csk);
          if constexpr (q == 5) csv = mfma32(ones, wc[2 * r + 1], csv);
          SCHED();
          if constexpr (q < 2) wc[2 * r + q] = trfrag(cb + q * 28672 + r * 4096, cb + q * 28672 + r * 4096 + 256);
          if constexpr (q >= 2) { constexpr int f = q - 2; if constexpr (f & 1) wv[4 * r + f / 2] = WFRAG(wb + r * 4096, 1, f / 2); else wk[4 * r + f / 2] = WFRAG(wb + r * 4096, 0, f / 2); }
          if constexpr (q == 0 || q == 3 || q == 5 || q == 8) READ_X1(r, (q == 0 ? 0 : q == 3 ? 1 : q == 5 ? 2 : 3))
        }
        constexpr int dm = q == 1 ? 0 : q == 2 ? 1 : q == 4 ? 2 : q == 6 ? 3 : q == 7 ? 4 : q == 9 ? 5 : -1;
        if constexpr (r + LA < 7 && dm >= 0) DMA_C(r + LA, dm)
        if constexpr (r == 4 && q == (LA == 3 ? 1 : 9)) { touch(rwq, tvq, 0, L); touch(mk(p.wo, D * D * 2), tvw, 0, L); }
        if constexpr (r == 6 && q == 0) asm volatile("buffer_load_dwordx4 %0, %1, %2, 0 offen sc1" : "=&v"(gx) : "v"(s0i), "s"(rws) : "memory");
        if constexpr (r == 5 && (q == 6 || q == 9)) DMA_W(0, q / 9);
        if constexpr (r == 6 && q % 3 == 2) DMA_W(0, 2 + q / 3);
        if constexpr (r == 7 && (q == 1 || q == 7)) DMA_W(0, 5 + q / 7);
        if constexpr (q == 3 || q == 8) {
          constexpr int i = 2 * r + (q == 8);
          if constexpr (i == 0) { kvs = KV_OFF + (hd * 64 + 4 * t + w) * 4096 + lane * 16; PINV(kvs); }
          if constexpr (i == 1) { oo = O_OFF + ((2 * t + (w >> 1)) * 16 + hd) * 4096 + (2 * (w & 1) + (r32 >> 4)) * 1024 + hh * 256 + c * 16; PINA(oo); }
          if constexpr (i == 2) { t1o = oob(lane < 32, (512 * (by >> 3) + 8 * ((by & 7) * 4 + w) + (lane >> 2)) * (F * 2) + (7 * (bx >> 1) + 3 * (bx & 1) + (lane & 3)) * 128); PINA(t1o); }
          if constexpr (i == 3) { wvo2 = ((112 * w + wrow) * 896 + dt * 128 + (lane & 7) * 16) ^ sx0; PINA(wvo2); }
          if constexpr (i == 4) { pw1 = ((512 * w + wrow) * (F * 2) + ft * 128 + (lane & 7) * 16) ^ sx0; PINA(pw1); }
          if constexpr (i == 5) { pw2 = ((448 * w + wrow) * (S * 2) + st * 128 + (lane & 7) * 16) ^ sx0; PINA(pw2); }
          if constexpr (i == 6) {
            const int tl = by * 98 + w * 25 + lane, trow = (tl * 9363) >> 16;
            tvq = oob(lane < 25 && w * 25 + lane < 98, trow * 2688 + (tl - 7 * trow) * 128); tvw = oob(lane < 25 && w * 25 + lane < 98, tl * 128);
            PINV(tvq); PINV(tvw);
          }
          if constexpr (i == 7) {
            const int tp = (t >= 2 && (lane & 1)) ? t - 2 : t > 0 ? t - 1 : 0;
            fo = FLAG_OFF + (((tp & 1) * 16 + hd) * 8 + (tp >> 1)) * 128;
            PINA(fo);
          }
          if constexpr (i == 8) { nk2 = (t < 2 && hh) ? 0xf149u : 0u; nk1 = (t < 1 && hh) ? 0xf149u : 0u; PINA(nk1); PINA(nk2); }
          if constexpr (i == 9) { kvw = L + L_KVW + w * 4096 + lane * 16; vw = L + L_KVW + w * 4096 + 2048 + ((lane * 16) ^ (hh << 6)); PINA(kvw); PINA(vw); }
          if constexpr (i == 10) { vr0 = L + L_KVW + w * 4096 + 2048 + ((((g & 1) * 1024 + (p4 & 1) * 512 + hh * 64 + q4 * 16) ^ ((p4 & 1) << 6) ^ ((g & 1) << 7)) + (p4 >> 1) * 8); PINA(vr0); }
          if constexpr (i == 11) {
            pb = (LAS float*)(L + L_CSP) + 32 + lane + 4 * (hd & 1);
            pw = (LAS float*)(L + (hh ? L_DUMP : L_CSP + (w * 96 + 32) * 4)) + r32;
            PINV(pb); PINV(pw);
          }
        }
        if constexpr (r == 6 && q == 7) { pw[0] = csk[0]; pw[32] = csv[0]; }
        if constexpr (r == 6 && q == 9) { bar(); pp[0] = pb[0]; pp[1] = pb[96]; pp[2] = pb[192]; pp[3] = pb[288]; }
        if constexpr (r == 7 && q == 2) *(LAS float*)(L + L_CS + (32 + lane) * 4) = (pp[0] + pp[1]) + (pp[2] + pp[3]);
        if constexpr (r == 7 && (q == 3 || q == 6)) { _Pragma("unroll") for (int j = 0; j < 4; ++j) cc[4 * (q / 6) + j] = CSUM(1, 4 * (q / 6) + j); }
        SCHED();
      });
    });
    asm volatile("s_waitcnt vmcnt(5)" : "+v"(gx) :: "memory");
    while (__builtin_amdgcn_ballot_w64((gx.y != epoch) | (gx.w != epoch)) != 0ull) {
      if (budget == 0u) break;
      --budget;
      asm volatile("buffer_load_dwordx4 %0, %1, %2, 0 offen sc1\n\ts_waitcnt vmcnt(0)" : "=&v"(gx) : "v"(s0i), "s"(rws) : "memory");
    }
    mean = asf(gx.x) * (1.f / D);
    rstd = __builtin_amdgcn_rsqf(fmaxf(asf(gx.z) * (1.f / D) - mean * mean, 0.f) + EPS);
    STAMP(34);
    MFMA_SETTLE2(ak, av);
#pragma unroll
    for (int i = 0; i < 16; ++i) ak[i] = (ak[i] - mean * cc[i >> 2][i & 3]) * rstd;
    KVOUT(ak, 0)
#pragma unroll
    for (int i = 0; i < 16; ++i) av[i] = (av[i] - mean * cc[4 + (i >> 2)][i & 3]) * rstd;
    KVOUT(av, 1)
  }

  unsigned b2r;
  v2u bb[4];
  v4u wz[30][4];
  sfor<2>([&](auto LY) INL {
    constexpr int layer = LY;
    constexpr int Y0 = layer ? KP : 0, Y2 = Y0 + (layer ? MO2 : NO2), YG = Y2 + (layer ? MO3 : NO3), YG2 = YG + (layer ? MG2 : NG2),
                  YH = YG2 + (layer ? MG3 : NG3), YE = layer ? 16 : Z_U, YH2 = layer ? YE : YH + NH2, YZ = layer ? 30 - NS : 30, YU3 = layer ? YZ : 30 - NU3;
    static_assert(YH <= YH2, "schedule");
    constexpr int QPUB = layer ? 14 : 10;
    GAS unsigned* fl = flags + layer * 5 * 256 * 32;
    STAMP(1 + layer * 16);
    v4u qf[2];
    v4u wo[4][4];
    v2u rr[4];
    int pring, pstash;
    const rsrc_t rw1 = mk(p.w1, on4 ? S * F * 2 : 0);
    const rsrc_t rw2 = mk(p.w2, on35 ? S * F * 2 : 0);
    {
      const bool hi = threadIdx.x & 32;
      FRESH();
      if constexpr (layer == 0) {
        vmw<4>();
        if (t == 15) vmw<0>();
        __builtin_amdgcn_s_barrier();
        FENCE();
      }
      v4u wc[7];
      f32x16 cs = {};
      LAS char* cb = L + L_WQ + (w * 16 + 8 * hh + q4) * 64 + (g & 1) * 32 + 8 * p4;
      v4u wq[28];
#pragma unroll
      for (int ks = 0; ks < 4; ++ks) wq[ks] = WFRAG(wbq, 0, ks);
      f32x16 aq;
      STAMP(2 + layer * 43);
      const rsrc_t rwo = mk(p.wo, on35 ? D * D * 2 : 0);
      pring = (int)(size_t)ring;
      pstash = (int)(size_t)(L + L_WQ) + w * 20480;
      PINS(pring); PINS(pstash);
      GAS unsigned* fp = (GAS unsigned*)((GAS char*)p.ws + fo);
      unsigned pf;
      sfor<28>([&](auto I) INL {
        constexpr int ks = I;
        if constexpr (ks == 0) MFMA32V0(aq, wq[ks], xf[ks]); else MFMA32VA(aq, wq[ks], xf[ks]);
        if constexpr (ks + 4 < 28) wq[ks + 4] = WFRAG(wbq + ((ks + 4) >> 2) * 4096, 0, ks & 3);
        constexpr int wi = layer ? -1 : ks - QPUB - 1;
        if constexpr (wi >= 0 && wi < 16) dmai<wi * 1024, (28 * (wi >> 2) + 8 * (wi & 3)) * 896>(rwo, (wi & 1) ? wvo2 ^ 64 : wvo2, pring);
        if constexpr (layer == 0) {
          if constexpr (ks < 7) wc[ks] = trfrag(cb + ks * 4096, cb + ks * 4096 + 256);
          if constexpr (ks >= 2 && ks < 9) cs = mfma32((v4u){0x3f803f80u, 0x3f803f80u, 0x3f803f80u, 0x3f803f80u}, wc[ks - 2], cs);
          if constexpr (ks == QPUB + 1) { _Pragma("unroll") for (int q = 0; q < 4; ++q) cs[q] = ((LAS float*)(L + L_CSP) + r32 + 4 * (hd & 1))[96 * q]; }
          if constexpr (ks == QPUB + 5) *(LAS float*)(L + L_CS + r32 * 4) = (cs[0] + cs[1]) + (cs[2] + cs[3]);
        }
        if constexpr (ks == 27) POLL(pf, fp);
        if constexpr (ks == QPUB) {
          if constexpr (layer == 0) { *(LAS float*)(L + (hh ? L_DUMP : L_CSP + w * 96 * 4) + r32 * 4) = cs[0]; }
          publish(fl + (0 * 256 + bid) * 32, epoch);
          STAMP(3 + layer * 16);
        }
        SCHED();
      });
      f32x4 cq[4];
#pragma unroll
      for (int a = 0; a < 4; ++a) cq[a] = CSUM(0, a);
      MFMA_SETTLE(aq);
      const float qs = rstd * (0.18898223650461362f * 1.4426950408889634f);
#pragma unroll
      for (int a = 0; a < 4; ++a) {
        qf[a >> 1][(a & 1) * 2] = pk((aq[4 * a] - mean * cq[a][0]) * qs, (aq[4 * a + 1] - mean * cq[a][1]) * qs);
        qf[a >> 1][(a & 1) * 2 + 1] = pk((aq[4 * a + 2] - mean * cq[a][2]) * qs, (aq[4 * a + 3] - mean * cq[a][3]) * qs);
      }
      if (hi) { qf[1][2] = 0x00003f80u; qf[1][3] = 0u; }
      PINV(qf[0]); PINV(qf[1]);
      f32x16 s8;
      asm volatile("s_nop 1" : "+v"(qf[0]), "+v"(qf[1]));
      MFMA32V0(s8, kv[0], qf[0]);
      MFMA32VA(s8, kv[1], qf[1]);
      SCHED();
      unsigned late;
      POLLED(pf, 0, fp)
      STAMP(4 + layer * 16);
      v4u ra[4], rb[4];
      {
        const rsrc_t r1 = mk(p.ws, t >= 1 ? WS_BYTES : 0), r2 = mk(p.ws, t >= 2 ? WS_BYTES : 0);
        const int kvo = kvs - 32768;
        rb[0] = ld16<SC1>(r2, kvo, 0); rb[1] = ld16<SC1>(r2, kvo, 1024);
        ra[0] = ld16<SC1>(r1, kvo, 16384); ra[1] = ld16<SC1>(r1, kvo, 16384 + 1024);
        SCHED();
        rb[2] = ld16<SC1>(r2, kvo, 2048); rb[3] = ld16<SC1>(r2, kvo, 3072);
        ra[2] = ld16<SC1>(r1, kvo, 16384 + 2048); ra[3] = ld16<SC1>(r1, kvo, 16384 + 3072);
        SCHED();
      }
#pragma unroll
      for (int j = 0; j < 16; ++j) wo[j >> 2][j & 3] = trfrag(wor[j & 3] + (j >> 2) * 4096, wor[(j & 3) ^ 2] + (j >> 2) * 4096 + 1024);
      SCHED();
      bar();
      LAS char* vw1 = X128(vw) + 1024;
      LAS char* vr1 = X128(vr0);
      *(LAS v4u*)(kvw + 32768) = kv[0];
      *(LAS v4u*)(kvw + 32768 + 1024) = kv[1];
      *(LAS v4u*)(vw + 32768) = kv[2];
      *(LAS v4u*)(vw1 + 32768) = kv[3];
      int rro = (64 * st + 16 * w + c) * 896 + dt * 128 + g * 8, b2o = (64 * st + 16 * w + c) * 2, b1o = ft * 128 + g * 8;
      unsigned pml[8], mk32[16], nk1l = nk1, nk2l = nk2;
      int ool = oo, t1l = t1o;
      LAS char* vr0l = vr0;
      if constexpr (layer == 0) { PINV(rro); PINV(b2o); PINV(b1o); }
      PINV(vw1); PINV(vr1); PINV(vr0l); PINV(ool); PINV(t1l); PINV(nk1l); PINV(nk2l);
#pragma unroll
      for (int i = 0; i < 8; ++i) { pml[i] = pm[i]; PINV(pml[i]); mk32[2 * i] = (unsigned)((int)(pml[i] << 16) >> 16); mk32[2 * i + 1] = (unsigned)((int)pml[i] >> 16); PINV(mk32[2 * i]); PINV(mk32[2 * i + 1]); }
      SCHED();
      f32x16 sc[8], se;
      v4u kf[8][2];
      float mx = -1e30f, mx2 = -1e30f;
#define MAX8(v, i) asm volatile("v_max3_f32 %0, %0, %2, %3\n\tv_max3_f32 %1, %1, %4, %5\n\tv_max3_f32 %0, %0, %6, %7\n\tv_max3_f32 %1, %1, %8, %9" \
        : "+v"(mx), "+v"(mx2) : "v"(v[i]), "v"(v[(i) + 1]), "v"(v[(i) + 2]), "v"(v[(i) + 3]), "v"(v[(i) + 4]), "v"(v[(i) + 5]), "v"(v[(i) + 6]), "v"(v[(i) + 7]))
      *(LAS v4u*)(kvw) = rb[0];
      MFMA32V0(sc[0], rb[0], qf[0]);
      rb[1][2] |= nk2l;
      *(LAS v4u*)(kvw + 1024) = rb[1];
      *(LAS v4u*)(kvw + 16384) = ra[0];
      SCHED();
      MFMA32VA(sc[0], rb[1], qf[1]);
      ra[1][2] |= nk1l;
      *(LAS v4u*)(kvw + 16384 + 1024) = ra[1];
      kf[4][0] = ra[0]; kf[4][1] = ra[1];
      MFMA32V0(sc[4], kf[4][0], qf[0]);
      bar();
      KEEP(late);
      STAMP(37 + layer * 4);
      MFMA32VA(sc[4], kf[4][1], qf[1]);
#pragma unroll
      for (int j = 1; j < 8; ++j) if (j != 4) { kf[j][0] = *(LAS v4u*)(kvw + j * 4096); kf[j][1] = *(LAS v4u*)(kvw + j * 4096 + 1024); }
      PINV(sc[0]);
#pragma unroll
      for (int i = 0; i < 16; ++i) se[i] = asf((asu(sc[0][i]) & mk32[i]) | (asu(s8[i]) & ~mk32[i]));
      PINV(se);
      SCHED();
      sfor<12>([&](auto N) INL {
        constexpr int n = N, jj = n >> 1, j = jj < 3 ? jj + 1 : jj + 2, hf = n & 1;
        if constexpr (hf == 0) MFMA32V0(sc[j], kf[j][0], qf[0]); else MFMA32VA(sc[j], kf[j][1], qf[1]);
        if constexpr (n < 2) MAX8(se, 8 * hf);
        else if constexpr (n < 4) MAX8(sc[4], 8 * hf);
        else MAX8(sc[jj < 5 ? jj - 1 : jj], 8 * hf);
        if constexpr (n == 2) *(LAS v4u*)(vw) = rb[2];
        if constexpr (n == 3) *(LAS v4u*)(vw1) = rb[3];
        if constexpr (n == 4) *(LAS v4u*)(vw + 16384) = ra[2];
        if constexpr (n == 5) *(LAS v4u*)(vw1 + 16384) = ra[3];
        if constexpr (n == 6) touch(mk(p.w1, S * F * 2), t1l, layer * KP * 32 * F * 2, L);
        if constexpr (n >= 7) asm volatile("s_nop 0");
        SCHED();
      });
      MAX8(sc[6], 0); MAX8(sc[6], 8); MAX8(sc[7], 0); MAX8(sc[7], 8);
      mx = max32(max3(mx, mx2, mx2));
      f32x16 nm;
#pragma unroll
      for (int i = 0; i < 16; ++i) nm[i] = -mx;
      PINV(nm);
      bar();
      STAMP(38 + layer * 4);
      f32x16 ao;
      v4u vf[9][2], pp[9][2];
      float ex[32][4];
#define VF(j, s) vf[j][s] = trfrag(vr0l + (j) * 4096 + (s) * 256, vr1 + (j) * 4096 + (s) * 256)
      sfor<33>([&](auto N) INL {
        constexpr int n = N, j = n >> 2, q = n & 3;
        if constexpr (j == 0) {
          if constexpr (q == 0) MFMA32VC(sc[1], kf[1][0], qf[0], nm);
          if constexpr (q == 1) MFMA32VA(sc[1], kf[1][1], qf[1]);
          if constexpr (q == 2) MFMA32VC(sc[2], kf[2][0], qf[0], nm);
          if constexpr (q == 3) MFMA32VA(sc[2], kf[2][1], qf[1]);
        } else if constexpr (j < 8) {
          if constexpr (q == 1) { if constexpr (j == 1) MFMA32V0(ao, vf[0][0], pp[0][0]); else MFMA32VA(ao, vf[j - 1][0], pp[j - 1][0]); }
          if constexpr (q == 2) MFMA32VA(ao, vf[j - 1][1], pp[j - 1][1]);
          if constexpr (j + 2 < 8) {
            if constexpr (q == 0) MFMA32VC(sc[j + 2], kf[j + 2][0], qf[0], nm);
            if constexpr (q == 3) MFMA32VA(sc[j + 2], kf[j + 2][1], qf[1]);
          } else if constexpr (j == 6) {
            if constexpr (q == 0) MFMA32VA(ao, vf[8][0], pp[8][0]);
            if constexpr (q == 3) MFMA32VA(ao, vf[8][1], pp[8][1]);
          }
        } else MFMA32VA(ao, vf[7][0], pp[7][0]);
        SCHED();
        if constexpr (n < 32) {
#pragma unroll
          for (int i = 0; i < 4; ++i) { if constexpr (j == 0) ex[n][i] = __builtin_amdgcn_exp2f(se[4 * q + i] - mx); else ex[n][i] = __builtin_amdgcn_exp2f(sc[j][4 * q + i]); }
        }
        SCHED();
        if constexpr (n > 0) {
          constexpr int jp = (n - 1) >> 2, qp = (n - 1) & 3;
#pragma unroll
          for (int i = 0; i < 2; ++i) {
            const unsigned u = pk(ex[n - 1][2 * i], ex[n - 1][2 * i + 1]);
            if constexpr (jp == 0) { pp[0][qp >> 1][(2 * qp + i) & 3] = u & pml[2 * qp + i]; pp[8][qp >> 1][(2 * qp + i) & 3] = u & ~pml[2 * qp + i]; }
            else pp[jp][qp >> 1][(2 * qp + i) & 3] = u;
          }
        }
        if constexpr (layer == 0) {
          if constexpr (n >= 2 && n < 10 && q >= 2) rr[(n >> 2) * 2 + q - 2] = ld8<0>(mk(p.x, on35 ? S * D * 2 : 0), rro, ((n >> 2) * 2 + q - 2) * 32);
          if constexpr (n == 10) b2r = (unsigned)__builtin_amdgcn_raw_buffer_load_b16(mk(p.b2, S * 2), b2o, 0, 0);
          if constexpr (n >= 14 && n < 22 && q >= 2) bb[((n >> 2) - 3) * 2 + q - 2] = ld8<0>(mk(p.b1, F * 2), b1o, (((n >> 2) - 3) * 2 + q - 2) * 32);
        }
        if constexpr (n == 22) KEEP(nm);
        if constexpr (j < 8 && q < 2) VF(j, q);
        if constexpr (j == 5 && q >= 2) VF(8, q - 2);
        SCHED();
      });
      asm volatile("s_nop 1" : "+v"(pp[7][1]));
      MFMA32VA(ao, vf[7][1], pp[7][1]);
      MFMA_SETTLE(ao);
      const float inv = __builtin_amdgcn_rcpf(hi32(ao[12]));
      v4u of[2];
#pragma unroll
      for (int i = 0; i < 16; i += 2) of[i >> 3][(i >> 1) & 3] = pk(ao[i] * inv, ao[i + 1] * inv);
      if (hi) { of[1][2] = 0u; of[1][3] = 0u; }
      st16<SC1>(of[0], rws, ool, 0);
      st16<SC1>(of[1], rws, ool, 512);
      SCHED();
      STAMP(39 + layer * 4);
    }
    if (on35) {
      {
        FRESH();
        const int tq = st >> 1;
        GAS unsigned* fp = fl + (1 * 256 + (((tq & 1) * 16 + 4 * w + (lane & 3)) * 8 + (tq >> 1))) * 32;
        int oo = O_OFF + layer * O_SZ + (st * 16 + 4 * w) * 4096 + lane * 16;
        const int sb = S3_OFF + layer * S3_SZ + st * 8192 + (16 * w + c) * 16;
        int sw = oob(g < 2, sb + dt * 1024 + g * 8), sa = sb + g * 1024, sc = oob(g < 3, sb + (g + 4) * 1024);
        int ho = HT_OFF + layer * HT_SZ + (dt * 64 + 2 * st + (w >> 1)) * 4096 + (g >> 1) * 1024 + (2 * (w & 1) + (g & 1)) * 256 + c * 16;
        const int hr = 8 * (g & 1) + q4, hst = L_HST + w * 2048;
        int h0 = hst + (raddr(hr) ^ ((g >> 1) << 5)), h1 = hst + (raddr(hr + 4) ^ ((g >> 1) << 5)), hw = hst + c * 128 + (swz(c) << 5) + g * 8;
        int ob[4];
#pragma unroll
        for (int b = 0; b < 4; ++b) { ob[b] = ((b + w) & 3) * 1024; PINS(ob[b]); }
        PINV(fp); PINV(oo); PINV(sw); PINV(sa); PINV(sc); PINV(ho); PINV(h0); PINV(h1); PINV(hw); PINV(red);
        publish(fl + (1 * 256 + bid) * 32, epoch);
        STAMP(5 + layer * 16);
        unsigned pf;
        DPUMP(Y0, Y0, Y2)
        if constexpr (layer == 0) {
#pragma unroll
          for (int a = 0; a < 4; ++a) { res[a] = unpack4(rr[a]); PINV(res[a]); }
        }
        POLL(pf, fp);
        DPUMP(Y0, Y2, YG)
        unsigned late;
        POLLED(pf, 4 * (YG - Y2), fp)
        STAMP(6 + layer * 16);
        v4u of[16];
#define LOAD_O(I) of[I] = ld16<SC1>(rws, oo + ((I) & 3) * 4096, ob[(((I) >> 2) + 1) & 3])
#pragma unroll
        for (int i = 0; i < WINO; ++i) { LOAD_O(i); SCHED(); }
        vmw<WINO>();
        KEEP(late);
        STAMP(50 + layer * 4);
        f32x4 acc[4][4], part[12];
        sfor<16>([&](auto I) INL {
          constexpr int i = I, b = ((i >> 2) + 1) & 3, k = i & 3;
#pragma unroll
          for (int a = 0; a < 4; ++a) {
            acc[a][b] = mfma16(wo[k][a], of[i], k > 0 ? acc[a][b] : (f32x4){0.f, 0.f, 0.f, 0.f});
            SCHED();
            constexpr int n0 = issued(i, 16, WINO, WO1), n1 = issued(i + 1, 16, WINO, WO1);
            if constexpr (n0 < n1) { if (a == 0) LOAD_O(n0); }
            if constexpr (n0 + 1 < n1) { if (a == 3) LOAD_O(n0 + 1); }
            if constexpr (i < 12) { if (a == 1) DTAIL1(Y0, YG, i) }
            if constexpr (i >= 12 && k < 2) { if (a == 1 || a == 3) REDW(2 * k + (a >> 1), 3); }
            if constexpr (i >= 4 && i < 12) { if (a == 2) REDW(k, i >> 2); }
            if constexpr (i == 14) { if (a == 0) bar(); }
            if constexpr (i >= 14) { if (a < 3) { const int j = 6 * (i - 14) + 2 * a; part[j] = REDR(j / 3, j % 3 + 1); part[j + 1] = REDR((j + 1) / 3, (j + 1) % 3 + 1); } }
            SCHED();
          }
        });
        STAMP(51 + layer * 4);
#pragma unroll
        for (int a = 0; a < 4; ++a) res[a] = ((res[a] + part[3 * a]) + (part[3 * a + 1] + part[3 * a + 2])) + acc[a][0];
        f32x2 s1 = {0.f, 0.f}, s2 = {0.f, 0.f};
        stats16(res, 15, s1, s2);
        st8<SC1>((v2u){asu(sum_ab(s1[0] + s1[1], s2[0] + s2[1])), epoch}, rws, sw, 0);
        SCHED();
        DTAIL1(Y0, YG, 12) DTAIL1(Y0, YG, 13) DTAIL1(Y0, YG, 14) DTAIL1(Y0, YG, 15)
        STAMP(7 + layer * 16);
        bar();
        DPUMP(YG, YG, YG2)
        v4u ga, gb;
        asm volatile("buffer_load_dwordx4 %0, %2, %4, 0 offen sc1\n\tbuffer_load_dwordx4 %1, %3, %4, 0 offen sc1" : "=&v"(ga), "=&v"(gb) : "v"(sa), "v"(sc), "s"(rws) : "memory");
        DPUMP(YG, YG2, YH)
        STASH(0, SG)
        asm volatile("s_waitcnt vmcnt(%2)" : "+v"(ga), "+v"(gb) : "n"(4 * (YH - YG2 + layer * SG)) : "memory");
        while ((__builtin_amdgcn_ballot_w64((ga.y != epoch) | (ga.w != epoch)) | (__builtin_amdgcn_ballot_w64((gb.y != epoch) | (gb.w != epoch)) << 16)) != 0ull) {
          if (budget == 0u) break;
          --budget;
          asm volatile("buffer_load_dwordx4 %0, %2, %4, 0 offen sc1\n\tbuffer_load_dwordx4 %1, %3, %4, 0 offen sc1\n\ts_waitcnt vmcnt(0)"
                       : "=&v"(ga), "=&v"(gb) : "v"(sa), "v"(sc), "s"(rws) : "memory");
        }
        STAMP(8 + layer * 16);
        const unsigned t12 = asu(sum_ab(asf(ga.x) + asf(gb.x), asf(ga.z) + asf(gb.z)));
        const auto tt = __builtin_amdgcn_permlane16_swap(t12, t12, false, false);
        const float t1 = asf(tt[0]), t2 = asf(tt[1]);
        const float m = t1 * (1.f / D), var = fmaxf(t2 * (1.f / D) - m * m, 0.f);
        const float r1 = __builtin_amdgcn_rsqf(var + EPS);
        const float r = r1 * __builtin_amdgcn_rsqf(var * r1 * r1 + EPS), nr = -m * r;
#pragma unroll
        for (int a = 0; a < 4; ++a) {
          const v2u v = {pk(res[a][0] * r + nr, res[a][1] * r + nr), pk(res[a][2] * r + nr, res[a][3] * r + nr)};
          *(LAS v2u*)(L + (hw ^ (a << 5))) = v;
        }
#pragma unroll
        for (int e = 0; e < 2; ++e) st16<SC1>(trfrag(L + (h0 ^ (e << 6)), L + (h1 ^ (e << 6))), rws, ho, e * 2048);
        SCHED();
      }
      if (on4) {
        FRESH();
        const int sp = 8 * w + (lane & 7);
        GAS unsigned* fp = fl + (2 * 256 + ((sp & 3) * 7 + dt4) * 8 + (sp >> 2)) * 32;
        int hv[4];
#pragma unroll
        for (int b = 0; b < 4; ++b) { hv[b] = HT_OFF + layer * HT_SZ + (dt4 * 64 + 16 * w) * 4096 + ((b + w) & 3) * 1024 + lane * 16; PINV(hv[b]); }
        int uo = U_OFF + layer * U_SZ + (dt4 * 56 + 2 * ft) * 4096 + w * 1024 + lane * 16;
        PINV(fp); PINV(uo); PINV(red);
        publish(fl + (2 * 256 + bid) * 32, epoch);
        STAMP(9 + layer * 16);
        unsigned pf;
        DPUMP(YG, YH, YH2)
        STASH(SG, NS - SH)
        POLL(pf, fp);
        DPUMP(YG, YH2, YE)
        STASH(NS - SH, NS)
        unsigned late;
        POLLED(pf, 4 * (YE - YH2 + layer * SH), fp)
        STAMP(10 + layer * 16);
        v4u af[64];
#define LOAD_H(I) { int so_; asm volatile("s_mov_b32 %1, %4\n\tbuffer_load_dwordx4 %0, %2, %3, %1 offen sc1" \
          : "=&v"(af[I]), "=&s"(so_) : "v"(hv[(((I) >> 4) + 1) & 3]), "s"(rws), "n"(((I) & 15) * 4096) : "memory"); }
        sfor<W0>([&](auto I) INL { LOAD_H(I) SCHED(); });
        f32x4 acc[4][4], part[12];
        sfor<64>([&](auto I) INL {
          constexpr int i = I, b = ((i >> 4) + 1) & 3, k = i & 15;
          constexpr int n0 = issued(i, 64, W0, WIN), n1 = issued(i + 1, 64, W0, WIN);
          constexpr int np = i <= 54 ? 0 : i <= 62 ? i - 54 : 10;
          asm volatile("s_waitcnt vmcnt(%1)" : "+v"(af[i]) : "n"(n0 - 1 - i + np) : "memory");
          if constexpr (i == 0) KEEP(late);
          SCHED();
          sfor<4>([&](auto A) INL {
            constexpr int a = A;
            acc[a][b] = mfma16(wz[k][a], af[i], k > 0 ? acc[a][b] : (f32x4){0.f, 0.f, 0.f, 0.f});
            SCHED();
            if constexpr (n0 < n1 && a == 0) LOAD_H(n0)
            if constexpr (n0 + 1 < n1 && a == 3) LOAD_H(n0 + 1)
            if constexpr (i < 8) { if constexpr (a == 1) DTAIL1(YG, YE, 2 * i) if constexpr (a == 2) DTAIL1(YG, YE, 2 * i + 1) }
            if constexpr (i >= 48 && k < 2 && (a & 1)) REDW(2 * k + (a >> 1), 3);
            if constexpr (i >= 16 && i < 48 && (k & 3) == 1 && a == 2) REDW(k >> 2, i >> 4);
            if constexpr (i == 50 && a == 3) bar();
            if constexpr (i >= 51 && i < 54) part[4 * (i - 51) + a] = REDR((4 * (i - 51) + a) / 3, (4 * (i - 51) + a) % 3 + 1);
            if constexpr (i == 54 && a == 1) bar();
            if constexpr (i >= 54 && a == 2) PIECE(Z_U, np)
            if constexpr (i >= 62 && a == 3) PIECE(Z_U, np + 1)
            if constexpr (i >= 55 && i < 59 && a == 0) {
              constexpr int q = i - 55;
              part[3 * q] = (part[3 * q] + unpack4(bb[q])) + (part[3 * q + 1] + part[3 * q + 2]);
              PINV(part[3 * q]);
            }
            SCHED();
          });
        });
        STAMP(11 + layer * 16);
        v4u uf[2];
        sfor<8>([&](auto H) INL {
          constexpr int h = H, a = h >> 1, j0 = 2 * (h & 1);
          float gl[2];
#pragma unroll
          for (int j = 0; j < 2; ++j) {
            const float u = acc[a][0][j0 + j] + part[3 * a][j0 + j];
            const float e = __builtin_amdgcn_exp2f(u * (u * u * (-0.10294324f) - 2.3022082f));
            gl[j] = u * __builtin_amdgcn_rcpf(1.f + e);
          }
          uf[h >> 2][h & 3] = pk(gl[0], gl[1]);
          if constexpr (h & 1) {
            PINV(uf[h >> 2]);
            if constexpr (h == 3) st16<SC1>(uf[0], rws, uo, 0);
            if constexpr (h == 7) st16<SC1>(uf[1], rws, uo, 4096);
            SCHED();
          }
        });
        STAMP(40 + layer * 4);
      } else {
        publish(fl + (2 * 256 + blockIdx.x) * 32, epoch);
        DPUMP(YG, YH, YE)
        STASH(SG, NS)
        DRAIN();
        DTAIL(YG, YE)
        LGKM0();
        DPUMP(Z_U, Z_U, Z_U + 3)
      }
      {
        FRESH();
        const int pi = wrap7(lane & 7);
        GAS unsigned* fp = fl + (3 * 256 + (pi == 3 ? (21 + (dt & 3)) * 8 + (dt >> 2) : (7 * (pi & 3) + dt) * 8 + (pi >> 2)) + 2 * w) * 32;
        int uv = U_OFF + layer * U_SZ + (dt * 56 + 14 * w) * 4096 + lane * 16;
        int ow = L_HST + w * 2048 + c * 128 + (swz(c) << 5) + g * 8, orr = L_HST + w * 2048 + ((wrow * 128 + (lane & 7) * 16) ^ sx0);
        int xb = layer ? (64 * st + 16 * w + wrow) * 896 + dt * 128 + (lane & 7) * 16
                       : X2_OFF + (2 * st + (w >> 1)) * 28672 + (4 * dt + (g >> 1)) * 1024 + ((g & 1) * 32 + (w & 1) * 16 + c) * 16;
        int so = oob(g < 2, S5_OFF + (st * 8 + dt) * 512 + (16 * w + c) * 8 + g * 4);
#pragma unroll
        for (int a = 0; a < 4; ++a) { res[a] += bflo(b2r); PINV(res[a]); }
        PINV(fp); PINV(uv); PINV(xb); PINV(red);
        if constexpr (layer) { PINV(ow); PINV(orr); } else PINV(so);
        if constexpr (layer == 1) {
          const int dlt = L_WQ + w * 20480 - (L_KVW + w * 16384);
#pragma unroll
          for (int i = 0; i < 4; ++i) {
            LAS char* ps = pr2[i] + dlt;
#pragma unroll
            for (int j = 0; j < NS; ++j) wz[30 - NS + j][i] = trfrag(ps + j * 4096, ps + j * 4096 + 2048);
          }
          SCHED();
        }
        if (on4) publish(fl + (3 * 256 + bid) * 32, epoch);
        STAMP(12 + layer * 16);
        unsigned pf;
        DPUMP(Z_U, Z_U + 3, YU3)
        POLL(pf, fp);
        DPUMP(Z_U, YU3, YZ)
        unsigned late;
        POLLED(pf, 4 * (YZ - YU3), fp)
        STAMP(13 + layer * 16);
        v4u af[56];
#define LOAD_U(I) af[I] = ld16<SC1>(rws, uv + ((I) / 14) * 1024, ((I) % 14) * 4096)
#pragma unroll
        for (int i = 0; i < W0; ++i) { LOAD_U(i); SCHED(); }
        vmw<W0>();
        KEEP(late);
        f32x4 acc[4][4], part[4][3];
        v2u xo[4];
        f32x2 s1 = {0.f, 0.f}, s2 = {0.f, 0.f};
        sfor<56>([&](auto I) INL {
          constexpr int i = I, a = i / 14, k = i % 14;
#pragma unroll
          for (int bq = 0; bq < 4; ++bq) {
            const int b = (bq + 1) & 3;
            if (a < 3 || b) acc[a][b] = mfma16(af[i], wz[16 + k][b], k > 0 ? acc[a][b] : (f32x4){0.f, 0.f, 0.f, 0.f});
            SCHED();
            constexpr int n0 = issued(i, 56, W0, WIN), n1 = issued(i + 1, 56, W0, WIN);
            if constexpr (n0 < n1) { if (bq == 0) LOAD_U(n0); }
            if constexpr (n0 + 1 < n1) { if (bq == 3) LOAD_U(n0 + 1); }
            if constexpr (i < 8) { if (bq == 1) DTAIL1(Z_U, YZ, 2 * i) if (bq == 2) DTAIL1(Z_U, YZ, 2 * i + 1) }
            if constexpr (a > 0 && k < 3) { if (bq == 2) REDW(a - 1, k + 1); }
            if constexpr (i == 46) { if (bq == 2) bar(); }
            if constexpr (i >= 47 && i < 50) { if (bq < 3) part[i - 47][bq] = REDR(i - 47, bq + 1); }
            if constexpr (i >= 50 && i < 53) { if (bq == 2) { res[i - 50] = ((res[i - 50] + part[i - 50][0]) + (part[i - 50][1] + part[i - 50][2])) + acc[i - 50][0]; PINV(res[i - 50]); } }
            if constexpr (i >= 53) { if (bq == 2) { xo[i - 53] = (v2u){pk(res[i - 53][0], res[i - 53][1]), pk(res[i - 53][2], res[i - 53][3])}; PINV(xo[i - 53]); if constexpr (layer == 0) { stats16(res, 1 << (i - 53), s1, s2); PINV(s1); PINV(s2); } } }
            SCHED();
          }
        });
        STAMP(14 + layer * 16);
        sfor<14>([&](auto K) INL {
          constexpr int k = K;
          acc[3][0] = mfma16(af[42 + k], wz[16 + k][0], k > 0 ? acc[3][0] : (f32x4){0.f, 0.f, 0.f, 0.f});
          SCHED();
          if constexpr (k >= 1 && k < 4) REDW(3, k);
          if constexpr (k == 5) {
            if constexpr (layer == 0) st16<SC1>((v4u){xo[0].x, xo[0].y, xo[1].x, xo[1].y}, rws, xb, 0);
            else { _Pragma("unroll") for (int a = 0; a < 3; ++a) *(LAS v2u*)(L + (ow ^ (a << 5))) = xo[a]; }
          }
          if constexpr (k == 7) { bar(); part[3][0] = REDR(3, 1); part[3][1] = REDR(3, 2); part[3][2] = REDR(3, 3); }
          SCHED();
        });
        res[3] = ((res[3] + part[3][0]) + (part[3][1] + part[3][2])) + acc[3][0];
        xo[3] = (v2u){pk(res[3][0], res[3][1]), pk(res[3][2], res[3][3])};
        if constexpr (layer == 0) {
          st16<SC1>((v4u){xo[2].x, xo[2].y, xo[3].x, xo[3].y}, rws, xb, 2048);
          SCHED();
          stats16(res, 8, s1, s2);
          __builtin_amdgcn_raw_buffer_store_b32(asu(sum_ab(s1[0] + s1[1], s2[0] + s2[1])), rws, so, 0, SC1);
          SCHED();
        } else {
          const rsrc_t ro = mk(p.out, S * D * 2);
          *(LAS v2u*)(L + (ow ^ (3 << 5))) = xo[3];
          const v4u r0 = *(LAS v4u*)(L + orr), r1 = *(LAS v4u*)(L + (orr ^ 64) + 1024);
          st16<SC1>(r0, ro, xb, 0);
          st16<SC1>(r1, ro, xb, 8 * 896);
        }
      }
    } else {
      publish(fl + (1 * 256 + blockIdx.x) * 32, epoch);
      FRESH();
      const int rw = 4 * (by - 28) + (lane >> 4) + 16 * ((lane >> 2) & 3);
      const int v1 = (512 * w + 256 + rw) * (F * 2) + (7 * (bx >> 1) + 3 * (bx & 1) + (lane & 3)) * 128;
#pragma unroll 1
      for (int i = layer * (KP / 2); i < 4; ++i) { touch(mk(p.w1, S * F * 2), v1, i * 64 * F * 2, L); vmw<1>(); }
      const int v2 = (448 * w + rw) * (S * 2) + bx * 512 + (lane & 3) * 128;
#pragma unroll 1
      for (int i = 0; i < (layer ? 7 - (Z_U - 16) / 2 : 7); ++i) { touch(mk(p.w2, S * F * 2), v2, (layer ? (i < 3 ? i + 4 : i - 3 + (Z_U - 16) / 2) : i) * 64 * S * 2, L); vmw<1>(); }
      DRAIN();
    }
    if constexpr (layer == 1) STAMP(31);
    if constexpr (layer == 0) {
      FRESH();
      const int st1 = 2 * t + (w >> 1);
      f32x16 ak, av;
      v4u wk[28], wv[28];
      LAS char* wb = WB(0) + 28672;
      PINV(wb);
      wk[0] = WFRAG(wb, 0, 0); wv[0] = WFRAG(wb, 1, 0); wk[1] = WFRAG(wb, 0, 1); wv[1] = WFRAG(wb, 1, 1);
      f32x4 ck[4], cv[4];
#pragma unroll
      for (int a = 0; a < 4; ++a) { ck[a] = CSUM(1, a); cv[a] = CSUM(2, a); PINV(ck[a]); PINV(cv[a]); }
      int xo = X2_OFF + (4 * t + w) * 28672 + lane * 16, so = S5_OFF + (st1 * 8 + hh * 4) * 512 + ((w & 1) * 32 + r32) * 8;
      int so3 = oob(!hh, so + 3 * 512);
      PINV(xo); PINV(so); PINV(so3);
      kvs += KV_SZ; oo += O_SZ; fo += 5 * 256 * 128;
      PINV(kvs); PINA(oo); PINA(fo);
      GAS unsigned* fp = flags + (4 * 256 + ((st1 & 3) * 7 + wrap7(lane & 7)) * 8 + (st1 >> 2)) * 32;
      PINV(fp);
      if (on35) publish(flags + (4 * 256 + bid) * 32, epoch);
      STAMP(15);
      unsigned pf, late;
      {
        const rsrc_t rwo = mk(p.wo, on35 ? D * D * 2 : 0);
        sfor<16>([&](auto I) INL {
          constexpr int wi = I;
          if constexpr (wi == 10) POLL(pf, fp);
          dmai<wi * 1024, (28 * (wi >> 2) + 8 * (wi & 3)) * 896>(rwo, (wi & 1) ? wvo2 ^ 64 : wvo2, pring);
        });
      }
      POLLED(pf, 6, fp)
      STAMP(18);
      v2u sp[4];
#define LOAD_X(I) xf[I] = ld16<SC1>(rws, xo + ((I) & 3) * 1024, ((I) >> 2) * 4096)
#pragma unroll
      for (int i = 0; i < WX0; ++i) { LOAD_X(i); SCHED(); }
      sfor<28>([&](auto I) INL {
        constexpr int ks = I;
        if constexpr (ks == 0) MFMA32V0(ak, wk[ks], xf[ks]); else MFMA32VA(ak, wk[ks], xf[ks]);
        if constexpr (ks + 2 < 28) wk[ks + 2] = WFRAG(wb + ((ks + 2) >> 2) * 4096, 0, (ks + 2) & 3);
        constexpr int n0 = issued(ks, 28, WX0, WINX), n1 = issued(ks + 1, 28, WX0, WINX);
        if constexpr (n0 < n1) LOAD_X(n0);
        SCHED();
        if constexpr (ks == 0) MFMA32V0(av, wv[ks], xf[ks]); else MFMA32VA(av, wv[ks], xf[ks]);
        if constexpr (ks + 2 < 28) wv[ks + 2] = WFRAG(wb + ((ks + 2) >> 2) * 4096, 1, (ks + 2) & 3);
        if constexpr (n0 + 1 < n1) LOAD_X(n0 + 1);
        if constexpr (ks >= 6 && ks < 10) sp[ks - 6] = ld8<SC1>(rws, ks == 9 ? so3 : so + (ks - 6) * 512, 0);
        if constexpr (ks == 2) KEEP(late);
        if constexpr (ks == 21) {
          float s1 = (asf(sp[0].x) + asf(sp[1].x)) + (asf(sp[2].x) + asf(sp[3].x));
          float s2 = (asf(sp[0].y) + asf(sp[1].y)) + (asf(sp[2].y) + asf(sp[3].y));
          s1 = sum32(s1); s2 = sum32(s2);
          mean = s1 * (1.f / D);
          rstd = s2 * (1.f / D) - mean * mean;
          PINV(mean); PINV(rstd);
        }
        if constexpr (ks == 22) { rstd = __builtin_amdgcn_rsqf(fmaxf(rstd, 0.f) + EPS); PINV(rstd); }
        if constexpr (ks >= 23 && ks < 27) { ck[ks - 23] *= -mean * rstd; cv[ks - 23] *= -mean * rstd; PINV(ck[ks - 23]); PINV(cv[ks - 23]); }
        SCHED();
      });
      MFMA_SETTLE2(ak, av);
#pragma unroll
      for (int i = 0; i < 16; ++i) ak[i] = ak[i] * rstd + ck[i >> 2][i & 3];
      KVOUT(ak, 0)
#pragma unroll
      for (int i = 0; i < 16; ++i) av[i] = av[i] * rstd + cv[i >> 2][i & 3];
      KVOUT(av, 1)
    }
  });
  asm volatile(".Lfk_code_to:");
  if (blockIdx.x == 0 && threadIdx.x == 0) __hip_atomic_store(ctrl, epoch, RLX_AGENT);
}

extern "C" void kernel_launch(void* const* d_in, const int* in_sizes, int n_in, void* d_out, int out_size, void* d_ws,
                              size_t ws_size, hipStream_t stream) {
  Params p;
  p.x = d_in[0]; p.wqkv = d_in[1]; p.wo = d_in[2];
  p.w1 = d_in[4]; p.b1 = d_in[5]; p.w2 = d_in[6]; p.b2 = d_in[7];
  p.out = d_out; p.ws = d_ws;
  (void)hipFuncSetAttribute((const void*)fused_kernel, hipFuncAttributeMaxDynamicSharedMemorySize, LDS_BYTES);
  fused_kernel<<<256, 256, LDS_BYTES, stream>>>(p);
}
```

```cpp
#include <hip/hip_runtime.h>
#include <utility>

#define GAS __attribute__((address_space(1)))
#define LAS __attribute__((address_space(3)))
#define DI __device__ __forceinline__
#define INL __attribute__((always_inline))
#define RLX_AGENT __ATOMIC_RELAXED, __HIP_MEMORY_SCOPE_AGENT
typedef unsigned v4u __attribute__((ext_vector_type(4)));
typedef unsigned v2u __attribute__((ext_vector_type(2)));
typedef float f32x2 __attribute__((ext_vector_type(2)));
typedef float f32x4 __attribute__((ext_vector_type(4)));
typedef float f32x16 __attribute__((ext_vector_type(16)));
typedef short s16x4 __attribute__((ext_vector_type(4)));
typedef __bf16 bf16x2 __attribute__((ext_vector_type(2)));
typedef __bf16 bf16x8 __attribute__((ext_vector_type(8)));
typedef __amdgpu_buffer_rsrc_t rsrc_t;

constexpr int S = 2048, D = 448, F = 1792;
constexpr float EPS = 1e-5f;
constexpr int FLAG_OFF = 4096;
constexpr int KV_OFF = 512 * 1024, KV_SZ = 16 * 64 * 4096;
constexpr int O_OFF = KV_OFF + 2 * KV_SZ, O_SZ = 32 * 16 * 4096;
constexpr int S3_OFF = O_OFF + 2 * O_SZ, S3_SZ = 32 * 8 * 1024;
constexpr int HT_OFF = S3_OFF + 2 * S3_SZ, HT_SZ = 7 * 64 * 4096;
constexpr int U_OFF = HT_OFF + 2 * HT_SZ, U_SZ = 7 * 56 * 4096;
constexpr int X2_OFF = U_OFF + 2 * U_SZ, X2_SZ = 64 * 28 * 1024;
constexpr int S5_OFF = X2_OFF + X2_SZ, S5_SZ = 32 * 8 * 512;
constexpr int S0_OFF = S5_OFF + S5_SZ, S0_SZ = S * 16;
constexpr int WS_BYTES = S0_OFF + S0_SZ;
constexpr int L_WQ = 0;
constexpr int L_KVW = 86016;
constexpr int L_CSP = L_KVW + 65536;
constexpr int L_CS = L_CSP + 1536 + 128;
constexpr int L_HST = L_CS + 384;
constexpr int L_DUMP = L_HST + 8192;
constexpr int LDS_BYTES = L_DUMP + 256;

static_assert(L_HST % 128 == 0 && LDS_BYTES <= 163840, "LDS map");
constexpr int SC1 = 16, OOB = 0x7ffff000;
constexpr int WIN = 12;
constexpr int NO2 = 4, NO3 = 2, NG2 = 3, NG3 = 2, NH2 = 4, NH3 = 3, NU3 = 2;
constexpr int KP = 4, MO2 = 4, MO3 = 1, MG2 = 3, MG3 = 1, NS = 5, SG = 1, SH = 2;
constexpr int W0 = 10, WINO = 9, WO1 = 9, WX0 = 9, WINX = 10;
constexpr int issued(int i, int n, int w0, int w) { const int c = w0 + i + (i < w - w0 ? i : w - w0); return c < n ? c : n; }
constexpr int Z_U = NO2 + NO3 + NG2 + NG3 + NH2 + NH3;
static_assert(Z_U >= 16 && Z_U + 3 + NU3 <= 30 && Z_U + 3 <= 30 - NS && NS <= 5 && SG + SH <= NS, "schedule");

struct Params {
  const void *x, *wqkv, *wo, *w1, *b1, *w2, *b2;
  void *out, *ws;
};

template <int... I, class Fn> DI void sfor_(std::integer_sequence<int, I...>, Fn&& f) { (f(std::integral_constant<int, I>{}), ...); }
template <int N, class Fn> DI void sfor(Fn&& f) { sfor_(std::make_integer_sequence<int, N>{}, f); }
DI float asf(unsigned u) { return __builtin_bit_cast(float, u); }
DI unsigned asu(float f) { return __builtin_bit_cast(unsigned, f); }
DI rsrc_t mk(const void* p, int bytes) { return __builtin_amdgcn_make_buffer_rsrc((void*)p, 0, bytes, 0x00020000); }
template <int AUX> DI v4u ld16(rsrc_t r, int voff, int soff) { return __builtin_amdgcn_raw_buffer_load_b128(r, voff, soff, AUX); }
template <int AUX> DI v2u ld8(rsrc_t r, int voff, int soff) { return __builtin_amdgcn_raw_buffer_load_b64(r, voff, soff, AUX); }
template <int AUX> DI void st16(v4u v, rsrc_t r, int voff, int soff) { __builtin_amdgcn_raw_buffer_store_b128(v, r, voff, soff, AUX); }
template <int AUX> DI void st8(v2u v, rsrc_t r, int voff, int soff) { __builtin_amdgcn_raw_buffer_store_b64(v, r, voff, soff, AUX); }
DI f32x4 mfma16(v4u a, v4u b, f32x4 c) {
  return __builtin_amdgcn_mfma_f32_16x16x32_bf16(__builtin_bit_cast(bf16x8, a), __builtin_bit_cast(bf16x8, b), c, 0, 0, 0);
}
DI f32x16 mfma32(v4u a, v4u b, f32x16 c) {
  return __builtin_amdgcn_mfma_f32_32x32x16_bf16(__builtin_bit_cast(bf16x8, a), __builtin_bit_cast(bf16x8, b), c, 0, 0, 0);
}
DI unsigned pk(float lo, float hi) { return __builtin_bit_cast(unsigned, __builtin_convertvector((f32x2){lo, hi}, bf16x2)); }
DI float bflo(unsigned u) { return asf(u << 16); }
DI float bfhi(unsigned u) { return asf(u & 0xffff0000u); }
DI f32x4 unpack4(v2u v) { return (f32x4){bflo(v.x), bfhi(v.x), bflo(v.y), bfhi(v.y)}; }
DI v2u trrd(LAS char* p) { return __builtin_bit_cast(v2u, __builtin_amdgcn_ds_read_tr16_b64_v4i16((LAS s16x4*)p)); }
DI v4u trfrag(LAS char* p0, LAS char* p1) { v2u a = trrd(p0), b = trrd(p1); return (v4u){a.x, a.y, b.x, b.y}; }
DI float hi32(float v) {
  unsigned u = asu(v);
  auto r = __builtin_amdgcn_permlane32_swap(u, u, false, false);
  return asf(r[1]);
}
DI float sum32(float v) {
  unsigned u = asu(v);
  auto r = __builtin_amdgcn_permlane32_swap(u, u, false, false);
  return asf(r[0]) + asf(r[1]);
}
DI float max32(float v) {
  unsigned u = asu(v);
  auto r = __builtin_amdgcn_permlane32_swap(u, u, false, false);
  return fmaxf(asf(r[0]), asf(r[1]));
}
DI float sum_ab(float a, float b) {
  auto r = __builtin_amdgcn_permlane16_swap(asu(a), asu(b), false, false);
  return sum32(asf(r[0]) + asf(r[1]));
}
template <int CTRL> DI float dpp_add(float v) { return v + asf((unsigned)__builtin_amdgcn_update_dpp(0, (int)asu(v), CTRL, 0xf, 0xf, true)); }
DI float sum_row(float v) { return dpp_add<0x140>(dpp_add<0x141>(dpp_add<0x4e>(dpp_add<0xb1>(v)))); }
DI float dot2(unsigned a, unsigned b, float c) { return __builtin_amdgcn_fdot2_f32_bf16(__builtin_bit_cast(bf16x2, a), __builtin_bit_cast(bf16x2, b), c, false); }
DI void stats16(const f32x4 (&v)[4], int n, f32x2& s1, f32x2& s2) {
#pragma unroll
  for (int a = 0; a < 4; ++a)
    if (n >> a & 1) {
      const f32x2 lo = {v[a][0], v[a][1]}, hi = {v[a][2], v[a][3]};
      s1 += lo; s2 += lo * lo; s1 += hi; s2 += hi * hi;
    }
}
#define FENCE() asm volatile("" ::: "memory")
#define SCHED() __builtin_amdgcn_sched_barrier(0)
#define DRAIN() asm volatile("s_waitcnt vmcnt(0)" ::: "memory")
#define PINV(x) asm volatile("" : "+v"(x))
#define PINA(x) asm volatile("" : "+a"(x))
#define PINS(x) asm volatile("" : "+s"(x))
#define KEEP(x) asm volatile("" :: "v"(x))
#define LGKM0() asm volatile("s_waitcnt lgkmcnt(0)" ::: "memory")
DI void bar() { LGKM0(); __builtin_amdgcn_s_barrier(); FENCE(); }
template <int N> DI void vmw() { asm volatile("s_waitcnt vmcnt(%0)" :: "n"(N) : "memory"); }
template <int LO, int MO> DI void dmai(rsrc_t r, int voff, int lds) {
  int so;
  asm volatile("s_add_u32 m0, %1, %4\n\ts_mov_b32 %0, %5\n\tbuffer_load_dwordx4 %2, %3, %0 offen lds"
               : "=&s"(so) : "s"(lds), "v"(voff), "s"(r), "n"(LO), "n"(MO) : "memory", "scc");
}
DI void touch(rsrc_t r, int voff, int soff, LAS char* L) {
  asm volatile("s_mov_b32 m0, %0\n\ts_nop 3\n\tbuffer_load_dword %1, %2, %3 offen lds"
               :: "s"((int)(size_t)(L + L_DUMP)), "v"(voff), "s"(r), "s"(soff) : "memory");
}
DI void publish(GAS unsigned* flag, unsigned epoch) {
  DRAIN();
  __builtin_amdgcn_s_waitcnt(0x0F70);
  bar();
  if (threadIdx.x == 0) __hip_atomic_store(flag, epoch, RLX_AGENT);
  FENCE();
}
#define POLL(f, ptr) asm volatile("global_load_dword %0, %1, off sc1" : "=&v"(f) : "v"(ptr) : "memory")
#define POLLED(f, N, ptr) { \
    unsigned f2_; \
    POLL(f2_, ptr); \
    asm volatile("s_waitcnt vmcnt(%1)" : "+v"(f) : "n"((N) + 1) : "memory"); \
    if (__builtin_amdgcn_ballot_w64(f != epoch) != 0ull) { \
      asm volatile("s_waitcnt vmcnt(0)" : "+v"(f2_) :: "memory"); \
      if (__builtin_amdgcn_ballot_w64(f2_ != epoch) != 0ull) wait_flags(ptr, epoch, budget); \
    } \
    FENCE(); \
    late = f2_; }
DI void wait_flags(GAS unsigned* flag, unsigned epoch, unsigned& budget) {
  FENCE();
  while (true) {
    unsigned f = __hip_atomic_load(flag, RLX_AGENT);
    if (__builtin_amdgcn_ballot_w64(f != epoch) == 0ull) break;
    if (budget == 0u) break;
    --budget;
  }
  __builtin_amdgcn_fence(__ATOMIC_ACQUIRE, "wavefront");
  FENCE();
}

DI int oob(bool in, int off) { return off | (in ? 0 : OOB); }
DI int wrap7(int v) { return v >= 7 ? v - 7 : v; }
DI int swz(int row) { return ((row >> 1) & 1) | (((row >> 3) ^ (row >> 2)) & 1) << 1; }

#define FRESH() \
  int tid = threadIdx.x, bid = blockIdx.x; \
  asm volatile("" : "+v"(tid)); \
  asm volatile("" : "+s"(bid)); \
  const int lane = tid & 63, w = __builtin_amdgcn_readfirstlane(tid >> 6), bx = bid & 7, by = bid >> 3; \
  const int c = lane & 15, g = lane >> 4, q4 = (lane & 15) >> 2, p4 = lane & 3, r32 = lane & 31, hh = lane >> 5; \
  const int by7 = __builtin_amdgcn_readfirstlane((by * 37) >> 8), bym = __builtin_amdgcn_readfirstlane(by - 7 * by7); \
  const int t = 2 * bx + (by >> 4), hd = by & 15;                \
  const int st = 4 * bx + by7, dt = bym;                         \
  const int dt4 = by < 21 ? bym : bym + 4 * (bx & 1), ft = 7 * (bx >> 1) + (by < 21 ? by7 + 4 * (bx & 1) : 3);    \
  LAS char* ring = L + L_KVW + w * 16384; \
  const int wrow = lane >> 3, sx0 = swz(wrow) << 5; \
  auto raddr = [&](int row) { return row * 128 + (swz(row) << 5) + 8 * p4; }; \
  LAS char* red = L + L_KVW + lane * 16; \
  (void)c; (void)g; (void)q4; (void)r32; (void)hh; (void)t; (void)hd; (void)st; (void)dt; (void)dt4; (void)ft; (void)ring; (void)sx0; (void)raddr; (void)red;

#define MFMA32V0(d, a, b) asm volatile("v_mfma_f32_32x32x16_bf16 %0, %1, %2, 0" : "=&v"(d) : "v"(a), "v"(b))
#define MFMA32VC(d, a, b, c) asm volatile("v_mfma_f32_32x32x16_bf16 %0, %1, %2, %3" : "=&v"(d) : "v"(a), "v"(b), "v"(c))
#define MFMA32VA(d, a, b) asm volatile("v_mfma_f32_32x32x16_bf16 %0, %1, %2, %0" : "+v"(d) : "v"(a), "v"(b))
#define MFMA_SETTLE(d) asm volatile("s_nop 11" : "+v"(d))
#define MFMA_SETTLE2(d, e) asm volatile("s_nop 11" : "+v"(d), "+v"(e))
DI float max3(float a, float b, float c) { float r; asm("v_max3_f32 %0, %1, %2, %3" : "=v"(r) : "v"(a), "v"(b), "v"(c)); return r; }

#define WB(cc) (L + L_WQ + (4 * hh + q4) * 64 + (g & 1) * 32 + 8 * p4 + (hd & 1) * 8 + (cc) * 4096)
#define WFRAG(wb, part, e) trfrag((wb) + (part) * 28672 + ((e) >> 1) * 2048 + ((e) & 1) * 512, (wb) + (part) * 28672 + ((e) >> 1) * 2048 + ((e) & 1) * 512 + 1024)
#define CSUM(part, a) (*(LAS f32x4*)(L + L_CS + ((part) * 32 + 8 * (a) + 4 * hh) * 4))
#define WZ_RD1(n, i, sl) wz[n][i] = (n) < 16 ? trfrag(pr1[i] + (sl) * 4096, pr1[(i) ^ 2] + (sl) * 4096 + 512) : trfrag(pr2[i] + (sl) * 4096, pr2[i] + (sl) * 4096 + 2048)
#define DMA_WZ1(n, m, sl) { \
    if ((n) < 16) dmai<(sl) * 4096 + (m) * 1024, (32 * ((n) & 15) + 8 * (m)) * F * 2>(rw1, ((m) & 1) ? pw1 ^ 64 : pw1, pring); \
    else dmai<(sl) * 4096 + (m) * 1024, (32 * ((n) & 15) + 8 * (m)) * S * 2>(rw2, ((m) & 1) ? pw2 ^ 64 : pw2, pring); }
#define PIECE(L0, q) { \
    constexpr int r_ = (q) / 4, m_ = (q) % 4, n_ = (L0) + r_; \
    if constexpr (m_ == 0 && r_ >= 4) asm volatile("s_waitcnt vmcnt(8) lgkmcnt(0)" ::: "memory"); \
    else if constexpr (m_ == 0 && r_ == 3) vmw<8>(); \
    DMA_WZ1(n_, m_, r_ & 3) \
    if constexpr (r_ >= 3) WZ_RD1(n_ - 3, m_, (r_ - 3) & 3); \
    SCHED(); }
#define DPUMP(L0, A, B) sfor<4 * ((B) - (A))>([&](auto I_) INL { PIECE(L0, 4 * ((A) - (L0)) + I_) });
#define STASH(A, B) if constexpr (layer == 1) sfor<4 * ((B) - (A))>([&](auto I_) INL { \
    constexpr int j_ = (A) + I_ / 4, m_ = I_ % 4; \
    dmai<j_ * 4096 + m_ * 1024, (32 * (14 - NS + j_) + 8 * m_) * S * 2>(rw2, (m_ & 1) ? pw2 ^ 64 : pw2, pstash); SCHED(); });
#define DTSTEP(L0, L1, sl) ((L1) - 1 - (((L1) - 1 - (L0) - (sl)) & 3))
#define DTAIL1(L0, L1, j) { if constexpr (DTSTEP(L0, L1, (j) >> 2) >= (L0) && DTSTEP(L0, L1, (j) >> 2) >= (L1) - 3) WZ_RD1(DTSTEP(L0, L1, (j) >> 2), (j) & 3, (j) >> 2); }
#define DTAIL(L0, L1) sfor<16>([&](auto J_) INL { constexpr int j_ = J_; DTAIL1(L0, L1, j_) }); SCHED();
#define REDW(a, b) *(LAS f32x4*)(red + w * 16384 + (((b) - 1) * 4 + (a)) * 1024) = acc[a][b]
#define REDR(a, b) (*(LAS f32x4*)(red + ((w + 4 - (b)) & 3) * 16384 + (((b) - 1) * 4 + (a)) * 1024))
__global__ void __launch_bounds__(256, 1) fused_kernel(Params p) {
  extern __shared__ __attribute__((aligned(16))) char smem[];
  LAS char* L = (LAS char*)smem;
  {
    unsigned long long pc;
    int len;
    asm volatile("s_getpc_b64 %0\n.Lfk_code_from:\n\ts_mov_b32 %1, .Lfk_code_to-.Lfk_code_from" : "=s"(pc), "=s"(len));
    touch(mk((const void*)pc, len > 0 ? len : 0), (threadIdx.x & 63) < 8 ? ((blockIdx.x >> 3) * 4 + (threadIdx.x >> 6) + (threadIdx.x & 7) * 128) * 128 : OOB, 0, L);
  }
  SCHED();
  const bool on35 = (blockIdx.x >> 3) < 28;
  const bool on4 = (blockIdx.x >> 3) < ((blockIdx.x & 1) ? 24 : 25);
  const rsrc_t rws = mk(p.ws, WS_BYTES);
  GAS unsigned* ctrl = (GAS unsigned*)p.ws;
  GAS unsigned* flags = (GAS unsigned*)((GAS char*)p.ws + FLAG_OFF);
  unsigned budget = 1u << 22;

  v4u xf[28];
  v4u kv[4];
  float mean = 0.f, rstd = 0.f;
  f32x4 res[4];
  unsigned epoch;
  int kvs, oo, t1o, wvo2, pw1, pw2, fo;
  unsigned nk1, nk2, pm[8];
  LAS char *kvw, *vw, *vr0, *wor[4], *pr1[4], *pr2[4], *wbq;
#define KVOUT(acc, h) { \
    _Pragma("unroll") for (int i = 0; i < 16; i += 2) kv[2 * (h) + (i >> 3)][(i >> 1) & 3] = pk(acc[i], acc[i + 1]); \
    if (hh) { kv[2 * (h) + 1][2] = (h) ? 0x00003f80u : 0u; kv[2 * (h) + 1][3] = 0u; } \
    const rsrc_t rkv = mk(p.ws, t < 15 ? WS_BYTES : 0);    \
    st16<SC1>(kv[2 * (h)], rkv, kvs + (h) * 2048, 0); \
    st16<SC1>(kv[2 * (h) + 1], rkv, kvs + (h) * 2048 + 1024, 0); \
    SCHED(); }
#define X128(p) ((LAS char*)(size_t)((unsigned)(size_t)(p) ^ 128u))
  {
    FRESH();
    const rsrc_t rwq = mk(p.wqkv, D * 3 * D * 2);
    const rsrc_t rx = mk(p.x, S * D * 2);
    const int gid = by * 4 + w, rg = (int)(size_t)ring;
    v4u xr0, xr1;
    asm volatile("s_nop 4\n\tbuffer_load_dword %2, %5, %6, 0 offen sc1\n\tbuffer_load_dwordx4 %0, %3, %4, 0 offen\n\tbuffer_load_dwordx4 %1, %3, %4, 0 offen offset:896"
                 : "=&v"(xr0), "=&v"(xr1), "=&v"(epoch) : "v"(oob(lane < 56, (t * 128 + hd * 8 + 2 * w) * 896 + lane * 16)), "s"(rx), "v"(0), "s"(rws) : "memory");
    touch(rwq, oob(lane < 14, gid * 2688 + 896 + lane * 128), 0, L);
    const int xvo = (t * 128 + w * 32 + wrow) * 896 + (((lane & 7) ^ (wrow >> 1)) << 4);
    const int wvo = (w * 16 + (lane >> 2)) * 2688 + hd * 56 - (hd & 1) * 8 + (lane & 3) * 16;
    const int wlb = (int)(size_t)(L + L_WQ) + w * 1024;
#define DMA_W(part, cc) dmai<(part) * 28672 + (cc) * 4096, (cc) * 64 * 2688 + (part) * 896>(rwq, wvo, wlb)
#define DMA_C(cc, m) { if constexpr ((m) < 4) dmai<((cc) & 3) * 4096 + ((m) & 3) * 1024, ((m) & 3) * 8 * 896 + (cc) * 128>(rx, ((m) & 1) ? xvo ^ 64 : xvo, rg); \
                       else DMA_W((m) - 3, cc); }
    const int tb = gid * 35 + lane, tbr = (tb * 18725) >> 18;
    int tvb = oob(lane < 35, (128 + tbr) * 2688 + 896 + (tb - 14 * tbr) * 128);
    sfor<6>([&](auto M) INL { constexpr int m = M; DMA_C(0, m) });
    SCHED();
    LAS char* xa[8];
#pragma unroll
    for (int e = 0; e < 8; ++e) { xa[e] = ring + r32 * 128 + 8 * hh + ((e ^ ((r32 >> 1) & 7)) << 4); PINV(xa[e]); }
    int epo = w == 0 ? FLAG_OFF + (bid - 256) * 128 + lane * 32768 : S3_OFF + (st * 8 + dt) * 1024 - 2 * S3_SZ + (lane >> 3) * S3_SZ + (lane & 7) * 128;
    epo = (w == 0 ? lane - 1u < 10u : w == 1 && on35 && (lane >> 4) == 1) ? epo : 0;
    int s0o = oob((lane & 15) == 0, S0_OFF + (t * 128 + hd * 8 + 2 * w) * 16 + g * 8), s0i = S0_OFF + (t * 128 + w * 32 + r32) * 16;
    PINV(s0o); PINV(s0i);
    wbq = WB(0);
    LAS char* wb = wbq + 28672;
    LAS char* cb = L + L_WQ + 28672 + (w * 16 + 8 * hh + q4) * 64 + (g & 1) * 32 + 8 * p4;
    PINV(tvb); PINV(epo); PINV(wbq); PINV(wb); PINV(cb);
#pragma unroll
    for (int i = 0; i < 8; ++i) {
      const int d = r32 - ((2 * i & 3) + 8 * (i >> 1)) - 4 * hh;
      pm[i] = ((unsigned)(d >> 31) & 0xffffu) | ((unsigned)((d - 1) >> 31) & 0xffff0000u);
      PINA(pm[i]);
    }
#pragma unroll
    for (int i = 0; i < 4; ++i) {
      wor[i] = ring + (raddr(16 * (g >> 1) + 4 * (g & 1) + q4) ^ (i << 5));
      pr1[i] = ring + (raddr(8 * g + q4) ^ (i << 5));
      pr2[i] = ring + (raddr(4 * g + q4) ^ (((i + w) & 3) << 5));
      PINA(wor[i]); PINA(pr1[i]); PINA(pr2[i]);
    }
    SCHED();
    sfor<6>([&](auto M) INL { constexpr int m = M; DMA_C(1, m) });
    asm volatile("s_mov_b32 m0, %0\n\ts_nop 3\n\tbuffer_load_dword %1, %2, 0 offen sc1 lds" :: "s"((int)(size_t)(L + L_DUMP)), "v"(epo), "s"(rws) : "memory");
    touch(rwq, tvb, 0, L);
    SCHED();
    {
      asm volatile("s_waitcnt vmcnt(15)" : "+v"(xr0), "+v"(xr1), "+v"(epoch) :: "memory");
      epoch = __builtin_amdgcn_readfirstlane(epoch) + 1u;
      float a1 = 0.f, a2 = 0.f, b1 = 0.f, b2 = 0.f;
#pragma unroll
      for (int j = 0; j < 4; ++j) { a1 = dot2(xr0[j], 0x3f803f80u, a1); a2 = dot2(xr0[j], xr0[j], a2); b1 = dot2(xr1[j], 0x3f803f80u, b1); b2 = dot2(xr1[j], xr1[j], b2); }
      const auto ra = __builtin_amdgcn_permlane16_swap(asu(a1), asu(a2), false, false), rb = __builtin_amdgcn_permlane16_swap(asu(b1), asu(b2), false, false);
      const auto rc = __builtin_amdgcn_permlane32_swap(asu(asf(ra[0]) + asf(ra[1])), asu(asf(rb[0]) + asf(rb[1])), false, false);
      st8<SC1>((v2u){asu(sum_row(asf(rc[0]) + asf(rc[1]))), epoch}, rws, s0o, 0);
      SCHED();
    }
#define READ_X1(cc, e) { \
      constexpr int ch = ((e) >> 1) * 4 + ((e) & 1); \
      const v2u a_ = *(LAS v2u*)(xa[ch] + ((cc) & 3) * 4096), b_ = *(LAS v2u*)(xa[ch + 2] + ((cc) & 3) * 4096); \
      xf[4 * (cc) + (e)] = (v4u){a_.x, a_.y, b_.x, b_.y}; }
    const v4u ones = {0x3f803f80u, 0x3f803f80u, 0x3f803f80u, 0x3f803f80u};
    f32x16 ak, av, csk = {}, csv = {};
    v4u wk[28], wv[28], wc[14], gx;
    float pp[4];
    f32x4 cc[8];
    int tvq, tvw;
    LAS float *pb, *pw;
    sfor<8>([&](auto R) INL {
      constexpr int r = R;
      if constexpr (r < 7) {
        vmw<(r < 2 ? 9 : r == 5 ? 8 : r == 6 ? 4 : 6)>();
        __builtin_amdgcn_s_barrier();
        FENCE();
        SCHED();
      }
      sfor<10>([&](auto Q) INL {
        constexpr int q = Q, e = q < 4 ? q >> 1 : q >= 6 ? (q >> 1) - 1 : -1;
        if constexpr (e >= 0 && r > 0) {
          constexpr int km = 4 * (r - 1) + e;
          if constexpr (q & 1) { if constexpr (km == 0) MFMA32V0(av, wv[km], xf[km]); else MFMA32VA(av, wv[km], xf[km]); }
          else { if constexpr (km == 0) MFMA32V0(ak, wk[km], xf[km]); else MFMA32VA(ak, wk[km], xf[km]); }
        }
        if constexpr (r < 7) {
          if constexpr (q == 4) csk = mfma32(ones, wc[2 * r], csk);
          if constexpr (q == 5) csv = mfma32(ones, wc[2 * r + 1], csv);
          SCHED();
          if constexpr (q < 2) wc[2 * r + q] = trfrag(cb + q * 28672 + r * 4096, cb + q * 28672 + r * 4096 + 256);
          if constexpr (q >= 2) { constexpr int f = q - 2; if constexpr (f & 1) wv[4 * r + f / 2] = WFRAG(wb + r * 4096, 1, f / 2); else wk[4 * r + f / 2] = WFRAG(wb + r * 4096, 0, f / 2); }
          if constexpr (q == 0 || q == 3 || q == 5 || q == 8) READ_X1(r, (q == 0 ? 0 : q == 3 ? 1 : q == 5 ? 2 : 3))
        }
        constexpr int dm = q == 1 ? 0 : q == 2 ? 1 : q == 4 ? 2 : q == 6 ? 3 : q == 7 ? 4 : q == 9 ? 5 : -1;
        if constexpr (r < 5 && dm >= 0) DMA_C(r + 2, dm)
        if constexpr (r == 4 && q == 9) { touch(rwq, tvq, 0, L); touch(mk(p.wo, D * D * 2), tvw, 0, L); }
        if constexpr (r == 6 && q == 0) asm volatile("buffer_load_dwordx4 %0, %1, %2, 0 offen sc1" : "=&v"(gx) : "v"(s0i), "s"(rws) : "memory");
        if constexpr (r == 5 && (q == 6 || q == 9)) DMA_W(0, q / 9);
        if constexpr (r == 6 && q % 3 == 2) DMA_W(0, 2 + q / 3);
        if constexpr (r == 7 && (q == 1 || q == 7)) DMA_W(0, 5 + q / 7);
        if constexpr (q == 3 || q == 8) {
          constexpr int i = 2 * r + (q == 8);
          if constexpr (i == 0) { kvs = KV_OFF + (hd * 64 + 4 * t + w) * 4096 + lane * 16; PINV(kvs); }
          if constexpr (i == 1) { oo = O_OFF + ((2 * t + (w >> 1)) * 16 + hd) * 4096 + (2 * (w & 1) + (r32 >> 4)) * 1024 + hh * 256 + c * 16; PINA(oo); }
          if constexpr (i == 2) { t1o = oob(lane < 32, (512 * (by >> 3) + 8 * ((by & 7) * 4 + w) + (lane >> 2)) * (F * 2) + (7 * (bx >> 1) + 3 * (bx & 1) + (lane & 3)) * 128); PINA(t1o); }
          if constexpr (i == 3) { wvo2 = ((112 * w + wrow) * 896 + dt * 128 + (lane & 7) * 16) ^ sx0; PINA(wvo2); }
          if constexpr (i == 4) { pw1 = ((512 * w + wrow) * (F * 2) + ft * 128 + (lane & 7) * 16) ^ sx0; PINA(pw1); }
          if constexpr (i == 5) { pw2 = ((448 * w + wrow) * (S * 2) + st * 128 + (lane & 7) * 16) ^ sx0; PINA(pw2); }
          if constexpr (i == 6) {
            const int tl = by * 98 + w * 25 + lane, trow = (tl * 9363) >> 16;
            tvq = oob(lane < 25 && w * 25 + lane < 98, trow * 2688 + (tl - 7 * trow) * 128); tvw = oob(lane < 25 && w * 25 + lane < 98, tl * 128);
            PINV(tvq); PINV(tvw);
          }
          if constexpr (i == 7) {
            const int tp = (t >= 2 && (lane & 1)) ? t - 2 : t > 0 ? t - 1 : 0;
            fo = FLAG_OFF + (((tp & 1) * 16 + hd) * 8 + (tp >> 1)) * 128;
            PINA(fo);
          }
          if constexpr (i == 8) { nk2 = (t < 2 && hh) ? 0xf149u : 0u; nk1 = (t < 1 && hh) ? 0xf149u : 0u; PINA(nk1); PINA(nk2); }
          if constexpr (i == 9) { kvw = L + L_KVW + w * 4096 + lane * 16; vw = L + L_KVW + w * 4096 + 2048 + ((lane * 16) ^ (hh << 6)); PINA(kvw); PINA(vw); }
          if constexpr (i == 10) { vr0 = L + L_KVW + w * 4096 + 2048 + ((((g & 1) * 1024 + (p4 & 1) * 512 + hh * 64 + q4 * 16) ^ ((p4 & 1) << 6) ^ ((g & 1) << 7)) + (p4 >> 1) * 8); PINA(vr0); }
          if constexpr (i == 11) {
            pb = (LAS float*)(L + L_CSP) + 32 + lane + 4 * (hd & 1);
            pw = (LAS float*)(L + (hh ? L_DUMP : L_CSP + (w * 96 + 32) * 4)) + r32;
            PINV(pb); PINV(pw);
          }
        }
        if constexpr (r == 6 && q == 7) { pw[0] = csk[0]; pw[32] = csv[0]; }
        if constexpr (r == 6 && q == 9) { bar(); pp[0] = pb[0]; pp[1] = pb[96]; pp[2] = pb[192]; pp[3] = pb[288]; }
        if constexpr (r == 7 && q == 2) *(LAS float*)(L + L_CS + (32 + lane) * 4) = (pp[0] + pp[1]) + (pp[2] + pp[3]);
        if constexpr (r == 7 && (q == 3 || q == 6)) { _Pragma("unroll") for (int j = 0; j < 4; ++j) cc[4 * (q / 6) + j] = CSUM(1, 4 * (q / 6) + j); }
        SCHED();
      });
    });
    asm volatile("s_waitcnt vmcnt(5)" : "+v"(gx) :: "memory");
    while (__builtin_amdgcn_ballot_w64((gx.y != epoch) | (gx.w != epoch)) != 0ull) {
      if (budget == 0u) break;
      --budget;
      asm volatile("buffer_load_dwordx4 %0, %1, %2, 0 offen sc1\n\ts_waitcnt vmcnt(0)" : "=&v"(gx) : "v"(s0i), "s"(rws) : "memory");
    }
    mean = asf(gx.x) * (1.f / D);
    rstd = __builtin_amdgcn_rsqf(fmaxf(asf(gx.z) * (1.f / D) - mean * mean, 0.f) + EPS);
    MFMA_SETTLE2(ak, av);
#pragma unroll
    for (int i = 0; i < 16; ++i) ak[i] = (ak[i] - mean * cc[i >> 2][i & 3]) * rstd;
    KVOUT(ak, 0)
#pragma unroll
    for (int i = 0; i < 16; ++i) av[i] = (av[i] - mean * cc[4 + (i >> 2)][i & 3]) * rstd;
    KVOUT(av, 1)
  }

  unsigned b2r;
  v2u bb[4];
  v4u wz[30][4];
  sfor<2>([&](auto LY) INL {
    constexpr int layer = LY;
    constexpr int Y0 = layer ? KP : 0, Y2 = Y0 + (layer ? MO2 : NO2), YG = Y2 + (layer ? MO3 : NO3), YG2 = YG + (layer ? MG2 : NG2),
                  YH = YG2 + (layer ? MG3 : NG3), YE = layer ? 16 : Z_U, YH2 = layer ? YE : YH + NH2, YZ = layer ? 30 - NS : 30, YU3 = layer ? YZ : 30 - NU3;
    static_assert(YH <= YH2, "schedule");
    constexpr int QPUB = layer ? 14 : 10;
    GAS unsigned* fl = flags + layer * 5 * 256 * 32;
    v4u qf[2];
    v4u wo[4][4];
    v2u rr[4];
    int pring, pstash;
    const rsrc_t rw1 = mk(p.w1, on4 ? S * F * 2 : 0);
    const rsrc_t rw2 = mk(p.w2, on35 ? S * F * 2 : 0);
    {
      const bool hi = threadIdx.x & 32;
      FRESH();
      if constexpr (layer == 0) {
        vmw<4>();
        if (t == 15) vmw<0>();
        __builtin_amdgcn_s_barrier();
        FENCE();
      }
      v4u wc[7];
      f32x16 cs = {};
      LAS char* cb = L + L_WQ + (w * 16 + 8 * hh + q4) * 64 + (g & 1) * 32 + 8 * p4;
      v4u wq[28];
#pragma unroll
      for (int ks = 0; ks < 4; ++ks) wq[ks] = WFRAG(wbq, 0, ks);
      f32x16 aq;
      const rsrc_t rwo = mk(p.wo, on35 ? D * D * 2 : 0);
      pring = (int)(size_t)ring;
      pstash = (int)(size_t)(L + L_WQ) + w * 20480;
      PINS(pring); PINS(pstash);
      GAS unsigned* fp = (GAS unsigned*)((GAS char*)p.ws + fo);
      unsigned pf;
      sfor<28>([&](auto I) INL {
        constexpr int ks = I;
        if constexpr (ks == 0) MFMA32V0(aq, wq[ks], xf[ks]); else MFMA32VA(aq, wq[ks], xf[ks]);
        if constexpr (ks + 4 < 28) wq[ks + 4] = WFRAG(wbq + ((ks + 4) >> 2) * 4096, 0, ks & 3);
        constexpr int wi = layer ? -1 : ks - QPUB - 1;
        if constexpr (wi >= 0 && wi < 16) dmai<wi * 1024, (28 * (wi >> 2) + 8 * (wi & 3)) * 896>(rwo, (wi & 1) ? wvo2 ^ 64 : wvo2, pring);
        if constexpr (layer == 0) {
          if constexpr (ks < 7) wc[ks] = trfrag(cb + ks * 4096, cb + ks * 4096 + 256);
          if constexpr (ks >= 2 && ks < 9) cs = mfma32((v4u){0x3f803f80u, 0x3f803f80u, 0x3f803f80u, 0x3f803f80u}, wc[ks - 2], cs);
          if constexpr (ks == QPUB + 1) { _Pragma("unroll") for (int q = 0; q < 4; ++q) cs[q] = ((LAS float*)(L + L_CSP) + r32 + 4 * (hd & 1))[96 * q]; }
          if constexpr (ks == QPUB + 5) *(LAS float*)(L + L_CS + r32 * 4) = (cs[0] + cs[1]) + (cs[2] + cs[3]);
        }
        if constexpr (ks == 27) POLL(pf, fp);
        if constexpr (ks == QPUB) {
          if constexpr (layer == 0) { *(LAS float*)(L + (hh ? L_DUMP : L_CSP + w * 96 * 4) + r32 * 4) = cs[0]; }
          publish(fl + (0 * 256 + bid) * 32, epoch);
        }
        SCHED();
      });
      f32x4 cq[4];
#pragma unroll
      for (int a = 0; a < 4; ++a) cq[a] = CSUM(0, a);
      MFMA_SETTLE(aq);
      const float qs = rstd * (0.18898223650461362f * 1.4426950408889634f);
#pragma unroll
      for (int a = 0; a < 4; ++a) {
        qf[a >> 1][(a & 1) * 2] = pk((aq[4 * a] - mean * cq[a][0]) * qs, (aq[4 * a + 1] - mean * cq[a][1]) * qs);
        qf[a >> 1][(a & 1) * 2 + 1] = pk((aq[4 * a + 2] - mean * cq[a][2]) * qs, (aq[4 * a + 3] - mean * cq[a][3]) * qs);
      }
      if (hi) { qf[1][2] = 0x00003f80u; qf[1][3] = 0u; }
      PINV(qf[0]); PINV(qf[1]);
      f32x16 s8;
      asm volatile("s_nop 1" : "+v"(qf[0]), "+v"(qf[1]));
      MFMA32V0(s8, kv[0], qf[0]);
      MFMA32VA(s8, kv[1], qf[1]);
      SCHED();
      unsigned late;
      POLLED(pf, 0, fp)
      v4u ra[4], rb[4];
      {
        const rsrc_t r1 = mk(p.ws, t >= 1 ? WS_BYTES : 0), r2 = mk(p.ws, t >= 2 ? WS_BYTES : 0);
        const int kvo = kvs - 32768;
        rb[0] = ld16<SC1>(r2, kvo, 0); rb[1] = ld16<SC1>(r2, kvo, 1024);
        ra[0] = ld16<SC1>(r1, kvo, 16384); ra[1] = ld16<SC1>(r1, kvo, 16384 + 1024);
        SCHED();
        rb[2] = ld16<SC1>(r2, kvo, 2048); rb[3] = ld16<SC1>(r2, kvo, 3072);
        ra[2] = ld16<SC1>(r1, kvo, 16384 + 2048); ra[3] = ld16<SC1>(r1, kvo, 16384 + 3072);
        SCHED();
      }
#pragma unroll
      for (int j = 0; j < 16; ++j) wo[j >> 2][j & 3] = trfrag(wor[j & 3] + (j >> 2) * 4096, wor[(j & 3) ^ 2] + (j >> 2) * 4096 + 1024);
      SCHED();
      bar();
      LAS char* vw1 = X128(vw) + 1024;
      LAS char* vr1 = X128(vr0);
      *(LAS v4u*)(kvw + 32768) = kv[0];
      *(LAS v4u*)(kvw + 32768 + 1024) = kv[1];
      *(LAS v4u*)(vw + 32768) = kv[2];
      *(LAS v4u*)(vw1 + 32768) = kv[3];
      int rro = (64 * st + 16 * w + c) * 896 + dt * 128 + g * 8, b2o = (64 * st + 16 * w + c) * 2, b1o = ft * 128 + g * 8;
      unsigned pml[8], mk32[16], nk1l = nk1, nk2l = nk2;
      int ool = oo, t1l = t1o;
      LAS char* vr0l = vr0;
      if constexpr (layer == 0) { PINV(rro); PINV(b2o); PINV(b1o); }
      PINV(vw1); PINV(vr1); PINV(vr0l); PINV(ool); PINV(t1l); PINV(nk1l); PINV(nk2l);
#pragma unroll
      for (int i = 0; i < 8; ++i) { pml[i] = pm[i]; PINV(pml[i]); mk32[2 * i] = (unsigned)((int)(pml[i] << 16) >> 16); mk32[2 * i + 1] = (unsigned)((int)pml[i] >> 16); PINV(mk32[2 * i]); PINV(mk32[2 * i + 1]); }
      SCHED();
      f32x16 sc[8], se;
      v4u kf[8][2];
      float mx = -1e30f, mx2 = -1e30f;
#define MAX8(v, i) asm volatile("v_max3_f32 %0, %0, %2, %3\n\tv_max3_f32 %1, %1, %4, %5\n\tv_max3_f32 %0, %0, %6, %7\n\tv_max3_f32 %1, %1, %8, %9" \
        : "+v"(mx), "+v"(mx2) : "v"(v[i]), "v"(v[(i) + 1]), "v"(v[(i) + 2]), "v"(v[(i) + 3]), "v"(v[(i) + 4]), "v"(v[(i) + 5]), "v"(v[(i) + 6]), "v"(v[(i) + 7]))
      *(LAS v4u*)(kvw) = rb[0];
      MFMA32V0(sc[0], rb[0], qf[0]);
      rb[1][2] |= nk2l;
      *(LAS v4u*)(kvw + 1024) = rb[1];
      *(LAS v4u*)(kvw + 16384) = ra[0];
      SCHED();
      MFMA32VA(sc[0], rb[1], qf[1]);
      ra[1][2] |= nk1l;
      *(LAS v4u*)(kvw + 16384 + 1024) = ra[1];
      kf[4][0] = ra[0]; kf[4][1] = ra[1];
      MFMA32V0(sc[4], kf[4][0], qf[0]);
      bar();
      KEEP(late);
      MFMA32VA(sc[4], kf[4][1], qf[1]);
#pragma unroll
      for (int j = 1; j < 8; ++j) if (j != 4) { kf[j][0] = *(LAS v4u*)(kvw + j * 4096); kf[j][1] = *(LAS v4u*)(kvw + j * 4096 + 1024); }
      PINV(sc[0]);
#pragma unroll
      for (int i = 0; i < 16; ++i) se[i] = asf((asu(sc[0][i]) & mk32[i]) | (asu(s8[i]) & ~mk32[i]));
      PINV(se);
      SCHED();
      sfor<12>([&](auto N) INL {
        constexpr int n = N, jj = n >> 1, j = jj < 3 ? jj + 1 : jj + 2, hf = n & 1;
        if constexpr (hf == 0) MFMA32V0(sc[j], kf[j][0], qf[0]); else MFMA32VA(sc[j], kf[j][1], qf[1]);
        if constexpr (n < 2) MAX8(se, 8 * hf);
        else if constexpr (n < 4) MAX8(sc[4], 8 * hf);
        else MAX8(sc[jj < 5 ? jj - 1 : jj], 8 * hf);
        if constexpr (n == 2) *(LAS v4u*)(vw) = rb[2];
        if constexpr (n == 3) *(LAS v4u*)(vw1) = rb[3];
        if constexpr (n == 4) *(LAS v4u*)(vw + 16384) = ra[2];
        if constexpr (n == 5) *(LAS v4u*)(vw1 + 16384) = ra[3];
        if constexpr (n == 6) touch(mk(p.w1, S * F * 2), t1l, layer * KP * 32 * F * 2, L);
        if constexpr (n >= 7) asm volatile("s_nop 0");
        SCHED();
      });
      MAX8(sc[6], 0); MAX8(sc[6], 8); MAX8(sc[7], 0); MAX8(sc[7], 8);
      mx = max32(max3(mx, mx2, mx2));
      f32x16 nm;
#pragma unroll
      for (int i = 0; i < 16; ++i) nm[i] = -mx;
      PINV(nm);
      bar();
      f32x16 ao;
      v4u vf[9][2], pp[9][2];
      float ex[32][4];
#define VF(j, s) vf[j][s] = trfrag(vr0l + (j) * 4096 + (s) * 256, vr1 + (j) * 4096 + (s) * 256)
      sfor<33>([&](auto N) INL {
        constexpr int n = N, j = n >> 2, q = n & 3;
        if constexpr (j == 0) {
          if constexpr (q == 0) MFMA32VC(sc[1], kf[1][0], qf[0], nm);
          if constexpr (q == 1) MFMA32VA(sc[1], kf[1][1], qf[1]);
          if constexpr (q == 2) MFMA32VC(sc[2], kf[2][0], qf[0], nm);
          if constexpr (q == 3) MFMA32VA(sc[2], kf[2][1], qf[1]);
        } else if constexpr (j < 8) {
          if constexpr (q == 1) { if constexpr (j == 1) MFMA32V0(ao, vf[0][0], pp[0][0]); else MFMA32VA(ao, vf[j - 1][0], pp[j - 1][0]); }
          if constexpr (q == 2) MFMA32VA(ao, vf[j - 1][1], pp[j - 1][1]);
          if constexpr (j + 2 < 8) {
            if constexpr (q == 0) MFMA32VC(sc[j + 2], kf[j + 2][0], qf[0], nm);
            if constexpr (q == 3) MFMA32VA(sc[j + 2], kf[j + 2][1], qf[1]);
          } else if constexpr (j == 6) {
            if constexpr (q == 0) MFMA32VA(ao, vf[8][0], pp[8][0]);
            if constexpr (q == 3) MFMA32VA(ao, vf[8][1], pp[8][1]);
          }
        } else MFMA32VA(ao, vf[7][0], pp[7][0]);
        SCHED();
        if constexpr (n < 32) {
#pragma unroll
          for (int i = 0; i < 4; ++i) { if constexpr (j == 0) ex[n][i] = __builtin_amdgcn_exp2f(se[4 * q + i] - mx); else ex[n][i] = __builtin_amdgcn_exp2f(sc[j][4 * q + i]); }
        }
        SCHED();
        if constexpr (n > 0) {
          constexpr int jp = (n - 1) >> 2, qp = (n - 1) & 3;
#pragma unroll
          for (int i = 0; i < 2; ++i) {
            const unsigned u = pk(ex[n - 1][2 * i], ex[n - 1][2 * i + 1]);
            if constexpr (jp == 0) { pp[0][qp >> 1][(2 * qp + i) & 3] = u & pml[2 * qp + i]; pp[8][qp >> 1][(2 * qp + i) & 3] = u & ~pml[2 * qp + i]; }
            else pp[jp][qp >> 1][(2 * qp + i) & 3] = u;
          }
        }
        if constexpr (layer == 0) {
          if constexpr (n >= 2 && n < 10 && q >= 2) rr[(n >> 2) * 2 + q - 2] = ld8<0>(mk(p.x, on35 ? S * D * 2 : 0), rro, ((n >> 2) * 2 + q - 2) * 32);
          if constexpr (n == 10) b2r = (unsigned)__builtin_amdgcn_raw_buffer_load_b16(mk(p.b2, S * 2), b2o, 0, 0);
          if constexpr (n >= 14 && n < 22 && q >= 2) bb[((n >> 2) - 3) * 2 + q - 2] = ld8<0>(mk(p.b1, F * 2), b1o, (((n >> 2) - 3) * 2 + q - 2) * 32);
        }
        if constexpr (n == 22) KEEP(nm);
        if constexpr (j < 8 && q < 2) VF(j, q);
        if constexpr (j == 5 && q >= 2) VF(8, q - 2);
        SCHED();
      });
      asm volatile("s_nop 1" : "+v"(pp[7][1]));
      MFMA32VA(ao, vf[7][1], pp[7][1]);
      MFMA_SETTLE(ao);
      const float inv = __builtin_amdgcn_rcpf(hi32(ao[12]));
      v4u of[2];
#pragma unroll
      for (int i = 0; i < 16; i += 2) of[i >> 3][(i >> 1) & 3] = pk(ao[i] * inv, ao[i + 1] * inv);
      if (hi) { of[1][2] = 0u; of[1][3] = 0u; }
      st16<SC1>(of[0], rws, ool, 0);
      st16<SC1>(of[1], rws, ool, 512);
      SCHED();
    }
    if (on35) {
      {
        FRESH();
        const int tq = st >> 1;
        GAS unsigned* fp = fl + (1 * 256 + (((tq & 1) * 16 + 4 * w + (lane & 3)) * 8 + (tq >> 1))) * 32;
        int oo = O_OFF + layer * O_SZ + (st * 16 + 4 * w) * 4096 + lane * 16;
        const int sb = S3_OFF + layer * S3_SZ + st * 8192 + (16 * w + c) * 16;
        int sw = oob(g < 2, sb + dt * 1024 + g * 8), sa = sb + g * 1024, sc = oob(g < 3, sb + (g + 4) * 1024);
        int ho = HT_OFF + layer * HT_SZ + (dt * 64 + 2 * st + (w >> 1)) * 4096 + (g >> 1) * 1024 + (2 * (w & 1) + (g & 1)) * 256 + c * 16;
        const int hr = 8 * (g & 1) + q4, hst = L_HST + w * 2048;
        int h0 = hst + (raddr(hr) ^ ((g >> 1) << 5)), h1 = hst + (raddr(hr + 4) ^ ((g >> 1) << 5)), hw = hst + c * 128 + (swz(c) << 5) + g * 8;
        int ob[4];
#pragma unroll
        for (int b = 0; b < 4; ++b) { ob[b] = ((b + w) & 3) * 1024; PINS(ob[b]); }
        PINV(fp); PINV(oo); PINV(sw); PINV(sa); PINV(sc); PINV(ho); PINV(h0); PINV(h1); PINV(hw); PINV(red);
        publish(fl + (1 * 256 + bid) * 32, epoch);
        unsigned pf;
        DPUMP(Y0, Y0, Y2)
        if constexpr (layer == 0) {
#pragma unroll
          for (int a = 0; a < 4; ++a) { res[a] = unpack4(rr[a]); PINV(res[a]); }
        }
        POLL(pf, fp);
        DPUMP(Y0, Y2, YG)
        unsigned late;
        POLLED(pf, 4 * (YG - Y2), fp)
        v4u of[16];
#define LOAD_O(I) of[I] = ld16<SC1>(rws, oo + ((I) & 3) * 4096, ob[(((I) >> 2) + 1) & 3])
#pragma unroll
        for (int i = 0; i < WINO; ++i) { LOAD_O(i); SCHED(); }
        vmw<WINO>();
        KEEP(late);
        f32x4 acc[4][4], part[12];
        sfor<16>([&](auto I) INL {
          constexpr int i = I, b = ((i >> 2) + 1) & 3, k = i & 3;
#pragma unroll
          for (int a = 0; a < 4; ++a) {
            acc[a][b] = mfma16(wo[k][a], of[i], k > 0 ? acc[a][b] : (f32x4){0.f, 0.f, 0.f, 0.f});
            SCHED();
            constexpr int n0 = issued(i, 16, WINO, WO1), n1 = issued(i + 1, 16, WINO, WO1);
            if constexpr (n0 < n1) { if (a == 0) LOAD_O(n0); }
            if constexpr (n0 + 1 < n1) { if (a == 3) LOAD_O(n0 + 1); }
            if constexpr (i < 12) { if (a == 1) DTAIL1(Y0, YG, i) }
            if constexpr (i >= 12 && k < 2) { if (a == 1 || a == 3) REDW(2 * k + (a >> 1), 3); }
            if constexpr (i >= 4 && i < 12) { if (a == 2) REDW(k, i >> 2); }
            if constexpr (i == 14) { if (a == 0) bar(); }
            if constexpr (i >= 14) { if (a < 3) { const int j = 6 * (i - 14) + 2 * a; part[j] = REDR(j / 3, j % 3 + 1); part[j + 1] = REDR((j + 1) / 3, (j + 1) % 3 + 1); } }
            SCHED();
          }
        });
#pragma unroll
        for (int a = 0; a < 4; ++a) res[a] = ((res[a] + part[3 * a]) + (part[3 * a + 1] + part[3 * a + 2])) + acc[a][0];
        f32x2 s1 = {0.f, 0.f}, s2 = {0.f, 0.f};
        stats16(res, 15, s1, s2);
        st8<SC1>((v2u){asu(sum_ab(s1[0] + s1[1], s2[0] + s2[1])), epoch}, rws, sw, 0);
        SCHED();
        DTAIL1(Y0, YG, 12) DTAIL1(Y0, YG, 13) DTAIL1(Y0, YG, 14) DTAIL1(Y0, YG, 15)
        bar();
        DPUMP(YG, YG, YG2)
        v4u ga, gb;
        asm volatile("buffer_load_dwordx4 %0, %2, %4, 0 offen sc1\n\tbuffer_load_dwordx4 %1, %3, %4, 0 offen sc1" : "=&v"(ga), "=&v"(gb) : "v"(sa), "v"(sc), "s"(rws) : "memory");
        DPUMP(YG, YG2, YH)
        STASH(0, SG)
        asm volatile("s_waitcnt vmcnt(%2)" : "+v"(ga), "+v"(gb) : "n"(4 * (YH - YG2 + layer * SG)) : "memory");
        while ((__builtin_amdgcn_ballot_w64((ga.y != epoch) | (ga.w != epoch)) | (__builtin_amdgcn_ballot_w64((gb.y != epoch) | (gb.w != epoch)) << 16)) != 0ull) {
          if (budget == 0u) break;
          --budget;
          asm volatile("buffer_load_dwordx4 %0, %2, %4, 0 offen sc1\n\tbuffer_load_dwordx4 %1, %3, %4, 0 offen sc1\n\ts_waitcnt vmcnt(0)"
                       : "=&v"(ga), "=&v"(gb) : "v"(sa), "v"(sc), "s"(rws) : "memory");
        }
        const unsigned t12 = asu(sum_ab(asf(ga.x) + asf(gb.x), asf(ga.z) + asf(gb.z)));
        const auto tt = __builtin_amdgcn_permlane16_swap(t12, t12, false, false);
        const float t1 = asf(tt[0]), t2 = asf(tt[1]);
        const float m = t1 * (1.f / D), var = fmaxf(t2 * (1.f / D) - m * m, 0.f);
        const float r1 = __builtin_amdgcn_rsqf(var + EPS);
        const float r = r1 * __builtin_amdgcn_rsqf(var * r1 * r1 + EPS), nr = -m * r;
#pragma unroll
        for (int a = 0; a < 4; ++a) {
          const v2u v = {pk(res[a][0] * r + nr, res[a][1] * r + nr), pk(res[a][2] * r + nr, res[a][3] * r + nr)};
          *(LAS v2u*)(L + (hw ^ (a << 5))) = v;
        }
#pragma unroll
        for (int e = 0; e < 2; ++e) st16<SC1>(trfrag(L + (h0 ^ (e << 6)), L + (h1 ^ (e << 6))), rws, ho, e * 2048);
        SCHED();
      }
      if (on4) {
        FRESH();
        const int sp = 8 * w + (lane & 7);
        GAS unsigned* fp = fl + (2 * 256 + ((sp & 3) * 7 + dt4) * 8 + (sp >> 2)) * 32;
        int hv[4];
#pragma unroll
        for (int b = 0; b < 4; ++b) { hv[b] = HT_OFF + layer * HT_SZ + (dt4 * 64 + 16 * w) * 4096 + ((b + w) & 3) * 1024 + lane * 16; PINV(hv[b]); }
        int uo = U_OFF + layer * U_SZ + (dt4 * 56 + 2 * ft) * 4096 + w * 1024 + lane * 16;
        PINV(fp); PINV(uo); PINV(red);
        publish(fl + (2 * 256 + bid) * 32, epoch);
        unsigned pf;
        DPUMP(YG, YH, YH2)
        STASH(SG, NS - SH)
        POLL(pf, fp);
        DPUMP(YG, YH2, YE)
        STASH(NS - SH, NS)
        unsigned late;
        POLLED(pf, 4 * (YE - YH2 + layer * SH), fp)
        v4u af[64];
#define LOAD_H(I) { int so_; asm volatile("s_mov_b32 %1, %4\n\tbuffer_load_dwordx4 %0, %2, %3, %1 offen sc1" \
          : "=&v"(af[I]), "=&s"(so_) : "v"(hv[(((I) >> 4) + 1) & 3]), "s"(rws), "n"(((I) & 15) * 4096) : "memory"); }
        sfor<W0>([&](auto I) INL { LOAD_H(I) SCHED(); });
        f32x4 acc[4][4], part[12];
        sfor<64>([&](auto I) INL {
          constexpr int i = I, b = ((i >> 4) + 1) & 3, k = i & 15;
          constexpr int n0 = issued(i, 64, W0, WIN), n1 = issued(i + 1, 64, W0, WIN);
          constexpr int np = i <= 54 ? 0 : i <= 62 ? i - 54 : 10;
          asm volatile("s_waitcnt vmcnt(%1)" : "+v"(af[i]) : "n"(n0 - 1 - i + np) : "memory");
          if constexpr (i == 0) KEEP(late);
          SCHED();
          sfor<4>([&](auto A) INL {
            constexpr int a = A;
            acc[a][b] = mfma16(wz[k][a], af[i], k > 0 ? acc[a][b] : (f32x4){0.f, 0.f, 0.f, 0.f});
            SCHED();
            if constexpr (n0 < n1 && a == 0) LOAD_H(n0)
            if constexpr (n0 + 1 < n1 && a == 3) LOAD_H(n0 + 1)
            if constexpr (i < 8) { if constexpr (a == 1) DTAIL1(YG, YE, 2 * i) if constexpr (a == 2) DTAIL1(YG, YE, 2 * i + 1) }
            if constexpr (i >= 48 && k < 2 && (a & 1)) REDW(2 * k + (a >> 1), 3);
            if constexpr (i >= 16 && i < 48 && (k & 3) == 1 && a == 2) REDW(k >> 2, i >> 4);
            if constexpr (i == 50 && a == 3) bar();
            if constexpr (i >= 51 && i < 54) part[4 * (i - 51) + a] = REDR((4 * (i - 51) + a) / 3, (4 * (i - 51) + a) % 3 + 1);
            if constexpr (i == 54 && a == 1) bar();
            if constexpr (i >= 54 && a == 2) PIECE(Z_U, np)
            if constexpr (i >= 62 && a == 3) PIECE(Z_U, np + 1)
            if constexpr (i >= 55 && i < 59 && a == 0) {
              constexpr int q = i - 55;
              part[3 * q] = (part[3 * q] + unpack4(bb[q])) + (part[3 * q + 1] + part[3 * q + 2]);
              PINV(part[3 * q]);
            }
            SCHED();
          });
        });
        v4u uf[2];
        sfor<8>([&](auto H) INL {
          constexpr int h = H, a = h >> 1, j0 = 2 * (h & 1);
          float gl[2];
#pragma unroll
          for (int j = 0; j < 2; ++j) {
            const float u = acc[a][0][j0 + j] + part[3 * a][j0 + j];
            const float e = __builtin_amdgcn_exp2f(u * (u * u * (-0.10294324f) - 2.3022082f));
            gl[j] = u * __builtin_amdgcn_rcpf(1.f + e);
          }
          uf[h >> 2][h & 3] = pk(gl[0], gl[1]);
          if constexpr (h & 1) {
            PINV(uf[h >> 2]);
            if constexpr (h == 3) st16<SC1>(uf[0], rws, uo, 0);
            if constexpr (h == 7) st16<SC1>(uf[1], rws, uo, 4096);
            SCHED();
          }
        });
      } else {
        publish(fl + (2 * 256 + blockIdx.x) * 32, epoch);
        DPUMP(YG, YH, YE)
        STASH(SG, NS)
        DRAIN();
        DTAIL(YG, YE)
        LGKM0();
        DPUMP(Z_U, Z_U, Z_U + 3)
      }
      {
        FRESH();
        const int pi = wrap7(lane & 7);
        GAS unsigned* fp = fl + (3 * 256 + (pi == 3 ? (21 + (dt & 3)) * 8 + (dt >> 2) : (7 * (pi & 3) + dt) * 8 + (pi >> 2)) + 2 * w) * 32;
        int uv = U_OFF + layer * U_SZ + (dt * 56 + 14 * w) * 4096 + lane * 16;
        int ow = L_HST + w * 2048 + c * 128 + (swz(c) << 5) + g * 8, orr = L_HST + w * 2048 + ((wrow * 128 + (lane & 7) * 16) ^ sx0);
        int xb = layer ? (64 * st + 16 * w + wrow) * 896 + dt * 128 + (lane & 7) * 16
                       : X2_OFF + (2 * st + (w >> 1)) * 28672 + (4 * dt + (g >> 1)) * 1024 + ((g & 1) * 32 + (w & 1) * 16 + c) * 16;
        int so = oob(g < 2, S5_OFF + (st * 8 + dt) * 512 + (16 * w + c) * 8 + g * 4);
#pragma unroll
        for (int a = 0; a < 4; ++a) { res[a] += bflo(b2r); PINV(res[a]); }
        PINV(fp); PINV(uv); PINV(xb); PINV(red);
        if constexpr (layer) { PINV(ow); PINV(orr); } else PINV(so);
        if constexpr (layer == 1) {
          const int dlt = L_WQ + w * 20480 - (L_KVW + w * 16384);
#pragma unroll
          for (int i = 0; i < 4; ++i) {
            LAS char* ps = pr2[i] + dlt;
#pragma unroll
            for (int j = 0; j < NS; ++j) wz[30 - NS + j][i] = trfrag(ps + j * 4096, ps + j * 4096 + 2048);
          }
          SCHED();
        }
        if (on4) publish(fl + (3 * 256 + bid) * 32, epoch);
        unsigned pf;
        DPUMP(Z_U, Z_U + 3, YU3)
        POLL(pf, fp);
        DPUMP(Z_U, YU3, YZ)
        unsigned late;
        POLLED(pf, 4 * (YZ - YU3), fp)
        v4u af[56];
#define LOAD_U(I) af[I] = ld16<SC1>(rws, uv + ((I) / 14) * 1024, ((I) % 14) * 4096)
#pragma unroll
        for (int i = 0; i < W0; ++i) { LOAD_U(i); SCHED(); }
        vmw<W0>();
        KEEP(late);
        f32x4 acc[4][4], part[4][3];
        v2u xo[4];
        f32x2 s1 = {0.f, 0.f}, s2 = {0.f, 0.f};
        sfor<56>([&](auto I) INL {
          constexpr int i = I, a = i / 14, k = i % 14;
#pragma unroll
          for (int bq = 0; bq < 4; ++bq) {
            const int b = (bq + 1) & 3;
            if (a < 3 || b) acc[a][b] = mfma16(af[i], wz[16 + k][b], k > 0 ? acc[a][b] : (f32x4){0.f, 0.f, 0.f, 0.f});
            SCHED();
            constexpr int n0 = issued(i, 56, W0, WIN), n1 = issued(i + 1, 56, W0, WIN);
            if constexpr (n0 < n1) { if (bq == 0) LOAD_U(n0); }
            if constexpr (n0 + 1 < n1) { if (bq == 3) LOAD_U(n0 + 1); }
            if constexpr (i < 8) { if (bq == 1) DTAIL1(Z_U, YZ, 2 * i) if (bq == 2) DTAIL1(Z_U, YZ, 2 * i + 1) }
            if constexpr (a > 0 && k < 3) { if (bq == 2) REDW(a - 1, k + 1); }
            if constexpr (i == 46) { if (bq == 2) bar(); }
            if constexpr (i >= 47 && i < 50) { if (bq < 3) part[i - 47][bq] = REDR(i - 47, bq + 1); }
            if constexpr (i >= 50 && i < 53) { if (bq == 2) { res[i - 50] = ((res[i - 50] + part[i - 50][0]) + (part[i - 50][1] + part[i - 50][2])) + acc[i - 50][0]; PINV(res[i - 50]); } }
            if constexpr (i >= 53) { if (bq == 2) { xo[i - 53] = (v2u){pk(res[i - 53][0], res[i - 53][1]), pk(res[i - 53][2], res[i - 53][3])}; PINV(xo[i - 53]); if constexpr (layer == 0) { stats16(res, 1 << (i - 53), s1, s2); PINV(s1); PINV(s2); } } }
            SCHED();
          }
        });
        sfor<14>([&](auto K) INL {
          constexpr int k = K;
          acc[3][0] = mfma16(af[42 + k], wz[16 + k][0], k > 0 ? acc[3][0] : (f32x4){0.f, 0.f, 0.f, 0.f});
          SCHED();
          if constexpr (k >= 1 && k < 4) REDW(3, k);
          if constexpr (k == 5) {
            if constexpr (layer == 0) st16<SC1>((v4u){xo[0].x, xo[0].y, xo[1].x, xo[1].y}, rws, xb, 0);
            else { _Pragma("unroll") for (int a = 0; a < 3; ++a) *(LAS v2u*)(L + (ow ^ (a << 5))) = xo[a]; }
          }
          if constexpr (k == 7) { bar(); part[3][0] = REDR(3, 1); part[3][1] = REDR(3, 2); part[3][2] = REDR(3, 3); }
          SCHED();
        });
        res[3] = ((res[3] + part[3][0]) + (part[3][1] + part[3][2])) + acc[3][0];
        xo[3] = (v2u){pk(res[3][0], res[3][1]), pk(res[3][2], res[3][3])};
        if constexpr (layer == 0) {
          st16<SC1>((v4u){xo[2].x, xo[2].y, xo[3].x, xo[3].y}, rws, xb, 2048);
          SCHED();
          stats16(res, 8, s1, s2);
          __builtin_amdgcn_raw_buffer_store_b32(asu(sum_ab(s1[0] + s1[1], s2[0] + s2[1])), rws, so, 0, SC1);
          SCHED();
        } else {
          const rsrc_t ro = mk(p.out, S * D * 2);
          *(LAS v2u*)(L + (ow ^ (3 << 5))) = xo[3];
          const v4u r0 = *(LAS v4u*)(L + orr), r1 = *(LAS v4u*)(L + (orr ^ 64) + 1024);
          st16<SC1>(r0, ro, xb, 0);
          st16<SC1>(r1, ro, xb, 8 * 896);
        }
      }
    } else {
      publish(fl + (1 * 256 + blockIdx.x) * 32, epoch);
      FRESH();
      const int rw = 4 * (by - 28) + (lane >> 4) + 16 * ((lane >> 2) & 3);
      const int v1 = (512 * w + 256 + rw) * (F * 2) + (7 * (bx >> 1) + 3 * (bx & 1) + (lane & 3)) * 128;
#pragma unroll 1
      for (int i = layer * (KP / 2); i < 4; ++i) { touch(mk(p.w1, S * F * 2), v1, i * 64 * F * 2, L); vmw<1>(); }
      const int v2 = (448 * w + rw) * (S * 2) + bx * 512 + (lane & 3) * 128;
#pragma unroll 1
      for (int i = 0; i < (layer ? 7 - (Z_U - 16) / 2 : 7); ++i) { touch(mk(p.w2, S * F * 2), v2, (layer ? (i < 3 ? i + 4 : i - 3 + (Z_U - 16) / 2) : i) * 64 * S * 2, L); vmw<1>(); }
      DRAIN();
    }
    if constexpr (layer == 0) {
      FRESH();
      const int st1 = 2 * t + (w >> 1);
      f32x16 ak, av;
      v4u wk[28], wv[28];
      LAS char* wb = WB(0) + 28672;
      PINV(wb);
      wk[0] = WFRAG(wb, 0, 0); wv[0] = WFRAG(wb, 1, 0); wk[1] = WFRAG(wb, 0, 1); wv[1] = WFRAG(wb, 1, 1);
      f32x4 ck[4], cv[4];
#pragma unroll
      for (int a = 0; a < 4; ++a) { ck[a] = CSUM(1, a); cv[a] = CSUM(2, a); PINV(ck[a]); PINV(cv[a]); }
      int xo = X2_OFF + (4 * t + w) * 28672 + lane * 16, so = S5_OFF + (st1 * 8 + hh * 4) * 512 + ((w & 1) * 32 + r32) * 8;
      int so3 = oob(!hh, so + 3 * 512);
      PINV(xo); PINV(so); PINV(so3);
      kvs += KV_SZ; oo += O_SZ; fo += 5 * 256 * 128;
      PINV(kvs); PINA(oo); PINA(fo);
      GAS unsigned* fp = flags + (4 * 256 + ((st1 & 3) * 7 + wrap7(lane & 7)) * 8 + (st1 >> 2)) * 32;
      PINV(fp);
      if (on35) publish(flags + (4 * 256 + bid) * 32, epoch);
      unsigned pf, late;
      {
        const rsrc_t rwo = mk(p.wo, on35 ? D * D * 2 : 0);
        sfor<16>([&](auto I) INL {
          constexpr int wi = I;
          if constexpr (wi == 10) POLL(pf, fp);
          dmai<wi * 1024, (28 * (wi >> 2) + 8 * (wi & 3)) * 896>(rwo, (wi & 1) ? wvo2 ^ 64 : wvo2, pring);
        });
      }
      POLLED(pf, 6, fp)
      v2u sp[4];
#define LOAD_X(I) xf[I] = ld16<SC1>(rws, xo + ((I) & 3) * 1024, ((I) >> 2) * 4096)
#pragma unroll
      for (int i = 0; i < WX0; ++i) { LOAD_X(i); SCHED(); }
      sfor<28>([&](auto I) INL {
        constexpr int ks = I;
        if constexpr (ks == 0) MFMA32V0(ak, wk[ks], xf[ks]); else MFMA32VA(ak, wk[ks], xf[ks]);
        if constexpr (ks + 2 < 28) wk[ks + 2] = WFRAG(wb + ((ks + 2) >> 2) * 4096, 0, (ks + 2) & 3);
        constexpr int n0 = issued(ks, 28, WX0, WINX), n1 = issued(ks + 1, 28, WX0, WINX);
        if constexpr (n0 < n1) LOAD_X(n0);
        SCHED();
        if constexpr (ks == 0) MFMA32V0(av, wv[ks], xf[ks]); else MFMA32VA(av, wv[ks], xf[ks]);
        if constexpr (ks + 2 < 28) wv[ks + 2] = WFRAG(wb + ((ks + 2) >> 2) * 4096, 1, (ks + 2) & 3);
        if constexpr (n0 + 1 < n1) LOAD_X(n0 + 1);
        if constexpr (ks >= 6 && ks < 10) sp[ks - 6] = ld8<SC1>(rws, ks == 9 ? so3 : so + (ks - 6) * 512, 0);
        if constexpr (ks == 2) KEEP(late);
        if constexpr (ks == 21) {
          float s1 = (asf(sp[0].x) + asf(sp[1].x)) + (asf(sp[2].x) + asf(sp[3].x));
          float s2 = (asf(sp[0].y) + asf(sp[1].y)) + (asf(sp[2].y) + asf(sp[3].y));
          s1 = sum32(s1); s2 = sum32(s2);
          mean = s1 * (1.f / D);
          rstd = s2 * (1.f / D) - mean * mean;
          PINV(mean); PINV(rstd);
        }
        if constexpr (ks == 22) { rstd = __builtin_amdgcn_rsqf(fmaxf(rstd, 0.f) + EPS); PINV(rstd); }
        if constexpr (ks >= 23 && ks < 27) { ck[ks - 23] *= -mean * rstd; cv[ks - 23] *= -mean * rstd; PINV(ck[ks - 23]); PINV(cv[ks - 23]); }
        SCHED();
      });
      MFMA_SETTLE2(ak, av);
#pragma unroll
      for (int i = 0; i < 16; ++i) ak[i] = ak[i] * rstd + ck[i >> 2][i & 3];
      KVOUT(ak, 0)
#pragma unroll
      for (int i = 0; i < 16; ++i) av[i] = av[i] * rstd + cv[i >> 2][i & 3];
      KVOUT(av, 1)
    }
  });
  asm volatile(".Lfk_code_to:");
  if (blockIdx.x == 0 && threadIdx.x == 0) __hip_atomic_store(ctrl, epoch, RLX_AGENT);
}

extern "C" void kernel_launch(void* const* d_in, const int* in_sizes, int n_in, void* d_out, int out_size, void* d_ws,
                              size_t ws_size, hipStream_t stream) {
  Params p;
  p.x = d_in[0]; p.wqkv = d_in[1]; p.wo = d_in[2];
  p.w1 = d_in[4]; p.b1 = d_in[5]; p.w2 = d_in[6]; p.b2 = d_in[7];
  p.out = d_out; p.ws = d_ws;
  (void)hipFuncSetAttribute((const void*)fused_kernel, hipFuncAttributeMaxDynamicSharedMemorySize, LDS_BYTES);
  fused_kernel<<<256, 256, LDS_BYTES, stream>>>(p);
}
```
